```python
import torch

def _softmax(x, axis=-1):
    return torch.softmax(x, dim=axis)

def _gelu(x):
    return torch.nn.functional.gelu(x, approximate='tanh')

def _silu(x):
    return torch.nn.functional.silu(x)

def _sigmoid(x):
    return torch.sigmoid(x)

def _layernorm(x, axis=-1, eps=1e-05):
    m = x.mean(dim=axis, keepdim=True)
    v = x.var(dim=axis, keepdim=True, unbiased=False)
    return (x - m) / torch.sqrt(v + eps)

def _scatter_add(tgt, idx, src, axis=0):
    return tgt.clone().index_add_(axis, idx, src)

def _segment_sum(src, seg, n):
    out = torch.zeros((n,) + src.shape[1:], dtype=src.dtype)
    return out.index_add_(0, seg, src)

def _pad(x, pw):
    flat = tuple((v for pair in reversed(pw) for v in pair))
    return torch.nn.functional.pad(x, flat)
kernel_name = 'sequential_preLN__grouped_kv_mqa__mixer_gmlp__prompt'

def setup_inputs():

    _w = lambda a: (lambda t: t.to(torch.bfloat16) if t.is_floating_point() else t)(torch.as_tensor(a))

    def _A_setup():
        B, S, D, H = (1, 2048, 1024, 32)
        Dh = D // H
        return {'x': _w((lambda *s: torch.randn(*s, dtype=torch.bfloat16) * 0.05)(B, S, D)), 'wq': _w((lambda *s: torch.randn(*s, dtype=torch.bfloat16) * 0.05)(D, H * Dh)), 'wk': _w((lambda *s: torch.randn(*s, dtype=torch.bfloat16) * 0.05)(D, Dh)), 'wv': _w((lambda *s: torch.randn(*s, dtype=torch.bfloat16) * 0.05)(D, Dh)), 'wo': _w((lambda *s: torch.randn(*s, dtype=torch.bfloat16) * 0.05)(H * Dh, D))}
    _A_in = _A_setup()

    def _B_setup():
        B, S, D, F = (1, 2048, 1024, 3584)
        Fh = F // 2
        return {'x': _w((lambda *s: torch.randn(*s, dtype=torch.bfloat16) * 0.05)(B, S, D)), 'w_in': _w((lambda *s: torch.randn(*s, dtype=torch.bfloat16) * 1.6)(D, 2 * Fh)), 'w_sp': _w(0.01 * (lambda *s: torch.randn(*s, dtype=torch.bfloat16) * 1.6)(S, S)), 'b_sp': _w(1.0 + 0.01 * (lambda *s: torch.randn(*s, dtype=torch.bfloat16) * 0.05)(S)), 'w_out': _w((lambda *s: torch.randn(*s, dtype=torch.bfloat16) * 0.05)(Fh, D))}
    _B_in = _B_setup()
    return {'A_x': _A_in['x'], 'A_wq': _A_in['wq'], 'A_wk': _A_in['wk'], 'A_wv': _A_in['wv'], 'A_wo': _A_in['wo'], 'B_x': _B_in['x'], 'B_w_in': _B_in['w_in'], 'B_w_sp': _B_in['w_sp'], 'B_b_sp': _B_in['b_sp'], 'B_w_out': _B_in['w_out']}

def reference(A_x, A_wq, A_wk, A_wv, A_wo, B_x, B_w_in, B_w_sp, B_b_sp, B_w_out):

    def _A_ref(x, wq, wk, wv, wo):
        B, S, D = x.shape
        H = 32
        Dh = D // H
        q = (lambda x, *d: x.permute(*d))((x @ wq).reshape(B, S, H, Dh), 0, 2, 1, 3)
        k = x @ wk
        v = x @ wv
        scores = torch.einsum('bhqd,bkd->bhqk', q, k) * Dh ** (-0.5)
        p = _softmax(scores, axis=-1)
        o = torch.einsum('bhqk,bkd->bhqd', p, v)
        return (lambda x, *d: x.permute(*d))(o, 0, 2, 1, 3).reshape(B, S, H * Dh) @ wo

    def _B_ref(x, w_in, w_sp, b_sp, w_out):
        B, S, D = x.shape
        F = w_in.shape[-1]
        Fh = F // 2
        z = _gelu(torch.einsum('bsd,df->bsf', x, w_in))
        z1 = z[..., :Fh].clone()
        z2 = _layernorm(z[..., Fh:], axis=-1)
        g = torch.einsum('ts,bsf->btf', w_sp, z2) + b_sp[None, :, None]
        return torch.einsum('bsf,fd->bsd', z1 * g, w_out)
    x = A_x
    h = x + _A_ref(_layernorm(x, axis=-1), A_wq, A_wk, A_wv, A_wo)
    h2 = h + _B_ref(_layernorm(h, axis=-1), B_w_in, B_w_sp, B_b_sp, B_w_out)
    return h2
```

```cpp
#include <hip/hip_runtime.h>
#include <hip/hip_bf16.h>
typedef unsigned short u16;
typedef short bf16x8 __attribute__((ext_vector_type(8)));
typedef float f32x4 __attribute__((ext_vector_type(4)));
__device__ __forceinline__ float b2f(u16 v) { return __uint_as_float((unsigned)v << 16); }
__device__ __forceinline__ u16 f2b(float f) { unsigned u = __float_as_uint(f); return (u16)((u + 0x7FFF + ((u >> 16) & 1)) >> 16); }

__global__ void ln_k(const u16* in, int ld, int n, u16* out, int ldo) {
  __shared__ float red[8];
  const u16* r = in + (size_t)blockIdx.x * ld;
  float s = 0; for (int i = threadIdx.x; i < n; i += 256) s += b2f(r[i]);
  for (int o = 32; o; o >>= 1) s += __shfl_xor(s, o);
  if ((threadIdx.x & 63) == 0) red[threadIdx.x >> 6] = s;
  __syncthreads();
  float m = (red[0] + red[1] + red[2] + red[3]) / n;
  float q = 0; for (int i = threadIdx.x; i < n; i += 256) { float d = b2f(r[i]) - m; q += d * d; }
  for (int o = 32; o; o >>= 1) q += __shfl_xor(q, o);
  if ((threadIdx.x & 63) == 0) red[4 + (threadIdx.x >> 6)] = q;
  __syncthreads();
  float rs = rsqrtf((red[4] + red[5] + red[6] + red[7]) / n + 1e-5f);
  for (int i = threadIdx.x; i < n; i += 256) out[(size_t)blockIdx.x * ldo + i] = f2b((b2f(r[i]) - m) * rs);
}
__global__ __launch_bounds__(256) void gemm_k(const u16* A, int lda, const u16* B, int ldb, u16* C, int ldc, int K, int N, int mode, const u16* res, int ldr, const u16* bias) {
  __shared__ u16 As[64][40], Bs[32][40];
  int tid = threadIdx.x, w = tid >> 6, l = tid & 63;
  int m0 = blockIdx.y * 64, n0 = blockIdx.x * 32;
  f32x4 acc[2] = {};
  for (int k0 = 0; k0 < K; k0 += 32) {
    { int r = tid >> 2, c = (tid & 3) * 8; *(bf16x8*)&As[r][c] = *(const bf16x8*)(A + (size_t)(m0 + r) * lda + k0 + c); }
    for (int i = tid; i < 1024; i += 256) { int kk = i >> 5, n = i & 31; Bs[n][kk] = B[(size_t)(k0 + kk) * ldb + n0 + n]; }
    __syncthreads();
    bf16x8 a = *(bf16x8*)&As[w * 16 + (l & 15)][(l >> 4) * 8];
    for (int j = 0; j < 2; ++j) { bf16x8 b = *(bf16x8*)&Bs[j * 16 + (l & 15)][(l >> 4) * 8];
      acc[j] = __builtin_amdgcn_mfma_f32_16x16x32_bf16(a, b, acc[j], 0, 0, 0); }
    __syncthreads();
  }
  for (int j = 0; j < 2; ++j) for (int i = 0; i < 4; ++i) {
    int r = m0 + w * 16 + (l >> 4) * 4 + i, c = n0 + j * 16 + (l & 15); float v = acc[j][i];
    if (mode == 1) v += b2f(res[(size_t)r * ldr + c]);
    if (mode == 2) { float u = 0.7978845608f * (v + 0.044715f * v * v * v); v = 0.5f * v * (1.f + tanhf(u)); }
    if (mode == 3) v = (v + b2f(bias[r])) * b2f(res[(size_t)r * ldr + c]);
    C[(size_t)r * ldc + c] = f2b(v);
  }
}
__global__ __launch_bounds__(256) void attn_k(const u16* q, const u16* k, const u16* v, u16* o) {
  __shared__ float ks[64][33], vs[64][33];
  int row = blockIdx.x * 256 + threadIdx.x;
  float qr[32], oa[32]; float m = -1e30f, l = 0;
  #pragma unroll
  for (int d = 0; d < 32; ++d) { qr[d] = b2f(q[(size_t)row * 32 + d]) * 0.17677669529f; oa[d] = 0; }
  for (int t = 0; t < 2048; t += 64) {
    __syncthreads();
    for (int i = threadIdx.x; i < 2048; i += 256) { ks[i >> 5][i & 31] = b2f(k[t * 32 + i]); vs[i >> 5][i & 31] = b2f(v[t * 32 + i]); }
    __syncthreads();
    for (int j = 0; j < 64; ++j) {
      float s = 0;
      #pragma unroll
      for (int d = 0; d < 32; ++d) s += qr[d] * ks[j][d];
      float mn = fmaxf(m, s), a = __expf(m - mn), p = __expf(s - mn); m = mn; l = l * a + p;
      #pragma unroll
      for (int d = 0; d < 32; ++d) oa[d] = oa[d] * a + p * vs[j][d];
    }
  }
  #pragma unroll
  for (int d = 0; d < 32; ++d) o[(size_t)row * 32 + d] = f2b(oa[d] / l);
}
extern "C" void kernel_launch(void* const* d_in, const int* in_sizes, int n_in, void* d_out, int out_size, void* d_ws, size_t ws_size, hipStream_t s) {
  const u16 *x = (const u16*)d_in[0], *wq = (const u16*)d_in[1], *wk = (const u16*)d_in[2], *wv = (const u16*)d_in[3], *wo = (const u16*)d_in[4];
  const u16 *win = (const u16*)d_in[6], *wsp = (const u16*)d_in[7], *bsp = (const u16*)d_in[8], *wout = (const u16*)d_in[9];
  u16* w = (u16*)d_ws; size_t M = 1 << 20;
  u16 *xn = w, *q = w + 2 * M, *k = w + 4 * M, *v = w + 5 * M, *o = w + 6 * M, *h = w + 8 * M, *hn = w + 10 * M, *z = w + 12 * M, *z2n = w + 20 * M, *g = w + 24 * M;
  ln_k<<<2048, 256, 0, s>>>(x, 1024, 1024, xn, 1024);
  gemm_k<<<dim3(32, 32), 256, 0, s>>>(xn, 1024, wq, 1024, q, 1024, 1024, 1024, 0, x, 0, x);
  gemm_k<<<dim3(1, 32), 256, 0, s>>>(xn, 1024, wk, 32, k, 32, 1024, 32, 0, x, 0, x);
  gemm_k<<<dim3(1, 32), 256, 0, s>>>(xn, 1024, wv, 32, v, 32, 1024, 32, 0, x, 0, x);
  attn_k<<<256, 256, 0, s>>>(q, k, v, o);
  gemm_k<<<dim3(32, 32), 256, 0, s>>>(o, 1024, wo, 1024, h, 1024, 1024, 1024, 1, x, 1024, x);
  ln_k<<<2048, 256, 0, s>>>(h, 1024, 1024, hn, 1024);
  gemm_k<<<dim3(112, 32), 256, 0, s>>>(hn, 1024, win, 3584, z, 3584, 1024, 3584, 2, x, 0, x);
  ln_k<<<2048, 256, 0, s>>>(z + 1792, 3584, 1792, z2n, 1792);
  gemm_k<<<dim3(56, 32), 256, 0, s>>>(wsp, 2048, z2n, 1792, g, 1792, 2048, 1792, 3, z, 3584, bsp);
  gemm_k<<<dim3(32, 32), 256, 0, s>>>(g, 1792, wout, 1024, (u16*)d_out, 1024, 1792, 1024, 1, h, 1024, x);
}
```

```cpp
#include <hip/hip_runtime.h>
#include <hip/hip_bf16.h>
typedef unsigned short u16;
typedef short bf16x8 __attribute__((ext_vector_type(8)));
typedef float f32x4 __attribute__((ext_vector_type(4)));
__device__ __forceinline__ float b2f(u16 v) { return __uint_as_float((unsigned)v << 16); }
__device__ __forceinline__ u16 f2b(float f) { unsigned u = __float_as_uint(f); return (u16)((u + 0x7FFF + ((u >> 16) & 1)) >> 16); }

__global__ void ln_k(const u16* in, int ld, int n, u16* out, int ldo) {
  __shared__ float red[8];
  const u16* r = in + (size_t)blockIdx.x * ld;
  float s = 0; for (int i = threadIdx.x; i < n; i += 256) s += b2f(r[i]);
  for (int o = 32; o; o >>= 1) s += __shfl_xor(s, o);
  if ((threadIdx.x & 63) == 0) red[threadIdx.x >> 6] = s;
  __syncthreads();
  float m = (red[0] + red[1] + red[2] + red[3]) / n;
  float q = 0; for (int i = threadIdx.x; i < n; i += 256) { float d = b2f(r[i]) - m; q += d * d; }
  for (int o = 32; o; o >>= 1) q += __shfl_xor(q, o);
  if ((threadIdx.x & 63) == 0) red[4 + (threadIdx.x >> 6)] = q;
  __syncthreads();
  float rs = rsqrtf((red[4] + red[5] + red[6] + red[7]) / n + 1e-5f);
  for (int i = threadIdx.x; i < n; i += 256) out[(size_t)blockIdx.x * ldo + i] = f2b((b2f(r[i]) - m) * rs);
}
__global__ __launch_bounds__(256) void gemm_k(const u16* A, int lda, const u16* B, int ldb, u16* C, int ldc, int K, int N, int mode, const u16* res, int ldr, const u16* bias) {
  __shared__ u16 As[64][40], Bs[32][40];
  int tid = threadIdx.x, w = tid >> 6, l = tid & 63;
  int m0 = blockIdx.y * 64, n0 = blockIdx.x * 32;
  f32x4 acc[2] = {};
  for (int k0 = 0; k0 < K; k0 += 32) {
    { int r = tid >> 2, c = (tid & 3) * 8; *(bf16x8*)&As[r][c] = *(const bf16x8*)(A + (size_t)(m0 + r) * lda + k0 + c); }
    for (int i = tid; i < 1024; i += 256) { int kk = i >> 5, n = i & 31; Bs[n][kk] = B[(size_t)(k0 + kk) * ldb + n0 + n]; }
    __syncthreads();
    bf16x8 a = *(bf16x8*)&As[w * 16 + (l & 15)][(l >> 4) * 8];
    for (int j = 0; j < 2; ++j) { bf16x8 b = *(bf16x8*)&Bs[j * 16 + (l & 15)][(l >> 4) * 8];
      acc[j] = __builtin_amdgcn_mfma_f32_16x16x32_bf16(a, b, acc[j], 0, 0, 0); }
    __syncthreads();
  }
  for (int j = 0; j < 2; ++j) for (int i = 0; i < 4; ++i) {
    int r = m0 + w * 16 + (l >> 4) * 4 + i, c = n0 + j * 16 + (l & 15); float v = acc[j][i];
    if (mode == 1) v += b2f(res[(size_t)r * ldr + c]);
    if (mode == 2) { float u = 0.7978845608f * (v + 0.044715f * v * v * v); v = 0.5f * v * (1.f + tanhf(u)); }
    if (mode == 3) v = (v + b2f(bias[r])) * b2f(res[(size_t)r * ldr + c]);
    C[(size_t)r * ldc + c] = f2b(v);
  }
}
__global__ __launch_bounds__(256) void attn_k(const u16* q, const u16* k, const u16* v, u16* o) {
  __shared__ float ks[64][33], vs[64][33];
  int row = blockIdx.x * 256 + threadIdx.x;
  float qr[32], oa[32]; float m = -1e30f, l = 0;
  #pragma unroll
  for (int d = 0; d < 32; ++d) { qr[d] = b2f(q[(size_t)row * 32 + d]) * 0.17677669529f; oa[d] = 0; }
  for (int t = 0; t < 2048; t += 64) {
    __syncthreads();
    for (int i = threadIdx.x; i < 2048; i += 256) { ks[i >> 5][i & 31] = b2f(k[t * 32 + i]); vs[i >> 5][i & 31] = b2f(v[t * 32 + i]); }
    __syncthreads();
    for (int j = 0; j < 64; ++j) {
      float s = 0;
      #pragma unroll
      for (int d = 0; d < 32; ++d) s += qr[d] * ks[j][d];
      float mn = fmaxf(m, s), a = __expf(m - mn), p = __expf(s - mn); m = mn; l = l * a + p;
      #pragma unroll
      for (int d = 0; d < 32; ++d) oa[d] = oa[d] * a + p * vs[j][d];
    }
  }
  #pragma unroll
  for (int d = 0; d < 32; ++d) o[(size_t)row * 32 + d] = f2b(oa[d] / l);
}

typedef unsigned u32x4 __attribute__((ext_vector_type(4)));
__global__ __launch_bounds__(512) void probe(const char* A, int bytes, int amp, unsigned* sink) {
  const char* s = A + (size_t)blockIdx.x * bytes + threadIdx.x * 16;
  u32x4 acc = {0,0,0,0};
  unsigned long long r0 = __builtin_amdgcn_s_memrealtime();
  __builtin_amdgcn_s_waitcnt(0xC07F);
  for (int o = 0; o < bytes; o += 8192) { asm volatile("global_load_dwordx4 %0, %1, off\n\ts_waitcnt vmcnt(16)" : "+v"(acc) : "v"(s + o)); }
  asm volatile("s_waitcnt vmcnt(0)" : "+v"(acc));
  __syncthreads();
  unsigned long long r1 = __builtin_amdgcn_s_memrealtime();
  unsigned long long lim = (r1 - r0) * amp; if (lim > 200000) lim = 200000;
  while (__builtin_amdgcn_s_memrealtime() - r1 < lim) __builtin_amdgcn_s_sleep(4);
  if (threadIdx.x == 0) sink[blockIdx.x] = acc[0];
}
__global__ void empty_k(unsigned* p) { if (p == 0) p[threadIdx.x] = 0; }
extern "C" void kernel_launch(void* const* d_in, const int* in_sizes, int n_in, void* d_out, int out_size, void* d_ws, size_t ws_size, hipStream_t s) {
  const u16 *x = (const u16*)d_in[0], *wq = (const u16*)d_in[1], *wk = (const u16*)d_in[2], *wv = (const u16*)d_in[3], *wo = (const u16*)d_in[4];
  const u16 *win = (const u16*)d_in[6], *wsp = (const u16*)d_in[7], *bsp = (const u16*)d_in[8], *wout = (const u16*)d_in[9];
  u16* w = (u16*)d_ws; size_t M = 1 << 20;
  u16 *xn = w, *q = w + 2 * M, *k = w + 4 * M, *v = w + 5 * M, *o = w + 6 * M, *h = w + 8 * M, *hn = w + 10 * M, *z = w + 12 * M, *z2n = w + 20 * M, *g = w + 24 * M;
  probe<<<256,512,0,s>>>((const char*)wsp, 32768, 0, (unsigned*)((char*)d_ws + (100u<<20)));
  probe<<<256,512,0,s>>>((const char*)wsp, 32768, 100, (unsigned*)((char*)d_ws + (100u<<20)));
  ln_k<<<2048, 256, 0, s>>>(x, 1024, 1024, xn, 1024);
  gemm_k<<<dim3(32, 32), 256, 0, s>>>(xn, 1024, wq, 1024, q, 1024, 1024, 1024, 0, x, 0, x);
  gemm_k<<<dim3(1, 32), 256, 0, s>>>(xn, 1024, wk, 32, k, 32, 1024, 32, 0, x, 0, x);
  gemm_k<<<dim3(1, 32), 256, 0, s>>>(xn, 1024, wv, 32, v, 32, 1024, 32, 0, x, 0, x);
  attn_k<<<256, 256, 0, s>>>(q, k, v, o);
  gemm_k<<<dim3(32, 32), 256, 0, s>>>(o, 1024, wo, 1024, h, 1024, 1024, 1024, 1, x, 1024, x);
  ln_k<<<2048, 256, 0, s>>>(h, 1024, 1024, hn, 1024);
  gemm_k<<<dim3(112, 32), 256, 0, s>>>(hn, 1024, win, 3584, z, 3584, 1024, 3584, 2, x, 0, x);
  ln_k<<<2048, 256, 0, s>>>(z + 1792, 3584, 1792, z2n, 1792);
  gemm_k<<<dim3(56, 32), 256, 0, s>>>(wsp, 2048, z2n, 1792, g, 1792, 2048, 1792, 3, z, 3584, bsp);
  gemm_k<<<dim3(32, 32), 256, 0, s>>>(g, 1792, wout, 1024, (u16*)d_out, 1024, 1792, 1024, 1, h, 1024, x);
}
```

```cpp
#include <hip/hip_runtime.h>
#include <hip/hip_bf16.h>

__global__ __attribute__((aligned(4096))) __launch_bounds__(256, 1) void preln_mqa_gmlp_kernel(
    const void* x, const void* wq, const void* wk, const void* wv, const void* wo, const void* win,
    const void* wsp, const void* bsp, const void* wout, void* out, void* ws) {
  unsigned tid = threadIdx.x;
  asm volatile(
".Lmk_entry:\n"
"s_getpc_b64 s[72:73]\n"
"s_load_dwordx16 s[4:19], %0, 0\n"
"s_load_dwordx4 s[20:23], %0, 64\n"
"s_load_dwordx2 s[24:25], %0, 80\n"
"s_mov_b32 s27, %1\n"
"s_and_b32 s42, %1, 7\n"
"s_lshr_b32 s43, %1, 3\n"
"v_lshrrev_b32 v11, 6, v0\n"
"v_and_b32 v1, 63, v0\n"
"v_readfirstlane_b32 s41, v11\n"
"v_lshlrev_b32 v2, 4, v1\n"
"v_add_u32 v3, 4096, v2\n"
"v_add_u32 v4, 8192, v2\n"
"v_add_u32 v5, 12288, v2\n"
"v_mov_b32 v6, v2\n"
"v_add_u32 v7, 0x10000, v2\n"
"v_add_u32 v8, 0x20000, v2\n"
"v_mov_b32 v9, 0\n"
"v_mov_b32 v10, 1\n"
"s_mov_b32 s55, 0\n"
"s_lshl_b32 s53, s41, 12\n"
"s_mul_i32 s54, s41, 7168\n"
"s_and_b32 s72, s72, 0xfffff000\n"
"s_and_b32 s73, s73, 0xffff\n"
"s_mov_b32 s74, 83836\n"
"s_mov_b32 s75, 0x20000\n"
"s_lshl_b32 s56, s43, 2\n"
"s_add_u32 s56, s56, s41\n"
"s_lshl_b32 s56, s56, 10\n"
"v_lshl_add_u32 v12, v1, 7, s56\n"
"s_mov_b64 exec, 0xff\n"
"buffer_load_dword v11, v12, s[72:75], 0 offen\n"
"s_mov_b64 exec, -1\n"
"s_mov_b32 s31, 0x20000\n"
"s_mov_b32 s35, 0x20000\n"
"s_mov_b32 s39, 0x20000\n"
"s_xor_b32 s56, s42, 1\n"
"s_brev_b32 s56, s56\n"
"s_lshr_b32 s56, s56, 30\n"
"s_lshl_b32 s56, s56, 3\n"
"s_and_b32 s57, s43, 7\n"
"s_add_u32 s44, s56, s57\n"
"s_lshr_b32 s56, s42, 2\n"
"s_lshl_b32 s56, s56, 2\n"
"s_lshr_b32 s57, s43, 3\n"
"s_add_u32 s45, s56, s57\n"
"s_lshl_b32 s40, s27, 2\n"
"s_add_u32 s40, s40, s41\n"
"v_bfe_u32 v15, v1, 3, 2\n"
"v_mul_u32_u24 v18, 2048, v15\n"
"v_bfe_u32 v15, v1, 5, 1\n"
"v_lshl_add_u32 v18, v15, 14, v18\n"
"v_lshrrev_b32 v12, 4, v1\n"
"v_xor_b32 v12, v12, v1\n"
"v_and_b32 v12, 7, v12\n"
"v_xor_b32 v13, 4, v12\n"
"v_lshl_add_u32 v19, v13, 4, v18\n"
"v_lshl_add_u32 v18, v12, 4, v18\n"
"v_add_u32 v19, 31744, v19\n"
"v_lshlrev_b32 v14, 7, v1\n"
"s_lshr_b32 s58, s45, 2\n"
"s_bfe_u32 s59, s45, 0x10001\n"
"s_mul_i32 s66, s44, 131072\n"
"s_mul_i32 s56, s58, 65536\n"
"s_add_u32 s66, s66, s56\n"
"s_mul_i32 s56, s59, 8192\n"
"s_add_u32 s66, s66, s56\n"
"s_mul_i32 s56, s41, 512\n"
"s_add_u32 s66, s66, s56\n"
"s_add_u32 s66, s66, 0\n"
"s_lshl_b32 s67, s41, 14\n"
"s_lshl_b32 s69, s41, 11\n"
"s_add_u32 s70, s53, 8192\n"
"s_lshl_b32 s68, s45, 1\n"
"s_add_u32 s68, s68, s43\n"
"s_lshl_b32 s60, s40, 11\n"
"s_and_b32 s58, s43, 7\n"
"s_mov_b32 s83, 0x20000\n"
"s_mul_i32 s57, s58, 8192\n"
"s_mul_i32 s56, s59, 4096\n"
"s_add_u32 s57, s57, s56\n"
"s_mul_i32 s56, s41, 1024\n"
"s_add_u32 s57, s57, s56\n"
"s_add_u32 s57, s57, 0\n"
"s_waitcnt lgkmcnt(0)\n"
"s_mov_b32 s36, s6\n"
"s_and_b32 s37, s7, 0xffff\n"
"s_mov_b32 s38, 2097152\n"
"s_bitcmp1_b32 s45, 0\n"
"s_cselect_b32 s80, s10, s8\n"
"s_cselect_b32 s81, s11, s9\n"
"s_and_b32 s81, s81, 0xffff\n"
"s_mov_b32 s82, 65536\n"
"s_mov_b32 s32, s4\n"
"s_and_b32 s33, s5, 0xffff\n"
"s_mov_b32 s34, 4194304\n"
"s_mov_b64 exec, 0xff\n"
"buffer_load_dword v11, v14, s[80:83], s57 offen\n"
"s_mov_b64 exec, -1\n"
"s_add_u32 s58, s68, 0\n"
"s_and_b32 s58, s58, 3\n"
"s_add_u32 m0, s69, 49152\n"
"s_lshl_b32 s64, s58, 7\n"
"s_add_u32 s64, s64, s66\n"
"buffer_load_dwordx4 v18, s[32:35], s64 offen offset:0 lds\n"
"buffer_load_dwordx4 v19, s[32:35], s64 offen offset:1024 lds\n"
"s_add_u32 s58, s68, 1\n"
"s_and_b32 s58, s58, 3\n"
"s_add_u32 m0, s69, 73728\n"
"s_lshl_b32 s64, s58, 7\n"
"s_add_u32 s64, s64, s66\n"
"buffer_load_dwordx4 v18, s[32:35], s64 offen offset:0 lds\n"
"buffer_load_dwordx4 v19, s[32:35], s64 offen offset:1024 lds\n"
"s_add_u32 s58, s68, 2\n"
"s_and_b32 s58, s58, 3\n"
"s_add_u32 m0, s69, 98304\n"
"s_lshl_b32 s64, s58, 7\n"
"s_add_u32 s64, s64, s66\n"
"buffer_load_dwordx4 v18, s[32:35], s64 offen offset:0 lds\n"
"buffer_load_dwordx4 v19, s[32:35], s64 offen offset:1024 lds\n"
"s_add_u32 s58, s68, 3\n"
"s_and_b32 s58, s58, 3\n"
"s_add_u32 m0, s69, 122880\n"
"s_lshl_b32 s64, s58, 7\n"
"s_add_u32 s64, s64, s66\n"
"buffer_load_dwordx4 v18, s[32:35], s64 offen offset:0 lds\n"
"buffer_load_dwordx4 v19, s[32:35], s64 offen offset:1024 lds\n"
"s_mov_b64 exec, 0xffff\n"
"buffer_load_dword v11, v14, s[36:39], s60 offen\n"
"s_mov_b64 exec, -1\n"
"s_mov_b64 exec, 0\n"
"s_cmp_eq_u32 s41, 0\n"
"s_cbranch_scc1 .Lmk_pg_1\n"
"s_cmp_eq_u32 s41, 1\n"
"s_cbranch_scc1 .Lmk_pg_2\n"
"s_cmp_eq_u32 s41, 2\n"
"s_cbranch_scc1 .Lmk_ret_0\n"
"s_branch .Lmk_ret_0\n"
".Lmk_ret_0:\n"
"s_mov_b64 exec, -1\n"
"s_nop 0\n"
"s_load_dword s26, s[24:25], 65536\n"
"s_mov_b32 s28, s24\n"
"s_and_b32 s29, s25, 0xffff\n"
"s_mov_b32 s30, 53477376\n"
"s_xor_b32 s56, s42, 1\n"
"s_brev_b32 s56, s56\n"
"s_lshr_b32 s56, s56, 30\n"
"s_lshl_b32 s56, s56, 3\n"
"s_and_b32 s57, s43, 7\n"
"s_add_u32 s46, s56, s57\n"
"s_lshr_b32 s56, s42, 2\n"
"s_lshl_b32 s56, s56, 2\n"
"s_lshr_b32 s57, s43, 3\n"
"s_add_u32 s47, s56, s57\n"
"s_and_b32 s56, s42, 1\n"
"s_lshl_b32 s56, s56, 3\n"
"s_and_b32 s57, s43, 7\n"
"s_add_u32 s48, s56, s57\n"
"s_lshr_b32 s56, s42, 1\n"
"s_lshl_b32 s56, s56, 2\n"
"s_lshr_b32 s57, s43, 3\n"
"s_add_u32 s49, s56, s57\n"
"s_xor_b32 s56, s42, 1\n"
"s_brev_b32 s56, s56\n"
"s_lshr_b32 s56, s56, 30\n"
"s_lshl_b32 s56, s56, 3\n"
"s_and_b32 s57, s43, 7\n"
"s_add_u32 s51, s56, s57\n"
"s_lshr_b32 s56, s42, 2\n"
"s_lshl_b32 s56, s56, 2\n"
"s_lshr_b32 s57, s43, 3\n"
"s_add_u32 s52, s56, s57\n"
"s_lshl_b32 s50, s48, 2\n"
"s_add_u32 s50, s50, s41\n"
"v_lshrrev_b32 v12, 4, v1\n"
"v_lshlrev_b32 v13, 1, v12\n"
"v_xor_b32 v13, v13, v1\n"
"v_and_b32 v13, 15, v13\n"
"v_lshlrev_b32 v12, 11, v12\n"
"v_lshl_add_u32 v20, v13, 4, v12\n"
"v_xor_b32 v22, 128, v20\n"
"v_add_u32 v21, 7168, v20\n"
"v_add_u32 v23, 21504, v22\n"
"v_add_u32 v22, 14336, v22\n"
"v_mov_b32 v252, 0x3f803f80\n"
"v_mov_b32 v253, 0x3f803f80\n"
"v_mov_b32 v254, 0x3f803f80\n"
"v_mov_b32 v255, 0x3f803f80\n"
"v_bfe_u32 v12, v1, 1, 3\n"
"v_lshrrev_b32 v13, 4, v1\n"
"v_xor_b32 v12, v12, v13\n"
"v_and_b32 v13, 15, v1\n"
"v_lshlrev_b32 v13, 7, v13\n"
"v_lshl_add_u32 v28, v12, 4, v13\n"
"v_xor_b32 v29, 64, v28\n"
"v_bfe_u32 v15, v1, 2, 2\n"
"v_mul_u32_u24 v12, 32, v15\n"
"v_bfe_u32 v15, v1, 4, 1\n"
"v_lshl_add_u32 v12, v15, 7, v12\n"
"s_lshl_b32 s58, s41, 6\n"
"v_xor_b32 v12, s58, v12\n"
"v_bfe_u32 v15, v1, 0, 2\n"
"v_mul_u32_u24 v34, 8, v15\n"
"v_bfe_u32 v15, v1, 2, 2\n"
"v_lshl_add_u32 v34, v15, 8, v34\n"
"v_bfe_u32 v15, v1, 4, 2\n"
"v_lshl_add_u32 v34, v15, 11, v34\n"
"v_add_u32 v34, 8192, v34\n"
"v_add_u32 v34, v34, v12\n"
"v_xor_b32 v35, 32, v34\n"
"v_add_u32 v30, 49152, v28\n"
"v_add_u32 v31, 49152, v29\n"
"v_add_u32 v32, 98304, v28\n"
"v_add_u32 v33, 98304, v29\n"
"v_add_u32 v36, 49152, v34\n"
"v_add_u32 v37, 49152, v35\n"
"v_add_u32 v38, 98304, v34\n"
"v_add_u32 v39, 98304, v35\n"
"v_add_u32 v40, s69, v30\n"
"v_add_u32 v41, s69, v31\n"
"v_add_u32 v42, s69, v32\n"
"v_add_u32 v43, s69, v33\n"
"v_bfe_u32 v15, v1, 0, 2\n"
"v_mul_u32_u24 v44, 8, v15\n"
"v_bfe_u32 v15, v1, 2, 2\n"
"v_lshl_add_u32 v44, v15, 6, v44\n"
"v_bfe_u32 v15, v1, 4, 2\n"
"v_lshl_add_u32 v44, v15, 9, v44\n"
"v_add_u32 v44, s70, v44\n"
"v_add_u32 v45, 98304, v44\n"
"v_add_u32 v44, 49152, v44\n"
"v_and_b32 v12, 3, v1\n"
"v_cmp_eq_u32 s[74:75], 1, v12\n"
"v_cmp_eq_u32 s[76:77], 2, v12\n"
"v_cmp_eq_u32 s[78:79], 3, v12\n"
"s_mul_i32 s58, s41, 3072\n"
"s_add_u32 s58, s58, 147456\n"
"v_add_u32 v166, s58, v2\n"
"v_lshl_add_u32 v167, v1, 3, s58\n"
"v_bfe_u32 v15, v1, 0, 4\n"
"v_mul_u32_u24 v168, 8, v15\n"
"v_bfe_u32 v15, v1, 2, 2\n"
"v_lshl_add_u32 v168, v15, 7, v168\n"
"v_add_u32 v168, 149504, v168\n"
"v_mov_b32 v228, 0\n"
"v_mov_b32 v229, 0\n"
"v_mov_b32 v230, 0\n"
"v_mov_b32 v231, 0\n"
"v_mov_b32 v232, 0\n"
"v_mov_b32 v233, 0\n"
"v_mov_b32 v234, 0\n"
"v_mov_b32 v235, 0\n"
"v_and_b32 v17, 7, v1\n"
"v_mov_b32 v225, 0x3f803f80\n"
"v_cmp_eq_u32 vcc, 0, v17\n"
"s_nop 1\n"
"v_cndmask_b32 v224, 0, v225, vcc\n"
"v_cmp_eq_u32 vcc, 4, v17\n"
"s_nop 1\n"
"v_cndmask_b32 v226, 0, v225, vcc\n"
"v_mov_b32 v225, v224\n"
"v_mov_b32 v227, v226\n"
"v_mov_b32 v17, 0xdddddddd\n"
"s_and_b32 s5, s5, 0xffff\n"
"s_and_b32 s13, s13, 0xffff\n"
"s_and_b32 s15, s15, 0xffff\n"
"s_and_b32 s17, s17, 0xffff\n"
"s_and_b32 s21, s21, 0xffff\n"
"s_lshl_b32 s71, s41, 13\n"
"s_sub_u32 s25, s40, 128\n"
"s_cmp_lt_u32 s40, 896\n"
"s_cselect_b32 s25, s40, s25\n"
"v_bfe_u32 v15, v1, 2, 4\n"
"v_mul_u32_u24 v188, 2048, v15\n"
"v_bfe_u32 v15, v1, 0, 2\n"
"v_lshl_add_u32 v188, v15, 4, v188\n"
"v_bfe_u32 v15, v1, 3, 3\n"
"v_mul_u32_u24 v189, 7168, v15\n"
"v_bfe_u32 v15, v1, 0, 3\n"
"v_lshl_add_u32 v189, v15, 4, v189\n"
"v_bfe_u32 v15, v1, 3, 3\n"
"v_mul_u32_u24 v190, 2048, v15\n"
"v_bfe_u32 v15, v1, 0, 3\n"
"v_lshl_add_u32 v190, v15, 4, v190\n"
"v_bfe_u32 v15, v1, 3, 3\n"
"v_mul_u32_u24 v191, 4096, v15\n"
"v_bfe_u32 v15, v1, 0, 3\n"
"v_lshl_add_u32 v191, v15, 4, v191\n"
"v_bfe_u32 v15, v1, 0, 2\n"
"v_mul_u32_u24 v184, 16, v15\n"
"s_mov_b64 s[72:73], s[56:57]\n"
"s_mov_b32 s61, s59\n"
"s_mov_b32 s63, s60\n"
"v_mov_b32 v13, v15\n"
"s_add_u32 s65, s68, 0\n"
"s_and_b32 s65, s65, 3\n"
"s_add_u32 m0, s70, 49152\n"
"s_lshl_b32 s65, s65, 12\n"
"s_add_u32 s65, s65, s67\n"
"v_bfe_u32 v15, v1, 0, 4\n"
"v_mul_u32_u24 v169, 16, v15\n"
"v_bfe_u32 v15, v1, 4, 1\n"
"v_lshl_add_u32 v169, v15, 10, v169\n"
"v_bfe_u32 v15, v1, 5, 1\n"
"buffer_load_dwordx4 v2, s[80:83], s65 offen offset:0 lds\n"
"v_lshl_add_u32 v169, v15, 9, v169\n"
"v_bfe_u32 v15, v1, 0, 4\n"
"v_mul_u32_u24 v170, 64, v15\n"
"v_bfe_u32 v15, v1, 4, 2\n"
"v_lshl_add_u32 v170, v15, 4, v170\n"
"buffer_load_dwordx4 v2, s[80:83], s65 offen offset:1024 lds\n"
"v_add_u32 v170, 159744, v170\n"
"v_bfe_u32 v15, v1, 0, 2\n"
"v_mul_u32_u24 v171, 8, v15\n"
"v_bfe_u32 v15, v1, 2, 2\n"
"v_lshl_add_u32 v171, v15, 6, v171\n"
"buffer_load_dwordx4 v2, s[80:83], s65 offen offset:2048 lds\n"
"v_bfe_u32 v15, v1, 4, 1\n"
"v_lshl_add_u32 v171, v15, 5, v171\n"
"v_bfe_u32 v15, v1, 5, 1\n"
"v_lshl_add_u32 v171, v15, 9, v171\n"
"buffer_load_dwordx4 v2, s[80:83], s65 offen offset:3072 lds\n"
"v_add_u32 v171, 159744, v171\n"
"s_lshr_b32 s57, s45, 2\n"
"s_bfe_u32 s59, s45, 0x10001\n"
"s_lshr_b32 s60, s45, 1\n"
"s_mul_i32 s87, s44, 512\n"
"s_add_u32 s65, s68, 1\n"
"s_and_b32 s65, s65, 3\n"
"s_add_u32 m0, s70, 73728\n"
"s_lshl_b32 s65, s65, 12\n"
"s_add_u32 s65, s65, s67\n"
"s_mul_i32 s56, s60, 128\n"
"s_add_u32 s87, s87, s56\n"
"s_add_u32 s87, s87, 98304\n"
"s_mul_i32 s60, s59, 1024\n"
"s_add_u32 s60, s60, 2048\n"
"buffer_load_dwordx4 v2, s[80:83], s65 offen offset:0 lds\n"
"s_lshl_b32 s59, s59, 8\n"
"s_bitcmp1_b32 s45, 0\n"
"s_cselect_b32 s59, s60, s59\n"
"s_mul_i32 s86, s44, 8192\n"
"s_mul_i32 s56, s57, 4096\n"
"buffer_load_dwordx4 v2, s[80:83], s65 offen offset:1024 lds\n"
"s_add_u32 s86, s86, s56\n"
"s_mul_i32 s56, s59, 1\n"
"s_add_u32 s86, s86, s56\n"
"s_add_u32 s86, s86, 524288\n"
"s_waitcnt lgkmcnt(0)\n"
"s_add_u32 s26, s26, 1\n"
"buffer_load_dwordx4 v2, s[80:83], s65 offen offset:2048 lds\n"
"s_lshl_b32 s58, s27, 8\n"
"s_mov_b64 exec, 3\n"
"buffer_store_dword v9, v14, s[28:31], s58 offen sc1\n"
"s_mov_b64 exec, -1\n"
"s_lshr_b32 s57, s41, 1\n"
"s_and_b32 s59, s41, 1\n"
"s_mul_i32 s66, s44, 131072\n"
"s_mul_i32 s56, s57, 65536\n"
"buffer_load_dwordx4 v2, s[80:83], s65 offen offset:3072 lds\n"
"s_add_u32 s66, s66, s56\n"
"s_mul_i32 s56, s59, 8192\n"
"s_add_u32 s66, s66, s56\n"
"s_add_u32 s66, s66, 0\n"
"s_waitcnt vmcnt(10)\n"
"ds_read_b128 v[46:49], v40 offset:0\n"
"ds_read_b128 v[50:53], v41 offset:0\n"
"ds_read_b128 v[54:57], v40 offset:24576\n"
"ds_read_b128 v[58:61], v41 offset:24576\n"
"ds_read_b128 v[62:65], v42 offset:0\n"
"ds_read_b128 v[66:69], v43 offset:0\n"
"ds_read_b128 v[70:73], v42 offset:24576\n"
"ds_read_b128 v[74:77], v43 offset:24576\n"
"s_add_u32 s65, s68, 2\n"
"s_and_b32 s65, s65, 3\n"
"s_add_u32 m0, s70, 98304\n"
"s_lshl_b32 s65, s65, 12\n"
"s_add_u32 s65, s65, s67\n"
"buffer_load_dwordx4 v2, s[80:83], s65 offen offset:0 lds\n"
"buffer_load_dwordx4 v2, s[80:83], s65 offen offset:1024 lds\n"
"buffer_load_dwordx4 v2, s[80:83], s65 offen offset:2048 lds\n"
"buffer_load_dwordx4 v2, s[80:83], s65 offen offset:3072 lds\n"
"s_waitcnt lgkmcnt(7)\n"
"v_mfma_f32_16x16x32_bf16 v[150:153], v[46:49], v[46:49], 0\n"
"s_add_u32 s65, s68, 3\n"
"s_and_b32 s65, s65, 3\n"
"s_add_u32 m0, s70, 122880\n"
"s_lshl_b32 s65, s65, 12\n"
"s_add_u32 s65, s65, s67\n"
"v_mfma_f32_16x16x32_bf16 v[154:157], v[252:255], v[46:49], 0\n"
"s_waitcnt lgkmcnt(6)\n"
"v_mfma_f32_16x16x32_bf16 v[150:153], v[50:53], v[50:53], v[150:153]\n"
"v_mfma_f32_16x16x32_bf16 v[154:157], v[252:255], v[50:53], v[154:157]\n"
"buffer_load_dwordx4 v2, s[80:83], s65 offen offset:0 lds\n"
"s_waitcnt lgkmcnt(5)\n"
"v_mfma_f32_16x16x32_bf16 v[150:153], v[54:57], v[54:57], v[150:153]\n"
"v_mfma_f32_16x16x32_bf16 v[154:157], v[252:255], v[54:57], v[154:157]\n"
"s_waitcnt lgkmcnt(4)\n"
"v_mfma_f32_16x16x32_bf16 v[150:153], v[58:61], v[58:61], v[150:153]\n"
"buffer_load_dwordx4 v2, s[80:83], s65 offen offset:1024 lds\n"
"v_mfma_f32_16x16x32_bf16 v[154:157], v[252:255], v[58:61], v[154:157]\n"
"s_waitcnt lgkmcnt(1)\n"
"v_mfma_f32_16x16x32_bf16 v[150:153], v[62:65], v[62:65], v[150:153]\n"
"v_mfma_f32_16x16x32_bf16 v[154:157], v[252:255], v[62:65], v[154:157]\n"
"buffer_load_dwordx4 v2, s[80:83], s65 offen offset:2048 lds\n"
"v_mfma_f32_16x16x32_bf16 v[150:153], v[66:69], v[66:69], v[150:153]\n"
"v_mfma_f32_16x16x32_bf16 v[154:157], v[252:255], v[66:69], v[154:157]\n"
"v_mfma_f32_16x16x32_bf16 v[150:153], v[70:73], v[70:73], v[150:153]\n"
"buffer_load_dwordx4 v2, s[80:83], s65 offen offset:3072 lds\n"
"v_mfma_f32_16x16x32_bf16 v[154:157], v[252:255], v[70:73], v[154:157]\n"
"s_waitcnt lgkmcnt(0)\n"
"v_mfma_f32_16x16x32_bf16 v[150:153], v[74:77], v[74:77], v[150:153]\n"
"v_mfma_f32_16x16x32_bf16 v[154:157], v[252:255], v[74:77], v[154:157]\n"
"s_mul_i32 s67, s45, 256\n"
"s_mul_i32 s56, s41, 32768\n"
"s_add_u32 s67, s67, s56\n"
"s_add_u32 s67, s67, 0\n"
"s_mul_i32 s68, s43, 0\n"
"s_mul_i32 s58, s42, 0\n"
"s_add_u32 s68, s68, s58\n"
"s_nop 0\n"
"v_cndmask_b32 v155, v150, v151, s[74:75]\n"
"v_cndmask_b32 v155, v155, v152, s[76:77]\n"
"v_cndmask_b32 v155, v155, v153, s[78:79]\n"
"ds_write_b64 v167, v[154:155] offset:2048\n"
"s_add_u32 m0, s69, 0\n"
"s_waitcnt vmcnt(13)\n"
"ds_read_b64_tr_b16 v[78:79], v44 offset:0\n"
"s_add_u32 s64, s66, 0\n"
"ds_read_b64_tr_b16 v[80:81], v44 offset:256\n"
"ds_read_b64_tr_b16 v[82:83], v44 offset:32\n"
"buffer_load_dwordx4 v18, s[32:35], s64 offen offset:0 lds\n"
"ds_read_b64_tr_b16 v[84:85], v44 offset:288\n"
"ds_read_b64_tr_b16 v[86:87], v44 offset:2048\n"
"buffer_load_dwordx4 v19, s[32:35], s64 offen offset:1024 lds\n"
"ds_read_b64_tr_b16 v[88:89], v44 offset:2304\n"
"ds_read_b64_tr_b16 v[90:91], v44 offset:2080\n"
"s_add_u32 m0, s70, 0\n"
"ds_read_b64_tr_b16 v[92:93], v44 offset:2336\n"
"s_add_u32 s65, s67, 0\n"
"s_waitcnt vmcnt(10)\n"
"ds_read_b64_tr_b16 v[94:95], v44 offset:24576\n"
"buffer_load_dwordx4 v20, s[36:39], s65 offen offset:0 lds\n"
"ds_read_b64_tr_b16 v[96:97], v44 offset:24832\n"
"ds_read_b64_tr_b16 v[98:99], v44 offset:24608\n"
"buffer_load_dwordx4 v21, s[36:39], s65 offen offset:1024 lds\n"
"ds_read_b64_tr_b16 v[100:101], v44 offset:24864\n"
"ds_read_b64_tr_b16 v[102:103], v44 offset:26624\n"
"buffer_load_dwordx4 v22, s[36:39], s65 offen offset:2048 lds\n"
"ds_read_b64_tr_b16 v[104:105], v44 offset:26880\n"
"ds_read_b64_tr_b16 v[106:107], v44 offset:26656\n"
"buffer_load_dwordx4 v23, s[36:39], s65 offen offset:3072 lds\n"
"ds_read_b64_tr_b16 v[108:109], v44 offset:26912\n"
"s_waitcnt lgkmcnt(15)\n"
"s_barrier\n"
"ds_read_b64 v[158:159], v168 offset:0\n"
"ds_read_b64 v[160:161], v168 offset:3072\n"
"ds_read_b64 v[162:163], v168 offset:6144\n"
"ds_read_b64 v[164:165], v168 offset:9216\n"
"s_waitcnt vmcnt(10)\n"
"ds_read_b64_tr_b16 v[110:111], v45 offset:0\n"
"ds_read_b64_tr_b16 v[112:113], v45 offset:256\n"
"ds_read_b64_tr_b16 v[114:115], v45 offset:32\n"
"ds_read_b64_tr_b16 v[116:117], v45 offset:288\n"
"ds_read_b64_tr_b16 v[118:119], v45 offset:2048\n"
"ds_read_b64_tr_b16 v[120:121], v45 offset:2304\n"
"ds_read_b64_tr_b16 v[122:123], v45 offset:2080\n"
"ds_read_b64_tr_b16 v[124:125], v45 offset:2336\n"
"s_waitcnt lgkmcnt(15)\n"
"v_mfma_f32_16x16x32_bf16 v[142:145], v[78:81], v[46:49], 0\n"
"s_waitcnt vmcnt(6)\n"
"ds_read_b64_tr_b16 v[126:127], v45 offset:24576\n"
"s_add_u32 m0, s69, 24576\n"
"v_mfma_f32_16x16x32_bf16 v[146:149], v[82:85], v[46:49], 0\n"
"ds_read_b64_tr_b16 v[128:129], v45 offset:24832\n"
"ds_read_b64_tr_b16 v[130:131], v45 offset:24608\n"
"s_add_u32 s64, s66, 128\n"
"v_mfma_f32_16x16x32_bf16 v[142:145], v[86:89], v[50:53], v[142:145]\n"
"ds_read_b64_tr_b16 v[132:133], v45 offset:24864\n"
"ds_read_b64_tr_b16 v[134:135], v45 offset:26624\n"
"v_mfma_f32_16x16x32_bf16 v[146:149], v[90:93], v[50:53], v[146:149]\n"
"ds_read_b64_tr_b16 v[136:137], v45 offset:26880\n"
"ds_read_b64_tr_b16 v[138:139], v45 offset:26656\n"
"buffer_load_dwordx4 v18, s[32:35], s64 offen offset:0 lds\n"
"v_mfma_f32_16x16x32_bf16 v[142:145], v[94:97], v[54:57], v[142:145]\n"
"ds_read_b64_tr_b16 v[140:141], v45 offset:26912\n"
"buffer_load_dwordx4 v19, s[32:35], s64 offen offset:1024 lds\n"
"v_mfma_f32_16x16x32_bf16 v[146:149], v[98:101], v[54:57], v[146:149]\n"
"s_waitcnt lgkmcnt(15)\n"
"v_add_f32 v158, v158, v162\n"
"v_add_f32 v159, v159, v163\n"
"v_mfma_f32_16x16x32_bf16 v[142:145], v[102:105], v[58:61], v[142:145]\n"
"s_add_u32 m0, s70, 24576\n"
"v_add_f32 v160, v160, v164\n"
"v_mfma_f32_16x16x32_bf16 v[146:149], v[106:109], v[58:61], v[146:149]\n"
"v_add_f32 v161, v161, v165\n"
"v_add_f32 v158, v158, v160\n"
"s_waitcnt lgkmcnt(8)\n"
"v_mfma_f32_16x16x32_bf16 v[142:145], v[110:113], v[62:65], v[142:145]\n"
"s_add_u32 s65, s67, 131072\n"
"v_add_f32 v159, v159, v161\n"
"v_mfma_f32_16x16x32_bf16 v[146:149], v[114:117], v[62:65], v[146:149]\n"
"buffer_load_dwordx4 v20, s[36:39], s65 offen offset:0 lds\n"
"v_mul_f32 v158, 0xba800000, v158\n"
"v_mul_f32 v159, 0x3a800000, v159\n"
"v_mfma_f32_16x16x32_bf16 v[142:145], v[118:121], v[66:69], v[142:145]\n"
"v_fma_f32 v159, -v158, v158, v159\n"
"v_mfma_f32_16x16x32_bf16 v[146:149], v[122:125], v[66:69], v[146:149]\n"
"buffer_load_dwordx4 v21, s[36:39], s65 offen offset:1024 lds\n"
"v_max_f32 v159, 0, v159\n"
"v_cvt_pk_bf16_f32 v24, v158, v158\n"
"s_waitcnt lgkmcnt(2)\n"
"v_mfma_f32_16x16x32_bf16 v[142:145], v[126:129], v[70:73], v[142:145]\n"
"buffer_load_dwordx4 v22, s[36:39], s65 offen offset:2048 lds\n"
"v_cvt_pk_bf16_f32 v25, v158, v158\n"
"v_mfma_f32_16x16x32_bf16 v[146:149], v[130:133], v[70:73], v[146:149]\n"
"v_cvt_pk_bf16_f32 v26, v158, v158\n"
"v_cvt_pk_bf16_f32 v27, v158, v158\n"
"v_mfma_f32_16x16x32_bf16 v[142:145], v[134:137], v[74:77], v[142:145]\n"
"buffer_load_dwordx4 v23, s[36:39], s65 offen offset:3072 lds\n"
"v_and_b32 v158, 0xffff0000, v24\n"
"s_waitcnt lgkmcnt(0)\n"
"v_mfma_f32_16x16x32_bf16 v[146:149], v[138:141], v[74:77], v[146:149]\n"
"v_mfma_f32_16x16x32_bf16 v[142:145], v[78:81], v[24:27], v[142:145]\n"
"s_add_u32 m0, s69, 49152\n"
"v_cvt_pk_bf16_f32 v159, v159, v159\n"
"v_mfma_f32_16x16x32_bf16 v[146:149], v[82:85], v[24:27], v[146:149]\n"
"s_add_u32 s64, s66, 256\n"
"v_and_b32 v159, 0xffff0000, v159\n"
"v_mfma_f32_16x16x32_bf16 v[142:145], v[86:89], v[24:27], v[142:145]\n"
"v_mfma_f32_16x16x32_bf16 v[146:149], v[90:93], v[24:27], v[146:149]\n"
"buffer_load_dwordx4 v18, s[32:35], s64 offen offset:0 lds\n"
"v_add_f32 v159, 0x3727c5ac, v159\n"
"v_mfma_f32_16x16x32_bf16 v[142:145], v[94:97], v[24:27], v[142:145]\n"
"buffer_load_dwordx4 v19, s[32:35], s64 offen offset:1024 lds\n"
"v_cvt_pk_bf16_f32 v159, v159, v159\n"
"v_mfma_f32_16x16x32_bf16 v[146:149], v[98:101], v[24:27], v[146:149]\n"
"v_mfma_f32_16x16x32_bf16 v[142:145], v[102:105], v[24:27], v[142:145]\n"
"s_add_u32 m0, s70, 49152\n"
"v_and_b32 v159, 0xffff0000, v159\n"
"v_mfma_f32_16x16x32_bf16 v[146:149], v[106:109], v[24:27], v[146:149]\n"
"v_sqrt_f32 v159, v159\n"
"v_mfma_f32_16x16x32_bf16 v[142:145], v[110:113], v[24:27], v[142:145]\n"
"s_add_u32 s65, s67, 262144\n"
"v_mfma_f32_16x16x32_bf16 v[146:149], v[114:117], v[24:27], v[146:149]\n"
"buffer_load_dwordx4 v20, s[36:39], s65 offen offset:0 lds\n"
"v_cvt_pk_bf16_f32 v159, v159, v159\n"
"v_mfma_f32_16x16x32_bf16 v[142:145], v[118:121], v[24:27], v[142:145]\n"
"v_and_b32 v159, 0xffff0000, v159\n"
"v_mfma_f32_16x16x32_bf16 v[146:149], v[122:125], v[24:27], v[146:149]\n"
"buffer_load_dwordx4 v21, s[36:39], s65 offen offset:1024 lds\n"
"v_mfma_f32_16x16x32_bf16 v[142:145], v[126:129], v[24:27], v[142:145]\n"
"buffer_load_dwordx4 v22, s[36:39], s65 offen offset:2048 lds\n"
"v_rcp_f32 v159, v159\n"
"v_mfma_f32_16x16x32_bf16 v[146:149], v[130:133], v[24:27], v[146:149]\n"
"v_mfma_f32_16x16x32_bf16 v[142:145], v[134:137], v[24:27], v[142:145]\n"
"buffer_load_dwordx4 v23, s[36:39], s65 offen offset:3072 lds\n"
"v_mfma_f32_16x16x32_bf16 v[146:149], v[138:141], v[24:27], v[146:149]\n"
"s_nop 5\n"
"ds_write_b128 v166, v[142:145] offset:0\n"
"s_nop 0\n"
"ds_write_b128 v166, v[146:149] offset:1024\n"
"s_waitcnt lgkmcnt(0)\n"
"s_waitcnt vmcnt(12)\n"
"s_barrier\n"
"s_add_u32 m0, s69, 73728\n"
"s_add_u32 s64, s66, 384\n"
"buffer_load_dwordx4 v18, s[32:35], s64 offen offset:0 lds\n"
"buffer_load_dwordx4 v19, s[32:35], s64 offen offset:1024 lds\n"
"s_add_u32 m0, s70, 73728\n"
"s_add_u32 s65, s67, 393216\n"
"buffer_load_dwordx4 v20, s[36:39], s65 offen offset:0 lds\n"
"buffer_load_dwordx4 v21, s[36:39], s65 offen offset:1024 lds\n"
"buffer_load_dwordx4 v22, s[36:39], s65 offen offset:2048 lds\n"
"buffer_load_dwordx4 v23, s[36:39], s65 offen offset:3072 lds\n"
"s_cmp_lg_u32 s41, 0\n"
"s_cbranch_scc1 .Lmk_kv_1\n"
"ds_read_b128 v[86:89], v166 offset:3072\n"
"ds_read_b128 v[90:93], v166 offset:4096\n"
"ds_read_b128 v[94:97], v166 offset:6144\n"
"ds_read_b128 v[98:101], v166 offset:7168\n"
"ds_read_b128 v[102:105], v166 offset:9216\n"
"ds_read_b128 v[106:109], v166 offset:10240\n"
"s_waitcnt lgkmcnt(5)\n"
"v_pk_add_f32 v[142:143], v[142:143], v[86:87]\n"
"v_pk_add_f32 v[144:145], v[144:145], v[88:89]\n"
"s_waitcnt lgkmcnt(4)\n"
"v_pk_add_f32 v[146:147], v[146:147], v[90:91]\n"
"v_pk_add_f32 v[148:149], v[148:149], v[92:93]\n"
"s_waitcnt lgkmcnt(3)\n"
"v_pk_add_f32 v[142:143], v[142:143], v[94:95]\n"
"v_pk_add_f32 v[144:145], v[144:145], v[96:97]\n"
"s_waitcnt lgkmcnt(2)\n"
"v_pk_add_f32 v[146:147], v[146:147], v[98:99]\n"
"v_pk_add_f32 v[148:149], v[148:149], v[100:101]\n"
"s_waitcnt lgkmcnt(1)\n"
"v_pk_add_f32 v[142:143], v[142:143], v[102:103]\n"
"v_pk_add_f32 v[144:145], v[144:145], v[104:105]\n"
"s_waitcnt lgkmcnt(0)\n"
"v_pk_add_f32 v[146:147], v[146:147], v[106:107]\n"
"v_pk_add_f32 v[148:149], v[148:149], v[108:109]\n"
"v_pk_mul_f32 v[142:143], v[142:143], v[158:159] op_sel:[0,1] op_sel_hi:[1,1]\n"
"v_pk_mul_f32 v[144:145], v[144:145], v[158:159] op_sel:[0,1] op_sel_hi:[1,1]\n"
"v_pk_mul_f32 v[146:147], v[146:147], v[158:159] op_sel:[0,1] op_sel_hi:[1,1]\n"
"v_pk_mul_f32 v[148:149], v[148:149], v[158:159] op_sel:[0,1] op_sel_hi:[1,1]\n"
"v_cvt_pk_bf16_f32 v150, v142, v143\n"
"v_cvt_pk_bf16_f32 v151, v144, v145\n"
"v_cvt_pk_bf16_f32 v152, v146, v147\n"
"v_cvt_pk_bf16_f32 v153, v148, v149\n"
"s_bitcmp1_b32 s45, 0\n"
"s_cbranch_scc1 .Lmk_isv_2\n"
"buffer_store_dwordx4 v[150:153], v169, s[28:31], s86 offen sc1\n"
"v_mul_f32 v159, 0x3e8293ee, v159\n"
"v_lshlrev_b32 v12, 3, v1\n"
"s_mov_b64 exec, 0xffff\n"
"buffer_store_dwordx2 v[158:159], v12, s[28:31], s87 offen sc1\n"
"s_mov_b64 exec, -1\n"
"s_branch .Lmk_kvd_3\n"
".Lmk_isv_2:\n"
"ds_write_b128 v170, v[150:153]\n"
"ds_read_b64_tr_b16 v[154:155], v171 offset:0\n"
"ds_read_b64_tr_b16 v[156:157], v171 offset:256\n"
"s_waitcnt lgkmcnt(0)\n"
"buffer_store_dwordx4 v[154:157], v2, s[28:31], s86 offen sc1\n"
".Lmk_kvd_3:\n"
"s_nop 1\n"
"v_mfma_f32_16x16x32_bf16 v[24:27], v[150:153], v[150:153], 0\n"
"s_branch .Lmk_kv_1j\n"
".Lmk_kv_1:\n"
"s_mov_b64 exec, 0\n"
"s_cmp_eq_u32 s41, 1\n"
"s_cbranch_scc1 .Lmk_pg_3\n"
"s_cmp_eq_u32 s41, 2\n"
"s_cbranch_scc1 .Lmk_pg_4\n"
"s_branch .Lmk_pg_5\n"
".Lmk_ret_1:\n"
"s_mov_b64 exec, -1\n"
"s_nop 0\n"
".Lmk_kv_1j:\n"
"s_mov_b64 s[56:57], s[72:73]\n"
"s_mov_b32 s59, s61\n"
"s_mov_b32 s60, s63\n"
"v_mov_b32 v15, v13\n"
"ds_read_b64_tr_b16 a[16:17], v34 offset:0\n"
"ds_read_b64_tr_b16 a[18:19], v34 offset:1024\n"
"ds_read_b64_tr_b16 a[20:21], v35 offset:0\n"
"ds_read_b64_tr_b16 a[22:23], v35 offset:1024\n"
"ds_read_b128 a[0:3], v28 offset:0\n"
"ds_read_b128 a[4:7], v28 offset:2048\n"
"ds_read_b128 a[8:11], v28 offset:4096\n"
"ds_read_b128 a[12:15], v28 offset:6144\n"
"ds_read_b64_tr_b16 a[40:41], v34 offset:8192\n"
".Lmk_pg_1:\n"
"s_cbranch_execz .Lmk_ret_0\n"
"ds_read_b64_tr_b16 a[42:43], v34 offset:9216\n"
"ds_read_b64_tr_b16 a[44:45], v35 offset:8192\n"
"ds_read_b64_tr_b16 a[46:47], v35 offset:9216\n"
"ds_read_b128 a[24:27], v29 offset:0\n"
"ds_read_b128 a[28:31], v29 offset:2048\n"
"ds_read_b128 a[32:35], v29 offset:4096\n"
"ds_read_b128 a[36:39], v29 offset:6144\n"
"s_waitcnt lgkmcnt(14)\n"
"v_mfma_f32_16x16x32_bf16 v[72:75], a[16:19], v[252:255], 0\n"
"s_waitcnt vmcnt(12)\n"
"s_barrier\n"
"s_waitcnt lgkmcnt(12)\n"
"v_mfma_f32_16x16x32_bf16 v[76:79], a[20:23], v[252:255], 0\n"
"ds_read_b64_tr_b16 a[64:65], v34 offset:24576\n"
"s_add_u32 m0, s69, 98304\n"
"s_waitcnt lgkmcnt(12)\n"
"v_mfma_f32_16x16x32_bf16 v[40:43], a[16:19], a[0:3], 0\n"
"ds_read_b64_tr_b16 a[66:67], v34 offset:25600\n"
"s_add_u32 s64, s66, 512\n"
"v_bfe_u32 v15, v1, 2, 2\n"
"v_mfma_f32_16x16x32_bf16 v[56:59], a[20:23], a[0:3], 0\n"
"ds_read_b64_tr_b16 a[68:69], v35 offset:24576\n"
"v_lshl_add_u32 v184, v15, 6, v184\n"
"s_waitcnt lgkmcnt(13)\n"
"v_mfma_f32_16x16x32_bf16 v[44:47], a[16:19], a[4:7], 0\n"
"ds_read_b64_tr_b16 a[70:71], v35 offset:25600\n"
"buffer_load_dwordx4 v18, s[32:35], s64 offen offset:0 lds\n"
"v_mfma_f32_16x16x32_bf16 v[60:63], a[20:23], a[4:7], 0\n"
"ds_read_b128 a[48:51], v28 offset:24576\n"
"v_bfe_u32 v15, v1, 4, 2\n"
"s_waitcnt lgkmcnt(13)\n"
"v_mfma_f32_16x16x32_bf16 v[48:51], a[16:19], a[8:11], 0\n"
"ds_read_b128 a[52:55], v28 offset:26624\n"
"buffer_load_dwordx4 v19, s[32:35], s64 offen offset:1024 lds\n"
"v_mfma_f32_16x16x32_bf16 v[64:67], a[20:23], a[8:11], 0\n"
"ds_read_b128 a[56:59], v28 offset:28672\n"
"v_lshl_add_u32 v184, v15, 9, v184\n"
"v_mfma_f32_16x16x32_bf16 v[52:55], a[16:19], a[12:15], 0\n"
"ds_read_b128 a[60:63], v28 offset:30720\n"
"s_add_u32 m0, s70, 98304\n"
"v_mfma_f32_16x16x32_bf16 v[68:71], a[20:23], a[12:15], 0\n"
"v_add_u32 v184, s71, v184\n"
"s_waitcnt lgkmcnt(8)\n"
"v_mfma_f32_16x16x32_bf16 v[72:75], a[40:43], v[252:255], v[72:75]\n"
"s_add_u32 s65, s67, 524288\n"
"v_mfma_f32_16x16x32_bf16 v[76:79], a[44:47], v[252:255], v[76:79]\n"
"ds_read_b64_tr_b16 a[16:17], v34 offset:32768\n"
"buffer_load_dwordx4 v20, s[36:39], s65 offen offset:0 lds\n"
"v_add_u32 v184, 65536, v184\n"
"v_mfma_f32_16x16x32_bf16 v[40:43], a[40:43], a[24:27], v[40:43]\n"
"ds_read_b64_tr_b16 a[18:19], v34 offset:33792\n"
"v_mfma_f32_16x16x32_bf16 v[56:59], a[44:47], a[24:27], v[56:59]\n"
"ds_read_b64_tr_b16 a[20:21], v35 offset:32768\n"
"buffer_load_dwordx4 v21, s[36:39], s65 offen offset:1024 lds\n"
"v_bfe_u32 v15, v1, 0, 2\n"
"v_mfma_f32_16x16x32_bf16 v[44:47], a[40:43], a[28:31], v[44:47]\n"
"ds_read_b64_tr_b16 a[22:23], v35 offset:33792\n"
"v_mul_u32_u24 v185, 8, v15\n"
"v_mfma_f32_16x16x32_bf16 v[60:63], a[44:47], a[28:31], v[60:63]\n"
"ds_read_b128 a[0:3], v29 offset:24576\n"
"buffer_load_dwordx4 v22, s[36:39], s65 offen offset:2048 lds\n"
"v_mfma_f32_16x16x32_bf16 v[48:51], a[40:43], a[32:35], v[48:51]\n"
"ds_read_b128 a[4:7], v29 offset:26624\n"
"v_bfe_u32 v15, v1, 2, 2\n"
"v_mfma_f32_16x16x32_bf16 v[64:67], a[44:47], a[32:35], v[64:67]\n"
"ds_read_b128 a[8:11], v29 offset:28672\n"
"buffer_load_dwordx4 v23, s[36:39], s65 offen offset:3072 lds\n"
"v_mfma_f32_16x16x32_bf16 v[52:55], a[40:43], a[36:39], v[52:55]\n"
"ds_read_b128 a[12:15], v29 offset:30720\n"
"v_lshl_add_u32 v185, v15, 7, v185\n"
"v_mfma_f32_16x16x32_bf16 v[68:71], a[44:47], a[36:39], v[68:71]\n"
"s_waitcnt lgkmcnt(8)\n"
"v_mfma_f32_16x16x32_bf16 v[72:75], a[64:67], v[252:255], v[72:75]\n"
"s_waitcnt vmcnt(12)\n"
"s_barrier\n"
"v_bfe_u32 v15, v1, 4, 2\n"
"v_mfma_f32_16x16x32_bf16 v[76:79], a[68:71], v[252:255], v[76:79]\n"
"ds_read_b64_tr_b16 a[40:41], v36 offset:0\n"
"s_add_u32 m0, s69, 122880\n"
"v_mfma_f32_16x16x32_bf16 v[40:43], a[64:67], a[48:51], v[40:43]\n"
"ds_read_b64_tr_b16 a[42:43], v36 offset:1024\n"
"s_add_u32 s64, s66, 640\n"
"v_lshl_add_u32 v185, v15, 10, v185\n"
"v_mfma_f32_16x16x32_bf16 v[56:59], a[68:71], a[48:51], v[56:59]\n"
"ds_read_b64_tr_b16 a[44:45], v37 offset:0\n"
"v_mfma_f32_16x16x32_bf16 v[44:47], a[64:67], a[52:55], v[44:47]\n"
"ds_read_b64_tr_b16 a[46:47], v37 offset:1024\n"
"buffer_load_dwordx4 v18, s[32:35], s64 offen offset:0 lds\n"
"v_add_u32 v185, s71, v185\n"
"v_mfma_f32_16x16x32_bf16 v[60:63], a[68:71], a[52:55], v[60:63]\n"
"ds_read_b128 a[24:27], v30 offset:0\n"
"v_add_u32 v185, 131072, v185\n"
"v_mfma_f32_16x16x32_bf16 v[48:51], a[64:67], a[56:59], v[48:51]\n"
"ds_read_b128 a[28:31], v30 offset:2048\n"
"buffer_load_dwordx4 v19, s[32:35], s64 offen offset:1024 lds\n"
"v_mfma_f32_16x16x32_bf16 v[64:67], a[68:71], a[56:59], v[64:67]\n"
"ds_read_b128 a[32:35], v30 offset:4096\n"
"v_bfe_u32 v15, v1, 0, 2\n"
"v_mfma_f32_16x16x32_bf16 v[52:55], a[64:67], a[60:63], v[52:55]\n"
"ds_read_b128 a[36:39], v30 offset:6144\n"
"s_add_u32 m0, s70, 122880\n"
"v_mfma_f32_16x16x32_bf16 v[68:71], a[68:71], a[60:63], v[68:71]\n"
"v_mul_u32_u24 v186, 16, v15\n"
"s_waitcnt lgkmcnt(8)\n"
"v_mfma_f32_16x16x32_bf16 v[72:75], a[16:19], v[252:255], v[72:75]\n"
"s_add_u32 s65, s67, 655360\n"
"v_mfma_f32_16x16x32_bf16 v[76:79], a[20:23], v[252:255], v[76:79]\n"
"ds_read_b64_tr_b16 a[64:65], v36 offset:8192\n"
"buffer_load_dwordx4 v20, s[36:39], s65 offen offset:0 lds\n"
"v_bfe_u32 v15, v1, 2, 2\n"
"v_mfma_f32_16x16x32_bf16 v[40:43], a[16:19], a[0:3], v[40:43]\n"
"ds_read_b64_tr_b16 a[66:67], v36 offset:9216\n"
"v_mfma_f32_16x16x32_bf16 v[56:59], a[20:23], a[0:3], v[56:59]\n"
"ds_read_b64_tr_b16 a[68:69], v37 offset:8192\n"
"buffer_load_dwordx4 v21, s[36:39], s65 offen offset:1024 lds\n"
"v_lshl_add_u32 v186, v15, 7, v186\n"
"v_mfma_f32_16x16x32_bf16 v[44:47], a[16:19], a[4:7], v[44:47]\n"
"ds_read_b64_tr_b16 a[70:71], v37 offset:9216\n"
"v_mfma_f32_16x16x32_bf16 v[60:63], a[20:23], a[4:7], v[60:63]\n"
"ds_read_b128 a[48:51], v31 offset:0\n"
"buffer_load_dwordx4 v22, s[36:39], s65 offen offset:2048 lds\n"
"v_bfe_u32 v15, v1, 4, 2\n"
"v_mfma_f32_16x16x32_bf16 v[48:51], a[16:19], a[8:11], v[48:51]\n"
"ds_read_b128 a[52:55], v31 offset:2048\n"
"v_mfma_f32_16x16x32_bf16 v[64:67], a[20:23], a[8:11], v[64:67]\n"
"ds_read_b128 a[56:59], v31 offset:4096\n"
"buffer_load_dwordx4 v23, s[36:39], s65 offen offset:3072 lds\n"
"v_lshl_add_u32 v186, v15, 9, v186\n"
"v_mfma_f32_16x16x32_bf16 v[52:55], a[16:19], a[12:15], v[52:55]\n"
"ds_read_b128 a[60:63], v31 offset:6144\n"
"v_add_u32 v186, s71, v186\n"
"v_mfma_f32_16x16x32_bf16 v[68:71], a[20:23], a[12:15], v[68:71]\n"
"s_waitcnt lgkmcnt(8)\n"
"v_mfma_f32_16x16x32_bf16 v[72:75], a[40:43], v[252:255], v[72:75]\n"
"s_waitcnt vmcnt(12)\n"
"s_barrier\n"
"v_add_u32 v186, 131072, v186\n"
"v_mfma_f32_16x16x32_bf16 v[76:79], a[44:47], v[252:255], v[76:79]\n"
"ds_read_b64_tr_b16 a[16:17], v36 offset:24576\n"
"s_add_u32 m0, s69, 0\n"
"v_mfma_f32_16x16x32_bf16 v[40:43], a[40:43], a[24:27], v[40:43]\n"
"ds_read_b64_tr_b16 a[18:19], v36 offset:25600\n"
"s_add_u32 s64, s66, 768\n"
"v_bfe_u32 v15, v1, 0, 2\n"
"v_mfma_f32_16x16x32_bf16 v[56:59], a[44:47], a[24:27], v[56:59]\n"
"ds_read_b64_tr_b16 a[20:21], v37 offset:24576\n"
"v_mfma_f32_16x16x32_bf16 v[44:47], a[40:43], a[28:31], v[44:47]\n"
"ds_read_b64_tr_b16 a[22:23], v37 offset:25600\n"
"buffer_load_dwordx4 v18, s[32:35], s64 offen offset:0 lds\n"
"v_mul_u32_u24 v187, 128, v15\n"
"v_mfma_f32_16x16x32_bf16 v[60:63], a[44:47], a[28:31], v[60:63]\n"
"ds_read_b128 a[0:3], v30 offset:24576\n"
"v_mfma_f32_16x16x32_bf16 v[48:51], a[40:43], a[32:35], v[48:51]\n"
"ds_read_b128 a[4:7], v30 offset:26624\n"
"buffer_load_dwordx4 v19, s[32:35], s64 offen offset:1024 lds\n"
"v_bfe_u32 v15, v1, 2, 2\n"
"v_mfma_f32_16x16x32_bf16 v[64:67], a[44:47], a[32:35], v[64:67]\n"
"ds_read_b128 a[8:11], v30 offset:28672\n"
"v_mfma_f32_16x16x32_bf16 v[52:55], a[40:43], a[36:39], v[52:55]\n"
"ds_read_b128 a[12:15], v30 offset:30720\n"
"s_add_u32 m0, s70, 0\n"
"v_lshl_add_u32 v187, v15, 10, v187\n"
"v_mfma_f32_16x16x32_bf16 v[68:71], a[44:47], a[36:39], v[68:71]\n"
"v_bfe_u32 v15, v1, 4, 2\n"
"s_waitcnt lgkmcnt(8)\n"
"v_mfma_f32_16x16x32_bf16 v[72:75], a[64:67], v[252:255], v[72:75]\n"
"s_add_u32 s65, s67, 786432\n"
"v_mfma_f32_16x16x32_bf16 v[76:79], a[68:71], v[252:255], v[76:79]\n"
"ds_read_b64_tr_b16 a[40:41], v36 offset:32768\n"
"buffer_load_dwordx4 v20, s[36:39], s65 offen offset:0 lds\n"
"v_lshl_add_u32 v187, v15, 4, v187\n"
"v_mfma_f32_16x16x32_bf16 v[40:43], a[64:67], a[48:51], v[40:43]\n"
"ds_read_b64_tr_b16 a[42:43], v36 offset:33792\n"
"v_mfma_f32_16x16x32_bf16 v[56:59], a[68:71], a[48:51], v[56:59]\n"
"ds_read_b64_tr_b16 a[44:45], v37 offset:32768\n"
"buffer_load_dwordx4 v21, s[36:39], s65 offen offset:1024 lds\n"
"v_add_u32 v187, s71, v187\n"
"v_mfma_f32_16x16x32_bf16 v[44:47], a[64:67], a[52:55], v[44:47]\n"
"ds_read_b64_tr_b16 a[46:47], v37 offset:33792\n"
"v_mfma_f32_16x16x32_bf16 v[60:63], a[68:71], a[52:55], v[60:63]\n"
"ds_read_b128 a[24:27], v31 offset:24576\n"
"buffer_load_dwordx4 v22, s[36:39], s65 offen offset:2048 lds\n"
"v_add_u32 v187, 65536, v187\n"
"v_mfma_f32_16x16x32_bf16 v[48:51], a[64:67], a[56:59], v[48:51]\n"
"ds_read_b128 a[28:31], v31 offset:26624\n"
"v_mfma_f32_16x16x32_bf16 v[64:67], a[68:71], a[56:59], v[64:67]\n"
"ds_read_b128 a[32:35], v31 offset:28672\n"
"buffer_load_dwordx4 v23, s[36:39], s65 offen offset:3072 lds\n"
"s_and_b32 s57, s40, 31\n"
"v_mfma_f32_16x16x32_bf16 v[52:55], a[64:67], a[60:63], v[52:55]\n"
"ds_read_b128 a[36:39], v31 offset:30720\n"
"v_mfma_f32_16x16x32_bf16 v[68:71], a[68:71], a[60:63], v[68:71]\n"
"s_lshr_b32 s59, s40, 5\n"
"s_waitcnt lgkmcnt(8)\n"
"v_mfma_f32_16x16x32_bf16 v[72:75], a[16:19], v[252:255], v[72:75]\n"
"s_waitcnt vmcnt(12)\n"
"s_barrier\n"
"v_mfma_f32_16x16x32_bf16 v[76:79], a[20:23], v[252:255], v[76:79]\n"
"s_cmp_lg_u32 s41, 0\n"
"s_cbranch_scc1 .Lmk_fl1_4\n"
"v_max3_f32 v25, v24, v25, v26\n"
"v_max_f32 v25, v25, v27\n"
"s_nop 1\n"
"v_max_f32_dpp v25, v25, v25 row_ror:8 row_mask:0xf bank_mask:0xf\n"
"s_nop 1\n"
"v_max_f32_dpp v25, v25, v25 row_ror:4 row_mask:0xf bank_mask:0xf\n"
"s_nop 1\n"
"v_max_f32_dpp v25, v25, v25 row_ror:2 row_mask:0xf bank_mask:0xf\n"
"s_nop 1\n"
"v_max_f32_dpp v25, v25, v25 row_ror:1 row_mask:0xf bank_mask:0xf\n"
"v_mov_b32 v24, v25\n"
"s_nop 1\n"
"v_permlane16_swap_b32 v25, v24\n"
"v_max_f32 v25, v25, v24\n"
"v_mov_b32 v24, v25\n"
"s_nop 1\n"
"v_permlane32_swap_b32 v25, v24\n"
"v_max_f32 v25, v25, v24\n"
"v_cmp_nge_f32 vcc, 0x5d800000, v25\n"
"v_mov_b32 v24, 0x7f800000\n"
"s_nop 0\n"
"v_cndmask_b32 v26, 0, v24, vcc\n"
"s_bitcmp1_b32 s45, 0\n"
"s_cselect_b64 vcc, -1, 0\n"
"v_cndmask_b32 v25, v25, v26, vcc\n"
"v_mov_b32 v24, s26\n"
"s_lshl_b32 s58, s27, 3\n"
"s_add_u32 s58, s58, 69632\n"
"s_mov_b64 exec, 1\n"
"buffer_store_dwordx2 v[24:25], v9, s[28:31], s58 offen sc1\n"
"s_mov_b64 exec, -1\n"
"s_nop 1\n"
".Lmk_fl1_4:\n"
"ds_read_b64_tr_b16 a[64:65], v38 offset:0\n"
"s_add_u32 m0, s69, 24576\n"
"s_mul_i32 s88, s57, 65536\n"
"v_mfma_f32_16x16x32_bf16 v[40:43], a[16:19], a[0:3], v[40:43]\n"
"ds_read_b64_tr_b16 a[66:67], v38 offset:1024\n"
"s_add_u32 s64, s66, 896\n"
"s_mul_i32 s56, s59, 64\n"
"v_mfma_f32_16x16x32_bf16 v[56:59], a[20:23], a[0:3], v[56:59]\n"
"ds_read_b64_tr_b16 a[68:69], v39 offset:0\n"
"v_mfma_f32_16x16x32_bf16 v[44:47], a[16:19], a[4:7], v[44:47]\n"
"ds_read_b64_tr_b16 a[70:71], v39 offset:1024\n"
"buffer_load_dwordx4 v18, s[32:35], s64 offen offset:0 lds\n"
"s_add_u32 s88, s88, s56\n"
"v_mfma_f32_16x16x32_bf16 v[60:63], a[20:23], a[4:7], v[60:63]\n"
"ds_read_b128 a[48:51], v32 offset:0\n"
"v_mfma_f32_16x16x32_bf16 v[48:51], a[16:19], a[8:11], v[48:51]\n"
"ds_read_b128 a[52:55], v32 offset:2048\n"
"buffer_load_dwordx4 v19, s[32:35], s64 offen offset:1024 lds\n"
"s_add_u32 s88, s88, 0\n"
"v_mfma_f32_16x16x32_bf16 v[64:67], a[20:23], a[8:11], v[64:67]\n"
"ds_read_b128 a[56:59], v32 offset:4096\n"
"v_mfma_f32_16x16x32_bf16 v[52:55], a[16:19], a[12:15], v[52:55]\n"
"ds_read_b128 a[60:63], v32 offset:6144\n"
"s_add_u32 m0, s70, 24576\n"
"s_mul_i32 s6, s40, 2048\n"
"v_mfma_f32_16x16x32_bf16 v[68:71], a[20:23], a[12:15], v[68:71]\n"
"s_waitcnt lgkmcnt(8)\n"
"v_mfma_f32_16x16x32_bf16 v[72:75], a[40:43], v[252:255], v[72:75]\n"
"s_add_u32 s65, s67, 917504\n"
"s_add_u32 s6, s6, 15728640\n"
"v_mfma_f32_16x16x32_bf16 v[76:79], a[44:47], v[252:255], v[76:79]\n"
"ds_read_b64_tr_b16 a[16:17], v38 offset:8192\n"
"buffer_load_dwordx4 v20, s[36:39], s65 offen offset:0 lds\n"
"v_mfma_f32_16x16x32_bf16 v[40:43], a[40:43], a[24:27], v[40:43]\n"
"ds_read_b64_tr_b16 a[18:19], v38 offset:9216\n"
"s_mul_i32 s62, s44, 32\n"
"v_mfma_f32_16x16x32_bf16 v[56:59], a[44:47], a[24:27], v[56:59]\n"
"ds_read_b64_tr_b16 a[20:21], v39 offset:8192\n"
"buffer_load_dwordx4 v21, s[36:39], s65 offen offset:1024 lds\n"
"s_mul_i32 s56, s45, 4\n"
"v_mfma_f32_16x16x32_bf16 v[44:47], a[40:43], a[28:31], v[44:47]\n"
"ds_read_b64_tr_b16 a[22:23], v39 offset:9216\n"
"v_mfma_f32_16x16x32_bf16 v[60:63], a[44:47], a[28:31], v[60:63]\n"
"ds_read_b128 a[0:3], v33 offset:0\n"
"buffer_load_dwordx4 v22, s[36:39], s65 offen offset:2048 lds\n"
"s_add_u32 s62, s62, s56\n"
"v_mfma_f32_16x16x32_bf16 v[48:51], a[40:43], a[32:35], v[48:51]\n"
"ds_read_b128 a[4:7], v33 offset:2048\n"
"v_mfma_f32_16x16x32_bf16 v[64:67], a[44:47], a[32:35], v[64:67]\n"
"ds_read_b128 a[8:11], v33 offset:4096\n"
"buffer_load_dwordx4 v23, s[36:39], s65 offen offset:3072 lds\n"
"s_mul_i32 s56, s41, 1\n"
"v_mfma_f32_16x16x32_bf16 v[52:55], a[40:43], a[36:39], v[52:55]\n"
"ds_read_b128 a[12:15], v33 offset:6144\n"
"v_mfma_f32_16x16x32_bf16 v[68:71], a[44:47], a[36:39], v[68:71]\n"
"s_add_u32 s62, s62, s56\n"
"s_waitcnt lgkmcnt(8)\n"
"v_mfma_f32_16x16x32_bf16 v[72:75], a[64:67], v[252:255], v[72:75]\n"
"s_waitcnt vmcnt(12)\n"
"s_barrier\n"
"v_mfma_f32_16x16x32_bf16 v[76:79], a[68:71], v[252:255], v[76:79]\n"
"ds_read_b64_tr_b16 a[40:41], v38 offset:24576\n"
"s_add_u32 m0, s69, 49152\n"
"s_add_u32 s62, s62, 0\n"
"v_mfma_f32_16x16x32_bf16 v[40:43], a[64:67], a[48:51], v[40:43]\n"
"ds_read_b64_tr_b16 a[42:43], v38 offset:25600\n"
"s_add_u32 s64, s66, 1024\n"
"v_mfma_f32_16x16x32_bf16 v[56:59], a[68:71], a[48:51], v[56:59]\n"
"ds_read_b64_tr_b16 a[44:45], v39 offset:24576\n"
"s_and_b32 s57, s62, 15\n"
"v_mfma_f32_16x16x32_bf16 v[44:47], a[64:67], a[52:55], v[44:47]\n"
"ds_read_b64_tr_b16 a[46:47], v39 offset:25600\n"
"buffer_load_dwordx4 v18, s[32:35], s64 offen offset:0 lds\n"
"s_lshr_b32 s59, s62, 4\n"
"v_mfma_f32_16x16x32_bf16 v[60:63], a[68:71], a[52:55], v[60:63]\n"
"ds_read_b128 a[24:27], v32 offset:24576\n"
"v_mfma_f32_16x16x32_bf16 v[48:51], a[64:67], a[56:59], v[48:51]\n"
"ds_read_b128 a[28:31], v32 offset:26624\n"
"buffer_load_dwordx4 v19, s[32:35], s64 offen offset:1024 lds\n"
"s_mul_i32 s92, s57, 256\n"
"v_mfma_f32_16x16x32_bf16 v[64:67], a[68:71], a[56:59], v[64:67]\n"
"ds_read_b128 a[32:35], v32 offset:28672\n"
"v_mfma_f32_16x16x32_bf16 v[52:55], a[64:67], a[60:63], v[52:55]\n"
"ds_read_b128 a[36:39], v32 offset:30720\n"
"s_add_u32 m0, s70, 49152\n"
"s_mul_i32 s56, s59, 131072\n"
"v_mfma_f32_16x16x32_bf16 v[68:71], a[68:71], a[60:63], v[68:71]\n"
"s_waitcnt lgkmcnt(8)\n"
"v_mfma_f32_16x16x32_bf16 v[72:75], a[16:19], v[252:255], v[72:75]\n"
"s_add_u32 s65, s67, 1048576\n"
"s_add_u32 s92, s92, s56\n"
"v_mfma_f32_16x16x32_bf16 v[76:79], a[20:23], v[252:255], v[76:79]\n"
"ds_read_b64_tr_b16 a[64:65], v38 offset:32768\n"
"buffer_load_dwordx4 v20, s[36:39], s65 offen offset:0 lds\n"
"v_mfma_f32_16x16x32_bf16 v[40:43], a[16:19], a[0:3], v[40:43]\n"
"ds_read_b64_tr_b16 a[66:67], v38 offset:33792\n"
"s_add_u32 s92, s92, 0\n"
"v_mfma_f32_16x16x32_bf16 v[56:59], a[20:23], a[0:3], v[56:59]\n"
"ds_read_b64_tr_b16 a[68:69], v39 offset:32768\n"
"buffer_load_dwordx4 v21, s[36:39], s65 offen offset:1024 lds\n"
"v_mfma_f32_16x16x32_bf16 v[44:47], a[16:19], a[4:7], v[44:47]\n"
"ds_read_b64_tr_b16 a[70:71], v39 offset:33792\n"
"s_and_b32 s57, s25, 15\n"
"v_mfma_f32_16x16x32_bf16 v[60:63], a[20:23], a[4:7], v[60:63]\n"
"ds_read_b128 a[48:51], v33 offset:24576\n"
"buffer_load_dwordx4 v22, s[36:39], s65 offen offset:2048 lds\n"
"v_mfma_f32_16x16x32_bf16 v[48:51], a[16:19], a[8:11], v[48:51]\n"
"ds_read_b128 a[52:55], v33 offset:26624\n"
"s_lshr_b32 s59, s25, 4\n"
"v_mfma_f32_16x16x32_bf16 v[64:67], a[20:23], a[8:11], v[64:67]\n"
"ds_read_b128 a[56:59], v33 offset:28672\n"
"buffer_load_dwordx4 v23, s[36:39], s65 offen offset:3072 lds\n"
"s_mul_i32 s89, s57, 458752\n"
"v_mfma_f32_16x16x32_bf16 v[52:55], a[16:19], a[12:15], v[52:55]\n"
"ds_read_b128 a[60:63], v33 offset:30720\n"
"v_mfma_f32_16x16x32_bf16 v[68:71], a[20:23], a[12:15], v[68:71]\n"
"s_mul_i32 s56, s59, 128\n"
"s_waitcnt lgkmcnt(8)\n"
"v_mfma_f32_16x16x32_bf16 v[72:75], a[40:43], v[252:255], v[72:75]\n"
"s_waitcnt vmcnt(12)\n"
"s_barrier\n"
"v_mfma_f32_16x16x32_bf16 v[76:79], a[44:47], v[252:255], v[76:79]\n"
"ds_read_b64_tr_b16 a[16:17], v34 offset:0\n"
"s_add_u32 m0, s69, 73728\n"
"s_add_u32 s89, s89, s56\n"
"v_mfma_f32_16x16x32_bf16 v[40:43], a[40:43], a[24:27], v[40:43]\n"
"ds_read_b64_tr_b16 a[18:19], v34 offset:1024\n"
"s_add_u32 s64, s66, 1152\n"
"v_mfma_f32_16x16x32_bf16 v[56:59], a[44:47], a[24:27], v[56:59]\n"
"ds_read_b64_tr_b16 a[20:21], v35 offset:0\n"
"s_add_u32 s89, s89, 0\n"
"v_mfma_f32_16x16x32_bf16 v[44:47], a[40:43], a[28:31], v[44:47]\n"
"ds_read_b64_tr_b16 a[22:23], v35 offset:1024\n"
"buffer_load_dwordx4 v18, s[32:35], s64 offen offset:0 lds\n"
"v_mfma_f32_16x16x32_bf16 v[60:63], a[44:47], a[28:31], v[60:63]\n"
"ds_read_b128 a[0:3], v28 offset:0\n"
"s_mul_i32 s11, s25, 256\n"
"v_mfma_f32_16x16x32_bf16 v[48:51], a[40:43], a[32:35], v[48:51]\n"
"ds_read_b128 a[4:7], v28 offset:2048\n"
"buffer_load_dwordx4 v19, s[32:35], s64 offen offset:1024 lds\n"
"v_mfma_f32_16x16x32_bf16 v[64:67], a[44:47], a[32:35], v[64:67]\n"
"ds_read_b128 a[8:11], v28 offset:4096\n"
"s_add_u32 s11, s11, 786432\n"
"v_mfma_f32_16x16x32_bf16 v[52:55], a[40:43], a[36:39], v[52:55]\n"
"ds_read_b128 a[12:15], v28 offset:6144\n"
"s_add_u32 m0, s70, 73728\n"
"s_mul_i32 s24, s59, 2048\n"
"v_mfma_f32_16x16x32_bf16 v[68:71], a[44:47], a[36:39], v[68:71]\n"
"s_waitcnt lgkmcnt(8)\n"
"v_mfma_f32_16x16x32_bf16 v[72:75], a[64:67], v[252:255], v[72:75]\n"
"s_add_u32 s65, s67, 1179648\n"
"s_mul_i32 s56, s57, 229376\n"
"v_mfma_f32_16x16x32_bf16 v[76:79], a[68:71], v[252:255], v[76:79]\n"
"ds_read_b64_tr_b16 a[40:41], v34 offset:8192\n"
"buffer_load_dwordx4 v20, s[36:39], s65 offen offset:0 lds\n"
"v_mfma_f32_16x16x32_bf16 v[40:43], a[64:67], a[48:51], v[40:43]\n"
"ds_read_b64_tr_b16 a[42:43], v34 offset:9216\n"
"s_add_u32 s24, s24, s56\n"
"v_mfma_f32_16x16x32_bf16 v[56:59], a[68:71], a[48:51], v[56:59]\n"
"ds_read_b64_tr_b16 a[44:45], v35 offset:8192\n"
"buffer_load_dwordx4 v21, s[36:39], s65 offen offset:1024 lds\n"
"v_mfma_f32_16x16x32_bf16 v[44:47], a[64:67], a[52:55], v[44:47]\n"
"ds_read_b64_tr_b16 a[46:47], v35 offset:9216\n"
"s_add_u32 s24, s24, 17825792\n"
"v_mfma_f32_16x16x32_bf16 v[60:63], a[68:71], a[52:55], v[60:63]\n"
"ds_read_b128 a[24:27], v29 offset:0\n"
"buffer_load_dwordx4 v22, s[36:39], s65 offen offset:2048 lds\n"
"v_mfma_f32_16x16x32_bf16 v[48:51], a[64:67], a[56:59], v[48:51]\n"
"ds_read_b128 a[28:31], v29 offset:2048\n"
"s_mul_i32 s60, s59, 9363\n"
"v_mfma_f32_16x16x32_bf16 v[64:67], a[68:71], a[56:59], v[64:67]\n"
"ds_read_b128 a[32:35], v29 offset:4096\n"
"buffer_load_dwordx4 v23, s[36:39], s65 offen offset:3072 lds\n"
"v_mfma_f32_16x16x32_bf16 v[52:55], a[64:67], a[60:63], v[52:55]\n"
"ds_read_b128 a[36:39], v29 offset:6144\n"
"s_lshr_b32 s60, s60, 16\n"
"v_mfma_f32_16x16x32_bf16 v[68:71], a[68:71], a[60:63], v[68:71]\n"
"s_waitcnt lgkmcnt(8)\n"
"v_mfma_f32_16x16x32_bf16 v[72:75], a[16:19], v[252:255], v[72:75]\n"
"s_waitcnt vmcnt(12)\n"
"s_barrier\n"
"s_mul_i32 s62, s60, 7\n"
"v_mfma_f32_16x16x32_bf16 v[76:79], a[20:23], v[252:255], v[76:79]\n"
"ds_read_b64_tr_b16 a[64:65], v34 offset:24576\n"
"s_add_u32 m0, s69, 98304\n"
"s_sub_u32 s62, s59, s62\n"
"v_mfma_f32_16x16x32_bf16 v[40:43], a[16:19], a[0:3], v[40:43]\n"
"ds_read_b64_tr_b16 a[66:67], v34 offset:25600\n"
"s_add_u32 s64, s66, 1280\n"
"v_mfma_f32_16x16x32_bf16 v[56:59], a[20:23], a[0:3], v[56:59]\n"
"ds_read_b64_tr_b16 a[68:69], v35 offset:24576\n"
"s_mul_i32 s60, s60, 224\n"
"v_mfma_f32_16x16x32_bf16 v[44:47], a[16:19], a[4:7], v[44:47]\n"
"ds_read_b64_tr_b16 a[70:71], v35 offset:25600\n"
"buffer_load_dwordx4 v18, s[32:35], s64 offen offset:0 lds\n"
"v_mfma_f32_16x16x32_bf16 v[60:63], a[20:23], a[4:7], v[60:63]\n"
"ds_read_b128 a[48:51], v28 offset:24576\n"
"s_lshl_b32 s90, s62, 5\n"
"v_mfma_f32_16x16x32_bf16 v[48:51], a[16:19], a[8:11], v[48:51]\n"
"ds_read_b128 a[52:55], v28 offset:26624\n"
"buffer_load_dwordx4 v19, s[32:35], s64 offen offset:1024 lds\n"
"v_mfma_f32_16x16x32_bf16 v[64:67], a[20:23], a[8:11], v[64:67]\n"
"ds_read_b128 a[56:59], v28 offset:28672\n"
"s_cmp_ge_u32 s62, 3\n"
"s_cselect_b32 s91, 16, 0\n"
"v_mfma_f32_16x16x32_bf16 v[52:55], a[16:19], a[12:15], v[52:55]\n"
"ds_read_b128 a[60:63], v28 offset:30720\n"
"s_add_u32 m0, s70, 98304\n"
"v_mfma_f32_16x16x32_bf16 v[68:71], a[20:23], a[12:15], v[68:71]\n"
"s_add_u32 s90, s90, s91\n"
"s_waitcnt lgkmcnt(8)\n"
"v_mfma_f32_16x16x32_bf16 v[72:75], a[40:43], v[252:255], v[72:75]\n"
"s_add_u32 s65, s67, 1310720\n"
"v_mfma_f32_16x16x32_bf16 v[76:79], a[44:47], v[252:255], v[76:79]\n"
"ds_read_b64_tr_b16 a[16:17], v34 offset:32768\n"
"buffer_load_dwordx4 v20, s[36:39], s65 offen offset:0 lds\n"
"s_cmp_eq_u32 s62, 6\n"
"s_cselect_b32 s90, 96, s90\n"
"s_cselect_b32 s91, 196608, 0\n"
"v_mfma_f32_16x16x32_bf16 v[40:43], a[40:43], a[24:27], v[40:43]\n"
"ds_read_b64_tr_b16 a[18:19], v34 offset:33792\n"
"s_add_u32 s91, s91, 15360\n"
"v_mfma_f32_16x16x32_bf16 v[56:59], a[44:47], a[24:27], v[56:59]\n"
"ds_read_b64_tr_b16 a[20:21], v35 offset:32768\n"
"buffer_load_dwordx4 v21, s[36:39], s65 offen offset:1024 lds\n"
"v_mfma_f32_16x16x32_bf16 v[44:47], a[40:43], a[28:31], v[44:47]\n"
"ds_read_b64_tr_b16 a[22:23], v35 offset:33792\n"
"s_add_u32 s60, s60, s90\n"
"v_mfma_f32_16x16x32_bf16 v[60:63], a[44:47], a[28:31], v[60:63]\n"
"ds_read_b128 a[0:3], v29 offset:24576\n"
"buffer_load_dwordx4 v22, s[36:39], s65 offen offset:2048 lds\n"
"v_mfma_f32_16x16x32_bf16 v[48:51], a[40:43], a[32:35], v[48:51]\n"
"ds_read_b128 a[4:7], v29 offset:26624\n"
"s_mul_i32 s90, s60, 2048\n"
"v_mfma_f32_16x16x32_bf16 v[64:67], a[44:47], a[32:35], v[64:67]\n"
"ds_read_b128 a[8:11], v29 offset:28672\n"
"buffer_load_dwordx4 v23, s[36:39], s65 offen offset:3072 lds\n"
"v_mfma_f32_16x16x32_bf16 v[52:55], a[40:43], a[36:39], v[52:55]\n"
"ds_read_b128 a[12:15], v29 offset:30720\n"
"s_mul_i32 s56, s57, 128\n"
"v_mfma_f32_16x16x32_bf16 v[68:71], a[44:47], a[36:39], v[68:71]\n"
"s_waitcnt lgkmcnt(8)\n"
"v_mfma_f32_16x16x32_bf16 v[72:75], a[64:67], v[252:255], v[72:75]\n"
"s_waitcnt vmcnt(12)\n"
"s_barrier\n"
"s_add_u32 s90, s90, s56\n"
"v_mfma_f32_16x16x32_bf16 v[76:79], a[68:71], v[252:255], v[76:79]\n"
"ds_read_b64_tr_b16 a[40:41], v36 offset:0\n"
"s_add_u32 m0, s69, 122880\n"
"v_mfma_f32_16x16x32_bf16 v[40:43], a[64:67], a[48:51], v[40:43]\n"
"ds_read_b64_tr_b16 a[42:43], v36 offset:1024\n"
"s_add_u32 s64, s66, 1408\n"
"s_add_u32 s90, s90, 0\n"
"v_mfma_f32_16x16x32_bf16 v[56:59], a[68:71], a[48:51], v[56:59]\n"
"ds_read_b64_tr_b16 a[44:45], v37 offset:0\n"
"s_lshl_b32 s59, s59, 2\n"
"v_mfma_f32_16x16x32_bf16 v[44:47], a[64:67], a[52:55], v[44:47]\n"
"ds_read_b64_tr_b16 a[46:47], v37 offset:1024\n"
"buffer_load_dwordx4 v18, s[32:35], s64 offen offset:0 lds\n"
"v_mfma_f32_16x16x32_bf16 v[60:63], a[68:71], a[52:55], v[60:63]\n"
"ds_read_b128 a[24:27], v30 offset:0\n"
"s_sub_u32 s60, s59, 112\n"
"v_mfma_f32_16x16x32_bf16 v[48:51], a[64:67], a[56:59], v[48:51]\n"
"ds_read_b128 a[28:31], v30 offset:2048\n"
"buffer_load_dwordx4 v19, s[32:35], s64 offen offset:1024 lds\n"
"v_mfma_f32_16x16x32_bf16 v[64:67], a[68:71], a[56:59], v[64:67]\n"
"ds_read_b128 a[32:35], v30 offset:4096\n"
"s_cmp_lt_u32 s59, 112\n"
"s_cselect_b32 s59, s59, s60\n"
"s_cselect_b32 s60, 3670016, 0\n"
"v_mfma_f32_16x16x32_bf16 v[52:55], a[64:67], a[60:63], v[52:55]\n"
"ds_read_b128 a[36:39], v30 offset:6144\n"
"s_add_u32 m0, s70, 122880\n"
"v_mfma_f32_16x16x32_bf16 v[68:71], a[68:71], a[60:63], v[68:71]\n"
"s_mul_i32 s62, s57, 14336\n"
"s_waitcnt lgkmcnt(8)\n"
"v_mfma_f32_16x16x32_bf16 v[72:75], a[16:19], v[252:255], v[72:75]\n"
"s_add_u32 s65, s67, 1441792\n"
"v_mfma_f32_16x16x32_bf16 v[76:79], a[20:23], v[252:255], v[76:79]\n"
"ds_read_b64_tr_b16 a[64:65], v36 offset:8192\n"
"buffer_load_dwordx4 v20, s[36:39], s65 offen offset:0 lds\n"
"s_mul_i32 s56, s59, 1024\n"
"v_mfma_f32_16x16x32_bf16 v[40:43], a[16:19], a[0:3], v[40:43]\n"
"ds_read_b64_tr_b16 a[66:67], v36 offset:9216\n"
"v_mfma_f32_16x16x32_bf16 v[56:59], a[20:23], a[0:3], v[56:59]\n"
"ds_read_b64_tr_b16 a[68:69], v37 offset:8192\n"
"buffer_load_dwordx4 v21, s[36:39], s65 offen offset:1024 lds\n"
"s_add_u32 s62, s62, s56\n"
"v_mfma_f32_16x16x32_bf16 v[44:47], a[16:19], a[4:7], v[44:47]\n"
"ds_read_b64_tr_b16 a[70:71], v37 offset:9216\n"
"v_mfma_f32_16x16x32_bf16 v[60:63], a[20:23], a[4:7], v[60:63]\n"
"ds_read_b128 a[48:51], v31 offset:0\n"
"buffer_load_dwordx4 v22, s[36:39], s65 offen offset:2048 lds\n"
"s_add_u32 s62, s62, 22020096\n"
"v_mfma_f32_16x16x32_bf16 v[48:51], a[16:19], a[8:11], v[48:51]\n"
"ds_read_b128 a[52:55], v31 offset:2048\n"
"s_add_u32 s60, s60, s62\n"
"v_mfma_f32_16x16x32_bf16 v[64:67], a[20:23], a[8:11], v[64:67]\n"
"ds_read_b128 a[56:59], v31 offset:4096\n"
"buffer_load_dwordx4 v23, s[36:39], s65 offen offset:3072 lds\n"
"v_mfma_f32_16x16x32_bf16 v[52:55], a[16:19], a[12:15], v[52:55]\n"
"ds_read_b128 a[60:63], v31 offset:6144\n"
"s_mul_i32 s62, s59, 9363\n"
"v_mfma_f32_16x16x32_bf16 v[68:71], a[20:23], a[12:15], v[68:71]\n"
"s_waitcnt lgkmcnt(8)\n"
"v_mfma_f32_16x16x32_bf16 v[72:75], a[40:43], v[252:255], v[72:75]\n"
"s_waitcnt vmcnt(12)\n"
"s_barrier\n"
"s_lshr_b32 s62, s62, 16\n"
"v_mfma_f32_16x16x32_bf16 v[76:79], a[44:47], v[252:255], v[76:79]\n"
"ds_read_b64_tr_b16 a[16:17], v36 offset:24576\n"
"s_add_u32 m0, s69, 0\n"
"v_mfma_f32_16x16x32_bf16 v[40:43], a[40:43], a[24:27], v[40:43]\n"
"ds_read_b64_tr_b16 a[18:19], v36 offset:25600\n"
"s_add_u32 s64, s66, 1536\n"
"s_mul_i32 s62, s62, 222208\n"
"v_mfma_f32_16x16x32_bf16 v[56:59], a[44:47], a[24:27], v[56:59]\n"
"ds_read_b64_tr_b16 a[20:21], v37 offset:24576\n"
"v_mfma_f32_16x16x32_bf16 v[44:47], a[40:43], a[28:31], v[44:47]\n"
"ds_read_b64_tr_b16 a[22:23], v37 offset:25600\n"
"buffer_load_dwordx4 v18, s[32:35], s64 offen offset:0 lds\n"
"s_add_u32 s7, s60, s62\n"
"v_mfma_f32_16x16x32_bf16 v[60:63], a[44:47], a[28:31], v[60:63]\n"
"ds_read_b128 a[0:3], v30 offset:24576\n"
"v_mfma_f32_16x16x32_bf16 v[48:51], a[40:43], a[32:35], v[48:51]\n"
"ds_read_b128 a[4:7], v30 offset:26624\n"
"buffer_load_dwordx4 v19, s[32:35], s64 offen offset:1024 lds\n"
"s_add_u32 s59, s59, 1\n"
"v_mfma_f32_16x16x32_bf16 v[64:67], a[44:47], a[32:35], v[64:67]\n"
"ds_read_b128 a[8:11], v30 offset:28672\n"
"s_add_u32 s60, s60, 1024\n"
"v_mfma_f32_16x16x32_bf16 v[52:55], a[40:43], a[36:39], v[52:55]\n"
"ds_read_b128 a[12:15], v30 offset:30720\n"
"s_add_u32 m0, s70, 0\n"
"v_mfma_f32_16x16x32_bf16 v[68:71], a[44:47], a[36:39], v[68:71]\n"
"s_mul_i32 s62, s59, 9363\n"
".Lmk_pg_2:\n"
"s_cbranch_execz .Lmk_ret_0\n"
"s_waitcnt lgkmcnt(8)\n"
"v_mfma_f32_16x16x32_bf16 v[72:75], a[64:67], v[252:255], v[72:75]\n"
"s_add_u32 s65, s67, 1572864\n"
"v_mfma_f32_16x16x32_bf16 v[76:79], a[68:71], v[252:255], v[76:79]\n"
"ds_read_b64_tr_b16 a[40:41], v36 offset:32768\n"
"buffer_load_dwordx4 v20, s[36:39], s65 offen offset:0 lds\n"
"s_lshr_b32 s62, s62, 16\n"
"v_mfma_f32_16x16x32_bf16 v[40:43], a[64:67], a[48:51], v[40:43]\n"
"ds_read_b64_tr_b16 a[42:43], v36 offset:33792\n"
"v_mfma_f32_16x16x32_bf16 v[56:59], a[68:71], a[48:51], v[56:59]\n"
"ds_read_b64_tr_b16 a[44:45], v37 offset:32768\n"
"buffer_load_dwordx4 v21, s[36:39], s65 offen offset:1024 lds\n"
"s_mul_i32 s62, s62, 222208\n"
"v_mfma_f32_16x16x32_bf16 v[44:47], a[64:67], a[52:55], v[44:47]\n"
"ds_read_b64_tr_b16 a[46:47], v37 offset:33792\n"
"v_mfma_f32_16x16x32_bf16 v[60:63], a[68:71], a[52:55], v[60:63]\n"
"ds_read_b128 a[24:27], v31 offset:24576\n"
"buffer_load_dwordx4 v22, s[36:39], s65 offen offset:2048 lds\n"
"s_add_u32 s8, s60, s62\n"
"v_mfma_f32_16x16x32_bf16 v[48:51], a[64:67], a[56:59], v[48:51]\n"
"ds_read_b128 a[28:31], v31 offset:26624\n"
"v_mfma_f32_16x16x32_bf16 v[64:67], a[68:71], a[56:59], v[64:67]\n"
"ds_read_b128 a[32:35], v31 offset:28672\n"
"buffer_load_dwordx4 v23, s[36:39], s65 offen offset:3072 lds\n"
"s_add_u32 s59, s59, 1\n"
"v_mfma_f32_16x16x32_bf16 v[52:55], a[64:67], a[60:63], v[52:55]\n"
"ds_read_b128 a[36:39], v31 offset:30720\n"
"v_mfma_f32_16x16x32_bf16 v[68:71], a[68:71], a[60:63], v[68:71]\n"
"s_add_u32 s60, s60, 1024\n"
"s_waitcnt lgkmcnt(8)\n"
"v_mfma_f32_16x16x32_bf16 v[72:75], a[16:19], v[252:255], v[72:75]\n"
"s_waitcnt vmcnt(12)\n"
"s_barrier\n"
"s_mul_i32 s62, s59, 9363\n"
"v_mfma_f32_16x16x32_bf16 v[76:79], a[20:23], v[252:255], v[76:79]\n"
"ds_read_b64_tr_b16 a[64:65], v38 offset:0\n"
"s_add_u32 m0, s69, 24576\n"
"v_mfma_f32_16x16x32_bf16 v[40:43], a[16:19], a[0:3], v[40:43]\n"
"ds_read_b64_tr_b16 a[66:67], v38 offset:1024\n"
"s_add_u32 s64, s66, 1664\n"
"s_lshr_b32 s62, s62, 16\n"
"v_mfma_f32_16x16x32_bf16 v[56:59], a[20:23], a[0:3], v[56:59]\n"
"ds_read_b64_tr_b16 a[68:69], v39 offset:0\n"
"v_mfma_f32_16x16x32_bf16 v[44:47], a[16:19], a[4:7], v[44:47]\n"
"ds_read_b64_tr_b16 a[70:71], v39 offset:1024\n"
"buffer_load_dwordx4 v18, s[32:35], s64 offen offset:0 lds\n"
"s_mul_i32 s62, s62, 222208\n"
"v_mfma_f32_16x16x32_bf16 v[60:63], a[20:23], a[4:7], v[60:63]\n"
"ds_read_b128 a[48:51], v32 offset:0\n"
"v_mfma_f32_16x16x32_bf16 v[48:51], a[16:19], a[8:11], v[48:51]\n"
"ds_read_b128 a[52:55], v32 offset:2048\n"
"buffer_load_dwordx4 v19, s[32:35], s64 offen offset:1024 lds\n"
"s_add_u32 s9, s60, s62\n"
"v_mfma_f32_16x16x32_bf16 v[64:67], a[20:23], a[8:11], v[64:67]\n"
"ds_read_b128 a[56:59], v32 offset:4096\n"
"v_mfma_f32_16x16x32_bf16 v[52:55], a[16:19], a[12:15], v[52:55]\n"
"ds_read_b128 a[60:63], v32 offset:6144\n"
"s_add_u32 m0, s70, 24576\n"
"s_add_u32 s59, s59, 1\n"
"v_mfma_f32_16x16x32_bf16 v[68:71], a[20:23], a[12:15], v[68:71]\n"
"s_waitcnt lgkmcnt(8)\n"
"v_mfma_f32_16x16x32_bf16 v[72:75], a[40:43], v[252:255], v[72:75]\n"
"s_add_u32 s65, s67, 1703936\n"
"s_add_u32 s60, s60, 1024\n"
"v_mfma_f32_16x16x32_bf16 v[76:79], a[44:47], v[252:255], v[76:79]\n"
"ds_read_b64_tr_b16 a[16:17], v38 offset:8192\n"
"buffer_load_dwordx4 v20, s[36:39], s65 offen offset:0 lds\n"
"s_mul_i32 s62, s59, 9363\n"
"v_mfma_f32_16x16x32_bf16 v[40:43], a[40:43], a[24:27], v[40:43]\n"
"ds_read_b64_tr_b16 a[18:19], v38 offset:9216\n"
"v_mfma_f32_16x16x32_bf16 v[56:59], a[44:47], a[24:27], v[56:59]\n"
"ds_read_b64_tr_b16 a[20:21], v39 offset:8192\n"
"buffer_load_dwordx4 v21, s[36:39], s65 offen offset:1024 lds\n"
"s_lshr_b32 s62, s62, 16\n"
"v_mfma_f32_16x16x32_bf16 v[44:47], a[40:43], a[28:31], v[44:47]\n"
"ds_read_b64_tr_b16 a[22:23], v39 offset:9216\n"
"v_mfma_f32_16x16x32_bf16 v[60:63], a[44:47], a[28:31], v[60:63]\n"
"ds_read_b128 a[0:3], v33 offset:0\n"
"buffer_load_dwordx4 v22, s[36:39], s65 offen offset:2048 lds\n"
"s_mul_i32 s62, s62, 222208\n"
"v_mfma_f32_16x16x32_bf16 v[48:51], a[40:43], a[32:35], v[48:51]\n"
"ds_read_b128 a[4:7], v33 offset:2048\n"
"v_mfma_f32_16x16x32_bf16 v[64:67], a[44:47], a[32:35], v[64:67]\n"
"ds_read_b128 a[8:11], v33 offset:4096\n"
"buffer_load_dwordx4 v23, s[36:39], s65 offen offset:3072 lds\n"
"s_add_u32 s10, s60, s62\n"
"v_mfma_f32_16x16x32_bf16 v[52:55], a[40:43], a[36:39], v[52:55]\n"
"ds_read_b128 a[12:15], v33 offset:6144\n"
"v_mfma_f32_16x16x32_bf16 v[68:71], a[44:47], a[36:39], v[68:71]\n"
"s_mul_i32 s25, s44, 262144\n"
"s_waitcnt lgkmcnt(8)\n"
"v_mfma_f32_16x16x32_bf16 v[72:75], a[64:67], v[252:255], v[72:75]\n"
"s_waitcnt vmcnt(12)\n"
"s_barrier\n"
"v_mfma_f32_16x16x32_bf16 v[76:79], a[68:71], v[252:255], v[76:79]\n"
"ds_read_b64_tr_b16 a[40:41], v38 offset:24576\n"
"s_add_u32 m0, s69, 49152\n"
"s_mul_i32 s56, s45, 32768\n"
"v_mfma_f32_16x16x32_bf16 v[40:43], a[64:67], a[48:51], v[40:43]\n"
"ds_read_b64_tr_b16 a[42:43], v38 offset:25600\n"
"s_add_u32 s64, s66, 1792\n"
"s_add_u32 s25, s25, s56\n"
"v_mfma_f32_16x16x32_bf16 v[56:59], a[68:71], a[48:51], v[56:59]\n"
"ds_read_b64_tr_b16 a[44:45], v39 offset:24576\n"
"v_mfma_f32_16x16x32_bf16 v[44:47], a[64:67], a[52:55], v[44:47]\n"
"ds_read_b64_tr_b16 a[46:47], v39 offset:25600\n"
"buffer_load_dwordx4 v18, s[32:35], s64 offen offset:0 lds\n"
"s_mul_i32 s56, s41, 8192\n"
"v_mfma_f32_16x16x32_bf16 v[60:63], a[68:71], a[52:55], v[60:63]\n"
"ds_read_b128 a[24:27], v32 offset:24576\n"
"v_mfma_f32_16x16x32_bf16 v[48:51], a[64:67], a[56:59], v[48:51]\n"
"ds_read_b128 a[28:31], v32 offset:26624\n"
"buffer_load_dwordx4 v19, s[32:35], s64 offen offset:1024 lds\n"
"s_add_u32 s25, s25, s56\n"
"v_mfma_f32_16x16x32_bf16 v[64:67], a[68:71], a[56:59], v[64:67]\n"
"ds_read_b128 a[32:35], v32 offset:28672\n"
"v_mfma_f32_16x16x32_bf16 v[52:55], a[64:67], a[60:63], v[52:55]\n"
"ds_read_b128 a[36:39], v32 offset:30720\n"
"s_add_u32 m0, s70, 49152\n"
"s_add_u32 s25, s25, 29360128\n"
"v_mfma_f32_16x16x32_bf16 v[68:71], a[68:71], a[60:63], v[68:71]\n"
"s_waitcnt lgkmcnt(8)\n"
"v_mfma_f32_16x16x32_bf16 v[72:75], a[16:19], v[252:255], v[72:75]\n"
"s_add_u32 s65, s67, 1835008\n"
"v_and_b32 v16, 12, v1\n"
"v_mfma_f32_16x16x32_bf16 v[76:79], a[20:23], v[252:255], v[76:79]\n"
"ds_read_b64_tr_b16 a[64:65], v38 offset:32768\n"
"buffer_load_dwordx4 v20, s[36:39], s65 offen offset:0 lds\n"
"v_mfma_f32_16x16x32_bf16 v[40:43], a[16:19], a[0:3], v[40:43]\n"
"ds_read_b64_tr_b16 a[66:67], v38 offset:33792\n"
"v_lshlrev_b32 v16, 1, v16\n"
"v_mfma_f32_16x16x32_bf16 v[56:59], a[20:23], a[0:3], v[56:59]\n"
"ds_read_b64_tr_b16 a[68:69], v39 offset:32768\n"
"buffer_load_dwordx4 v21, s[36:39], s65 offen offset:1024 lds\n"
"v_mfma_f32_16x16x32_bf16 v[44:47], a[16:19], a[4:7], v[44:47]\n"
"ds_read_b64_tr_b16 a[70:71], v39 offset:33792\n"
"v_and_b32 v15, 3, v1\n"
"v_mfma_f32_16x16x32_bf16 v[60:63], a[20:23], a[4:7], v[60:63]\n"
"ds_read_b128 a[48:51], v33 offset:24576\n"
"buffer_load_dwordx4 v22, s[36:39], s65 offen offset:2048 lds\n"
"v_add_u32 v16, v16, v15\n"
"v_mfma_f32_16x16x32_bf16 v[48:51], a[16:19], a[8:11], v[48:51]\n"
"ds_read_b128 a[52:55], v33 offset:26624\n"
"v_mfma_f32_16x16x32_bf16 v[64:67], a[20:23], a[8:11], v[64:67]\n"
"ds_read_b128 a[56:59], v33 offset:28672\n"
"buffer_load_dwordx4 v23, s[36:39], s65 offen offset:3072 lds\n"
"v_lshlrev_b32 v16, 11, v16\n"
"v_mfma_f32_16x16x32_bf16 v[52:55], a[16:19], a[12:15], v[52:55]\n"
"ds_read_b128 a[60:63], v33 offset:30720\n"
"v_mfma_f32_16x16x32_bf16 v[68:71], a[20:23], a[12:15], v[68:71]\n"
"v_and_b32 v15, 48, v1\n"
"s_waitcnt lgkmcnt(8)\n"
"v_mfma_f32_16x16x32_bf16 v[72:75], a[40:43], v[252:255], v[72:75]\n"
"s_waitcnt vmcnt(12)\n"
"s_barrier\n"
"v_mfma_f32_16x16x32_bf16 v[76:79], a[44:47], v[252:255], v[76:79]\n"
"ds_read_b64_tr_b16 a[16:17], v34 offset:0\n"
"s_add_u32 m0, s69, 73728\n"
"v_add_u32 v16, v16, v15\n"
"v_mfma_f32_16x16x32_bf16 v[40:43], a[40:43], a[24:27], v[40:43]\n"
"ds_read_b64_tr_b16 a[18:19], v34 offset:1024\n"
"s_add_u32 s64, s66, 1920\n"
"v_mfma_f32_16x16x32_bf16 v[56:59], a[44:47], a[24:27], v[56:59]\n"
"ds_read_b64_tr_b16 a[20:21], v35 offset:0\n"
"v_mov_b32 v236, 3217080256\n"
"v_mfma_f32_16x16x32_bf16 v[44:47], a[40:43], a[28:31], v[44:47]\n"
"ds_read_b64_tr_b16 a[22:23], v35 offset:1024\n"
"buffer_load_dwordx4 v18, s[32:35], s64 offen offset:0 lds\n"
"v_mfma_f32_16x16x32_bf16 v[60:63], a[44:47], a[28:31], v[60:63]\n"
"ds_read_b128 a[0:3], v28 offset:0\n"
"v_mov_b32 v237, 3217080256\n"
"v_mfma_f32_16x16x32_bf16 v[48:51], a[40:43], a[32:35], v[48:51]\n"
"ds_read_b128 a[4:7], v28 offset:2048\n"
"buffer_load_dwordx4 v19, s[32:35], s64 offen offset:1024 lds\n"
"v_mov_b32 v238, 3217080256\n"
"v_mfma_f32_16x16x32_bf16 v[64:67], a[44:47], a[32:35], v[64:67]\n"
"ds_read_b128 a[8:11], v28 offset:4096\n"
"v_mfma_f32_16x16x32_bf16 v[52:55], a[40:43], a[36:39], v[52:55]\n"
"ds_read_b128 a[12:15], v28 offset:6144\n"
"s_add_u32 m0, s70, 73728\n"
"v_mov_b32 v239, 3217080256\n"
"v_mfma_f32_16x16x32_bf16 v[68:71], a[44:47], a[36:39], v[68:71]\n"
"s_waitcnt lgkmcnt(8)\n"
"v_mfma_f32_16x16x32_bf16 v[72:75], a[64:67], v[252:255], v[72:75]\n"
"s_add_u32 s65, s67, 1966080\n"
"v_mfma_f32_32x32x16_bf16 v[152:167], v[228:231], v[228:231], 0\n"
"v_mfma_f32_16x16x32_bf16 v[76:79], a[68:71], v[252:255], v[76:79]\n"
"ds_read_b64_tr_b16 a[40:41], v34 offset:8192\n"
"buffer_load_dwordx4 v20, s[36:39], s65 offen offset:0 lds\n"
"v_mfma_f32_16x16x32_bf16 v[40:43], a[64:67], a[48:51], v[40:43]\n"
"ds_read_b64_tr_b16 a[42:43], v34 offset:9216\n"
"v_mfma_f32_32x32x16_bf16 v[168:183], v[228:231], v[228:231], 0\n"
"v_mfma_f32_16x16x32_bf16 v[56:59], a[68:71], a[48:51], v[56:59]\n"
"ds_read_b64_tr_b16 a[44:45], v35 offset:8192\n"
"buffer_load_dwordx4 v21, s[36:39], s65 offen offset:1024 lds\n"
"v_mfma_f32_16x16x32_bf16 v[44:47], a[64:67], a[52:55], v[44:47]\n"
"ds_read_b64_tr_b16 a[46:47], v35 offset:9216\n"
"v_mfma_f32_32x32x16_bf16 v[192:207], v[252:255], v[236:239], 0\n"
"v_mfma_f32_16x16x32_bf16 v[60:63], a[68:71], a[52:55], v[60:63]\n"
"ds_read_b128 a[24:27], v29 offset:0\n"
"buffer_load_dwordx4 v22, s[36:39], s65 offen offset:2048 lds\n"
"v_mfma_f32_16x16x32_bf16 v[48:51], a[64:67], a[56:59], v[48:51]\n"
"ds_read_b128 a[28:31], v29 offset:2048\n"
"v_mfma_f32_32x32x16_bf16 v[208:223], v[252:255], v[236:239], 0\n"
"v_mfma_f32_16x16x32_bf16 v[64:67], a[68:71], a[56:59], v[64:67]\n"
"ds_read_b128 a[32:35], v29 offset:4096\n"
"buffer_load_dwordx4 v23, s[36:39], s65 offen offset:3072 lds\n"
"v_mfma_f32_16x16x32_bf16 v[52:55], a[64:67], a[60:63], v[52:55]\n"
"ds_read_b128 a[36:39], v29 offset:6144\n"
"v_mfma_f32_16x16x32_bf16 v[68:71], a[68:71], a[60:63], v[68:71]\n"
"s_waitcnt lgkmcnt(8)\n"
"v_mfma_f32_16x16x32_bf16 v[72:75], a[16:19], v[252:255], v[72:75]\n"
"s_waitcnt vmcnt(12)\n"
"s_barrier\n"
"s_getpc_b64 s[36:37]\n"
".Lmk_pc:\n"
"s_sub_u32 s36, s36, .Lmk_pc-.Lmk_entry\n"
"s_subb_u32 s37, s37, 0\n"
"s_and_b32 s37, s37, 0xffff\n"
"s_mov_b32 s38, 83836\n"
"v_mfma_f32_16x16x32_bf16 v[76:79], a[20:23], v[252:255], v[76:79]\n"
"ds_read_b64_tr_b16 a[64:65], v34 offset:24576\n"
"s_mul_i32 s67, s46, 131072\n"
"s_mul_i32 s56, s47, 256\n"
"s_add_u32 s67, s67, s56\n"
"s_mul_i32 s56, s41, 64\n"
"s_add_u32 s67, s67, s56\n"
"s_add_u32 s67, s67, 0\n"
"v_mfma_f32_16x16x32_bf16 v[40:43], a[16:19], a[0:3], v[40:43]\n"
"ds_read_b64_tr_b16 a[66:67], v34 offset:25600\n"
"v_mfma_f32_16x16x32_bf16 v[56:59], a[20:23], a[0:3], v[56:59]\n"
"ds_read_b64_tr_b16 a[68:69], v35 offset:24576\n"
"s_mov_b32 s82, 69632\n"
"s_mul_i32 s66, s47, 262144\n"
"s_mul_i32 s56, s41, 65536\n"
"s_add_u32 s66, s66, s56\n"
"s_add_u32 s66, s66, 15728640\n"
"v_mfma_f32_16x16x32_bf16 v[44:47], a[16:19], a[4:7], v[44:47]\n"
"ds_read_b64_tr_b16 a[70:71], v35 offset:25600\n"
"buffer_load_dwordx4 v[80:83], v2, s[28:31], s82 offen offset:0 sc1\n"
"v_mfma_f32_16x16x32_bf16 v[60:63], a[20:23], a[4:7], v[60:63]\n"
"ds_read_b128 a[48:51], v28 offset:24576\n"
"buffer_load_dwordx4 v[84:87], v2, s[28:31], s82 offen offset:1024 sc1\n"
"s_lshl_b32 s59, s47, 2\n"
"s_bfm_b32 s59, s59, 0\n"
"s_and_b32 s60, s59, 0x76543210\n"
"s_lshl_b32 s60, s60, 4\n"
"s_lshl_b32 s59, s59, 4\n"
"s_or_b32 s59, s59, 15\n"
"s_andn2_b32 s68, 0x76543210, s59\n"
"s_or_b32 s68, s68, s60\n"
"s_or_b32 s68, s68, s47\n"
"v_mfma_f32_16x16x32_bf16 v[48:51], a[16:19], a[8:11], v[48:51]\n"
"ds_read_b128 a[52:55], v28 offset:26624\n"
"s_mul_i32 s65, s44, 131072\n"
"s_mul_i32 s56, s45, 16384\n"
"s_add_u32 s65, s65, s56\n"
"s_mul_i32 s56, s41, 4096\n"
"s_add_u32 s65, s65, s56\n"
"s_add_u32 s65, s65, 5242880\n"
"v_mfma_f32_16x16x32_bf16 v[64:67], a[20:23], a[8:11], v[64:67]\n"
"ds_read_b128 a[56:59], v28 offset:28672\n"
"v_mfma_f32_16x16x32_bf16 v[52:55], a[16:19], a[12:15], v[52:55]\n"
"ds_read_b128 a[60:63], v28 offset:30720\n"
"v_bfe_u32 v15, v1, 0, 4\n"
"v_mul_u32_u24 v14, 16, v15\n"
"v_bfe_u32 v15, v1, 4, 1\n"
"v_lshl_add_u32 v14, v15, 10, v14\n"
"v_bfe_u32 v15, v1, 5, 1\n"
"v_lshl_add_u32 v14, v15, 9, v14\n"
"v_mfma_f32_16x16x32_bf16 v[68:71], a[20:23], a[12:15], v[68:71]\n"
"s_waitcnt lgkmcnt(4)\n"
"v_mfma_f32_16x16x32_bf16 v[72:75], a[40:43], v[252:255], v[72:75]\n"
"v_mfma_f32_16x16x32_bf16 v[76:79], a[44:47], v[252:255], v[76:79]\n"
"ds_read_b64_tr_b16 a[16:17], v34 offset:32768\n"
"v_mfma_f32_16x16x32_bf16 v[40:43], a[40:43], a[24:27], v[40:43]\n"
"ds_read_b64_tr_b16 a[18:19], v34 offset:33792\n"
"v_mfma_f32_16x16x32_bf16 v[56:59], a[44:47], a[24:27], v[56:59]\n"
"ds_read_b64_tr_b16 a[20:21], v35 offset:32768\n"
"v_mfma_f32_16x16x32_bf16 v[44:47], a[40:43], a[28:31], v[44:47]\n"
"ds_read_b64_tr_b16 a[22:23], v35 offset:33792\n"
"v_mfma_f32_16x16x32_bf16 v[60:63], a[44:47], a[28:31], v[60:63]\n"
"ds_read_b128 a[0:3], v29 offset:24576\n"
"v_mfma_f32_16x16x32_bf16 v[48:51], a[40:43], a[32:35], v[48:51]\n"
"ds_read_b128 a[4:7], v29 offset:26624\n"
"v_mfma_f32_16x16x32_bf16 v[64:67], a[44:47], a[32:35], v[64:67]\n"
"ds_read_b128 a[8:11], v29 offset:28672\n"
"v_mfma_f32_16x16x32_bf16 v[52:55], a[40:43], a[36:39], v[52:55]\n"
"ds_read_b128 a[12:15], v29 offset:30720\n"
"v_mfma_f32_16x16x32_bf16 v[68:71], a[44:47], a[36:39], v[68:71]\n"
"v_mfma_f32_16x16x32_bf16 v[72:75], a[64:67], v[252:255], v[72:75]\n"
"s_waitcnt vmcnt(8)\n"
"s_barrier\n"
"v_mfma_f32_16x16x32_bf16 v[76:79], a[68:71], v[252:255], v[76:79]\n"
"ds_read_b64_tr_b16 a[40:41], v36 offset:0\n"
"s_waitcnt lgkmcnt(9)\n"
"v_mfma_f32_16x16x32_bf16 v[40:43], a[64:67], a[48:51], v[40:43]\n"
"ds_read_b64_tr_b16 a[42:43], v36 offset:1024\n"
"v_mfma_f32_16x16x32_bf16 v[56:59], a[68:71], a[48:51], v[56:59]\n"
"ds_read_b64_tr_b16 a[44:45], v37 offset:0\n"
"v_mfma_f32_16x16x32_bf16 v[44:47], a[64:67], a[52:55], v[44:47]\n"
"ds_read_b64_tr_b16 a[46:47], v37 offset:1024\n"
"v_mfma_f32_16x16x32_bf16 v[60:63], a[68:71], a[52:55], v[60:63]\n"
"ds_read_b128 a[24:27], v30 offset:0\n"
"v_mfma_f32_16x16x32_bf16 v[48:51], a[64:67], a[56:59], v[48:51]\n"
"ds_read_b128 a[28:31], v30 offset:2048\n"
"v_mfma_f32_16x16x32_bf16 v[64:67], a[68:71], a[56:59], v[64:67]\n"
"ds_read_b128 a[32:35], v30 offset:4096\n"
"v_mfma_f32_16x16x32_bf16 v[52:55], a[64:67], a[60:63], v[52:55]\n"
"ds_read_b128 a[36:39], v30 offset:6144\n"
"v_mfma_f32_16x16x32_bf16 v[68:71], a[68:71], a[60:63], v[68:71]\n"
"s_waitcnt lgkmcnt(10)\n"
"v_mfma_f32_16x16x32_bf16 v[72:75], a[16:19], v[252:255], v[72:75]\n"
"v_mfma_f32_16x16x32_bf16 v[76:79], a[20:23], v[252:255], v[76:79]\n"
"ds_read_b64_tr_b16 a[64:65], v36 offset:8192\n"
"v_mfma_f32_16x16x32_bf16 v[40:43], a[16:19], a[0:3], v[40:43]\n"
"ds_read_b64_tr_b16 a[66:67], v36 offset:9216\n"
"s_waitcnt vmcnt(1)\n"
"v_bitop3_b32 v80, v80, v82, s26 bitop3:0x7e\n"
"s_waitcnt vmcnt(0)\n"
"v_bitop3_b32 v84, v84, v86, s26 bitop3:0x7e\n"
"v_or_b32 v80, v80, v84\n"
"v_cmp_ne_u32 vcc, 0, v80\n"
"s_nop 0\n"
"s_cbranch_vccnz .Lmk_fspin_5\n"
".Lmk_fback_6:\n"
"v_mfma_f32_16x16x32_bf16 v[56:59], a[20:23], a[0:3], v[56:59]\n"
"ds_read_b64_tr_b16 a[68:69], v37 offset:8192\n"
"s_and_b32 s81, s43, 7\n"
"s_lshl_b32 s81, s81, 15\n"
"s_add_u32 s81, s81, s53\n"
"v_mfma_f32_16x16x32_bf16 v[44:47], a[16:19], a[4:7], v[44:47]\n"
"ds_read_b64_tr_b16 a[70:71], v37 offset:9216\n"
"v_bfe_u32 v15, v1, 0, 4\n"
"v_mul_u32_u24 v12, 8, v15\n"
"v_mfma_f32_16x16x32_bf16 v[60:63], a[20:23], a[4:7], v[60:63]\n"
"ds_read_b128 a[48:51], v31 offset:0\n"
"s_waitcnt lgkmcnt(7)\n"
"v_mfma_f32_16x16x32_bf16 v[48:51], a[16:19], a[8:11], v[48:51]\n"
"ds_read_b128 a[52:55], v31 offset:2048\n"
"s_mul_i32 s58, s44, 512\n"
"s_add_u32 s58, s58, 98304\n"
"v_mfma_f32_16x16x32_bf16 v[64:67], a[20:23], a[8:11], v[64:67]\n"
"ds_read_b128 a[56:59], v31 offset:4096\n"
"buffer_load_dwordx2 v[88:89], v12, s[28:31], s58 offen offset:0 sc1\n"
"v_mfma_f32_16x16x32_bf16 v[52:55], a[16:19], a[12:15], v[52:55]\n"
"ds_read_b128 a[60:63], v31 offset:6144\n"
"v_mfma_f32_16x16x32_bf16 v[68:71], a[20:23], a[12:15], v[68:71]\n"
"buffer_load_dwordx2 v[90:91], v12, s[28:31], s58 offen offset:128 sc1\n"
"v_mfma_f32_16x16x32_bf16 v[72:75], a[40:43], v[252:255], v[72:75]\n"
"s_barrier\n"
"buffer_load_dwordx2 v[92:93], v12, s[28:31], s58 offen offset:256 sc1\n"
"v_mfma_f32_16x16x32_bf16 v[76:79], a[44:47], v[252:255], v[76:79]\n"
"ds_read_b64_tr_b16 a[16:17], v36 offset:24576\n"
"v_mfma_f32_16x16x32_bf16 v[40:43], a[40:43], a[24:27], v[40:43]\n"
"ds_read_b64_tr_b16 a[18:19], v36 offset:25600\n"
"buffer_load_dwordx2 v[94:95], v12, s[28:31], s58 offen offset:384 sc1\n"
"v_mfma_f32_16x16x32_bf16 v[56:59], a[44:47], a[24:27], v[56:59]\n"
"ds_read_b64_tr_b16 a[20:21], v37 offset:24576\n"
"s_add_u32 s81, s81, 0x8000\n"
"s_and_b32 s81, s81, 0x3ffff\n"
"s_add_u32 s80, s81, 524288\n"
"v_mfma_f32_16x16x32_bf16 v[44:47], a[40:43], a[28:31], v[44:47]\n"
"ds_read_b64_tr_b16 a[22:23], v37 offset:25600\n"
"v_mfma_f32_16x16x32_bf16 v[60:63], a[44:47], a[28:31], v[60:63]\n"
"ds_read_b128 a[0:3], v30 offset:24576\n"
"s_add_u32 m0, s53, 98304\n"
"s_waitcnt lgkmcnt(8)\n"
"v_mfma_f32_16x16x32_bf16 v[48:51], a[40:43], a[32:35], v[48:51]\n"
"ds_read_b128 a[4:7], v30 offset:26624\n"
"buffer_load_dwordx4 v2, s[28:31], s80 offen offset:0 sc1 lds\n"
"v_mfma_f32_16x16x32_bf16 v[64:67], a[44:47], a[32:35], v[64:67]\n"
"ds_read_b128 a[8:11], v30 offset:28672\n"
"v_mfma_f32_16x16x32_bf16 v[52:55], a[40:43], a[36:39], v[52:55]\n"
"ds_read_b128 a[12:15], v30 offset:30720\n"
"buffer_load_dwordx4 v2, s[28:31], s80 offen offset:1024 sc1 lds\n"
"v_mfma_f32_16x16x32_bf16 v[68:71], a[44:47], a[36:39], v[68:71]\n"
"buffer_load_dwordx4 v2, s[28:31], s80 offen offset:2048 sc1 lds\n"
"v_mfma_f32_16x16x32_bf16 v[72:75], a[64:67], v[252:255], v[72:75]\n"
"v_mfma_f32_16x16x32_bf16 v[76:79], a[68:71], v[252:255], v[76:79]\n"
"ds_read_b64_tr_b16 a[40:41], v36 offset:32768\n"
"buffer_load_dwordx4 v2, s[28:31], s80 offen offset:3072 sc1 lds\n"
"v_mfma_f32_16x16x32_bf16 v[40:43], a[64:67], a[48:51], v[40:43]\n"
"ds_read_b64_tr_b16 a[42:43], v36 offset:33792\n"
"s_add_u32 s80, s80, 0x4000\n"
"v_mfma_f32_16x16x32_bf16 v[56:59], a[68:71], a[48:51], v[56:59]\n"
"ds_read_b64_tr_b16 a[44:45], v37 offset:32768\n"
"s_waitcnt lgkmcnt(8)\n"
"v_mfma_f32_16x16x32_bf16 v[44:47], a[64:67], a[52:55], v[44:47]\n"
"ds_read_b64_tr_b16 a[46:47], v37 offset:33792\n"
"s_add_u32 m0, s53, 114688\n"
"v_mfma_f32_16x16x32_bf16 v[60:63], a[68:71], a[52:55], v[60:63]\n"
"ds_read_b128 a[24:27], v31 offset:24576\n"
"buffer_load_dwordx4 v2, s[28:31], s80 offen offset:0 sc1 lds\n"
"v_mfma_f32_16x16x32_bf16 v[48:51], a[64:67], a[56:59], v[48:51]\n"
"ds_read_b128 a[28:31], v31 offset:26624\n"
"buffer_load_dwordx4 v2, s[28:31], s80 offen offset:1024 sc1 lds\n"
"v_mfma_f32_16x16x32_bf16 v[64:67], a[68:71], a[56:59], v[64:67]\n"
"ds_read_b128 a[32:35], v31 offset:28672\n"
"v_mfma_f32_16x16x32_bf16 v[52:55], a[64:67], a[60:63], v[52:55]\n"
"ds_read_b128 a[36:39], v31 offset:30720\n"
"buffer_load_dwordx4 v2, s[28:31], s80 offen offset:2048 sc1 lds\n"
"v_mfma_f32_16x16x32_bf16 v[68:71], a[68:71], a[60:63], v[68:71]\n"
"buffer_load_dwordx4 v2, s[28:31], s80 offen offset:3072 sc1 lds\n"
"v_mfma_f32_16x16x32_bf16 v[72:75], a[16:19], v[252:255], v[72:75]\n"
"s_waitcnt lgkmcnt(8)\n"
"v_mfma_f32_16x16x32_bf16 v[76:79], a[20:23], v[252:255], v[76:79]\n"
"v_max3_f32 v81, v81, v83, v85\n"
"v_mfma_f32_16x16x32_bf16 v[40:43], a[16:19], a[0:3], v[40:43]\n"
"v_max_f32 v81, v81, v87\n"
"v_mfma_f32_16x16x32_bf16 v[56:59], a[20:23], a[0:3], v[56:59]\n"
"v_mfma_f32_16x16x32_bf16 v[44:47], a[16:19], a[4:7], v[44:47]\n"
"v_max_f32_dpp v81, v81, v81 row_ror:8 row_mask:0xf bank_mask:0xf\n"
"v_mfma_f32_16x16x32_bf16 v[60:63], a[20:23], a[4:7], v[60:63]\n"
"s_nop 0\n"
"v_max_f32_dpp v81, v81, v81 row_ror:4 row_mask:0xf bank_mask:0xf\n"
"v_mfma_f32_16x16x32_bf16 v[48:51], a[16:19], a[8:11], v[48:51]\n"
"v_mfma_f32_16x16x32_bf16 v[64:67], a[20:23], a[8:11], v[64:67]\n"
"v_max_f32_dpp v81, v81, v81 row_ror:2 row_mask:0xf bank_mask:0xf\n"
"v_mfma_f32_16x16x32_bf16 v[52:55], a[16:19], a[12:15], v[52:55]\n"
"s_nop 0\n"
"v_max_f32_dpp v81, v81, v81 row_ror:1 row_mask:0xf bank_mask:0xf\n"
"v_mfma_f32_16x16x32_bf16 v[68:71], a[20:23], a[12:15], v[68:71]\n"
"s_waitcnt lgkmcnt(2)\n"
"v_mfma_f32_16x16x32_bf16 v[72:75], a[40:43], v[252:255], v[72:75]\n"
"v_mov_b32 v13, v81\n"
"v_mfma_f32_16x16x32_bf16 v[76:79], a[44:47], v[252:255], v[76:79]\n"
"s_nop 0\n"
"v_permlane16_swap_b32 v81, v13\n"
"v_mfma_f32_16x16x32_bf16 v[40:43], a[40:43], a[24:27], v[40:43]\n"
"v_mfma_f32_16x16x32_bf16 v[56:59], a[44:47], a[24:27], v[56:59]\n"
"v_max_f32 v81, v81, v13\n"
"v_mfma_f32_16x16x32_bf16 v[44:47], a[40:43], a[28:31], v[44:47]\n"
"v_mov_b32 v13, v81\n"
"v_mfma_f32_16x16x32_bf16 v[60:63], a[44:47], a[28:31], v[60:63]\n"
"s_waitcnt lgkmcnt(0)\n"
"v_mfma_f32_16x16x32_bf16 v[48:51], a[40:43], a[32:35], v[48:51]\n"
"v_permlane32_swap_b32 v81, v13\n"
"v_mfma_f32_16x16x32_bf16 v[64:67], a[44:47], a[32:35], v[64:67]\n"
"v_max_f32 v81, v81, v13\n"
"v_mfma_f32_16x16x32_bf16 v[52:55], a[40:43], a[36:39], v[52:55]\n"
"v_mfma_f32_16x16x32_bf16 v[68:71], a[44:47], a[36:39], v[68:71]\n"
"s_waitcnt vmcnt(4)\n"
"s_barrier\n"
"ds_read_b128 v[128:131], v7 offset:32768\n"
"ds_read_b128 v[132:135], v7 offset:33792\n"
"ds_read_b128 v[136:139], v7 offset:36864\n"
"ds_read_b128 v[140:143], v7 offset:37888\n"
"ds_read_b128 v[104:107], v7 offset:34816\n"
"ds_read_b128 v[108:111], v7 offset:35840\n"
"ds_read_b128 v[144:147], v7 offset:40960\n"
"ds_read_b128 v[148:151], v7 offset:41984\n"
"ds_read_b128 v[112:115], v7 offset:38912\n"
"ds_read_b128 v[116:119], v7 offset:39936\n"
"v_pk_fma_f32 v[40:41], v[72:73], v[88:89], v[40:41] op_sel_hi:[1,0,1]\n"
"v_pk_fma_f32 v[42:43], v[74:75], v[88:89], v[42:43] op_sel_hi:[1,0,1]\n"
"v_pk_fma_f32 v[56:57], v[76:77], v[88:89], v[56:57] op_sel_hi:[1,0,1]\n"
"v_pk_fma_f32 v[58:59], v[78:79], v[88:89], v[58:59] op_sel_hi:[1,0,1]\n"
"v_pk_fma_f32 v[44:45], v[72:73], v[90:91], v[44:45] op_sel_hi:[1,0,1]\n"
"v_pk_fma_f32 v[46:47], v[74:75], v[90:91], v[46:47] op_sel_hi:[1,0,1]\n"
"v_pk_fma_f32 v[60:61], v[76:77], v[90:91], v[60:61] op_sel_hi:[1,0,1]\n"
"v_pk_fma_f32 v[62:63], v[78:79], v[90:91], v[62:63] op_sel_hi:[1,0,1]\n"
"v_pk_mul_f32 v[40:41], v[40:41], v[88:89] op_sel:[0,1] op_sel_hi:[1,1]\n"
"v_pk_mul_f32 v[42:43], v[42:43], v[88:89] op_sel:[0,1] op_sel_hi:[1,1]\n"
"v_pk_mul_f32 v[56:57], v[56:57], v[88:89] op_sel:[0,1] op_sel_hi:[1,1]\n"
"v_pk_mul_f32 v[58:59], v[58:59], v[88:89] op_sel:[0,1] op_sel_hi:[1,1]\n"
"v_pk_mul_f32 v[44:45], v[44:45], v[90:91] op_sel:[0,1] op_sel_hi:[1,1]\n"
"v_pk_mul_f32 v[46:47], v[46:47], v[90:91] op_sel:[0,1] op_sel_hi:[1,1]\n"
"v_pk_mul_f32 v[60:61], v[60:61], v[90:91] op_sel:[0,1] op_sel_hi:[1,1]\n"
"v_pk_mul_f32 v[62:63], v[62:63], v[90:91] op_sel:[0,1] op_sel_hi:[1,1]\n"
"v_cvt_pk_bf16_f32 v236, v40, v41\n"
"v_cvt_pk_bf16_f32 v237, v42, v43\n"
"v_cvt_pk_bf16_f32 v238, v56, v57\n"
"v_cvt_pk_bf16_f32 v239, v58, v59\n"
"v_cvt_pk_bf16_f32 v240, v44, v45\n"
"v_cvt_pk_bf16_f32 v241, v46, v47\n"
"v_cvt_pk_bf16_f32 v242, v60, v61\n"
"v_cvt_pk_bf16_f32 v243, v62, v63\n"
"v_mfma_f32_16x16x32_bf16 v[40:43], v[236:239], v[236:239], 0\n"
"s_nop 0\n"
"v_mfma_f32_16x16x32_bf16 v[44:47], v[240:243], v[240:243], 0\n"
"v_permlane16_swap_b32 v236, v240\n"
"v_permlane16_swap_b32 v237, v241\n"
"v_permlane16_swap_b32 v238, v242\n"
"v_permlane16_swap_b32 v239, v243\n"
"s_nop 0\n"
"v_pk_fma_f32 v[48:49], v[72:73], v[92:93], v[48:49] op_sel_hi:[1,0,1]\n"
"v_pk_fma_f32 v[50:51], v[74:75], v[92:93], v[50:51] op_sel_hi:[1,0,1]\n"
"v_pk_fma_f32 v[64:65], v[76:77], v[92:93], v[64:65] op_sel_hi:[1,0,1]\n"
"v_pk_fma_f32 v[66:67], v[78:79], v[92:93], v[66:67] op_sel_hi:[1,0,1]\n"
"v_pk_fma_f32 v[52:53], v[72:73], v[94:95], v[52:53] op_sel_hi:[1,0,1]\n"
"v_pk_fma_f32 v[54:55], v[74:75], v[94:95], v[54:55] op_sel_hi:[1,0,1]\n"
"v_pk_fma_f32 v[68:69], v[76:77], v[94:95], v[68:69] op_sel_hi:[1,0,1]\n"
"v_pk_fma_f32 v[70:71], v[78:79], v[94:95], v[70:71] op_sel_hi:[1,0,1]\n"
"v_pk_mul_f32 v[48:49], v[48:49], v[92:93] op_sel:[0,1] op_sel_hi:[1,1]\n"
"v_pk_mul_f32 v[50:51], v[50:51], v[92:93] op_sel:[0,1] op_sel_hi:[1,1]\n"
"v_pk_mul_f32 v[64:65], v[64:65], v[92:93] op_sel:[0,1] op_sel_hi:[1,1]\n"
"v_pk_mul_f32 v[66:67], v[66:67], v[92:93] op_sel:[0,1] op_sel_hi:[1,1]\n"
"v_pk_mul_f32 v[52:53], v[52:53], v[94:95] op_sel:[0,1] op_sel_hi:[1,1]\n"
"v_pk_mul_f32 v[54:55], v[54:55], v[94:95] op_sel:[0,1] op_sel_hi:[1,1]\n"
"v_pk_mul_f32 v[68:69], v[68:69], v[94:95] op_sel:[0,1] op_sel_hi:[1,1]\n"
"v_pk_mul_f32 v[70:71], v[70:71], v[94:95] op_sel:[0,1] op_sel_hi:[1,1]\n"
"v_cvt_pk_bf16_f32 v244, v48, v49\n"
"v_cvt_pk_bf16_f32 v245, v50, v51\n"
"v_cvt_pk_bf16_f32 v246, v64, v65\n"
"v_cvt_pk_bf16_f32 v247, v66, v67\n"
"v_cvt_pk_bf16_f32 v248, v52, v53\n"
"v_cvt_pk_bf16_f32 v249, v54, v55\n"
"v_cvt_pk_bf16_f32 v250, v68, v69\n"
"v_cvt_pk_bf16_f32 v251, v70, v71\n"
"v_mfma_f32_16x16x32_bf16 v[48:51], v[244:247], v[244:247], 0\n"
"s_nop 0\n"
"v_mfma_f32_16x16x32_bf16 v[52:55], v[248:251], v[248:251], 0\n"
"v_permlane16_swap_b32 v244, v248\n"
"v_permlane16_swap_b32 v245, v249\n"
"v_permlane16_swap_b32 v246, v250\n"
"v_permlane16_swap_b32 v247, v251\n"
"s_waitcnt lgkmcnt(0)\n"
"v_mfma_f32_32x32x16_bf16 v[18:33], v[128:131], v[236:239], v[192:207]\n"
"v_mfma_f32_32x32x16_bf16 v[18:33], v[132:135], v[240:243], v[18:33]\n"
"v_max3_f32 v40, v40, v41, v42\n"
"v_max3_f32 v40, v40, v43, v44\n"
"v_max3_f32 v40, v40, v45, v46\n"
"v_max3_f32 v40, v40, v47, v48\n"
"v_max3_f32 v40, v40, v49, v50\n"
"v_max3_f32 v40, v40, v51, v52\n"
"v_max3_f32 v40, v40, v53, v54\n"
"v_max_f32 v40, v40, v55\n"
"v_mul_f32 v40, v40, v81\n"
"v_cmp_nge_f32 vcc, 0x45a20000, v40\n"
"s_nop 0\n"
"s_cbranch_vccnz .Lmk_slow_8\n"
".Lmk_fast_9:\n"
"v_mfma_f32_32x32x16_bf16 v[40:55], v[128:131], v[244:247], v[208:223]\n"
"s_add_u32 s81, s81, 0x8000\n"
"v_exp_f32 v18, v18\n"
"v_exp_f32 v19, v19\n"
"v_exp_f32 v20, v20\n"
"v_mfma_f32_32x32x16_bf16 v[40:55], v[132:135], v[248:251], v[40:55]\n"
"s_and_b32 s81, s81, 0x3ffff\n"
"v_exp_f32 v21, v21\n"
"v_exp_f32 v22, v22\n"
"v_exp_f32 v23, v23\n"
"v_exp_f32 v24, v24\n"
"v_exp_f32 v25, v25\n"
"v_exp_f32 v26, v26\n"
"ds_read_b128 v[128:131], v7 offset:45056\n"
"v_exp_f32 v27, v27\n"
"v_exp_f32 v28, v28\n"
"s_add_u32 s80, s81, 524288\n"
"v_exp_f32 v29, v29\n"
"v_exp_f32 v30, v30\n"
"v_exp_f32 v31, v31\n"
"ds_read_b128 v[132:135], v7 offset:46080\n"
"v_exp_f32 v32, v32\n"
"v_exp_f32 v33, v33\n"
"v_mfma_f32_32x32x16_bf16 v[56:71], v[136:139], v[236:239], v[192:207]\n"
"s_add_u32 m0, s53, 0\n"
"v_exp_f32 v40, v40\n"
"v_exp_f32 v41, v41\n"
"v_exp_f32 v42, v42\n"
"v_mfma_f32_32x32x16_bf16 v[56:71], v[140:143], v[240:243], v[56:71]\n"
"v_exp_f32 v43, v43\n"
"v_exp_f32 v44, v44\n"
"v_exp_f32 v45, v45\n"
"v_smfmac_f32_32x32x32_bf16 v[152:167], v[104:107], v[18:25], v17\n"
"buffer_load_dwordx4 v2, s[28:31], s80 offen offset:0 sc1 lds\n"
"v_exp_f32 v46, v46\n"
"v_exp_f32 v47, v47\n"
"v_exp_f32 v48, v48\n"
"v_smfmac_f32_16x16x64_bf16 v[228:231], v[224:227], v[18:25], v17\n"
"ds_read_b128 v[120:123], v7 offset:43008\n"
"v_exp_f32 v49, v49\n"
"v_exp_f32 v50, v50\n"
"v_smfmac_f32_32x32x32_bf16 v[152:167], v[108:111], v[26:33], v17\n"
"buffer_load_dwordx4 v2, s[28:31], s80 offen offset:1024 sc1 lds\n"
"v_exp_f32 v51, v51\n"
"v_exp_f32 v52, v52\n"
"v_exp_f32 v53, v53\n"
"v_smfmac_f32_16x16x64_bf16 v[228:231], v[224:227], v[26:33], v17\n"
".Lmk_pg_3:\n"
"s_cbranch_execz .Lmk_pg_6\n"
"ds_read_b128 v[124:127], v7 offset:44032\n"
"v_exp_f32 v54, v54\n"
"v_exp_f32 v55, v55\n"
"v_mfma_f32_32x32x16_bf16 v[18:33], v[136:139], v[244:247], v[208:223]\n"
"s_waitcnt vmcnt(2)\n"
"v_exp_f32 v56, v56\n"
"v_exp_f32 v57, v57\n"
"v_exp_f32 v58, v58\n"
"v_mfma_f32_32x32x16_bf16 v[18:33], v[140:143], v[248:251], v[18:33]\n"
"s_barrier\n"
"v_exp_f32 v59, v59\n"
"v_exp_f32 v60, v60\n"
"v_exp_f32 v61, v61\n"
"v_smfmac_f32_32x32x32_bf16 v[168:183], v[104:107], v[40:47], v17\n"
"buffer_load_dwordx4 v2, s[28:31], s80 offen offset:2048 sc1 lds\n"
"v_exp_f32 v62, v62\n"
"v_exp_f32 v63, v63\n"
"v_exp_f32 v64, v64\n"
"v_smfmac_f32_16x16x64_bf16 v[232:235], v[224:227], v[40:47], v17\n"
"ds_read_b128 v[136:139], v7 offset:49152\n"
"v_exp_f32 v65, v65\n"
"v_exp_f32 v66, v66\n"
"v_smfmac_f32_32x32x32_bf16 v[168:183], v[108:111], v[48:55], v17\n"
"buffer_load_dwordx4 v2, s[28:31], s80 offen offset:3072 sc1 lds\n"
"v_exp_f32 v67, v67\n"
"v_exp_f32 v68, v68\n"
"v_exp_f32 v69, v69\n"
"v_smfmac_f32_16x16x64_bf16 v[232:235], v[224:227], v[48:55], v17\n"
"ds_read_b128 v[140:143], v7 offset:50176\n"
"v_exp_f32 v70, v70\n"
"v_exp_f32 v71, v71\n"
"v_mfma_f32_32x32x16_bf16 v[40:55], v[144:147], v[236:239], v[192:207]\n"
"s_add_u32 s80, s80, 0x4000\n"
"v_exp_f32 v18, v18\n"
"v_exp_f32 v19, v19\n"
"v_exp_f32 v20, v20\n"
"v_mfma_f32_32x32x16_bf16 v[40:55], v[148:151], v[240:243], v[40:55]\n"
"s_waitcnt lgkmcnt(2)\n"
"v_exp_f32 v21, v21\n"
"v_exp_f32 v22, v22\n"
"v_exp_f32 v23, v23\n"
"v_smfmac_f32_32x32x32_bf16 v[152:167], v[112:115], v[56:63], v17\n"
"s_add_u32 m0, s53, 16384\n"
"v_exp_f32 v24, v24\n"
"v_exp_f32 v25, v25\n"
"v_exp_f32 v26, v26\n"
"v_smfmac_f32_16x16x64_bf16 v[228:231], v[224:227], v[56:63], v17\n"
"ds_read_b128 v[104:107], v7 offset:47104\n"
"v_exp_f32 v27, v27\n"
"v_exp_f32 v28, v28\n"
"v_smfmac_f32_32x32x32_bf16 v[152:167], v[116:119], v[64:71], v17\n"
"buffer_load_dwordx4 v2, s[28:31], s80 offen offset:0 sc1 lds\n"
"v_exp_f32 v29, v29\n"
"v_exp_f32 v30, v30\n"
"v_exp_f32 v31, v31\n"
"v_smfmac_f32_16x16x64_bf16 v[228:231], v[224:227], v[64:71], v17\n"
"ds_read_b128 v[108:111], v7 offset:48128\n"
"v_exp_f32 v32, v32\n"
"v_exp_f32 v33, v33\n"
"v_mfma_f32_32x32x16_bf16 v[56:71], v[144:147], v[244:247], v[208:223]\n"
"buffer_load_dwordx4 v2, s[28:31], s80 offen offset:1024 sc1 lds\n"
"v_exp_f32 v40, v40\n"
"v_exp_f32 v41, v41\n"
"v_exp_f32 v42, v42\n"
"v_mfma_f32_32x32x16_bf16 v[56:71], v[148:151], v[248:251], v[56:71]\n"
"v_exp_f32 v43, v43\n"
"v_exp_f32 v44, v44\n"
"v_exp_f32 v45, v45\n"
"v_smfmac_f32_32x32x32_bf16 v[168:183], v[112:115], v[18:25], v17\n"
"buffer_load_dwordx4 v2, s[28:31], s80 offen offset:2048 sc1 lds\n"
"v_exp_f32 v46, v46\n"
"v_exp_f32 v47, v47\n"
"v_exp_f32 v48, v48\n"
"v_smfmac_f32_16x16x64_bf16 v[232:235], v[224:227], v[18:25], v17\n"
"ds_read_b128 v[144:147], v7 offset:53248\n"
"v_exp_f32 v49, v49\n"
"v_exp_f32 v50, v50\n"
"v_smfmac_f32_32x32x32_bf16 v[168:183], v[116:119], v[26:33], v17\n"
"buffer_load_dwordx4 v2, s[28:31], s80 offen offset:3072 sc1 lds\n"
"v_exp_f32 v51, v51\n"
"v_exp_f32 v52, v52\n"
"v_exp_f32 v53, v53\n"
"v_smfmac_f32_16x16x64_bf16 v[232:235], v[224:227], v[26:33], v17\n"
"ds_read_b128 v[148:151], v7 offset:54272\n"
"v_exp_f32 v54, v54\n"
"v_exp_f32 v55, v55\n"
"v_mfma_f32_32x32x16_bf16 v[18:33], v[128:131], v[236:239], v[192:207]\n"
"s_mov_b64 s[32:33], s[12:13]\n"
"v_exp_f32 v56, v56\n"
"v_exp_f32 v57, v57\n"
"v_exp_f32 v58, v58\n"
"v_mfma_f32_32x32x16_bf16 v[18:33], v[132:135], v[240:243], v[18:33]\n"
"s_waitcnt lgkmcnt(2)\n"
"v_exp_f32 v59, v59\n"
"v_exp_f32 v60, v60\n"
"v_exp_f32 v61, v61\n"
"v_smfmac_f32_32x32x32_bf16 v[152:167], v[120:123], v[40:47], v17\n"
"v_exp_f32 v62, v62\n"
"v_exp_f32 v63, v63\n"
"v_exp_f32 v64, v64\n"
"v_smfmac_f32_16x16x64_bf16 v[228:231], v[224:227], v[40:47], v17\n"
"ds_read_b128 v[112:115], v7 offset:51200\n"
"v_exp_f32 v65, v65\n"
"v_exp_f32 v66, v66\n"
"v_smfmac_f32_32x32x32_bf16 v[152:167], v[124:127], v[48:55], v17\n"
"s_mov_b32 s34, 2097152\n"
"v_exp_f32 v67, v67\n"
"v_exp_f32 v68, v68\n"
"v_exp_f32 v69, v69\n"
"v_smfmac_f32_16x16x64_bf16 v[228:231], v[224:227], v[48:55], v17\n"
"ds_read_b128 v[116:119], v7 offset:52224\n"
"v_exp_f32 v70, v70\n"
"v_exp_f32 v71, v71\n"
"v_mfma_f32_32x32x16_bf16 v[40:55], v[128:131], v[244:247], v[208:223]\n"
"v_exp_f32 v18, v18\n"
"v_exp_f32 v19, v19\n"
"v_exp_f32 v20, v20\n"
"v_mfma_f32_32x32x16_bf16 v[40:55], v[132:135], v[248:251], v[40:55]\n"
"s_add_u32 m0, s71, 65536\n"
"v_exp_f32 v21, v21\n"
"v_exp_f32 v22, v22\n"
"v_exp_f32 v23, v23\n"
"v_smfmac_f32_32x32x32_bf16 v[168:183], v[120:123], v[56:63], v17\n"
"v_exp_f32 v24, v24\n"
"v_exp_f32 v25, v25\n"
"v_exp_f32 v26, v26\n"
"v_smfmac_f32_16x16x64_bf16 v[232:235], v[224:227], v[56:63], v17\n"
"ds_read_b128 v[128:131], v7 offset:57344\n"
"v_exp_f32 v27, v27\n"
"v_exp_f32 v28, v28\n"
"v_smfmac_f32_32x32x32_bf16 v[168:183], v[124:127], v[64:71], v17\n"
"v_exp_f32 v29, v29\n"
"v_exp_f32 v30, v30\n"
"v_exp_f32 v31, v31\n"
"v_smfmac_f32_16x16x64_bf16 v[232:235], v[224:227], v[64:71], v17\n"
"ds_read_b128 v[132:135], v7 offset:58368\n"
"v_exp_f32 v32, v32\n"
"v_exp_f32 v33, v33\n"
"v_mfma_f32_32x32x16_bf16 v[56:71], v[136:139], v[236:239], v[192:207]\n"
"s_add_u32 s57, s88, 31744\n"
"v_exp_f32 v40, v40\n"
"v_exp_f32 v41, v41\n"
"v_exp_f32 v42, v42\n"
"v_mfma_f32_32x32x16_bf16 v[56:71], v[140:143], v[240:243], v[56:71]\n"
"s_waitcnt lgkmcnt(2)\n"
"v_exp_f32 v43, v43\n"
"v_exp_f32 v44, v44\n"
"v_exp_f32 v45, v45\n"
"v_smfmac_f32_32x32x32_bf16 v[152:167], v[104:107], v[18:25], v17\n"
"v_exp_f32 v46, v46\n"
"v_exp_f32 v47, v47\n"
"v_exp_f32 v48, v48\n"
"v_smfmac_f32_16x16x64_bf16 v[228:231], v[224:227], v[18:25], v17\n"
"ds_read_b128 v[120:123], v7 offset:55296\n"
"v_exp_f32 v49, v49\n"
"v_exp_f32 v50, v50\n"
"v_smfmac_f32_32x32x32_bf16 v[152:167], v[108:111], v[26:33], v17\n"
"buffer_load_dwordx4 v188, s[32:35], s88 offen offset:0 lds\n"
"v_exp_f32 v51, v51\n"
"v_exp_f32 v52, v52\n"
"v_exp_f32 v53, v53\n"
"v_smfmac_f32_16x16x64_bf16 v[228:231], v[224:227], v[26:33], v17\n"
"ds_read_b128 v[124:127], v7 offset:56320\n"
"v_exp_f32 v54, v54\n"
"v_exp_f32 v55, v55\n"
"v_mfma_f32_32x32x16_bf16 v[18:33], v[136:139], v[244:247], v[208:223]\n"
"v_exp_f32 v56, v56\n"
"v_exp_f32 v57, v57\n"
"v_exp_f32 v58, v58\n"
"v_mfma_f32_32x32x16_bf16 v[18:33], v[140:143], v[248:251], v[18:33]\n"
"v_exp_f32 v59, v59\n"
"v_exp_f32 v60, v60\n"
"v_exp_f32 v61, v61\n"
"v_smfmac_f32_32x32x32_bf16 v[168:183], v[104:107], v[40:47], v17\n"
"buffer_load_dwordx4 v188, s[32:35], s57 offen offset:1024 lds\n"
"v_exp_f32 v62, v62\n"
"v_exp_f32 v63, v63\n"
"v_exp_f32 v64, v64\n"
"v_smfmac_f32_16x16x64_bf16 v[232:235], v[224:227], v[40:47], v17\n"
"ds_read_b128 v[136:139], v7 offset:61440\n"
"v_exp_f32 v65, v65\n"
"v_exp_f32 v66, v66\n"
"v_smfmac_f32_32x32x32_bf16 v[168:183], v[108:111], v[48:55], v17\n"
"v_exp_f32 v67, v67\n"
"v_exp_f32 v68, v68\n"
"v_exp_f32 v69, v69\n"
"v_smfmac_f32_16x16x64_bf16 v[232:235], v[224:227], v[48:55], v17\n"
"ds_read_b128 v[140:143], v7 offset:62464\n"
"v_exp_f32 v70, v70\n"
"v_exp_f32 v71, v71\n"
"v_mfma_f32_32x32x16_bf16 v[40:55], v[144:147], v[236:239], v[192:207]\n"
"v_exp_f32 v18, v18\n"
"v_exp_f32 v19, v19\n"
"v_exp_f32 v20, v20\n"
"v_mfma_f32_32x32x16_bf16 v[40:55], v[148:151], v[240:243], v[40:55]\n"
"s_waitcnt lgkmcnt(2)\n"
"v_exp_f32 v21, v21\n"
"v_exp_f32 v22, v22\n"
"v_exp_f32 v23, v23\n"
"v_smfmac_f32_32x32x32_bf16 v[152:167], v[112:115], v[56:63], v17\n"
"v_exp_f32 v24, v24\n"
"v_exp_f32 v25, v25\n"
"v_exp_f32 v26, v26\n"
"v_smfmac_f32_16x16x64_bf16 v[228:231], v[224:227], v[56:63], v17\n"
"ds_read_b128 v[104:107], v7 offset:59392\n"
"v_exp_f32 v27, v27\n"
"v_exp_f32 v28, v28\n"
"v_smfmac_f32_32x32x32_bf16 v[152:167], v[116:119], v[64:71], v17\n"
"v_exp_f32 v29, v29\n"
"v_exp_f32 v30, v30\n"
"v_exp_f32 v31, v31\n"
"v_smfmac_f32_16x16x64_bf16 v[228:231], v[224:227], v[64:71], v17\n"
"ds_read_b128 v[108:111], v7 offset:60416\n"
"v_exp_f32 v32, v32\n"
"v_exp_f32 v33, v33\n"
"v_mfma_f32_32x32x16_bf16 v[56:71], v[144:147], v[244:247], v[208:223]\n"
"s_waitcnt vmcnt(2)\n"
"v_exp_f32 v40, v40\n"
"v_exp_f32 v41, v41\n"
"v_exp_f32 v42, v42\n"
"v_mfma_f32_32x32x16_bf16 v[56:71], v[148:151], v[248:251], v[56:71]\n"
"s_barrier\n"
"v_exp_f32 v43, v43\n"
"v_exp_f32 v44, v44\n"
"v_exp_f32 v45, v45\n"
"v_smfmac_f32_32x32x32_bf16 v[168:183], v[112:115], v[18:25], v17\n"
"s_add_u32 s81, s81, 0x8000\n"
"v_exp_f32 v46, v46\n"
"v_exp_f32 v47, v47\n"
"v_exp_f32 v48, v48\n"
"v_smfmac_f32_16x16x64_bf16 v[232:235], v[224:227], v[18:25], v17\n"
"ds_read_b128 v[144:147], v6 offset:0\n"
"v_exp_f32 v49, v49\n"
"v_exp_f32 v50, v50\n"
"v_smfmac_f32_32x32x32_bf16 v[168:183], v[116:119], v[26:33], v17\n"
"s_and_b32 s81, s81, 0x3ffff\n"
"v_exp_f32 v51, v51\n"
"v_exp_f32 v52, v52\n"
"v_exp_f32 v53, v53\n"
"v_smfmac_f32_16x16x64_bf16 v[232:235], v[224:227], v[26:33], v17\n"
"ds_read_b128 v[148:151], v6 offset:1024\n"
"v_exp_f32 v54, v54\n"
"v_exp_f32 v55, v55\n"
"v_mfma_f32_32x32x16_bf16 v[18:33], v[128:131], v[236:239], v[192:207]\n"
"s_add_u32 s80, s81, 524288\n"
"v_exp_f32 v56, v56\n"
"v_exp_f32 v57, v57\n"
"v_exp_f32 v58, v58\n"
"v_mfma_f32_32x32x16_bf16 v[18:33], v[132:135], v[240:243], v[18:33]\n"
"s_waitcnt lgkmcnt(2)\n"
"v_exp_f32 v59, v59\n"
"v_exp_f32 v60, v60\n"
"v_exp_f32 v61, v61\n"
"v_smfmac_f32_32x32x32_bf16 v[152:167], v[120:123], v[40:47], v17\n"
"s_add_u32 m0, s53, 32768\n"
"v_exp_f32 v62, v62\n"
"v_exp_f32 v63, v63\n"
"v_exp_f32 v64, v64\n"
"v_smfmac_f32_16x16x64_bf16 v[228:231], v[224:227], v[40:47], v17\n"
"ds_read_b128 v[112:115], v7 offset:63488\n"
"v_exp_f32 v65, v65\n"
"v_exp_f32 v66, v66\n"
"v_smfmac_f32_32x32x32_bf16 v[152:167], v[124:127], v[48:55], v17\n"
"buffer_load_dwordx4 v2, s[28:31], s80 offen offset:0 sc1 lds\n"
"v_exp_f32 v67, v67\n"
"v_exp_f32 v68, v68\n"
"v_exp_f32 v69, v69\n"
"v_smfmac_f32_16x16x64_bf16 v[228:231], v[224:227], v[48:55], v17\n"
"ds_read_b128 v[116:119], v7 offset:64512\n"
"v_exp_f32 v70, v70\n"
"v_exp_f32 v71, v71\n"
"v_mfma_f32_32x32x16_bf16 v[40:55], v[128:131], v[244:247], v[208:223]\n"
"v_exp_f32 v18, v18\n"
"v_exp_f32 v19, v19\n"
"v_exp_f32 v20, v20\n"
"v_mfma_f32_32x32x16_bf16 v[40:55], v[132:135], v[248:251], v[40:55]\n"
"buffer_load_dwordx4 v2, s[28:31], s80 offen offset:1024 sc1 lds\n"
"v_exp_f32 v21, v21\n"
"v_exp_f32 v22, v22\n"
"v_exp_f32 v23, v23\n"
"v_smfmac_f32_32x32x32_bf16 v[168:183], v[120:123], v[56:63], v17\n"
"buffer_load_dwordx4 v2, s[28:31], s80 offen offset:2048 sc1 lds\n"
"v_exp_f32 v24, v24\n"
"v_exp_f32 v25, v25\n"
"v_exp_f32 v26, v26\n"
"v_smfmac_f32_16x16x64_bf16 v[232:235], v[224:227], v[56:63], v17\n"
"ds_read_b128 v[128:131], v6 offset:4096\n"
"v_exp_f32 v27, v27\n"
"v_exp_f32 v28, v28\n"
"v_smfmac_f32_32x32x32_bf16 v[168:183], v[124:127], v[64:71], v17\n"
"buffer_load_dwordx4 v2, s[28:31], s80 offen offset:3072 sc1 lds\n"
"v_exp_f32 v29, v29\n"
"v_exp_f32 v30, v30\n"
"v_exp_f32 v31, v31\n"
"v_smfmac_f32_16x16x64_bf16 v[232:235], v[224:227], v[64:71], v17\n"
"ds_read_b128 v[132:135], v6 offset:5120\n"
"v_exp_f32 v32, v32\n"
"v_exp_f32 v33, v33\n"
"v_mfma_f32_32x32x16_bf16 v[56:71], v[136:139], v[236:239], v[192:207]\n"
"s_add_u32 s80, s80, 0x4000\n"
"v_exp_f32 v40, v40\n"
"v_exp_f32 v41, v41\n"
"v_exp_f32 v42, v42\n"
"v_mfma_f32_32x32x16_bf16 v[56:71], v[140:143], v[240:243], v[56:71]\n"
"s_waitcnt lgkmcnt(2)\n"
"v_exp_f32 v43, v43\n"
"v_exp_f32 v44, v44\n"
"v_exp_f32 v45, v45\n"
"v_smfmac_f32_32x32x32_bf16 v[152:167], v[104:107], v[18:25], v17\n"
"s_add_u32 m0, s53, 49152\n"
"v_exp_f32 v46, v46\n"
"v_exp_f32 v47, v47\n"
"v_exp_f32 v48, v48\n"
"v_smfmac_f32_16x16x64_bf16 v[228:231], v[224:227], v[18:25], v17\n"
"ds_read_b128 v[120:123], v6 offset:2048\n"
"v_exp_f32 v49, v49\n"
"v_exp_f32 v50, v50\n"
"v_smfmac_f32_32x32x32_bf16 v[152:167], v[108:111], v[26:33], v17\n"
"buffer_load_dwordx4 v2, s[28:31], s80 offen offset:0 sc1 lds\n"
"v_exp_f32 v51, v51\n"
"v_exp_f32 v52, v52\n"
"v_exp_f32 v53, v53\n"
"v_smfmac_f32_16x16x64_bf16 v[228:231], v[224:227], v[26:33], v17\n"
"ds_read_b128 v[124:127], v6 offset:3072\n"
"v_exp_f32 v54, v54\n"
"v_exp_f32 v55, v55\n"
"v_mfma_f32_32x32x16_bf16 v[18:33], v[136:139], v[244:247], v[208:223]\n"
"buffer_load_dwordx4 v2, s[28:31], s80 offen offset:1024 sc1 lds\n"
"v_exp_f32 v56, v56\n"
"v_exp_f32 v57, v57\n"
"v_exp_f32 v58, v58\n"
"v_mfma_f32_32x32x16_bf16 v[18:33], v[140:143], v[248:251], v[18:33]\n"
"buffer_load_dwordx4 v2, s[28:31], s80 offen offset:2048 sc1 lds\n"
"v_exp_f32 v59, v59\n"
"v_exp_f32 v60, v60\n"
"v_exp_f32 v61, v61\n"
"v_smfmac_f32_32x32x32_bf16 v[168:183], v[104:107], v[40:47], v17\n"
"buffer_load_dwordx4 v2, s[28:31], s80 offen offset:3072 sc1 lds\n"
"v_exp_f32 v62, v62\n"
"v_exp_f32 v63, v63\n"
"v_exp_f32 v64, v64\n"
"v_smfmac_f32_16x16x64_bf16 v[232:235], v[224:227], v[40:47], v17\n"
"ds_read_b128 v[136:139], v6 offset:8192\n"
"v_exp_f32 v65, v65\n"
"v_exp_f32 v66, v66\n"
"v_smfmac_f32_32x32x32_bf16 v[168:183], v[108:111], v[48:55], v17\n"
"s_mov_b64 s[32:33], s[14:15]\n"
"v_exp_f32 v67, v67\n"
"v_exp_f32 v68, v68\n"
"v_exp_f32 v69, v69\n"
"v_smfmac_f32_16x16x64_bf16 v[232:235], v[224:227], v[48:55], v17\n"
"ds_read_b128 v[140:143], v6 offset:9216\n"
"v_exp_f32 v70, v70\n"
"v_exp_f32 v71, v71\n"
"v_mfma_f32_32x32x16_bf16 v[40:55], v[144:147], v[236:239], v[192:207]\n"
"s_mov_b32 s34, 7340032\n"
"v_exp_f32 v18, v18\n"
"v_exp_f32 v19, v19\n"
"v_exp_f32 v20, v20\n"
"v_mfma_f32_32x32x16_bf16 v[40:55], v[148:151], v[240:243], v[40:55]\n"
"s_waitcnt lgkmcnt(2)\n"
"v_exp_f32 v21, v21\n"
"v_exp_f32 v22, v22\n"
"v_exp_f32 v23, v23\n"
"v_smfmac_f32_32x32x32_bf16 v[152:167], v[112:115], v[56:63], v17\n"
"s_add_u32 m0, s71, 131072\n"
"v_exp_f32 v24, v24\n"
"v_exp_f32 v25, v25\n"
"v_exp_f32 v26, v26\n"
"v_smfmac_f32_16x16x64_bf16 v[228:231], v[224:227], v[56:63], v17\n"
"ds_read_b128 v[104:107], v6 offset:6144\n"
"v_exp_f32 v27, v27\n"
"v_exp_f32 v28, v28\n"
"v_smfmac_f32_32x32x32_bf16 v[152:167], v[116:119], v[64:71], v17\n"
"s_add_u32 s57, s89, 56320\n"
"v_exp_f32 v29, v29\n"
"v_exp_f32 v30, v30\n"
"v_exp_f32 v31, v31\n"
"v_smfmac_f32_16x16x64_bf16 v[228:231], v[224:227], v[64:71], v17\n"
"ds_read_b128 v[108:111], v6 offset:7168\n"
"v_exp_f32 v32, v32\n"
"v_exp_f32 v33, v33\n"
"v_mfma_f32_32x32x16_bf16 v[56:71], v[144:147], v[244:247], v[208:223]\n"
"s_add_u32 s59, s57, 56320\n"
"v_exp_f32 v40, v40\n"
"v_exp_f32 v41, v41\n"
"v_exp_f32 v42, v42\n"
"v_mfma_f32_32x32x16_bf16 v[56:71], v[148:151], v[248:251], v[56:71]\n"
"s_add_u32 s60, s59, 56320\n"
"v_exp_f32 v43, v43\n"
"v_exp_f32 v44, v44\n"
"v_exp_f32 v45, v45\n"
"v_smfmac_f32_32x32x32_bf16 v[168:183], v[112:115], v[18:25], v17\n"
"buffer_load_dwordx4 v189, s[32:35], s89 offen offset:0 lds\n"
"v_exp_f32 v46, v46\n"
"v_exp_f32 v47, v47\n"
"v_exp_f32 v48, v48\n"
"v_smfmac_f32_16x16x64_bf16 v[232:235], v[224:227], v[18:25], v17\n"
"ds_read_b128 v[144:147], v6 offset:12288\n"
"v_exp_f32 v49, v49\n"
"v_exp_f32 v50, v50\n"
"v_smfmac_f32_32x32x32_bf16 v[168:183], v[116:119], v[26:33], v17\n"
"buffer_load_dwordx4 v189, s[32:35], s57 offen offset:1024 lds\n"
"v_exp_f32 v51, v51\n"
"v_exp_f32 v52, v52\n"
"v_exp_f32 v53, v53\n"
"v_smfmac_f32_16x16x64_bf16 v[232:235], v[224:227], v[26:33], v17\n"
"ds_read_b128 v[148:151], v6 offset:13312\n"
"v_exp_f32 v54, v54\n"
"v_exp_f32 v55, v55\n"
"v_mfma_f32_32x32x16_bf16 v[18:33], v[128:131], v[236:239], v[192:207]\n"
"v_exp_f32 v56, v56\n"
"v_exp_f32 v57, v57\n"
"v_exp_f32 v58, v58\n"
"v_mfma_f32_32x32x16_bf16 v[18:33], v[132:135], v[240:243], v[18:33]\n"
"s_waitcnt lgkmcnt(2)\n"
"v_exp_f32 v59, v59\n"
"v_exp_f32 v60, v60\n"
"v_exp_f32 v61, v61\n"
"v_smfmac_f32_32x32x32_bf16 v[152:167], v[120:123], v[40:47], v17\n"
"buffer_load_dwordx4 v189, s[32:35], s59 offen offset:2048 lds\n"
"v_exp_f32 v62, v62\n"
"v_exp_f32 v63, v63\n"
"v_exp_f32 v64, v64\n"
"v_smfmac_f32_16x16x64_bf16 v[228:231], v[224:227], v[40:47], v17\n"
"ds_read_b128 v[112:115], v6 offset:10240\n"
"v_exp_f32 v65, v65\n"
"v_exp_f32 v66, v66\n"
"v_smfmac_f32_32x32x32_bf16 v[152:167], v[124:127], v[48:55], v17\n"
"buffer_load_dwordx4 v189, s[32:35], s60 offen offset:3072 lds\n"
"v_exp_f32 v67, v67\n"
"v_exp_f32 v68, v68\n"
"v_exp_f32 v69, v69\n"
"v_smfmac_f32_16x16x64_bf16 v[228:231], v[224:227], v[48:55], v17\n"
"ds_read_b128 v[116:119], v6 offset:11264\n"
"v_exp_f32 v70, v70\n"
"v_exp_f32 v71, v71\n"
"v_mfma_f32_32x32x16_bf16 v[40:55], v[128:131], v[244:247], v[208:223]\n"
"v_exp_f32 v18, v18\n"
"v_exp_f32 v19, v19\n"
"v_exp_f32 v20, v20\n"
"v_mfma_f32_32x32x16_bf16 v[40:55], v[132:135], v[248:251], v[40:55]\n"
"v_exp_f32 v21, v21\n"
"v_exp_f32 v22, v22\n"
"v_exp_f32 v23, v23\n"
"v_smfmac_f32_32x32x32_bf16 v[168:183], v[120:123], v[56:63], v17\n"
"v_exp_f32 v24, v24\n"
"v_exp_f32 v25, v25\n"
"v_exp_f32 v26, v26\n"
"v_smfmac_f32_16x16x64_bf16 v[232:235], v[224:227], v[56:63], v17\n"
"ds_read_b128 v[128:131], v6 offset:16384\n"
"v_exp_f32 v27, v27\n"
"v_exp_f32 v28, v28\n"
"v_smfmac_f32_32x32x32_bf16 v[168:183], v[124:127], v[64:71], v17\n"
"s_waitcnt vmcnt(12)\n"
"v_exp_f32 v29, v29\n"
"v_exp_f32 v30, v30\n"
"v_exp_f32 v31, v31\n"
"v_smfmac_f32_16x16x64_bf16 v[232:235], v[224:227], v[64:71], v17\n"
"ds_read_b128 v[132:135], v6 offset:17408\n"
"v_exp_f32 v32, v32\n"
"v_exp_f32 v33, v33\n"
"v_mfma_f32_32x32x16_bf16 v[56:71], v[136:139], v[236:239], v[192:207]\n"
"v_exp_f32 v40, v40\n"
"v_exp_f32 v41, v41\n"
"v_exp_f32 v42, v42\n"
"v_mfma_f32_32x32x16_bf16 v[56:71], v[140:143], v[240:243], v[56:71]\n"
"s_waitcnt lgkmcnt(2)\n"
"v_exp_f32 v43, v43\n"
"v_exp_f32 v44, v44\n"
"v_exp_f32 v45, v45\n"
"v_smfmac_f32_32x32x32_bf16 v[152:167], v[104:107], v[18:25], v17\n"
"v_exp_f32 v46, v46\n"
"v_exp_f32 v47, v47\n"
"v_exp_f32 v48, v48\n"
"v_smfmac_f32_16x16x64_bf16 v[228:231], v[224:227], v[18:25], v17\n"
"ds_read_b128 v[120:123], v6 offset:14336\n"
"v_exp_f32 v49, v49\n"
"v_exp_f32 v50, v50\n"
"v_smfmac_f32_32x32x32_bf16 v[152:167], v[108:111], v[26:33], v17\n"
"ds_read_b64_tr_b16 a[240:241], v184 offset:0\n"
"ds_read_b64_tr_b16 a[242:243], v184 offset:256\n"
"v_exp_f32 v51, v51\n"
"v_exp_f32 v52, v52\n"
"v_exp_f32 v53, v53\n"
"v_smfmac_f32_16x16x64_bf16 v[228:231], v[224:227], v[26:33], v17\n"
"ds_read_b128 v[124:127], v6 offset:15360\n"
"v_exp_f32 v54, v54\n"
"v_exp_f32 v55, v55\n"
"v_mfma_f32_32x32x16_bf16 v[18:33], v[136:139], v[244:247], v[208:223]\n"
"v_exp_f32 v56, v56\n"
"v_exp_f32 v57, v57\n"
"v_exp_f32 v58, v58\n"
"v_mfma_f32_32x32x16_bf16 v[18:33], v[140:143], v[248:251], v[18:33]\n"
"v_exp_f32 v59, v59\n"
"v_exp_f32 v60, v60\n"
"v_exp_f32 v61, v61\n"
"v_smfmac_f32_32x32x32_bf16 v[168:183], v[104:107], v[40:47], v17\n"
"v_exp_f32 v62, v62\n"
"v_exp_f32 v63, v63\n"
"v_exp_f32 v64, v64\n"
"v_smfmac_f32_16x16x64_bf16 v[232:235], v[224:227], v[40:47], v17\n"
"ds_read_b128 v[136:139], v6 offset:20480\n"
"v_exp_f32 v65, v65\n"
"v_exp_f32 v66, v66\n"
"v_smfmac_f32_32x32x32_bf16 v[168:183], v[108:111], v[48:55], v17\n"
"ds_read_b64_tr_b16 a[244:245], v184 offset:8\n"
"ds_read_b64_tr_b16 a[246:247], v184 offset:264\n"
"v_exp_f32 v67, v67\n"
"v_exp_f32 v68, v68\n"
"v_exp_f32 v69, v69\n"
"v_smfmac_f32_16x16x64_bf16 v[232:235], v[224:227], v[48:55], v17\n"
"ds_read_b128 v[140:143], v6 offset:21504\n"
"v_exp_f32 v70, v70\n"
"v_exp_f32 v71, v71\n"
"v_mfma_f32_32x32x16_bf16 v[40:55], v[144:147], v[236:239], v[192:207]\n"
"v_exp_f32 v18, v18\n"
"v_exp_f32 v19, v19\n"
"v_exp_f32 v20, v20\n"
"v_mfma_f32_32x32x16_bf16 v[40:55], v[148:151], v[240:243], v[40:55]\n"
"s_waitcnt lgkmcnt(4)\n"
"v_exp_f32 v21, v21\n"
"v_exp_f32 v22, v22\n"
"v_exp_f32 v23, v23\n"
"v_smfmac_f32_32x32x32_bf16 v[152:167], v[112:115], v[56:63], v17\n"
"v_exp_f32 v24, v24\n"
"v_exp_f32 v25, v25\n"
"v_exp_f32 v26, v26\n"
"v_smfmac_f32_16x16x64_bf16 v[228:231], v[224:227], v[56:63], v17\n"
"ds_read_b128 v[104:107], v6 offset:18432\n"
"v_exp_f32 v27, v27\n"
"v_exp_f32 v28, v28\n"
"v_smfmac_f32_32x32x32_bf16 v[152:167], v[116:119], v[64:71], v17\n"
"v_exp_f32 v29, v29\n"
"v_exp_f32 v30, v30\n"
"v_exp_f32 v31, v31\n"
"v_smfmac_f32_16x16x64_bf16 v[228:231], v[224:227], v[64:71], v17\n"
"ds_read_b128 v[108:111], v6 offset:19456\n"
"v_exp_f32 v32, v32\n"
"v_exp_f32 v33, v33\n"
"v_mfma_f32_32x32x16_bf16 v[56:71], v[144:147], v[244:247], v[208:223]\n"
"s_waitcnt lgkmcnt(3)\n"
"v_exp_f32 v40, v40\n"
"v_exp_f32 v41, v41\n"
"v_exp_f32 v42, v42\n"
"v_mfma_f32_32x32x16_bf16 v[56:71], v[148:151], v[248:251], v[56:71]\n"
"v_exp_f32 v43, v43\n"
"v_exp_f32 v44, v44\n"
"v_exp_f32 v45, v45\n"
"v_smfmac_f32_32x32x32_bf16 v[168:183], v[112:115], v[18:25], v17\n"
"v_exp_f32 v46, v46\n"
"v_exp_f32 v47, v47\n"
"v_exp_f32 v48, v48\n"
"v_smfmac_f32_16x16x64_bf16 v[232:235], v[224:227], v[18:25], v17\n"
"ds_read_b128 v[144:147], v6 offset:24576\n"
"v_exp_f32 v49, v49\n"
"v_exp_f32 v50, v50\n"
"v_smfmac_f32_32x32x32_bf16 v[168:183], v[116:119], v[26:33], v17\n"
"v_exp_f32 v51, v51\n"
"v_exp_f32 v52, v52\n"
"v_exp_f32 v53, v53\n"
"v_smfmac_f32_16x16x64_bf16 v[232:235], v[224:227], v[26:33], v17\n"
"ds_read_b128 v[148:151], v6 offset:25600\n"
"v_exp_f32 v54, v54\n"
"v_exp_f32 v55, v55\n"
"v_mfma_f32_32x32x16_bf16 v[18:33], v[128:131], v[236:239], v[192:207]\n"
"v_exp_f32 v56, v56\n"
"v_exp_f32 v57, v57\n"
"v_exp_f32 v58, v58\n"
"v_mfma_f32_32x32x16_bf16 v[18:33], v[132:135], v[240:243], v[18:33]\n"
"s_waitcnt lgkmcnt(2)\n"
"v_exp_f32 v59, v59\n"
"v_exp_f32 v60, v60\n"
"v_exp_f32 v61, v61\n"
"v_smfmac_f32_32x32x32_bf16 v[152:167], v[120:123], v[40:47], v17\n"
"buffer_store_dwordx4 a[240:243], v2, s[28:31], s6 offen offset:0 sc1\n"
"v_exp_f32 v62, v62\n"
"v_exp_f32 v63, v63\n"
"v_exp_f32 v64, v64\n"
"v_smfmac_f32_16x16x64_bf16 v[228:231], v[224:227], v[40:47], v17\n"
"ds_read_b128 v[112:115], v6 offset:22528\n"
"v_exp_f32 v65, v65\n"
"v_exp_f32 v66, v66\n"
"v_smfmac_f32_32x32x32_bf16 v[152:167], v[124:127], v[48:55], v17\n"
"v_exp_f32 v67, v67\n"
"v_exp_f32 v68, v68\n"
"v_exp_f32 v69, v69\n"
"v_smfmac_f32_16x16x64_bf16 v[228:231], v[224:227], v[48:55], v17\n"
"ds_read_b128 v[116:119], v6 offset:23552\n"
"v_exp_f32 v70, v70\n"
"v_exp_f32 v71, v71\n"
"v_mfma_f32_32x32x16_bf16 v[40:55], v[128:131], v[244:247], v[208:223]\n"
"v_exp_f32 v18, v18\n"
"v_exp_f32 v19, v19\n"
"v_exp_f32 v20, v20\n"
"v_mfma_f32_32x32x16_bf16 v[40:55], v[132:135], v[248:251], v[40:55]\n"
"v_exp_f32 v21, v21\n"
"v_exp_f32 v22, v22\n"
"v_exp_f32 v23, v23\n"
"v_smfmac_f32_32x32x32_bf16 v[168:183], v[120:123], v[56:63], v17\n"
"buffer_store_dwordx4 a[244:247], v2, s[28:31], s6 offen offset:1024 sc1\n"
"v_exp_f32 v24, v24\n"
"v_exp_f32 v25, v25\n"
"v_exp_f32 v26, v26\n"
"v_smfmac_f32_16x16x64_bf16 v[232:235], v[224:227], v[56:63], v17\n"
"ds_read_b128 v[128:131], v6 offset:28672\n"
"v_exp_f32 v27, v27\n"
"v_exp_f32 v28, v28\n"
"v_smfmac_f32_32x32x32_bf16 v[168:183], v[124:127], v[64:71], v17\n"
"v_exp_f32 v29, v29\n"
"v_exp_f32 v30, v30\n"
"v_exp_f32 v31, v31\n"
"v_smfmac_f32_16x16x64_bf16 v[232:235], v[224:227], v[64:71], v17\n"
"ds_read_b128 v[132:135], v6 offset:29696\n"
"v_exp_f32 v32, v32\n"
"v_exp_f32 v33, v33\n"
"v_mfma_f32_32x32x16_bf16 v[56:71], v[136:139], v[236:239], v[192:207]\n"
"v_exp_f32 v40, v40\n"
"v_exp_f32 v41, v41\n"
"v_exp_f32 v42, v42\n"
"v_mfma_f32_32x32x16_bf16 v[56:71], v[140:143], v[240:243], v[56:71]\n"
"s_waitcnt lgkmcnt(2)\n"
"v_exp_f32 v43, v43\n"
"v_exp_f32 v44, v44\n"
"v_exp_f32 v45, v45\n"
"v_smfmac_f32_32x32x32_bf16 v[152:167], v[104:107], v[18:25], v17\n"
"v_exp_f32 v46, v46\n"
"v_exp_f32 v47, v47\n"
"v_exp_f32 v48, v48\n"
"v_smfmac_f32_16x16x64_bf16 v[228:231], v[224:227], v[18:25], v17\n"
"ds_read_b128 v[120:123], v6 offset:26624\n"
"v_exp_f32 v49, v49\n"
"v_exp_f32 v50, v50\n"
"v_smfmac_f32_32x32x32_bf16 v[152:167], v[108:111], v[26:33], v17\n"
"v_exp_f32 v51, v51\n"
"v_exp_f32 v52, v52\n"
"v_exp_f32 v53, v53\n"
"v_smfmac_f32_16x16x64_bf16 v[228:231], v[224:227], v[26:33], v17\n"
"ds_read_b128 v[124:127], v6 offset:27648\n"
"v_exp_f32 v54, v54\n"
"v_exp_f32 v55, v55\n"
"v_mfma_f32_32x32x16_bf16 v[18:33], v[136:139], v[244:247], v[208:223]\n"
"s_waitcnt vmcnt(6)\n"
"v_exp_f32 v56, v56\n"
"v_exp_f32 v57, v57\n"
"v_exp_f32 v58, v58\n"
"v_mfma_f32_32x32x16_bf16 v[18:33], v[140:143], v[248:251], v[18:33]\n"
"s_barrier\n"
"v_exp_f32 v59, v59\n"
"v_exp_f32 v60, v60\n"
"v_exp_f32 v61, v61\n"
"v_smfmac_f32_32x32x32_bf16 v[168:183], v[104:107], v[40:47], v17\n"
"s_add_u32 s81, s81, 0x8000\n"
"v_exp_f32 v62, v62\n"
"v_exp_f32 v63, v63\n"
"v_exp_f32 v64, v64\n"
"v_smfmac_f32_16x16x64_bf16 v[232:235], v[224:227], v[40:47], v17\n"
"ds_read_b128 v[136:139], v6 offset:32768\n"
"v_exp_f32 v65, v65\n"
"v_exp_f32 v66, v66\n"
"v_smfmac_f32_32x32x32_bf16 v[168:183], v[108:111], v[48:55], v17\n"
"s_and_b32 s81, s81, 0x3ffff\n"
"v_exp_f32 v67, v67\n"
"v_exp_f32 v68, v68\n"
"v_exp_f32 v69, v69\n"
"v_smfmac_f32_16x16x64_bf16 v[232:235], v[224:227], v[48:55], v17\n"
"ds_read_b128 v[140:143], v6 offset:33792\n"
"v_exp_f32 v70, v70\n"
"v_exp_f32 v71, v71\n"
"v_mfma_f32_32x32x16_bf16 v[40:55], v[144:147], v[236:239], v[192:207]\n"
"s_add_u32 s80, s81, 524288\n"
"v_exp_f32 v18, v18\n"
"v_exp_f32 v19, v19\n"
"v_exp_f32 v20, v20\n"
"v_mfma_f32_32x32x16_bf16 v[40:55], v[148:151], v[240:243], v[40:55]\n"
"s_waitcnt lgkmcnt(2)\n"
"v_exp_f32 v21, v21\n"
"v_exp_f32 v22, v22\n"
"v_exp_f32 v23, v23\n"
"v_smfmac_f32_32x32x32_bf16 v[152:167], v[112:115], v[56:63], v17\n"
"s_add_u32 m0, s53, 98304\n"
"v_exp_f32 v24, v24\n"
"v_exp_f32 v25, v25\n"
"v_exp_f32 v26, v26\n"
"v_smfmac_f32_16x16x64_bf16 v[228:231], v[224:227], v[56:63], v17\n"
"ds_read_b128 v[104:107], v6 offset:30720\n"
"v_exp_f32 v27, v27\n"
"v_exp_f32 v28, v28\n"
"v_smfmac_f32_32x32x32_bf16 v[152:167], v[116:119], v[64:71], v17\n"
"buffer_load_dwordx4 v2, s[28:31], s80 offen offset:0 sc1 lds\n"
"v_exp_f32 v29, v29\n"
"v_exp_f32 v30, v30\n"
"v_exp_f32 v31, v31\n"
"v_smfmac_f32_16x16x64_bf16 v[228:231], v[224:227], v[64:71], v17\n"
"ds_read_b128 v[108:111], v6 offset:31744\n"
"v_exp_f32 v32, v32\n"
"v_exp_f32 v33, v33\n"
"v_mfma_f32_32x32x16_bf16 v[56:71], v[144:147], v[244:247], v[208:223]\n"
"v_exp_f32 v40, v40\n"
"v_exp_f32 v41, v41\n"
"v_exp_f32 v42, v42\n"
"v_mfma_f32_32x32x16_bf16 v[56:71], v[148:151], v[248:251], v[56:71]\n"
"buffer_load_dwordx4 v2, s[28:31], s80 offen offset:1024 sc1 lds\n"
"v_exp_f32 v43, v43\n"
"v_exp_f32 v44, v44\n"
"v_exp_f32 v45, v45\n"
"v_smfmac_f32_32x32x32_bf16 v[168:183], v[112:115], v[18:25], v17\n"
"buffer_load_dwordx4 v2, s[28:31], s80 offen offset:2048 sc1 lds\n"
"v_exp_f32 v46, v46\n"
"v_exp_f32 v47, v47\n"
"v_exp_f32 v48, v48\n"
"v_smfmac_f32_16x16x64_bf16 v[232:235], v[224:227], v[18:25], v17\n"
"ds_read_b128 v[144:147], v6 offset:36864\n"
"v_exp_f32 v49, v49\n"
"v_exp_f32 v50, v50\n"
"v_smfmac_f32_32x32x32_bf16 v[168:183], v[116:119], v[26:33], v17\n"
"buffer_load_dwordx4 v2, s[28:31], s80 offen offset:3072 sc1 lds\n"
"v_exp_f32 v51, v51\n"
"v_exp_f32 v52, v52\n"
"v_exp_f32 v53, v53\n"
"v_smfmac_f32_16x16x64_bf16 v[232:235], v[224:227], v[26:33], v17\n"
"ds_read_b128 v[148:151], v6 offset:37888\n"
"v_exp_f32 v54, v54\n"
"v_exp_f32 v55, v55\n"
"v_mfma_f32_32x32x16_bf16 v[18:33], v[128:131], v[236:239], v[192:207]\n"
"s_add_u32 s80, s80, 0x4000\n"
"v_exp_f32 v56, v56\n"
"v_exp_f32 v57, v57\n"
"v_exp_f32 v58, v58\n"
"v_mfma_f32_32x32x16_bf16 v[18:33], v[132:135], v[240:243], v[18:33]\n"
"s_waitcnt lgkmcnt(2)\n"
"v_exp_f32 v59, v59\n"
"v_exp_f32 v60, v60\n"
"v_exp_f32 v61, v61\n"
"v_smfmac_f32_32x32x32_bf16 v[152:167], v[120:123], v[40:47], v17\n"
"s_add_u32 m0, s53, 114688\n"
"v_exp_f32 v62, v62\n"
"v_exp_f32 v63, v63\n"
"v_exp_f32 v64, v64\n"
"v_smfmac_f32_16x16x64_bf16 v[228:231], v[224:227], v[40:47], v17\n"
"ds_read_b128 v[112:115], v6 offset:34816\n"
"v_exp_f32 v65, v65\n"
"v_exp_f32 v66, v66\n"
"v_smfmac_f32_32x32x32_bf16 v[152:167], v[124:127], v[48:55], v17\n"
"buffer_load_dwordx4 v2, s[28:31], s80 offen offset:0 sc1 lds\n"
"v_exp_f32 v67, v67\n"
".Lmk_pg_4:\n"
"s_cbranch_execz .Lmk_pg_7\n"
"v_exp_f32 v68, v68\n"
"v_exp_f32 v69, v69\n"
"v_smfmac_f32_16x16x64_bf16 v[228:231], v[224:227], v[48:55], v17\n"
"ds_read_b128 v[116:119], v6 offset:35840\n"
"v_exp_f32 v70, v70\n"
"v_exp_f32 v71, v71\n"
"v_mfma_f32_32x32x16_bf16 v[40:55], v[128:131], v[244:247], v[208:223]\n"
"buffer_load_dwordx4 v2, s[28:31], s80 offen offset:1024 sc1 lds\n"
"v_exp_f32 v18, v18\n"
"v_exp_f32 v19, v19\n"
"v_exp_f32 v20, v20\n"
"v_mfma_f32_32x32x16_bf16 v[40:55], v[132:135], v[248:251], v[40:55]\n"
"buffer_load_dwordx4 v2, s[28:31], s80 offen offset:2048 sc1 lds\n"
"v_exp_f32 v21, v21\n"
"v_exp_f32 v22, v22\n"
"v_exp_f32 v23, v23\n"
"v_smfmac_f32_32x32x32_bf16 v[168:183], v[120:123], v[56:63], v17\n"
"buffer_load_dwordx4 v2, s[28:31], s80 offen offset:3072 sc1 lds\n"
"v_exp_f32 v24, v24\n"
"v_exp_f32 v25, v25\n"
"v_exp_f32 v26, v26\n"
"v_smfmac_f32_16x16x64_bf16 v[232:235], v[224:227], v[56:63], v17\n"
"ds_read_b128 v[128:131], v6 offset:40960\n"
"v_exp_f32 v27, v27\n"
"v_exp_f32 v28, v28\n"
"v_smfmac_f32_32x32x32_bf16 v[168:183], v[124:127], v[64:71], v17\n"
"s_add_u32 m0, s71, 135168\n"
"v_exp_f32 v29, v29\n"
"v_exp_f32 v30, v30\n"
"v_exp_f32 v31, v31\n"
"v_smfmac_f32_16x16x64_bf16 v[232:235], v[224:227], v[64:71], v17\n"
"ds_read_b128 v[132:135], v6 offset:41984\n"
"v_exp_f32 v32, v32\n"
"v_exp_f32 v33, v33\n"
"v_mfma_f32_32x32x16_bf16 v[56:71], v[136:139], v[236:239], v[192:207]\n"
"s_add_u32 s89, s89, 229376\n"
"v_exp_f32 v40, v40\n"
"v_exp_f32 v41, v41\n"
"v_exp_f32 v42, v42\n"
"v_mfma_f32_32x32x16_bf16 v[56:71], v[140:143], v[240:243], v[56:71]\n"
"s_waitcnt lgkmcnt(2)\n"
"v_exp_f32 v43, v43\n"
"v_exp_f32 v44, v44\n"
"v_exp_f32 v45, v45\n"
"v_smfmac_f32_32x32x32_bf16 v[152:167], v[104:107], v[18:25], v17\n"
"s_add_u32 s57, s89, 56320\n"
"v_exp_f32 v46, v46\n"
"v_exp_f32 v47, v47\n"
"v_exp_f32 v48, v48\n"
"v_smfmac_f32_16x16x64_bf16 v[228:231], v[224:227], v[18:25], v17\n"
"ds_read_b128 v[120:123], v6 offset:38912\n"
"v_exp_f32 v49, v49\n"
"v_exp_f32 v50, v50\n"
"v_smfmac_f32_32x32x32_bf16 v[152:167], v[108:111], v[26:33], v17\n"
"s_add_u32 s59, s57, 56320\n"
"v_exp_f32 v51, v51\n"
"v_exp_f32 v52, v52\n"
"v_exp_f32 v53, v53\n"
"v_smfmac_f32_16x16x64_bf16 v[228:231], v[224:227], v[26:33], v17\n"
"ds_read_b128 v[124:127], v6 offset:39936\n"
"v_exp_f32 v54, v54\n"
"v_exp_f32 v55, v55\n"
"v_mfma_f32_32x32x16_bf16 v[18:33], v[136:139], v[244:247], v[208:223]\n"
"s_add_u32 s60, s59, 56320\n"
"v_exp_f32 v56, v56\n"
"v_exp_f32 v57, v57\n"
"v_exp_f32 v58, v58\n"
"v_mfma_f32_32x32x16_bf16 v[18:33], v[140:143], v[248:251], v[18:33]\n"
"v_exp_f32 v59, v59\n"
"v_exp_f32 v60, v60\n"
"v_exp_f32 v61, v61\n"
"v_smfmac_f32_32x32x32_bf16 v[168:183], v[104:107], v[40:47], v17\n"
"buffer_load_dwordx4 v189, s[32:35], s89 offen offset:0 lds\n"
"v_exp_f32 v62, v62\n"
"v_exp_f32 v63, v63\n"
"v_exp_f32 v64, v64\n"
"v_smfmac_f32_16x16x64_bf16 v[232:235], v[224:227], v[40:47], v17\n"
"ds_read_b128 v[136:139], v6 offset:45056\n"
"v_exp_f32 v65, v65\n"
"v_exp_f32 v66, v66\n"
"v_smfmac_f32_32x32x32_bf16 v[168:183], v[108:111], v[48:55], v17\n"
"buffer_load_dwordx4 v189, s[32:35], s57 offen offset:1024 lds\n"
"v_exp_f32 v67, v67\n"
"v_exp_f32 v68, v68\n"
"v_exp_f32 v69, v69\n"
"v_smfmac_f32_16x16x64_bf16 v[232:235], v[224:227], v[48:55], v17\n"
"ds_read_b128 v[140:143], v6 offset:46080\n"
"v_exp_f32 v70, v70\n"
"v_exp_f32 v71, v71\n"
"v_mfma_f32_32x32x16_bf16 v[40:55], v[144:147], v[236:239], v[192:207]\n"
"v_exp_f32 v18, v18\n"
"v_exp_f32 v19, v19\n"
"v_exp_f32 v20, v20\n"
"v_mfma_f32_32x32x16_bf16 v[40:55], v[148:151], v[240:243], v[40:55]\n"
"s_waitcnt lgkmcnt(2)\n"
"v_exp_f32 v21, v21\n"
"v_exp_f32 v22, v22\n"
"v_exp_f32 v23, v23\n"
"v_smfmac_f32_32x32x32_bf16 v[152:167], v[112:115], v[56:63], v17\n"
"buffer_load_dwordx4 v189, s[32:35], s59 offen offset:2048 lds\n"
"v_exp_f32 v24, v24\n"
"v_exp_f32 v25, v25\n"
"v_exp_f32 v26, v26\n"
"v_smfmac_f32_16x16x64_bf16 v[228:231], v[224:227], v[56:63], v17\n"
"ds_read_b128 v[104:107], v6 offset:43008\n"
"v_exp_f32 v27, v27\n"
"v_exp_f32 v28, v28\n"
"v_smfmac_f32_32x32x32_bf16 v[152:167], v[116:119], v[64:71], v17\n"
"buffer_load_dwordx4 v189, s[32:35], s60 offen offset:3072 lds\n"
"v_exp_f32 v29, v29\n"
"v_exp_f32 v30, v30\n"
"v_exp_f32 v31, v31\n"
"v_smfmac_f32_16x16x64_bf16 v[228:231], v[224:227], v[64:71], v17\n"
"ds_read_b128 v[108:111], v6 offset:44032\n"
"v_exp_f32 v32, v32\n"
"v_exp_f32 v33, v33\n"
"v_mfma_f32_32x32x16_bf16 v[56:71], v[144:147], v[244:247], v[208:223]\n"
"v_exp_f32 v40, v40\n"
"v_exp_f32 v41, v41\n"
"v_exp_f32 v42, v42\n"
"v_mfma_f32_32x32x16_bf16 v[56:71], v[148:151], v[248:251], v[56:71]\n"
"v_exp_f32 v43, v43\n"
"v_exp_f32 v44, v44\n"
"v_exp_f32 v45, v45\n"
"v_smfmac_f32_32x32x32_bf16 v[168:183], v[112:115], v[18:25], v17\n"
"v_exp_f32 v46, v46\n"
"v_exp_f32 v47, v47\n"
"v_exp_f32 v48, v48\n"
"v_smfmac_f32_16x16x64_bf16 v[232:235], v[224:227], v[18:25], v17\n"
"ds_read_b128 v[144:147], v6 offset:49152\n"
"v_exp_f32 v49, v49\n"
"v_exp_f32 v50, v50\n"
"v_smfmac_f32_32x32x32_bf16 v[168:183], v[116:119], v[26:33], v17\n"
"s_waitcnt vmcnt(14)\n"
"v_exp_f32 v51, v51\n"
"v_exp_f32 v52, v52\n"
"v_exp_f32 v53, v53\n"
"v_smfmac_f32_16x16x64_bf16 v[232:235], v[224:227], v[26:33], v17\n"
"ds_read_b128 v[148:151], v6 offset:50176\n"
"v_exp_f32 v54, v54\n"
"v_exp_f32 v55, v55\n"
"v_mfma_f32_32x32x16_bf16 v[18:33], v[128:131], v[236:239], v[192:207]\n"
"v_exp_f32 v56, v56\n"
"v_exp_f32 v57, v57\n"
"v_exp_f32 v58, v58\n"
"v_mfma_f32_32x32x16_bf16 v[18:33], v[132:135], v[240:243], v[18:33]\n"
"s_waitcnt lgkmcnt(2)\n"
"v_exp_f32 v59, v59\n"
"v_exp_f32 v60, v60\n"
"v_exp_f32 v61, v61\n"
"v_smfmac_f32_32x32x32_bf16 v[152:167], v[120:123], v[40:47], v17\n"
"ds_read_b64_tr_b16 a[240:241], v185 offset:0\n"
"ds_read_b64_tr_b16 a[242:243], v185 offset:512\n"
"v_exp_f32 v62, v62\n"
"v_exp_f32 v63, v63\n"
"v_exp_f32 v64, v64\n"
"v_smfmac_f32_16x16x64_bf16 v[228:231], v[224:227], v[40:47], v17\n"
"ds_read_b128 v[112:115], v6 offset:47104\n"
"v_exp_f32 v65, v65\n"
"v_exp_f32 v66, v66\n"
"v_smfmac_f32_32x32x32_bf16 v[152:167], v[124:127], v[48:55], v17\n"
"ds_read_b64_tr_b16 a[244:245], v185 offset:32\n"
"ds_read_b64_tr_b16 a[246:247], v185 offset:544\n"
"v_exp_f32 v67, v67\n"
"v_exp_f32 v68, v68\n"
"v_exp_f32 v69, v69\n"
"v_smfmac_f32_16x16x64_bf16 v[228:231], v[224:227], v[48:55], v17\n"
"ds_read_b128 v[116:119], v6 offset:48128\n"
"v_exp_f32 v70, v70\n"
"v_exp_f32 v71, v71\n"
"v_mfma_f32_32x32x16_bf16 v[40:55], v[128:131], v[244:247], v[208:223]\n"
"v_exp_f32 v18, v18\n"
"v_exp_f32 v19, v19\n"
"v_exp_f32 v20, v20\n"
"v_mfma_f32_32x32x16_bf16 v[40:55], v[132:135], v[248:251], v[40:55]\n"
"v_exp_f32 v21, v21\n"
"v_exp_f32 v22, v22\n"
"v_exp_f32 v23, v23\n"
"v_smfmac_f32_32x32x32_bf16 v[168:183], v[120:123], v[56:63], v17\n"
"ds_read_b64_tr_b16 a[248:249], v185 offset:64\n"
"ds_read_b64_tr_b16 a[250:251], v185 offset:576\n"
"v_exp_f32 v24, v24\n"
"v_exp_f32 v25, v25\n"
"v_exp_f32 v26, v26\n"
"v_smfmac_f32_16x16x64_bf16 v[232:235], v[224:227], v[56:63], v17\n"
"ds_read_b128 v[128:131], v6 offset:53248\n"
"v_exp_f32 v27, v27\n"
"v_exp_f32 v28, v28\n"
"v_smfmac_f32_32x32x32_bf16 v[168:183], v[124:127], v[64:71], v17\n"
"ds_read_b64_tr_b16 a[252:253], v185 offset:96\n"
"ds_read_b64_tr_b16 a[254:255], v185 offset:608\n"
"v_exp_f32 v29, v29\n"
"v_exp_f32 v30, v30\n"
"v_exp_f32 v31, v31\n"
"v_smfmac_f32_16x16x64_bf16 v[232:235], v[224:227], v[64:71], v17\n"
"ds_read_b128 v[132:135], v6 offset:54272\n"
"v_exp_f32 v32, v32\n"
"v_exp_f32 v33, v33\n"
"v_mfma_f32_32x32x16_bf16 v[56:71], v[136:139], v[236:239], v[192:207]\n"
"s_waitcnt lgkmcnt(1)\n"
"v_exp_f32 v40, v40\n"
"v_exp_f32 v41, v41\n"
"v_exp_f32 v42, v42\n"
"v_mfma_f32_32x32x16_bf16 v[56:71], v[140:143], v[240:243], v[56:71]\n"
"v_exp_f32 v43, v43\n"
"v_exp_f32 v44, v44\n"
"v_exp_f32 v45, v45\n"
"v_smfmac_f32_32x32x32_bf16 v[152:167], v[104:107], v[18:25], v17\n"
"buffer_store_dwordx4 a[240:243], v2, s[28:31], s7 offen offset:0 sc1\n"
"v_exp_f32 v46, v46\n"
"v_exp_f32 v47, v47\n"
"v_exp_f32 v48, v48\n"
"v_smfmac_f32_16x16x64_bf16 v[228:231], v[224:227], v[18:25], v17\n"
"ds_read_b128 v[120:123], v6 offset:51200\n"
"v_exp_f32 v49, v49\n"
"v_exp_f32 v50, v50\n"
"v_smfmac_f32_32x32x32_bf16 v[152:167], v[108:111], v[26:33], v17\n"
"v_exp_f32 v51, v51\n"
"v_exp_f32 v52, v52\n"
"v_exp_f32 v53, v53\n"
"v_smfmac_f32_16x16x64_bf16 v[228:231], v[224:227], v[26:33], v17\n"
"ds_read_b128 v[124:127], v6 offset:52224\n"
"v_exp_f32 v54, v54\n"
"v_exp_f32 v55, v55\n"
"v_mfma_f32_32x32x16_bf16 v[18:33], v[136:139], v[244:247], v[208:223]\n"
"v_exp_f32 v56, v56\n"
"v_exp_f32 v57, v57\n"
"v_exp_f32 v58, v58\n"
"v_mfma_f32_32x32x16_bf16 v[18:33], v[140:143], v[248:251], v[18:33]\n"
"v_exp_f32 v59, v59\n"
"v_exp_f32 v60, v60\n"
"v_exp_f32 v61, v61\n"
"v_smfmac_f32_32x32x32_bf16 v[168:183], v[104:107], v[40:47], v17\n"
"buffer_store_dwordx4 a[244:247], v2, s[28:31], s8 offen offset:0 sc1\n"
"v_exp_f32 v62, v62\n"
"v_exp_f32 v63, v63\n"
"v_exp_f32 v64, v64\n"
"v_smfmac_f32_16x16x64_bf16 v[232:235], v[224:227], v[40:47], v17\n"
"ds_read_b128 v[136:139], v6 offset:57344\n"
"v_exp_f32 v65, v65\n"
"v_exp_f32 v66, v66\n"
"v_smfmac_f32_32x32x32_bf16 v[168:183], v[108:111], v[48:55], v17\n"
"buffer_store_dwordx4 a[248:251], v2, s[28:31], s9 offen offset:0 sc1\n"
"v_exp_f32 v67, v67\n"
"v_exp_f32 v68, v68\n"
"v_exp_f32 v69, v69\n"
"v_smfmac_f32_16x16x64_bf16 v[232:235], v[224:227], v[48:55], v17\n"
"ds_read_b128 v[140:143], v6 offset:58368\n"
"v_exp_f32 v70, v70\n"
"v_exp_f32 v71, v71\n"
"v_mfma_f32_32x32x16_bf16 v[40:55], v[144:147], v[236:239], v[192:207]\n"
"v_exp_f32 v18, v18\n"
"v_exp_f32 v19, v19\n"
"v_exp_f32 v20, v20\n"
"v_mfma_f32_32x32x16_bf16 v[40:55], v[148:151], v[240:243], v[40:55]\n"
"s_waitcnt lgkmcnt(2)\n"
"v_exp_f32 v21, v21\n"
"v_exp_f32 v22, v22\n"
"v_exp_f32 v23, v23\n"
"v_smfmac_f32_32x32x32_bf16 v[152:167], v[112:115], v[56:63], v17\n"
"buffer_store_dwordx4 a[252:255], v2, s[28:31], s10 offen offset:0 sc1\n"
"v_mfma_f32_16x16x32_bf16 v[72:75], v[252:255], a[240:243], 0\n"
"v_exp_f32 v24, v24\n"
"v_exp_f32 v25, v25\n"
"v_exp_f32 v26, v26\n"
"v_smfmac_f32_16x16x64_bf16 v[228:231], v[224:227], v[56:63], v17\n"
"ds_read_b128 v[104:107], v6 offset:55296\n"
"v_mfma_f32_16x16x32_bf16 v[76:79], v[252:255], a[244:247], 0\n"
"v_exp_f32 v27, v27\n"
"v_exp_f32 v28, v28\n"
"v_smfmac_f32_32x32x32_bf16 v[152:167], v[116:119], v[64:71], v17\n"
"v_exp_f32 v29, v29\n"
"v_exp_f32 v30, v30\n"
"v_exp_f32 v31, v31\n"
"v_smfmac_f32_16x16x64_bf16 v[228:231], v[224:227], v[64:71], v17\n"
"ds_read_b128 v[108:111], v6 offset:56320\n"
"v_exp_f32 v32, v32\n"
"v_exp_f32 v33, v33\n"
"v_mfma_f32_32x32x16_bf16 v[56:71], v[144:147], v[244:247], v[208:223]\n"
"v_mfma_f32_16x16x32_bf16 v[80:83], v[252:255], a[248:251], 0\n"
"v_exp_f32 v40, v40\n"
"v_exp_f32 v41, v41\n"
"v_exp_f32 v42, v42\n"
"v_mfma_f32_32x32x16_bf16 v[56:71], v[148:151], v[248:251], v[56:71]\n"
"v_exp_f32 v43, v43\n"
"v_exp_f32 v44, v44\n"
"v_exp_f32 v45, v45\n"
"v_smfmac_f32_32x32x32_bf16 v[168:183], v[112:115], v[18:25], v17\n"
"v_exp_f32 v46, v46\n"
"v_exp_f32 v47, v47\n"
"v_exp_f32 v48, v48\n"
"v_smfmac_f32_16x16x64_bf16 v[232:235], v[224:227], v[18:25], v17\n"
"ds_read_b128 v[144:147], v6 offset:61440\n"
"v_mfma_f32_16x16x32_bf16 v[84:87], v[252:255], a[252:255], 0\n"
"v_exp_f32 v49, v49\n"
"v_exp_f32 v50, v50\n"
"v_smfmac_f32_32x32x32_bf16 v[168:183], v[116:119], v[26:33], v17\n"
"v_exp_f32 v51, v51\n"
"v_exp_f32 v52, v52\n"
"v_exp_f32 v53, v53\n"
"v_smfmac_f32_16x16x64_bf16 v[232:235], v[224:227], v[26:33], v17\n"
"ds_read_b128 v[148:151], v6 offset:62464\n"
"v_exp_f32 v54, v54\n"
"v_exp_f32 v55, v55\n"
"v_mfma_f32_32x32x16_bf16 v[18:33], v[128:131], v[236:239], v[192:207]\n"
"v_exp_f32 v56, v56\n"
"v_exp_f32 v57, v57\n"
"v_exp_f32 v58, v58\n"
"v_mfma_f32_32x32x16_bf16 v[18:33], v[132:135], v[240:243], v[18:33]\n"
"s_waitcnt lgkmcnt(2)\n"
"v_exp_f32 v59, v59\n"
"v_exp_f32 v60, v60\n"
"v_exp_f32 v61, v61\n"
"v_smfmac_f32_32x32x32_bf16 v[152:167], v[120:123], v[40:47], v17\n"
"v_exp_f32 v62, v62\n"
"v_exp_f32 v63, v63\n"
"v_exp_f32 v64, v64\n"
"v_smfmac_f32_16x16x64_bf16 v[228:231], v[224:227], v[40:47], v17\n"
"ds_read_b128 v[112:115], v6 offset:59392\n"
"v_exp_f32 v65, v65\n"
"v_exp_f32 v66, v66\n"
"v_smfmac_f32_32x32x32_bf16 v[152:167], v[124:127], v[48:55], v17\n"
"v_exp_f32 v67, v67\n"
"v_exp_f32 v68, v68\n"
"v_exp_f32 v69, v69\n"
"v_smfmac_f32_16x16x64_bf16 v[228:231], v[224:227], v[48:55], v17\n"
"ds_read_b128 v[116:119], v6 offset:60416\n"
"v_exp_f32 v70, v70\n"
"v_exp_f32 v71, v71\n"
"v_mfma_f32_32x32x16_bf16 v[40:55], v[128:131], v[244:247], v[208:223]\n"
"s_waitcnt vmcnt(16)\n"
"s_waitcnt vmcnt(8)\n"
"v_exp_f32 v18, v18\n"
"v_exp_f32 v19, v19\n"
"v_exp_f32 v20, v20\n"
"v_mfma_f32_32x32x16_bf16 v[40:55], v[132:135], v[248:251], v[40:55]\n"
"s_barrier\n"
"v_exp_f32 v21, v21\n"
"v_exp_f32 v22, v22\n"
"v_exp_f32 v23, v23\n"
"v_smfmac_f32_32x32x32_bf16 v[168:183], v[120:123], v[56:63], v17\n"
"s_add_u32 s81, s81, 0x8000\n"
"v_exp_f32 v24, v24\n"
"v_exp_f32 v25, v25\n"
"v_exp_f32 v26, v26\n"
"v_smfmac_f32_16x16x64_bf16 v[232:235], v[224:227], v[56:63], v17\n"
"ds_read_b128 v[128:131], v7 offset:32768\n"
"v_exp_f32 v27, v27\n"
"v_exp_f32 v28, v28\n"
"v_smfmac_f32_32x32x32_bf16 v[168:183], v[124:127], v[64:71], v17\n"
"s_and_b32 s81, s81, 0x3ffff\n"
"v_exp_f32 v29, v29\n"
"v_exp_f32 v30, v30\n"
"v_exp_f32 v31, v31\n"
"v_smfmac_f32_16x16x64_bf16 v[232:235], v[224:227], v[64:71], v17\n"
"ds_read_b128 v[132:135], v7 offset:33792\n"
"v_exp_f32 v32, v32\n"
"v_exp_f32 v33, v33\n"
"v_mfma_f32_32x32x16_bf16 v[56:71], v[136:139], v[236:239], v[192:207]\n"
"s_add_u32 s80, s81, 524288\n"
"v_exp_f32 v40, v40\n"
"v_exp_f32 v41, v41\n"
"v_exp_f32 v42, v42\n"
"v_mfma_f32_32x32x16_bf16 v[56:71], v[140:143], v[240:243], v[56:71]\n"
"s_waitcnt lgkmcnt(2)\n"
"v_exp_f32 v43, v43\n"
"v_exp_f32 v44, v44\n"
"v_exp_f32 v45, v45\n"
"v_smfmac_f32_32x32x32_bf16 v[152:167], v[104:107], v[18:25], v17\n"
"s_add_u32 m0, s53, 0\n"
"v_exp_f32 v46, v46\n"
"v_exp_f32 v47, v47\n"
"v_exp_f32 v48, v48\n"
"v_smfmac_f32_16x16x64_bf16 v[228:231], v[224:227], v[18:25], v17\n"
"ds_read_b128 v[120:123], v6 offset:63488\n"
"v_exp_f32 v49, v49\n"
"v_exp_f32 v50, v50\n"
"v_smfmac_f32_32x32x32_bf16 v[152:167], v[108:111], v[26:33], v17\n"
"buffer_load_dwordx4 v2, s[28:31], s80 offen offset:0 sc1 lds\n"
"v_exp_f32 v51, v51\n"
"v_exp_f32 v52, v52\n"
"v_exp_f32 v53, v53\n"
"v_smfmac_f32_16x16x64_bf16 v[228:231], v[224:227], v[26:33], v17\n"
"ds_read_b128 v[124:127], v6 offset:64512\n"
"v_exp_f32 v54, v54\n"
"v_exp_f32 v55, v55\n"
"v_mfma_f32_32x32x16_bf16 v[18:33], v[136:139], v[244:247], v[208:223]\n"
"v_exp_f32 v56, v56\n"
"v_exp_f32 v57, v57\n"
"v_exp_f32 v58, v58\n"
"v_mfma_f32_32x32x16_bf16 v[18:33], v[140:143], v[248:251], v[18:33]\n"
"buffer_load_dwordx4 v2, s[28:31], s80 offen offset:1024 sc1 lds\n"
"v_exp_f32 v59, v59\n"
"v_exp_f32 v60, v60\n"
"v_exp_f32 v61, v61\n"
"v_smfmac_f32_32x32x32_bf16 v[168:183], v[104:107], v[40:47], v17\n"
"buffer_load_dwordx4 v2, s[28:31], s80 offen offset:2048 sc1 lds\n"
"v_exp_f32 v62, v62\n"
"v_exp_f32 v63, v63\n"
"v_exp_f32 v64, v64\n"
"v_smfmac_f32_16x16x64_bf16 v[232:235], v[224:227], v[40:47], v17\n"
"ds_read_b128 v[136:139], v7 offset:36864\n"
"v_exp_f32 v65, v65\n"
"v_exp_f32 v66, v66\n"
"v_smfmac_f32_32x32x32_bf16 v[168:183], v[108:111], v[48:55], v17\n"
"buffer_load_dwordx4 v2, s[28:31], s80 offen offset:3072 sc1 lds\n"
"v_exp_f32 v67, v67\n"
"v_exp_f32 v68, v68\n"
"v_exp_f32 v69, v69\n"
"v_smfmac_f32_16x16x64_bf16 v[232:235], v[224:227], v[48:55], v17\n"
"ds_read_b128 v[140:143], v7 offset:37888\n"
"v_exp_f32 v70, v70\n"
"v_exp_f32 v71, v71\n"
"v_mfma_f32_32x32x16_bf16 v[40:55], v[144:147], v[236:239], v[192:207]\n"
"s_add_u32 s80, s80, 0x4000\n"
"v_exp_f32 v18, v18\n"
"v_exp_f32 v19, v19\n"
"v_exp_f32 v20, v20\n"
"v_mfma_f32_32x32x16_bf16 v[40:55], v[148:151], v[240:243], v[40:55]\n"
"s_waitcnt lgkmcnt(2)\n"
"v_exp_f32 v21, v21\n"
"v_exp_f32 v22, v22\n"
"v_exp_f32 v23, v23\n"
"v_smfmac_f32_32x32x32_bf16 v[152:167], v[112:115], v[56:63], v17\n"
"s_add_u32 m0, s53, 16384\n"
"v_exp_f32 v24, v24\n"
"v_exp_f32 v25, v25\n"
"v_exp_f32 v26, v26\n"
"v_smfmac_f32_16x16x64_bf16 v[228:231], v[224:227], v[56:63], v17\n"
"ds_read_b128 v[104:107], v7 offset:34816\n"
"v_exp_f32 v27, v27\n"
"v_exp_f32 v28, v28\n"
"v_smfmac_f32_32x32x32_bf16 v[152:167], v[116:119], v[64:71], v17\n"
"buffer_load_dwordx4 v2, s[28:31], s80 offen offset:0 sc1 lds\n"
"v_exp_f32 v29, v29\n"
"v_exp_f32 v30, v30\n"
"v_exp_f32 v31, v31\n"
"v_smfmac_f32_16x16x64_bf16 v[228:231], v[224:227], v[64:71], v17\n"
"ds_read_b128 v[108:111], v7 offset:35840\n"
"v_exp_f32 v32, v32\n"
"v_exp_f32 v33, v33\n"
"v_mfma_f32_32x32x16_bf16 v[56:71], v[144:147], v[244:247], v[208:223]\n"
"buffer_load_dwordx4 v2, s[28:31], s80 offen offset:1024 sc1 lds\n"
"v_exp_f32 v40, v40\n"
"v_exp_f32 v41, v41\n"
"v_exp_f32 v42, v42\n"
"v_mfma_f32_32x32x16_bf16 v[56:71], v[148:151], v[248:251], v[56:71]\n"
"buffer_load_dwordx4 v2, s[28:31], s80 offen offset:2048 sc1 lds\n"
"v_exp_f32 v43, v43\n"
"v_exp_f32 v44, v44\n"
"v_exp_f32 v45, v45\n"
"v_smfmac_f32_32x32x32_bf16 v[168:183], v[112:115], v[18:25], v17\n"
"buffer_load_dwordx4 v2, s[28:31], s80 offen offset:3072 sc1 lds\n"
"v_exp_f32 v46, v46\n"
"v_exp_f32 v47, v47\n"
"v_exp_f32 v48, v48\n"
"v_smfmac_f32_16x16x64_bf16 v[232:235], v[224:227], v[18:25], v17\n"
"ds_read_b128 v[144:147], v7 offset:40960\n"
"s_cmp_lg_u32 s41, 0\n"
"s_cbranch_scc1 .Lmk_fl_12\n"
"s_lshl_b32 s58, s27, 2\n"
"s_add_u32 s58, s58, 73728\n"
"v_mov_b32 v11, s26\n"
"s_mov_b64 exec, 1\n"
"buffer_store_dword v11, v9, s[28:31], s58 offen sc1\n"
"s_mov_b64 exec, -1\n"
"s_nop 1\n"
".Lmk_fl_12:\n"
"v_exp_f32 v49, v49\n"
"v_exp_f32 v50, v50\n"
"v_smfmac_f32_32x32x32_bf16 v[168:183], v[116:119], v[26:33], v17\n"
"s_mov_b64 s[32:33], s[16:17]\n"
"v_exp_f32 v51, v51\n"
"v_exp_f32 v52, v52\n"
"v_exp_f32 v53, v53\n"
"v_smfmac_f32_16x16x64_bf16 v[232:235], v[224:227], v[26:33], v17\n"
"ds_read_b128 v[148:151], v7 offset:41984\n"
"v_exp_f32 v54, v54\n"
"v_exp_f32 v55, v55\n"
"v_mfma_f32_32x32x16_bf16 v[18:33], v[128:131], v[236:239], v[192:207]\n"
"s_mov_b32 s34, 8388608\n"
"v_exp_f32 v56, v56\n"
"v_exp_f32 v57, v57\n"
"v_exp_f32 v58, v58\n"
"v_mfma_f32_32x32x16_bf16 v[18:33], v[132:135], v[240:243], v[18:33]\n"
"s_waitcnt lgkmcnt(2)\n"
"v_exp_f32 v59, v59\n"
"v_exp_f32 v60, v60\n"
"v_exp_f32 v61, v61\n"
"v_smfmac_f32_32x32x32_bf16 v[152:167], v[120:123], v[40:47], v17\n"
"s_add_u32 m0, s71, 65536\n"
"v_exp_f32 v62, v62\n"
"v_exp_f32 v63, v63\n"
"v_exp_f32 v64, v64\n"
"v_smfmac_f32_16x16x64_bf16 v[228:231], v[224:227], v[40:47], v17\n"
"ds_read_b128 v[112:115], v7 offset:38912\n"
"v_exp_f32 v65, v65\n"
"v_exp_f32 v66, v66\n"
"v_smfmac_f32_32x32x32_bf16 v[152:167], v[124:127], v[48:55], v17\n"
"s_add_u32 s57, s92, 31744\n"
"v_exp_f32 v67, v67\n"
"v_exp_f32 v68, v68\n"
"v_exp_f32 v69, v69\n"
"v_smfmac_f32_16x16x64_bf16 v[228:231], v[224:227], v[48:55], v17\n"
"ds_read_b128 v[116:119], v7 offset:39936\n"
"v_exp_f32 v70, v70\n"
"v_exp_f32 v71, v71\n"
"v_mfma_f32_32x32x16_bf16 v[40:55], v[128:131], v[244:247], v[208:223]\n"
"s_add_u32 s59, s57, 31744\n"
"v_exp_f32 v18, v18\n"
"v_exp_f32 v19, v19\n"
"v_exp_f32 v20, v20\n"
"v_mfma_f32_32x32x16_bf16 v[40:55], v[132:135], v[248:251], v[40:55]\n"
"s_add_u32 s60, s59, 31744\n"
"v_exp_f32 v21, v21\n"
"v_exp_f32 v22, v22\n"
"v_exp_f32 v23, v23\n"
"v_smfmac_f32_32x32x32_bf16 v[168:183], v[120:123], v[56:63], v17\n"
"v_exp_f32 v24, v24\n"
"v_exp_f32 v25, v25\n"
"v_exp_f32 v26, v26\n"
"v_smfmac_f32_16x16x64_bf16 v[232:235], v[224:227], v[56:63], v17\n"
"ds_read_b128 v[128:131], v7 offset:45056\n"
"v_exp_f32 v27, v27\n"
"v_exp_f32 v28, v28\n"
"v_smfmac_f32_32x32x32_bf16 v[168:183], v[124:127], v[64:71], v17\n"
"buffer_load_dwordx4 v191, s[32:35], s92 offen offset:0 lds\n"
"v_exp_f32 v29, v29\n"
"v_exp_f32 v30, v30\n"
"v_exp_f32 v31, v31\n"
"v_smfmac_f32_16x16x64_bf16 v[232:235], v[224:227], v[64:71], v17\n"
"ds_read_b128 v[132:135], v7 offset:46080\n"
"v_exp_f32 v32, v32\n"
"v_exp_f32 v33, v33\n"
"v_mfma_f32_32x32x16_bf16 v[56:71], v[136:139], v[236:239], v[192:207]\n"
"v_exp_f32 v40, v40\n"
"v_exp_f32 v41, v41\n"
"v_exp_f32 v42, v42\n"
"v_mfma_f32_32x32x16_bf16 v[56:71], v[140:143], v[240:243], v[56:71]\n"
"s_waitcnt lgkmcnt(2)\n"
"v_exp_f32 v43, v43\n"
"v_exp_f32 v44, v44\n"
"v_exp_f32 v45, v45\n"
"v_smfmac_f32_32x32x32_bf16 v[152:167], v[104:107], v[18:25], v17\n"
"buffer_load_dwordx4 v191, s[32:35], s57 offen offset:1024 lds\n"
"v_exp_f32 v46, v46\n"
"v_exp_f32 v47, v47\n"
"v_exp_f32 v48, v48\n"
"v_smfmac_f32_16x16x64_bf16 v[228:231], v[224:227], v[18:25], v17\n"
"ds_read_b128 v[120:123], v7 offset:43008\n"
"v_exp_f32 v49, v49\n"
"v_exp_f32 v50, v50\n"
"v_smfmac_f32_32x32x32_bf16 v[152:167], v[108:111], v[26:33], v17\n"
"buffer_load_dwordx4 v191, s[32:35], s59 offen offset:2048 lds\n"
"v_exp_f32 v51, v51\n"
"v_exp_f32 v52, v52\n"
"v_exp_f32 v53, v53\n"
"v_smfmac_f32_16x16x64_bf16 v[228:231], v[224:227], v[26:33], v17\n"
"ds_read_b128 v[124:127], v7 offset:44032\n"
"v_exp_f32 v54, v54\n"
"v_exp_f32 v55, v55\n"
"v_mfma_f32_32x32x16_bf16 v[18:33], v[136:139], v[244:247], v[208:223]\n"
"buffer_load_dwordx4 v191, s[32:35], s60 offen offset:3072 lds\n"
"v_exp_f32 v56, v56\n"
"v_exp_f32 v57, v57\n"
"v_exp_f32 v58, v58\n"
"v_mfma_f32_32x32x16_bf16 v[18:33], v[140:143], v[248:251], v[18:33]\n"
"v_exp_f32 v59, v59\n"
"v_exp_f32 v60, v60\n"
"v_exp_f32 v61, v61\n"
"v_smfmac_f32_32x32x32_bf16 v[168:183], v[104:107], v[40:47], v17\n"
"v_exp_f32 v62, v62\n"
"v_exp_f32 v63, v63\n"
"v_exp_f32 v64, v64\n"
"v_smfmac_f32_16x16x64_bf16 v[232:235], v[224:227], v[40:47], v17\n"
"ds_read_b128 v[136:139], v7 offset:49152\n"
"v_exp_f32 v65, v65\n"
"v_exp_f32 v66, v66\n"
"v_smfmac_f32_32x32x32_bf16 v[168:183], v[108:111], v[48:55], v17\n"
"s_waitcnt vmcnt(16)\n"
"v_exp_f32 v67, v67\n"
"v_exp_f32 v68, v68\n"
"v_exp_f32 v69, v69\n"
"v_smfmac_f32_16x16x64_bf16 v[232:235], v[224:227], v[48:55], v17\n"
"ds_read_b128 v[140:143], v7 offset:50176\n"
"v_exp_f32 v70, v70\n"
"v_exp_f32 v71, v71\n"
"v_mfma_f32_32x32x16_bf16 v[40:55], v[144:147], v[236:239], v[192:207]\n"
"v_exp_f32 v18, v18\n"
"v_exp_f32 v19, v19\n"
"v_exp_f32 v20, v20\n"
"v_mfma_f32_32x32x16_bf16 v[40:55], v[148:151], v[240:243], v[40:55]\n"
"s_waitcnt lgkmcnt(2)\n"
"v_exp_f32 v21, v21\n"
"v_exp_f32 v22, v22\n"
"v_exp_f32 v23, v23\n"
"v_smfmac_f32_32x32x32_bf16 v[152:167], v[112:115], v[56:63], v17\n"
"ds_read_b64_tr_b16 a[240:241], v185 offset:4096\n"
"ds_read_b64_tr_b16 a[242:243], v185 offset:4608\n"
"v_exp_f32 v24, v24\n"
"v_exp_f32 v25, v25\n"
"v_exp_f32 v26, v26\n"
"v_smfmac_f32_16x16x64_bf16 v[228:231], v[224:227], v[56:63], v17\n"
"ds_read_b128 v[104:107], v7 offset:47104\n"
"v_exp_f32 v27, v27\n"
"v_exp_f32 v28, v28\n"
"v_smfmac_f32_32x32x32_bf16 v[152:167], v[116:119], v[64:71], v17\n"
"ds_read_b64_tr_b16 a[244:245], v185 offset:4128\n"
"ds_read_b64_tr_b16 a[246:247], v185 offset:4640\n"
"v_exp_f32 v29, v29\n"
"v_exp_f32 v30, v30\n"
"v_exp_f32 v31, v31\n"
"v_smfmac_f32_16x16x64_bf16 v[228:231], v[224:227], v[64:71], v17\n"
"ds_read_b128 v[108:111], v7 offset:48128\n"
"v_exp_f32 v32, v32\n"
"v_exp_f32 v33, v33\n"
"v_mfma_f32_32x32x16_bf16 v[56:71], v[144:147], v[244:247], v[208:223]\n"
"v_exp_f32 v40, v40\n"
"v_exp_f32 v41, v41\n"
"v_exp_f32 v42, v42\n"
"v_mfma_f32_32x32x16_bf16 v[56:71], v[148:151], v[248:251], v[56:71]\n"
"v_exp_f32 v43, v43\n"
"v_exp_f32 v44, v44\n"
"v_exp_f32 v45, v45\n"
"v_smfmac_f32_32x32x32_bf16 v[168:183], v[112:115], v[18:25], v17\n"
"ds_read_b64_tr_b16 a[248:249], v185 offset:4160\n"
"ds_read_b64_tr_b16 a[250:251], v185 offset:4672\n"
"v_exp_f32 v46, v46\n"
"v_exp_f32 v47, v47\n"
"v_exp_f32 v48, v48\n"
"v_smfmac_f32_16x16x64_bf16 v[232:235], v[224:227], v[18:25], v17\n"
"ds_read_b128 v[144:147], v7 offset:53248\n"
"v_exp_f32 v49, v49\n"
"v_exp_f32 v50, v50\n"
"v_smfmac_f32_32x32x32_bf16 v[168:183], v[116:119], v[26:33], v17\n"
"ds_read_b64_tr_b16 a[252:253], v185 offset:4192\n"
"ds_read_b64_tr_b16 a[254:255], v185 offset:4704\n"
"v_exp_f32 v51, v51\n"
"v_exp_f32 v52, v52\n"
"v_exp_f32 v53, v53\n"
"v_smfmac_f32_16x16x64_bf16 v[232:235], v[224:227], v[26:33], v17\n"
"ds_read_b128 v[148:151], v7 offset:54272\n"
"v_exp_f32 v54, v54\n"
"v_exp_f32 v55, v55\n"
"v_mfma_f32_32x32x16_bf16 v[18:33], v[128:131], v[236:239], v[192:207]\n"
"s_waitcnt lgkmcnt(1)\n"
"v_exp_f32 v56, v56\n"
"v_exp_f32 v57, v57\n"
"v_exp_f32 v58, v58\n"
"v_mfma_f32_32x32x16_bf16 v[18:33], v[132:135], v[240:243], v[18:33]\n"
"v_exp_f32 v59, v59\n"
"v_exp_f32 v60, v60\n"
"v_exp_f32 v61, v61\n"
"v_smfmac_f32_32x32x32_bf16 v[152:167], v[120:123], v[40:47], v17\n"
"buffer_store_dwordx4 a[240:243], v3, s[28:31], s7 offen offset:3072 sc1\n"
"v_exp_f32 v62, v62\n"
"v_exp_f32 v63, v63\n"
"v_exp_f32 v64, v64\n"
"v_smfmac_f32_16x16x64_bf16 v[228:231], v[224:227], v[40:47], v17\n"
"ds_read_b128 v[112:115], v7 offset:51200\n"
"v_exp_f32 v65, v65\n"
"v_exp_f32 v66, v66\n"
"v_smfmac_f32_32x32x32_bf16 v[152:167], v[124:127], v[48:55], v17\n"
"buffer_store_dwordx4 a[244:247], v3, s[28:31], s8 offen offset:3072 sc1\n"
"v_exp_f32 v67, v67\n"
"v_exp_f32 v68, v68\n"
"v_exp_f32 v69, v69\n"
"v_smfmac_f32_16x16x64_bf16 v[228:231], v[224:227], v[48:55], v17\n"
"ds_read_b128 v[116:119], v7 offset:52224\n"
"v_exp_f32 v70, v70\n"
"v_exp_f32 v71, v71\n"
"v_mfma_f32_32x32x16_bf16 v[40:55], v[128:131], v[244:247], v[208:223]\n"
"buffer_store_dwordx4 a[248:251], v3, s[28:31], s9 offen offset:3072 sc1\n"
"v_exp_f32 v18, v18\n"
"v_exp_f32 v19, v19\n"
"v_exp_f32 v20, v20\n"
"v_mfma_f32_32x32x16_bf16 v[40:55], v[132:135], v[248:251], v[40:55]\n"
"v_exp_f32 v21, v21\n"
"v_exp_f32 v22, v22\n"
"v_exp_f32 v23, v23\n"
"v_smfmac_f32_32x32x32_bf16 v[168:183], v[120:123], v[56:63], v17\n"
"buffer_store_dwordx4 a[252:255], v3, s[28:31], s10 offen offset:3072 sc1\n"
"v_mfma_f32_16x16x32_bf16 v[72:75], v[252:255], a[240:243], v[72:75]\n"
"v_mfma_f32_16x16x32_bf16 v[76:79], v[252:255], a[244:247], v[76:79]\n"
"v_exp_f32 v24, v24\n"
"v_exp_f32 v25, v25\n"
"v_exp_f32 v26, v26\n"
"v_smfmac_f32_16x16x64_bf16 v[232:235], v[224:227], v[56:63], v17\n"
"ds_read_b128 v[128:131], v7 offset:57344\n"
"v_exp_f32 v27, v27\n"
"v_exp_f32 v28, v28\n"
"v_smfmac_f32_32x32x32_bf16 v[168:183], v[124:127], v[64:71], v17\n"
"v_mfma_f32_16x16x32_bf16 v[80:83], v[252:255], a[248:251], v[80:83]\n"
"v_exp_f32 v29, v29\n"
"v_exp_f32 v30, v30\n"
"v_exp_f32 v31, v31\n"
"v_smfmac_f32_16x16x64_bf16 v[232:235], v[224:227], v[64:71], v17\n"
"ds_read_b128 v[132:135], v7 offset:58368\n"
"v_exp_f32 v32, v32\n"
"v_exp_f32 v33, v33\n"
"v_mfma_f32_32x32x16_bf16 v[56:71], v[136:139], v[236:239], v[192:207]\n"
"v_mfma_f32_16x16x32_bf16 v[84:87], v[252:255], a[252:255], v[84:87]\n"
"v_exp_f32 v40, v40\n"
"v_exp_f32 v41, v41\n"
"v_exp_f32 v42, v42\n"
"v_mfma_f32_32x32x16_bf16 v[56:71], v[140:143], v[240:243], v[56:71]\n"
"s_waitcnt lgkmcnt(2)\n"
"v_exp_f32 v43, v43\n"
"v_exp_f32 v44, v44\n"
"v_exp_f32 v45, v45\n"
"v_smfmac_f32_32x32x32_bf16 v[152:167], v[104:107], v[18:25], v17\n"
"v_mov_b32_dpp v72, v76 quad_perm:[0,1,2,3] row_mask:2 bank_mask:0xf\n"
"v_exp_f32 v46, v46\n"
"v_exp_f32 v47, v47\n"
"v_exp_f32 v48, v48\n"
"v_smfmac_f32_16x16x64_bf16 v[228:231], v[224:227], v[18:25], v17\n"
"ds_read_b128 v[120:123], v7 offset:55296\n"
"v_exp_f32 v49, v49\n"
"v_exp_f32 v50, v50\n"
"v_smfmac_f32_32x32x32_bf16 v[152:167], v[108:111], v[26:33], v17\n"
"v_mov_b32_dpp v72, v80 quad_perm:[0,1,2,3] row_mask:4 bank_mask:0xf\n"
"v_exp_f32 v51, v51\n"
"v_exp_f32 v52, v52\n"
"v_exp_f32 v53, v53\n"
"v_smfmac_f32_16x16x64_bf16 v[228:231], v[224:227], v[26:33], v17\n"
"ds_read_b128 v[124:127], v7 offset:56320\n"
"v_exp_f32 v54, v54\n"
"v_exp_f32 v55, v55\n"
"v_mfma_f32_32x32x16_bf16 v[18:33], v[136:139], v[244:247], v[208:223]\n"
"v_mov_b32_dpp v72, v84 quad_perm:[0,1,2,3] row_mask:8 bank_mask:0xf\n"
"v_exp_f32 v56, v56\n"
"v_exp_f32 v57, v57\n"
"v_exp_f32 v58, v58\n"
"v_mfma_f32_32x32x16_bf16 v[18:33], v[140:143], v[248:251], v[18:33]\n"
"v_exp_f32 v59, v59\n"
"v_exp_f32 v60, v60\n"
"v_exp_f32 v61, v61\n"
"v_smfmac_f32_32x32x32_bf16 v[168:183], v[104:107], v[40:47], v17\n"
"v_lshlrev_b32 v73, 2, v1\n"
"v_exp_f32 v62, v62\n"
".Lmk_pg_5:\n"
"s_cbranch_execz .Lmk_ret_1\n"
"v_exp_f32 v63, v63\n"
"v_exp_f32 v64, v64\n"
"v_smfmac_f32_16x16x64_bf16 v[232:235], v[224:227], v[40:47], v17\n"
"ds_read_b128 v[136:139], v7 offset:61440\n"
"v_exp_f32 v65, v65\n"
"v_exp_f32 v66, v66\n"
"v_smfmac_f32_32x32x32_bf16 v[168:183], v[108:111], v[48:55], v17\n"
"buffer_store_dword v72, v73, s[28:31], s11 offen sc1\n"
"v_exp_f32 v67, v67\n"
"v_exp_f32 v68, v68\n"
"v_exp_f32 v69, v69\n"
"v_smfmac_f32_16x16x64_bf16 v[232:235], v[224:227], v[48:55], v17\n"
"ds_read_b128 v[140:143], v7 offset:62464\n"
"v_exp_f32 v70, v70\n"
"v_exp_f32 v71, v71\n"
"v_mfma_f32_32x32x16_bf16 v[40:55], v[144:147], v[236:239], v[192:207]\n"
"v_exp_f32 v18, v18\n"
"v_exp_f32 v19, v19\n"
"v_exp_f32 v20, v20\n"
"v_mfma_f32_32x32x16_bf16 v[40:55], v[148:151], v[240:243], v[40:55]\n"
"s_waitcnt lgkmcnt(2)\n"
"v_exp_f32 v21, v21\n"
"v_exp_f32 v22, v22\n"
"v_exp_f32 v23, v23\n"
"v_smfmac_f32_32x32x32_bf16 v[152:167], v[112:115], v[56:63], v17\n"
"v_exp_f32 v24, v24\n"
"v_exp_f32 v25, v25\n"
"v_exp_f32 v26, v26\n"
"v_smfmac_f32_16x16x64_bf16 v[228:231], v[224:227], v[56:63], v17\n"
"ds_read_b128 v[104:107], v7 offset:59392\n"
"v_exp_f32 v27, v27\n"
"v_exp_f32 v28, v28\n"
"v_smfmac_f32_32x32x32_bf16 v[152:167], v[116:119], v[64:71], v17\n"
"v_exp_f32 v29, v29\n"
"v_exp_f32 v30, v30\n"
"v_exp_f32 v31, v31\n"
"v_smfmac_f32_16x16x64_bf16 v[228:231], v[224:227], v[64:71], v17\n"
"ds_read_b128 v[108:111], v7 offset:60416\n"
"v_exp_f32 v32, v32\n"
"v_exp_f32 v33, v33\n"
"v_mfma_f32_32x32x16_bf16 v[56:71], v[144:147], v[244:247], v[208:223]\n"
"s_waitcnt vmcnt(9)\n"
"v_exp_f32 v40, v40\n"
"v_exp_f32 v41, v41\n"
"v_exp_f32 v42, v42\n"
"v_mfma_f32_32x32x16_bf16 v[56:71], v[148:151], v[248:251], v[56:71]\n"
"s_barrier\n"
"v_exp_f32 v43, v43\n"
"v_exp_f32 v44, v44\n"
"v_exp_f32 v45, v45\n"
"v_smfmac_f32_32x32x32_bf16 v[168:183], v[112:115], v[18:25], v17\n"
"s_add_u32 s81, s81, 0x8000\n"
"v_exp_f32 v46, v46\n"
"v_exp_f32 v47, v47\n"
"v_exp_f32 v48, v48\n"
"v_smfmac_f32_16x16x64_bf16 v[232:235], v[224:227], v[18:25], v17\n"
"ds_read_b128 v[144:147], v6 offset:0\n"
"v_exp_f32 v49, v49\n"
"v_exp_f32 v50, v50\n"
"v_smfmac_f32_32x32x32_bf16 v[168:183], v[116:119], v[26:33], v17\n"
"s_and_b32 s81, s81, 0x3ffff\n"
"v_exp_f32 v51, v51\n"
"v_exp_f32 v52, v52\n"
"v_exp_f32 v53, v53\n"
"v_smfmac_f32_16x16x64_bf16 v[232:235], v[224:227], v[26:33], v17\n"
"ds_read_b128 v[148:151], v6 offset:1024\n"
"v_exp_f32 v54, v54\n"
"v_exp_f32 v55, v55\n"
"v_mfma_f32_32x32x16_bf16 v[18:33], v[128:131], v[236:239], v[192:207]\n"
"s_add_u32 s80, s81, 524288\n"
"v_exp_f32 v56, v56\n"
"v_exp_f32 v57, v57\n"
"v_exp_f32 v58, v58\n"
"v_mfma_f32_32x32x16_bf16 v[18:33], v[132:135], v[240:243], v[18:33]\n"
"s_waitcnt lgkmcnt(2)\n"
"v_exp_f32 v59, v59\n"
"v_exp_f32 v60, v60\n"
"v_exp_f32 v61, v61\n"
"v_smfmac_f32_32x32x32_bf16 v[152:167], v[120:123], v[40:47], v17\n"
"s_add_u32 m0, s53, 32768\n"
"v_exp_f32 v62, v62\n"
"v_exp_f32 v63, v63\n"
"v_exp_f32 v64, v64\n"
"v_smfmac_f32_16x16x64_bf16 v[228:231], v[224:227], v[40:47], v17\n"
"ds_read_b128 v[112:115], v7 offset:63488\n"
"v_exp_f32 v65, v65\n"
"v_exp_f32 v66, v66\n"
"v_smfmac_f32_32x32x32_bf16 v[152:167], v[124:127], v[48:55], v17\n"
"buffer_load_dwordx4 v2, s[28:31], s80 offen offset:0 sc1 lds\n"
"v_exp_f32 v67, v67\n"
"v_exp_f32 v68, v68\n"
"v_exp_f32 v69, v69\n"
"v_smfmac_f32_16x16x64_bf16 v[228:231], v[224:227], v[48:55], v17\n"
"ds_read_b128 v[116:119], v7 offset:64512\n"
"v_exp_f32 v70, v70\n"
"v_exp_f32 v71, v71\n"
"v_mfma_f32_32x32x16_bf16 v[40:55], v[128:131], v[244:247], v[208:223]\n"
"v_exp_f32 v18, v18\n"
"v_exp_f32 v19, v19\n"
"v_exp_f32 v20, v20\n"
"v_mfma_f32_32x32x16_bf16 v[40:55], v[132:135], v[248:251], v[40:55]\n"
"buffer_load_dwordx4 v2, s[28:31], s80 offen offset:1024 sc1 lds\n"
"v_exp_f32 v21, v21\n"
"v_exp_f32 v22, v22\n"
"v_exp_f32 v23, v23\n"
"v_smfmac_f32_32x32x32_bf16 v[168:183], v[120:123], v[56:63], v17\n"
"buffer_load_dwordx4 v2, s[28:31], s80 offen offset:2048 sc1 lds\n"
"v_exp_f32 v24, v24\n"
"v_exp_f32 v25, v25\n"
"v_exp_f32 v26, v26\n"
"v_smfmac_f32_16x16x64_bf16 v[232:235], v[224:227], v[56:63], v17\n"
"ds_read_b128 v[128:131], v6 offset:4096\n"
"v_exp_f32 v27, v27\n"
"v_exp_f32 v28, v28\n"
"v_smfmac_f32_32x32x32_bf16 v[168:183], v[124:127], v[64:71], v17\n"
"buffer_load_dwordx4 v2, s[28:31], s80 offen offset:3072 sc1 lds\n"
"v_exp_f32 v29, v29\n"
"v_exp_f32 v30, v30\n"
"v_exp_f32 v31, v31\n"
"v_smfmac_f32_16x16x64_bf16 v[232:235], v[224:227], v[64:71], v17\n"
"ds_read_b128 v[132:135], v6 offset:5120\n"
"v_exp_f32 v32, v32\n"
"v_exp_f32 v33, v33\n"
"v_mfma_f32_32x32x16_bf16 v[56:71], v[136:139], v[236:239], v[192:207]\n"
"s_add_u32 s80, s80, 0x4000\n"
"v_exp_f32 v40, v40\n"
"v_exp_f32 v41, v41\n"
"v_exp_f32 v42, v42\n"
"v_mfma_f32_32x32x16_bf16 v[56:71], v[140:143], v[240:243], v[56:71]\n"
"s_waitcnt lgkmcnt(2)\n"
"v_exp_f32 v43, v43\n"
"v_exp_f32 v44, v44\n"
"v_exp_f32 v45, v45\n"
"v_smfmac_f32_32x32x32_bf16 v[152:167], v[104:107], v[18:25], v17\n"
"s_add_u32 m0, s53, 49152\n"
"v_exp_f32 v46, v46\n"
"v_exp_f32 v47, v47\n"
"v_exp_f32 v48, v48\n"
"v_smfmac_f32_16x16x64_bf16 v[228:231], v[224:227], v[18:25], v17\n"
"ds_read_b128 v[120:123], v6 offset:2048\n"
"v_exp_f32 v49, v49\n"
"v_exp_f32 v50, v50\n"
"v_smfmac_f32_32x32x32_bf16 v[152:167], v[108:111], v[26:33], v17\n"
"buffer_load_dwordx4 v2, s[28:31], s80 offen offset:0 sc1 lds\n"
"v_exp_f32 v51, v51\n"
"v_exp_f32 v52, v52\n"
"v_exp_f32 v53, v53\n"
"v_smfmac_f32_16x16x64_bf16 v[228:231], v[224:227], v[26:33], v17\n"
"ds_read_b128 v[124:127], v6 offset:3072\n"
"v_exp_f32 v54, v54\n"
"v_exp_f32 v55, v55\n"
"v_mfma_f32_32x32x16_bf16 v[18:33], v[136:139], v[244:247], v[208:223]\n"
"buffer_load_dwordx4 v2, s[28:31], s80 offen offset:1024 sc1 lds\n"
"v_exp_f32 v56, v56\n"
"v_exp_f32 v57, v57\n"
"v_exp_f32 v58, v58\n"
"v_mfma_f32_32x32x16_bf16 v[18:33], v[140:143], v[248:251], v[18:33]\n"
"buffer_load_dwordx4 v2, s[28:31], s80 offen offset:2048 sc1 lds\n"
"v_exp_f32 v59, v59\n"
"v_exp_f32 v60, v60\n"
"v_exp_f32 v61, v61\n"
"v_smfmac_f32_32x32x32_bf16 v[168:183], v[104:107], v[40:47], v17\n"
"buffer_load_dwordx4 v2, s[28:31], s80 offen offset:3072 sc1 lds\n"
"v_exp_f32 v62, v62\n"
"v_exp_f32 v63, v63\n"
"v_exp_f32 v64, v64\n"
"v_smfmac_f32_16x16x64_bf16 v[232:235], v[224:227], v[40:47], v17\n"
"ds_read_b128 v[136:139], v6 offset:8192\n"
"v_exp_f32 v65, v65\n"
"v_exp_f32 v66, v66\n"
"v_smfmac_f32_32x32x32_bf16 v[168:183], v[108:111], v[48:55], v17\n"
"s_add_u32 m0, s71, 69632\n"
"v_exp_f32 v67, v67\n"
"v_exp_f32 v68, v68\n"
"v_exp_f32 v69, v69\n"
"v_smfmac_f32_16x16x64_bf16 v[232:235], v[224:227], v[48:55], v17\n"
"ds_read_b128 v[140:143], v6 offset:9216\n"
"v_exp_f32 v70, v70\n"
"v_exp_f32 v71, v71\n"
"v_mfma_f32_32x32x16_bf16 v[40:55], v[144:147], v[236:239], v[192:207]\n"
"s_add_u32 s92, s92, 128\n"
"v_exp_f32 v18, v18\n"
"v_exp_f32 v19, v19\n"
"v_exp_f32 v20, v20\n"
"v_mfma_f32_32x32x16_bf16 v[40:55], v[148:151], v[240:243], v[40:55]\n"
"s_waitcnt lgkmcnt(2)\n"
"v_exp_f32 v21, v21\n"
"v_exp_f32 v22, v22\n"
"v_exp_f32 v23, v23\n"
"v_smfmac_f32_32x32x32_bf16 v[152:167], v[112:115], v[56:63], v17\n"
"s_add_u32 s57, s92, 31744\n"
"v_exp_f32 v24, v24\n"
"v_exp_f32 v25, v25\n"
"v_exp_f32 v26, v26\n"
"v_smfmac_f32_16x16x64_bf16 v[228:231], v[224:227], v[56:63], v17\n"
"ds_read_b128 v[104:107], v6 offset:6144\n"
"v_exp_f32 v27, v27\n"
"v_exp_f32 v28, v28\n"
"v_smfmac_f32_32x32x32_bf16 v[152:167], v[116:119], v[64:71], v17\n"
"s_add_u32 s59, s57, 31744\n"
"v_exp_f32 v29, v29\n"
"v_exp_f32 v30, v30\n"
"v_exp_f32 v31, v31\n"
"v_smfmac_f32_16x16x64_bf16 v[228:231], v[224:227], v[64:71], v17\n"
"ds_read_b128 v[108:111], v6 offset:7168\n"
"v_exp_f32 v32, v32\n"
"v_exp_f32 v33, v33\n"
"v_mfma_f32_32x32x16_bf16 v[56:71], v[144:147], v[244:247], v[208:223]\n"
"s_add_u32 s60, s59, 31744\n"
"v_exp_f32 v40, v40\n"
"v_exp_f32 v41, v41\n"
"v_exp_f32 v42, v42\n"
"v_mfma_f32_32x32x16_bf16 v[56:71], v[148:151], v[248:251], v[56:71]\n"
"v_exp_f32 v43, v43\n"
"v_exp_f32 v44, v44\n"
"v_exp_f32 v45, v45\n"
"v_smfmac_f32_32x32x32_bf16 v[168:183], v[112:115], v[18:25], v17\n"
"buffer_load_dwordx4 v191, s[32:35], s92 offen offset:0 lds\n"
"v_exp_f32 v46, v46\n"
"v_exp_f32 v47, v47\n"
"v_exp_f32 v48, v48\n"
"v_smfmac_f32_16x16x64_bf16 v[232:235], v[224:227], v[18:25], v17\n"
"ds_read_b128 v[144:147], v6 offset:12288\n"
"v_exp_f32 v49, v49\n"
"v_exp_f32 v50, v50\n"
"v_smfmac_f32_32x32x32_bf16 v[168:183], v[116:119], v[26:33], v17\n"
"buffer_load_dwordx4 v191, s[32:35], s57 offen offset:1024 lds\n"
"v_exp_f32 v51, v51\n"
"v_exp_f32 v52, v52\n"
"v_exp_f32 v53, v53\n"
"v_smfmac_f32_16x16x64_bf16 v[232:235], v[224:227], v[26:33], v17\n"
"ds_read_b128 v[148:151], v6 offset:13312\n"
"v_exp_f32 v54, v54\n"
"v_exp_f32 v55, v55\n"
"v_mfma_f32_32x32x16_bf16 v[18:33], v[128:131], v[236:239], v[192:207]\n"
"v_exp_f32 v56, v56\n"
"v_exp_f32 v57, v57\n"
"v_exp_f32 v58, v58\n"
"v_mfma_f32_32x32x16_bf16 v[18:33], v[132:135], v[240:243], v[18:33]\n"
"s_waitcnt lgkmcnt(2)\n"
"v_exp_f32 v59, v59\n"
"v_exp_f32 v60, v60\n"
"v_exp_f32 v61, v61\n"
"v_smfmac_f32_32x32x32_bf16 v[152:167], v[120:123], v[40:47], v17\n"
"buffer_load_dwordx4 v191, s[32:35], s59 offen offset:2048 lds\n"
"v_exp_f32 v62, v62\n"
"v_exp_f32 v63, v63\n"
"v_exp_f32 v64, v64\n"
"v_smfmac_f32_16x16x64_bf16 v[228:231], v[224:227], v[40:47], v17\n"
"ds_read_b128 v[112:115], v6 offset:10240\n"
"v_exp_f32 v65, v65\n"
"v_exp_f32 v66, v66\n"
"v_smfmac_f32_32x32x32_bf16 v[152:167], v[124:127], v[48:55], v17\n"
"buffer_load_dwordx4 v191, s[32:35], s60 offen offset:3072 lds\n"
"v_exp_f32 v67, v67\n"
"v_exp_f32 v68, v68\n"
"v_exp_f32 v69, v69\n"
"v_smfmac_f32_16x16x64_bf16 v[228:231], v[224:227], v[48:55], v17\n"
"ds_read_b128 v[116:119], v6 offset:11264\n"
"v_exp_f32 v70, v70\n"
"v_exp_f32 v71, v71\n"
"v_mfma_f32_32x32x16_bf16 v[40:55], v[128:131], v[244:247], v[208:223]\n"
"v_exp_f32 v18, v18\n"
"v_exp_f32 v19, v19\n"
"v_exp_f32 v20, v20\n"
"v_mfma_f32_32x32x16_bf16 v[40:55], v[132:135], v[248:251], v[40:55]\n"
"v_exp_f32 v21, v21\n"
"v_exp_f32 v22, v22\n"
"v_exp_f32 v23, v23\n"
"v_smfmac_f32_32x32x32_bf16 v[168:183], v[120:123], v[56:63], v17\n"
"v_exp_f32 v24, v24\n"
"v_exp_f32 v25, v25\n"
"v_exp_f32 v26, v26\n"
"v_smfmac_f32_16x16x64_bf16 v[232:235], v[224:227], v[56:63], v17\n"
"ds_read_b128 v[128:131], v6 offset:16384\n"
"v_exp_f32 v27, v27\n"
"v_exp_f32 v28, v28\n"
"v_smfmac_f32_32x32x32_bf16 v[168:183], v[124:127], v[64:71], v17\n"
"s_waitcnt vmcnt(17)\n"
"v_exp_f32 v29, v29\n"
"v_exp_f32 v30, v30\n"
"v_exp_f32 v31, v31\n"
"v_smfmac_f32_16x16x64_bf16 v[232:235], v[224:227], v[64:71], v17\n"
"ds_read_b128 v[132:135], v6 offset:17408\n"
"v_exp_f32 v32, v32\n"
"v_exp_f32 v33, v33\n"
"v_mfma_f32_32x32x16_bf16 v[56:71], v[136:139], v[236:239], v[192:207]\n"
"v_exp_f32 v40, v40\n"
"v_exp_f32 v41, v41\n"
"v_exp_f32 v42, v42\n"
"v_mfma_f32_32x32x16_bf16 v[56:71], v[140:143], v[240:243], v[56:71]\n"
"s_waitcnt lgkmcnt(2)\n"
"v_exp_f32 v43, v43\n"
"v_exp_f32 v44, v44\n"
"v_exp_f32 v45, v45\n"
"v_smfmac_f32_32x32x32_bf16 v[152:167], v[104:107], v[18:25], v17\n"
"ds_read_b128 a[240:243], v187 offset:0\n"
"ds_read_b128 a[244:247], v187 offset:512\n"
"v_exp_f32 v46, v46\n"
"v_exp_f32 v47, v47\n"
"v_exp_f32 v48, v48\n"
"v_smfmac_f32_16x16x64_bf16 v[228:231], v[224:227], v[18:25], v17\n"
"ds_read_b128 v[120:123], v6 offset:14336\n"
"v_exp_f32 v49, v49\n"
"v_exp_f32 v50, v50\n"
"v_smfmac_f32_32x32x32_bf16 v[152:167], v[108:111], v[26:33], v17\n"
"ds_read_b128 a[248:251], v187 offset:64\n"
"ds_read_b128 a[252:255], v187 offset:576\n"
"v_exp_f32 v51, v51\n"
"v_exp_f32 v52, v52\n"
"v_exp_f32 v53, v53\n"
"v_smfmac_f32_16x16x64_bf16 v[228:231], v[224:227], v[26:33], v17\n"
"ds_read_b128 v[124:127], v6 offset:15360\n"
"v_exp_f32 v54, v54\n"
"v_exp_f32 v55, v55\n"
"v_mfma_f32_32x32x16_bf16 v[18:33], v[136:139], v[244:247], v[208:223]\n"
"s_waitcnt lgkmcnt(1)\n"
"v_exp_f32 v56, v56\n"
"v_exp_f32 v57, v57\n"
"v_exp_f32 v58, v58\n"
"v_mfma_f32_32x32x16_bf16 v[18:33], v[140:143], v[248:251], v[18:33]\n"
"v_exp_f32 v59, v59\n"
"v_exp_f32 v60, v60\n"
"v_exp_f32 v61, v61\n"
"v_smfmac_f32_32x32x32_bf16 v[168:183], v[104:107], v[40:47], v17\n"
"buffer_store_dwordx4 a[240:243], v2, s[28:31], s25 offen offset:0 sc1\n"
"v_exp_f32 v62, v62\n"
"v_exp_f32 v63, v63\n"
"v_exp_f32 v64, v64\n"
"v_smfmac_f32_16x16x64_bf16 v[232:235], v[224:227], v[40:47], v17\n"
"ds_read_b128 v[136:139], v6 offset:20480\n"
"v_exp_f32 v65, v65\n"
"v_exp_f32 v66, v66\n"
"v_smfmac_f32_32x32x32_bf16 v[168:183], v[108:111], v[48:55], v17\n"
"buffer_store_dwordx4 a[244:247], v2, s[28:31], s25 offen offset:1024 sc1\n"
"v_exp_f32 v67, v67\n"
"v_exp_f32 v68, v68\n"
"v_exp_f32 v69, v69\n"
"v_smfmac_f32_16x16x64_bf16 v[232:235], v[224:227], v[48:55], v17\n"
"ds_read_b128 v[140:143], v6 offset:21504\n"
"v_exp_f32 v70, v70\n"
"v_exp_f32 v71, v71\n"
"v_mfma_f32_32x32x16_bf16 v[40:55], v[144:147], v[236:239], v[192:207]\n"
"v_exp_f32 v18, v18\n"
"v_exp_f32 v19, v19\n"
"v_exp_f32 v20, v20\n"
"v_mfma_f32_32x32x16_bf16 v[40:55], v[148:151], v[240:243], v[40:55]\n"
"s_waitcnt lgkmcnt(2)\n"
"v_exp_f32 v21, v21\n"
"v_exp_f32 v22, v22\n"
"v_exp_f32 v23, v23\n"
"v_smfmac_f32_32x32x32_bf16 v[152:167], v[112:115], v[56:63], v17\n"
"buffer_store_dwordx4 a[248:251], v2, s[28:31], s25 offen offset:2048 sc1\n"
"v_exp_f32 v24, v24\n"
"v_exp_f32 v25, v25\n"
"v_exp_f32 v26, v26\n"
"v_smfmac_f32_16x16x64_bf16 v[228:231], v[224:227], v[56:63], v17\n"
"ds_read_b128 v[104:107], v6 offset:18432\n"
"v_exp_f32 v27, v27\n"
"v_exp_f32 v28, v28\n"
"v_smfmac_f32_32x32x32_bf16 v[152:167], v[116:119], v[64:71], v17\n"
"buffer_store_dwordx4 a[252:255], v2, s[28:31], s25 offen offset:3072 sc1\n"
"v_exp_f32 v29, v29\n"
"v_exp_f32 v30, v30\n"
"v_exp_f32 v31, v31\n"
"v_smfmac_f32_16x16x64_bf16 v[228:231], v[224:227], v[64:71], v17\n"
"ds_read_b128 v[108:111], v6 offset:19456\n"
"v_exp_f32 v32, v32\n"
"v_exp_f32 v33, v33\n"
"v_mfma_f32_32x32x16_bf16 v[56:71], v[144:147], v[244:247], v[208:223]\n"
"s_mov_b64 s[32:33], s[4:5]\n"
"v_exp_f32 v40, v40\n"
"v_exp_f32 v41, v41\n"
"v_exp_f32 v42, v42\n"
"v_mfma_f32_32x32x16_bf16 v[56:71], v[148:151], v[248:251], v[56:71]\n"
"s_mov_b32 s34, 4194304\n"
"v_exp_f32 v43, v43\n"
"v_exp_f32 v44, v44\n"
"v_exp_f32 v45, v45\n"
"v_smfmac_f32_32x32x32_bf16 v[168:183], v[112:115], v[18:25], v17\n"
"s_add_u32 s57, s67, 8192\n"
"v_exp_f32 v46, v46\n"
"v_exp_f32 v47, v47\n"
"v_exp_f32 v48, v48\n"
"v_smfmac_f32_16x16x64_bf16 v[232:235], v[224:227], v[18:25], v17\n"
"ds_read_b128 v[144:147], v6 offset:24576\n"
"v_exp_f32 v49, v49\n"
"v_exp_f32 v50, v50\n"
"v_smfmac_f32_32x32x32_bf16 v[168:183], v[116:119], v[26:33], v17\n"
"s_add_u32 s59, s67, 65536\n"
"v_exp_f32 v51, v51\n"
"v_exp_f32 v52, v52\n"
"v_exp_f32 v53, v53\n"
"v_smfmac_f32_16x16x64_bf16 v[232:235], v[224:227], v[26:33], v17\n"
"ds_read_b128 v[148:151], v6 offset:25600\n"
"v_exp_f32 v54, v54\n"
"v_exp_f32 v55, v55\n"
"v_mfma_f32_32x32x16_bf16 v[18:33], v[128:131], v[236:239], v[192:207]\n"
"s_add_u32 s60, s67, 73728\n"
"v_exp_f32 v56, v56\n"
"v_exp_f32 v57, v57\n"
"v_exp_f32 v58, v58\n"
"v_mfma_f32_32x32x16_bf16 v[18:33], v[132:135], v[240:243], v[18:33]\n"
"s_waitcnt lgkmcnt(2)\n"
"v_exp_f32 v59, v59\n"
"v_exp_f32 v60, v60\n"
"v_exp_f32 v61, v61\n"
"v_smfmac_f32_32x32x32_bf16 v[152:167], v[120:123], v[40:47], v17\n"
"buffer_load_dwordx4 v[72:75], v16, s[32:35], s67 offen\n"
"v_exp_f32 v62, v62\n"
"v_exp_f32 v63, v63\n"
"v_exp_f32 v64, v64\n"
"v_smfmac_f32_16x16x64_bf16 v[228:231], v[224:227], v[40:47], v17\n"
"ds_read_b128 v[112:115], v6 offset:22528\n"
"v_exp_f32 v65, v65\n"
"v_exp_f32 v66, v66\n"
"v_smfmac_f32_32x32x32_bf16 v[152:167], v[124:127], v[48:55], v17\n"
"buffer_load_dwordx4 v[76:79], v16, s[32:35], s57 offen\n"
"v_exp_f32 v67, v67\n"
"v_exp_f32 v68, v68\n"
"v_exp_f32 v69, v69\n"
"v_smfmac_f32_16x16x64_bf16 v[228:231], v[224:227], v[48:55], v17\n"
"ds_read_b128 v[116:119], v6 offset:23552\n"
"v_exp_f32 v70, v70\n"
"v_exp_f32 v71, v71\n"
"v_mfma_f32_32x32x16_bf16 v[40:55], v[128:131], v[244:247], v[208:223]\n"
"v_exp_f32 v18, v18\n"
"v_exp_f32 v19, v19\n"
"v_exp_f32 v20, v20\n"
"v_mfma_f32_32x32x16_bf16 v[40:55], v[132:135], v[248:251], v[40:55]\n"
"v_exp_f32 v21, v21\n"
"v_exp_f32 v22, v22\n"
"v_exp_f32 v23, v23\n"
"v_smfmac_f32_32x32x32_bf16 v[168:183], v[120:123], v[56:63], v17\n"
"buffer_load_dwordx4 v[80:83], v16, s[32:35], s59 offen\n"
"v_exp_f32 v24, v24\n"
"v_exp_f32 v25, v25\n"
"v_exp_f32 v26, v26\n"
"v_smfmac_f32_16x16x64_bf16 v[232:235], v[224:227], v[56:63], v17\n"
"ds_read_b128 v[128:131], v6 offset:28672\n"
"v_exp_f32 v27, v27\n"
"v_exp_f32 v28, v28\n"
"v_smfmac_f32_32x32x32_bf16 v[168:183], v[124:127], v[64:71], v17\n"
"buffer_load_dwordx4 v[84:87], v16, s[32:35], s60 offen\n"
"v_exp_f32 v29, v29\n"
"v_exp_f32 v30, v30\n"
"v_exp_f32 v31, v31\n"
"v_smfmac_f32_16x16x64_bf16 v[232:235], v[224:227], v[64:71], v17\n"
"ds_read_b128 v[132:135], v6 offset:29696\n"
"v_exp_f32 v32, v32\n"
"v_exp_f32 v33, v33\n"
"v_mfma_f32_32x32x16_bf16 v[56:71], v[136:139], v[236:239], v[192:207]\n"
"s_mov_b32 s82, 73728\n"
"v_exp_f32 v40, v40\n"
"v_exp_f32 v41, v41\n"
"v_exp_f32 v42, v42\n"
"v_mfma_f32_32x32x16_bf16 v[56:71], v[140:143], v[240:243], v[56:71]\n"
"s_waitcnt lgkmcnt(2)\n"
"v_exp_f32 v43, v43\n"
"v_exp_f32 v44, v44\n"
"v_exp_f32 v45, v45\n"
"v_smfmac_f32_32x32x32_bf16 v[152:167], v[104:107], v[18:25], v17\n"
"buffer_load_dwordx4 v[34:37], v2, s[28:31], s82 offen sc1\n"
"v_exp_f32 v46, v46\n"
"v_exp_f32 v47, v47\n"
"v_exp_f32 v48, v48\n"
"v_smfmac_f32_16x16x64_bf16 v[228:231], v[224:227], v[18:25], v17\n"
"ds_read_b128 v[120:123], v6 offset:26624\n"
"v_exp_f32 v49, v49\n"
"v_exp_f32 v50, v50\n"
"v_smfmac_f32_32x32x32_bf16 v[152:167], v[108:111], v[26:33], v17\n"
"v_exp_f32 v51, v51\n"
"v_exp_f32 v52, v52\n"
"v_exp_f32 v53, v53\n"
"v_smfmac_f32_16x16x64_bf16 v[228:231], v[224:227], v[26:33], v17\n"
"ds_read_b128 v[124:127], v6 offset:27648\n"
"v_exp_f32 v54, v54\n"
"v_exp_f32 v55, v55\n"
"v_mfma_f32_32x32x16_bf16 v[18:33], v[136:139], v[244:247], v[208:223]\n"
"s_waitcnt vmcnt(21)\n"
"s_waitcnt vmcnt(13)\n"
"v_exp_f32 v56, v56\n"
"v_exp_f32 v57, v57\n"
"v_exp_f32 v58, v58\n"
"v_mfma_f32_32x32x16_bf16 v[18:33], v[140:143], v[248:251], v[18:33]\n"
"s_barrier\n"
"v_exp_f32 v59, v59\n"
"v_exp_f32 v60, v60\n"
"v_exp_f32 v61, v61\n"
"v_smfmac_f32_32x32x32_bf16 v[168:183], v[104:107], v[40:47], v17\n"
"s_add_u32 s81, s81, 0x8000\n"
"v_exp_f32 v62, v62\n"
"v_exp_f32 v63, v63\n"
"v_exp_f32 v64, v64\n"
"v_smfmac_f32_16x16x64_bf16 v[232:235], v[224:227], v[40:47], v17\n"
"ds_read_b128 v[136:139], v6 offset:32768\n"
"v_exp_f32 v65, v65\n"
"v_exp_f32 v66, v66\n"
"v_smfmac_f32_32x32x32_bf16 v[168:183], v[108:111], v[48:55], v17\n"
"s_and_b32 s81, s81, 0x3ffff\n"
"v_exp_f32 v67, v67\n"
"v_exp_f32 v68, v68\n"
"v_exp_f32 v69, v69\n"
"v_smfmac_f32_16x16x64_bf16 v[232:235], v[224:227], v[48:55], v17\n"
"ds_read_b128 v[140:143], v6 offset:33792\n"
"v_exp_f32 v70, v70\n"
"v_exp_f32 v71, v71\n"
"v_mfma_f32_32x32x16_bf16 v[40:55], v[144:147], v[236:239], v[192:207]\n"
"s_add_u32 s80, s81, 524288\n"
"v_exp_f32 v18, v18\n"
"v_exp_f32 v19, v19\n"
"v_exp_f32 v20, v20\n"
"v_mfma_f32_32x32x16_bf16 v[40:55], v[148:151], v[240:243], v[40:55]\n"
"s_waitcnt lgkmcnt(2)\n"
"v_exp_f32 v21, v21\n"
"v_exp_f32 v22, v22\n"
"v_exp_f32 v23, v23\n"
"v_smfmac_f32_32x32x32_bf16 v[152:167], v[112:115], v[56:63], v17\n"
"s_add_u32 m0, s53, 98304\n"
"v_exp_f32 v24, v24\n"
"v_exp_f32 v25, v25\n"
"v_exp_f32 v26, v26\n"
"v_smfmac_f32_16x16x64_bf16 v[228:231], v[224:227], v[56:63], v17\n"
"ds_read_b128 v[104:107], v6 offset:30720\n"
"v_exp_f32 v27, v27\n"
"v_exp_f32 v28, v28\n"
"v_smfmac_f32_32x32x32_bf16 v[152:167], v[116:119], v[64:71], v17\n"
"buffer_load_dwordx4 v2, s[28:31], s80 offen offset:0 sc1 lds\n"
"v_exp_f32 v29, v29\n"
"v_exp_f32 v30, v30\n"
"v_exp_f32 v31, v31\n"
"v_smfmac_f32_16x16x64_bf16 v[228:231], v[224:227], v[64:71], v17\n"
"ds_read_b128 v[108:111], v6 offset:31744\n"
"v_exp_f32 v32, v32\n"
"v_exp_f32 v33, v33\n"
"v_mfma_f32_32x32x16_bf16 v[56:71], v[144:147], v[244:247], v[208:223]\n"
"v_exp_f32 v40, v40\n"
"v_exp_f32 v41, v41\n"
"v_exp_f32 v42, v42\n"
"v_mfma_f32_32x32x16_bf16 v[56:71], v[148:151], v[248:251], v[56:71]\n"
"buffer_load_dwordx4 v2, s[28:31], s80 offen offset:1024 sc1 lds\n"
"v_exp_f32 v43, v43\n"
"v_exp_f32 v44, v44\n"
"v_exp_f32 v45, v45\n"
"v_smfmac_f32_32x32x32_bf16 v[168:183], v[112:115], v[18:25], v17\n"
"buffer_load_dwordx4 v2, s[28:31], s80 offen offset:2048 sc1 lds\n"
"v_exp_f32 v46, v46\n"
"v_exp_f32 v47, v47\n"
"v_exp_f32 v48, v48\n"
"v_smfmac_f32_16x16x64_bf16 v[232:235], v[224:227], v[18:25], v17\n"
"ds_read_b128 v[144:147], v6 offset:36864\n"
"v_exp_f32 v49, v49\n"
"v_exp_f32 v50, v50\n"
"v_smfmac_f32_32x32x32_bf16 v[168:183], v[116:119], v[26:33], v17\n"
"buffer_load_dwordx4 v2, s[28:31], s80 offen offset:3072 sc1 lds\n"
"v_exp_f32 v51, v51\n"
"v_exp_f32 v52, v52\n"
"v_exp_f32 v53, v53\n"
"v_smfmac_f32_16x16x64_bf16 v[232:235], v[224:227], v[26:33], v17\n"
"ds_read_b128 v[148:151], v6 offset:37888\n"
"v_exp_f32 v54, v54\n"
"v_exp_f32 v55, v55\n"
"v_mfma_f32_32x32x16_bf16 v[18:33], v[128:131], v[236:239], v[192:207]\n"
"s_add_u32 s80, s80, 0x4000\n"
"v_exp_f32 v56, v56\n"
"v_exp_f32 v57, v57\n"
"v_exp_f32 v58, v58\n"
"v_mfma_f32_32x32x16_bf16 v[18:33], v[132:135], v[240:243], v[18:33]\n"
"s_waitcnt lgkmcnt(2)\n"
"v_exp_f32 v59, v59\n"
"v_exp_f32 v60, v60\n"
"v_exp_f32 v61, v61\n"
"v_smfmac_f32_32x32x32_bf16 v[152:167], v[120:123], v[40:47], v17\n"
"s_add_u32 m0, s53, 114688\n"
"v_exp_f32 v62, v62\n"
"v_exp_f32 v63, v63\n"
"v_exp_f32 v64, v64\n"
"v_smfmac_f32_16x16x64_bf16 v[228:231], v[224:227], v[40:47], v17\n"
"ds_read_b128 v[112:115], v6 offset:34816\n"
"v_exp_f32 v65, v65\n"
"v_exp_f32 v66, v66\n"
"v_smfmac_f32_32x32x32_bf16 v[152:167], v[124:127], v[48:55], v17\n"
"buffer_load_dwordx4 v2, s[28:31], s80 offen offset:0 sc1 lds\n"
"v_exp_f32 v67, v67\n"
"v_exp_f32 v68, v68\n"
"v_exp_f32 v69, v69\n"
"v_smfmac_f32_16x16x64_bf16 v[228:231], v[224:227], v[48:55], v17\n"
"ds_read_b128 v[116:119], v6 offset:35840\n"
"v_exp_f32 v70, v70\n"
"v_exp_f32 v71, v71\n"
"v_mfma_f32_32x32x16_bf16 v[40:55], v[128:131], v[244:247], v[208:223]\n"
"buffer_load_dwordx4 v2, s[28:31], s80 offen offset:1024 sc1 lds\n"
"v_exp_f32 v18, v18\n"
"v_exp_f32 v19, v19\n"
"v_exp_f32 v20, v20\n"
"v_mfma_f32_32x32x16_bf16 v[40:55], v[132:135], v[248:251], v[40:55]\n"
"buffer_load_dwordx4 v2, s[28:31], s80 offen offset:2048 sc1 lds\n"
"v_exp_f32 v21, v21\n"
"v_exp_f32 v22, v22\n"
"v_exp_f32 v23, v23\n"
"v_smfmac_f32_32x32x32_bf16 v[168:183], v[120:123], v[56:63], v17\n"
"buffer_load_dwordx4 v2, s[28:31], s80 offen offset:3072 sc1 lds\n"
"v_exp_f32 v24, v24\n"
"v_exp_f32 v25, v25\n"
"v_exp_f32 v26, v26\n"
"v_smfmac_f32_16x16x64_bf16 v[232:235], v[224:227], v[56:63], v17\n"
"ds_read_b128 v[128:131], v6 offset:40960\n"
"s_waitcnt vmcnt(8)\n"
"v_bitop3_b32 v34, v34, v35, s26 bitop3:0x7e\n"
"v_exp_f32 v27, v27\n"
"v_exp_f32 v28, v28\n"
"v_smfmac_f32_32x32x32_bf16 v[168:183], v[124:127], v[64:71], v17\n"
"v_bitop3_b32 v36, v36, v37, s26 bitop3:0x7e\n"
"v_exp_f32 v29, v29\n"
"v_exp_f32 v30, v30\n"
"v_exp_f32 v31, v31\n"
"v_smfmac_f32_16x16x64_bf16 v[232:235], v[224:227], v[64:71], v17\n"
"ds_read_b128 v[132:135], v6 offset:41984\n"
"v_or_b32 v34, v34, v36\n"
"v_exp_f32 v32, v32\n"
"v_exp_f32 v33, v33\n"
"v_mfma_f32_32x32x16_bf16 v[56:71], v[136:139], v[236:239], v[192:207]\n"
"v_cmp_ne_u32 vcc, 0, v34\n"
"s_nop 0\n"
"s_cbranch_vccnz .Lmk_fspin_13\n"
".Lmk_fback_14:\n"
"v_exp_f32 v40, v40\n"
"v_exp_f32 v41, v41\n"
"v_exp_f32 v42, v42\n"
"v_mfma_f32_32x32x16_bf16 v[56:71], v[140:143], v[240:243], v[56:71]\n"
"s_waitcnt lgkmcnt(2)\n"
"v_exp_f32 v43, v43\n"
"v_exp_f32 v44, v44\n"
"v_exp_f32 v45, v45\n"
"v_smfmac_f32_32x32x32_bf16 v[152:167], v[104:107], v[18:25], v17\n"
"s_cmp_lg_u32 s41, 0\n"
"s_cbranch_scc1 .Lmk_fl_15\n"
"s_lshl_b32 s58, s27, 2\n"
"s_add_u32 s58, s58, 77824\n"
"v_mov_b32 v11, s26\n"
"s_mov_b64 exec, 1\n"
"buffer_store_dword v11, v9, s[28:31], s58 offen sc1\n"
"s_mov_b64 exec, -1\n"
"s_nop 1\n"
".Lmk_fl_15:\n"
"v_exp_f32 v46, v46\n"
"v_exp_f32 v47, v47\n"
"v_exp_f32 v48, v48\n"
"v_smfmac_f32_16x16x64_bf16 v[228:231], v[224:227], v[18:25], v17\n"
"ds_read_b128 v[120:123], v6 offset:38912\n"
"v_exp_f32 v49, v49\n"
"v_exp_f32 v50, v50\n"
"v_smfmac_f32_32x32x32_bf16 v[152:167], v[108:111], v[26:33], v17\n"
"s_mov_b64 s[32:33], s[20:21]\n"
"v_exp_f32 v51, v51\n"
"v_exp_f32 v52, v52\n"
"v_exp_f32 v53, v53\n"
"v_smfmac_f32_16x16x64_bf16 v[228:231], v[224:227], v[26:33], v17\n"
"ds_read_b128 v[124:127], v6 offset:39936\n"
"v_exp_f32 v54, v54\n"
"v_exp_f32 v55, v55\n"
"v_mfma_f32_32x32x16_bf16 v[18:33], v[136:139], v[244:247], v[208:223]\n"
"s_mov_b32 s34, 3670016\n"
"v_exp_f32 v56, v56\n"
"v_exp_f32 v57, v57\n"
"v_exp_f32 v58, v58\n"
"v_mfma_f32_32x32x16_bf16 v[18:33], v[140:143], v[248:251], v[18:33]\n"
"s_add_u32 m0, s71, 131072\n"
"v_exp_f32 v59, v59\n"
"v_exp_f32 v60, v60\n"
"v_exp_f32 v61, v61\n"
"v_smfmac_f32_32x32x32_bf16 v[168:183], v[104:107], v[40:47], v17\n"
"s_add_u32 s57, s90, 15360\n"
"v_exp_f32 v62, v62\n"
"v_exp_f32 v63, v63\n"
"v_exp_f32 v64, v64\n"
"v_smfmac_f32_16x16x64_bf16 v[232:235], v[224:227], v[40:47], v17\n"
"ds_read_b128 v[136:139], v6 offset:45056\n"
"v_exp_f32 v65, v65\n"
"v_exp_f32 v66, v66\n"
"v_smfmac_f32_32x32x32_bf16 v[168:183], v[108:111], v[48:55], v17\n"
"s_add_u32 s59, s57, s91\n"
"v_exp_f32 v67, v67\n"
"v_exp_f32 v68, v68\n"
"v_exp_f32 v69, v69\n"
"v_smfmac_f32_16x16x64_bf16 v[232:235], v[224:227], v[48:55], v17\n"
"ds_read_b128 v[140:143], v6 offset:46080\n"
"v_exp_f32 v70, v70\n"
"v_exp_f32 v71, v71\n"
"v_mfma_f32_32x32x16_bf16 v[40:55], v[144:147], v[236:239], v[192:207]\n"
"s_add_u32 s60, s59, 15360\n"
"v_exp_f32 v18, v18\n"
"v_exp_f32 v19, v19\n"
"v_exp_f32 v20, v20\n"
"v_mfma_f32_32x32x16_bf16 v[40:55], v[148:151], v[240:243], v[40:55]\n"
"s_waitcnt lgkmcnt(2)\n"
"v_exp_f32 v21, v21\n"
"v_exp_f32 v22, v22\n"
"v_exp_f32 v23, v23\n"
"v_smfmac_f32_32x32x32_bf16 v[152:167], v[112:115], v[56:63], v17\n"
"buffer_load_dwordx4 v190, s[32:35], s90 offen offset:0 lds\n"
"v_exp_f32 v24, v24\n"
"v_exp_f32 v25, v25\n"
"v_exp_f32 v26, v26\n"
"v_smfmac_f32_16x16x64_bf16 v[228:231], v[224:227], v[56:63], v17\n"
"ds_read_b128 v[104:107], v6 offset:43008\n"
"v_exp_f32 v27, v27\n"
"v_exp_f32 v28, v28\n"
"v_smfmac_f32_32x32x32_bf16 v[152:167], v[116:119], v[64:71], v17\n"
"buffer_load_dwordx4 v190, s[32:35], s57 offen offset:1024 lds\n"
"v_exp_f32 v29, v29\n"
"v_exp_f32 v30, v30\n"
"v_exp_f32 v31, v31\n"
"v_smfmac_f32_16x16x64_bf16 v[228:231], v[224:227], v[64:71], v17\n"
"ds_read_b128 v[108:111], v6 offset:44032\n"
"v_exp_f32 v32, v32\n"
"v_exp_f32 v33, v33\n"
"v_mfma_f32_32x32x16_bf16 v[56:71], v[144:147], v[244:247], v[208:223]\n"
"buffer_load_dwordx4 v190, s[32:35], s59 offen offset:2048 lds\n"
"v_exp_f32 v40, v40\n"
"v_exp_f32 v41, v41\n"
"v_exp_f32 v42, v42\n"
"v_mfma_f32_32x32x16_bf16 v[56:71], v[148:151], v[248:251], v[56:71]\n"
"buffer_load_dwordx4 v190, s[32:35], s60 offen offset:3072 lds\n"
"v_exp_f32 v43, v43\n"
"v_exp_f32 v44, v44\n"
"v_exp_f32 v45, v45\n"
"v_smfmac_f32_32x32x32_bf16 v[168:183], v[112:115], v[18:25], v17\n"
"v_exp_f32 v46, v46\n"
"v_exp_f32 v47, v47\n"
"v_exp_f32 v48, v48\n"
"v_smfmac_f32_16x16x64_bf16 v[232:235], v[224:227], v[18:25], v17\n"
"ds_read_b128 v[144:147], v6 offset:49152\n"
"v_exp_f32 v49, v49\n"
"v_exp_f32 v50, v50\n"
"v_smfmac_f32_32x32x32_bf16 v[168:183], v[116:119], v[26:33], v17\n"
"v_exp_f32 v51, v51\n"
"v_exp_f32 v52, v52\n"
"v_exp_f32 v53, v53\n"
"v_smfmac_f32_16x16x64_bf16 v[232:235], v[224:227], v[26:33], v17\n"
"ds_read_b128 v[148:151], v6 offset:50176\n"
"v_exp_f32 v54, v54\n"
"v_exp_f32 v55, v55\n"
"v_mfma_f32_32x32x16_bf16 v[18:33], v[128:131], v[236:239], v[192:207]\n"
".Lmk_pg_6:\n"
"s_cbranch_execz .Lmk_ret_1\n"
"v_exp_f32 v56, v56\n"
"v_exp_f32 v57, v57\n"
"v_exp_f32 v58, v58\n"
"v_mfma_f32_32x32x16_bf16 v[18:33], v[132:135], v[240:243], v[18:33]\n"
"s_waitcnt lgkmcnt(2)\n"
"v_exp_f32 v59, v59\n"
"v_exp_f32 v60, v60\n"
"v_exp_f32 v61, v61\n"
"v_smfmac_f32_32x32x32_bf16 v[152:167], v[120:123], v[40:47], v17\n"
"ds_read_b128 a[240:243], v187 offset:4096\n"
"ds_read_b128 a[244:247], v187 offset:4608\n"
"v_exp_f32 v62, v62\n"
"v_exp_f32 v63, v63\n"
"v_exp_f32 v64, v64\n"
"v_smfmac_f32_16x16x64_bf16 v[228:231], v[224:227], v[40:47], v17\n"
"ds_read_b128 v[112:115], v6 offset:47104\n"
"v_exp_f32 v65, v65\n"
"v_exp_f32 v66, v66\n"
"v_smfmac_f32_32x32x32_bf16 v[152:167], v[124:127], v[48:55], v17\n"
"v_exp_f32 v67, v67\n"
"v_exp_f32 v68, v68\n"
"v_exp_f32 v69, v69\n"
"v_smfmac_f32_16x16x64_bf16 v[228:231], v[224:227], v[48:55], v17\n"
"ds_read_b128 v[116:119], v6 offset:48128\n"
"v_exp_f32 v70, v70\n"
"v_exp_f32 v71, v71\n"
"v_mfma_f32_32x32x16_bf16 v[40:55], v[128:131], v[244:247], v[208:223]\n"
"v_exp_f32 v18, v18\n"
"v_exp_f32 v19, v19\n"
"v_exp_f32 v20, v20\n"
"v_mfma_f32_32x32x16_bf16 v[40:55], v[132:135], v[248:251], v[40:55]\n"
"v_exp_f32 v21, v21\n"
"v_exp_f32 v22, v22\n"
"v_exp_f32 v23, v23\n"
"v_smfmac_f32_32x32x32_bf16 v[168:183], v[120:123], v[56:63], v17\n"
"ds_read_b128 a[248:251], v187 offset:4160\n"
"ds_read_b128 a[252:255], v187 offset:4672\n"
"v_exp_f32 v24, v24\n"
"v_exp_f32 v25, v25\n"
"v_exp_f32 v26, v26\n"
"v_smfmac_f32_16x16x64_bf16 v[232:235], v[224:227], v[56:63], v17\n"
"ds_read_b128 v[128:131], v6 offset:53248\n"
"v_exp_f32 v27, v27\n"
"v_exp_f32 v28, v28\n"
"v_smfmac_f32_32x32x32_bf16 v[168:183], v[124:127], v[64:71], v17\n"
"v_exp_f32 v29, v29\n"
"v_exp_f32 v30, v30\n"
"v_exp_f32 v31, v31\n"
"v_smfmac_f32_16x16x64_bf16 v[232:235], v[224:227], v[64:71], v17\n"
"ds_read_b128 v[132:135], v6 offset:54272\n"
"v_exp_f32 v32, v32\n"
"v_exp_f32 v33, v33\n"
"v_mfma_f32_32x32x16_bf16 v[56:71], v[136:139], v[236:239], v[192:207]\n"
"s_waitcnt lgkmcnt(2)\n"
"v_exp_f32 v40, v40\n"
"v_exp_f32 v41, v41\n"
"v_exp_f32 v42, v42\n"
"v_mfma_f32_32x32x16_bf16 v[56:71], v[140:143], v[240:243], v[56:71]\n"
"v_exp_f32 v43, v43\n"
"v_exp_f32 v44, v44\n"
"v_exp_f32 v45, v45\n"
"v_smfmac_f32_32x32x32_bf16 v[152:167], v[104:107], v[18:25], v17\n"
"v_exp_f32 v46, v46\n"
"v_exp_f32 v47, v47\n"
"v_exp_f32 v48, v48\n"
"v_smfmac_f32_16x16x64_bf16 v[228:231], v[224:227], v[18:25], v17\n"
"ds_read_b128 v[120:123], v6 offset:51200\n"
"v_exp_f32 v49, v49\n"
"v_exp_f32 v50, v50\n"
"v_smfmac_f32_32x32x32_bf16 v[152:167], v[108:111], v[26:33], v17\n"
"v_exp_f32 v51, v51\n"
"v_exp_f32 v52, v52\n"
"v_exp_f32 v53, v53\n"
"v_smfmac_f32_16x16x64_bf16 v[228:231], v[224:227], v[26:33], v17\n"
"ds_read_b128 v[124:127], v6 offset:52224\n"
"v_exp_f32 v54, v54\n"
"v_exp_f32 v55, v55\n"
"v_mfma_f32_32x32x16_bf16 v[18:33], v[136:139], v[244:247], v[208:223]\n"
"v_exp_f32 v56, v56\n"
"v_exp_f32 v57, v57\n"
"v_exp_f32 v58, v58\n"
"v_mfma_f32_32x32x16_bf16 v[18:33], v[140:143], v[248:251], v[18:33]\n"
"v_exp_f32 v59, v59\n"
"v_exp_f32 v60, v60\n"
"v_exp_f32 v61, v61\n"
"v_smfmac_f32_32x32x32_bf16 v[168:183], v[104:107], v[40:47], v17\n"
"buffer_store_dwordx4 a[240:243], v3, s[28:31], s25 offen offset:0 sc1\n"
"v_exp_f32 v62, v62\n"
"v_exp_f32 v63, v63\n"
"v_exp_f32 v64, v64\n"
"v_smfmac_f32_16x16x64_bf16 v[232:235], v[224:227], v[40:47], v17\n"
"ds_read_b128 v[136:139], v6 offset:57344\n"
"v_exp_f32 v65, v65\n"
"v_exp_f32 v66, v66\n"
"v_smfmac_f32_32x32x32_bf16 v[168:183], v[108:111], v[48:55], v17\n"
"buffer_store_dwordx4 a[244:247], v3, s[28:31], s25 offen offset:1024 sc1\n"
"v_exp_f32 v67, v67\n"
"v_exp_f32 v68, v68\n"
"v_exp_f32 v69, v69\n"
"v_smfmac_f32_16x16x64_bf16 v[232:235], v[224:227], v[48:55], v17\n"
"ds_read_b128 v[140:143], v6 offset:58368\n"
"v_exp_f32 v70, v70\n"
"v_exp_f32 v71, v71\n"
"v_mfma_f32_32x32x16_bf16 v[40:55], v[144:147], v[236:239], v[192:207]\n"
"v_exp_f32 v18, v18\n"
"v_exp_f32 v19, v19\n"
"v_exp_f32 v20, v20\n"
"v_mfma_f32_32x32x16_bf16 v[40:55], v[148:151], v[240:243], v[40:55]\n"
"s_waitcnt lgkmcnt(2)\n"
"v_exp_f32 v21, v21\n"
"v_exp_f32 v22, v22\n"
"v_exp_f32 v23, v23\n"
"v_smfmac_f32_32x32x32_bf16 v[152:167], v[112:115], v[56:63], v17\n"
"buffer_store_dwordx4 a[248:251], v3, s[28:31], s25 offen offset:2048 sc1\n"
"v_exp_f32 v24, v24\n"
"v_exp_f32 v25, v25\n"
"v_exp_f32 v26, v26\n"
"v_smfmac_f32_16x16x64_bf16 v[228:231], v[224:227], v[56:63], v17\n"
"ds_read_b128 v[104:107], v6 offset:55296\n"
"v_exp_f32 v27, v27\n"
"v_exp_f32 v28, v28\n"
"v_smfmac_f32_32x32x32_bf16 v[152:167], v[116:119], v[64:71], v17\n"
"v_exp_f32 v29, v29\n"
"v_exp_f32 v30, v30\n"
"v_exp_f32 v31, v31\n"
"v_smfmac_f32_16x16x64_bf16 v[228:231], v[224:227], v[64:71], v17\n"
"ds_read_b128 v[108:111], v6 offset:56320\n"
"v_exp_f32 v32, v32\n"
"v_exp_f32 v33, v33\n"
"v_mfma_f32_32x32x16_bf16 v[56:71], v[144:147], v[244:247], v[208:223]\n"
"v_exp_f32 v40, v40\n"
"v_exp_f32 v41, v41\n"
"v_exp_f32 v42, v42\n"
"v_mfma_f32_32x32x16_bf16 v[56:71], v[148:151], v[248:251], v[56:71]\n"
"v_exp_f32 v43, v43\n"
"v_exp_f32 v44, v44\n"
"v_exp_f32 v45, v45\n"
"v_smfmac_f32_32x32x32_bf16 v[168:183], v[112:115], v[18:25], v17\n"
"buffer_store_dwordx4 a[252:255], v3, s[28:31], s25 offen offset:3072 sc1\n"
"v_exp_f32 v46, v46\n"
"v_exp_f32 v47, v47\n"
"v_exp_f32 v48, v48\n"
"v_smfmac_f32_16x16x64_bf16 v[232:235], v[224:227], v[18:25], v17\n"
"ds_read_b128 v[144:147], v6 offset:61440\n"
"v_exp_f32 v49, v49\n"
"v_exp_f32 v50, v50\n"
"v_smfmac_f32_32x32x32_bf16 v[168:183], v[116:119], v[26:33], v17\n"
"v_exp_f32 v51, v51\n"
"v_exp_f32 v52, v52\n"
"v_exp_f32 v53, v53\n"
"v_smfmac_f32_16x16x64_bf16 v[232:235], v[224:227], v[26:33], v17\n"
"ds_read_b128 v[148:151], v6 offset:62464\n"
"v_exp_f32 v54, v54\n"
"v_exp_f32 v55, v55\n"
"v_mfma_f32_32x32x16_bf16 v[18:33], v[128:131], v[236:239], v[192:207]\n"
"v_exp_f32 v56, v56\n"
"v_exp_f32 v57, v57\n"
"v_exp_f32 v58, v58\n"
"v_mfma_f32_32x32x16_bf16 v[18:33], v[132:135], v[240:243], v[18:33]\n"
"s_waitcnt lgkmcnt(2)\n"
"v_exp_f32 v59, v59\n"
"v_exp_f32 v60, v60\n"
"v_exp_f32 v61, v61\n"
"v_smfmac_f32_32x32x32_bf16 v[152:167], v[120:123], v[40:47], v17\n"
"v_exp_f32 v62, v62\n"
"v_exp_f32 v63, v63\n"
"v_exp_f32 v64, v64\n"
"v_smfmac_f32_16x16x64_bf16 v[228:231], v[224:227], v[40:47], v17\n"
"ds_read_b128 v[112:115], v6 offset:59392\n"
"v_exp_f32 v65, v65\n"
"v_exp_f32 v66, v66\n"
"v_smfmac_f32_32x32x32_bf16 v[152:167], v[124:127], v[48:55], v17\n"
"v_exp_f32 v67, v67\n"
"v_exp_f32 v68, v68\n"
"v_exp_f32 v69, v69\n"
"v_smfmac_f32_16x16x64_bf16 v[228:231], v[224:227], v[48:55], v17\n"
"ds_read_b128 v[116:119], v6 offset:60416\n"
"v_exp_f32 v70, v70\n"
"v_exp_f32 v71, v71\n"
"v_mfma_f32_32x32x16_bf16 v[40:55], v[128:131], v[244:247], v[208:223]\n"
"s_waitcnt vmcnt(8)\n"
"v_exp_f32 v18, v18\n"
"v_exp_f32 v19, v19\n"
"v_exp_f32 v20, v20\n"
"v_mfma_f32_32x32x16_bf16 v[40:55], v[132:135], v[248:251], v[40:55]\n"
"s_barrier\n"
"v_exp_f32 v21, v21\n"
"v_exp_f32 v22, v22\n"
"v_exp_f32 v23, v23\n"
"v_smfmac_f32_32x32x32_bf16 v[168:183], v[120:123], v[56:63], v17\n"
"s_add_u32 s81, s81, 0x8000\n"
"v_exp_f32 v24, v24\n"
"v_exp_f32 v25, v25\n"
"v_exp_f32 v26, v26\n"
"v_smfmac_f32_16x16x64_bf16 v[232:235], v[224:227], v[56:63], v17\n"
"ds_read_b128 v[128:131], v7 offset:32768\n"
"v_exp_f32 v27, v27\n"
"v_exp_f32 v28, v28\n"
"v_smfmac_f32_32x32x32_bf16 v[168:183], v[124:127], v[64:71], v17\n"
"s_and_b32 s81, s81, 0x3ffff\n"
"v_exp_f32 v29, v29\n"
"v_exp_f32 v30, v30\n"
"v_exp_f32 v31, v31\n"
"v_smfmac_f32_16x16x64_bf16 v[232:235], v[224:227], v[64:71], v17\n"
"ds_read_b128 v[132:135], v7 offset:33792\n"
"v_exp_f32 v32, v32\n"
"v_exp_f32 v33, v33\n"
"v_mfma_f32_32x32x16_bf16 v[56:71], v[136:139], v[236:239], v[192:207]\n"
"s_add_u32 s80, s81, 524288\n"
"v_exp_f32 v40, v40\n"
"v_exp_f32 v41, v41\n"
"v_exp_f32 v42, v42\n"
"v_mfma_f32_32x32x16_bf16 v[56:71], v[140:143], v[240:243], v[56:71]\n"
"s_waitcnt lgkmcnt(2)\n"
"v_exp_f32 v43, v43\n"
"v_exp_f32 v44, v44\n"
"v_exp_f32 v45, v45\n"
"v_smfmac_f32_32x32x32_bf16 v[152:167], v[104:107], v[18:25], v17\n"
"s_add_u32 m0, s53, 0\n"
"v_exp_f32 v46, v46\n"
"v_exp_f32 v47, v47\n"
"v_exp_f32 v48, v48\n"
"v_smfmac_f32_16x16x64_bf16 v[228:231], v[224:227], v[18:25], v17\n"
"ds_read_b128 v[120:123], v6 offset:63488\n"
"v_exp_f32 v49, v49\n"
"v_exp_f32 v50, v50\n"
"v_smfmac_f32_32x32x32_bf16 v[152:167], v[108:111], v[26:33], v17\n"
"buffer_load_dwordx4 v2, s[28:31], s80 offen offset:0 sc1 lds\n"
"v_exp_f32 v51, v51\n"
"v_exp_f32 v52, v52\n"
"v_exp_f32 v53, v53\n"
"v_smfmac_f32_16x16x64_bf16 v[228:231], v[224:227], v[26:33], v17\n"
"ds_read_b128 v[124:127], v6 offset:64512\n"
"v_exp_f32 v54, v54\n"
"v_exp_f32 v55, v55\n"
"v_mfma_f32_32x32x16_bf16 v[18:33], v[136:139], v[244:247], v[208:223]\n"
"v_exp_f32 v56, v56\n"
"v_exp_f32 v57, v57\n"
"v_exp_f32 v58, v58\n"
"v_mfma_f32_32x32x16_bf16 v[18:33], v[140:143], v[248:251], v[18:33]\n"
"buffer_load_dwordx4 v2, s[28:31], s80 offen offset:1024 sc1 lds\n"
"v_exp_f32 v59, v59\n"
"v_exp_f32 v60, v60\n"
"v_exp_f32 v61, v61\n"
"v_smfmac_f32_32x32x32_bf16 v[168:183], v[104:107], v[40:47], v17\n"
"buffer_load_dwordx4 v2, s[28:31], s80 offen offset:2048 sc1 lds\n"
"v_exp_f32 v62, v62\n"
"v_exp_f32 v63, v63\n"
"v_exp_f32 v64, v64\n"
"v_smfmac_f32_16x16x64_bf16 v[232:235], v[224:227], v[40:47], v17\n"
"ds_read_b128 v[136:139], v7 offset:36864\n"
"v_exp_f32 v65, v65\n"
"v_exp_f32 v66, v66\n"
"v_smfmac_f32_32x32x32_bf16 v[168:183], v[108:111], v[48:55], v17\n"
"buffer_load_dwordx4 v2, s[28:31], s80 offen offset:3072 sc1 lds\n"
"v_exp_f32 v67, v67\n"
"v_exp_f32 v68, v68\n"
"v_exp_f32 v69, v69\n"
"v_smfmac_f32_16x16x64_bf16 v[232:235], v[224:227], v[48:55], v17\n"
"ds_read_b128 v[140:143], v7 offset:37888\n"
"v_exp_f32 v70, v70\n"
"v_exp_f32 v71, v71\n"
"v_mfma_f32_32x32x16_bf16 v[40:55], v[144:147], v[236:239], v[192:207]\n"
"s_add_u32 s80, s80, 0x4000\n"
"v_exp_f32 v18, v18\n"
"v_exp_f32 v19, v19\n"
"v_exp_f32 v20, v20\n"
"v_mfma_f32_32x32x16_bf16 v[40:55], v[148:151], v[240:243], v[40:55]\n"
"s_waitcnt lgkmcnt(2)\n"
"v_exp_f32 v21, v21\n"
"v_exp_f32 v22, v22\n"
"v_exp_f32 v23, v23\n"
"v_smfmac_f32_32x32x32_bf16 v[152:167], v[112:115], v[56:63], v17\n"
"s_add_u32 m0, s53, 16384\n"
"v_exp_f32 v24, v24\n"
"v_exp_f32 v25, v25\n"
"v_exp_f32 v26, v26\n"
"v_smfmac_f32_16x16x64_bf16 v[228:231], v[224:227], v[56:63], v17\n"
"ds_read_b128 v[104:107], v7 offset:34816\n"
"v_exp_f32 v27, v27\n"
"v_exp_f32 v28, v28\n"
"v_smfmac_f32_32x32x32_bf16 v[152:167], v[116:119], v[64:71], v17\n"
"buffer_load_dwordx4 v2, s[28:31], s80 offen offset:0 sc1 lds\n"
"v_exp_f32 v29, v29\n"
"v_exp_f32 v30, v30\n"
"v_exp_f32 v31, v31\n"
"v_smfmac_f32_16x16x64_bf16 v[228:231], v[224:227], v[64:71], v17\n"
"ds_read_b128 v[108:111], v7 offset:35840\n"
"v_exp_f32 v32, v32\n"
"v_exp_f32 v33, v33\n"
"v_mfma_f32_32x32x16_bf16 v[56:71], v[144:147], v[244:247], v[208:223]\n"
"buffer_load_dwordx4 v2, s[28:31], s80 offen offset:1024 sc1 lds\n"
"v_exp_f32 v40, v40\n"
"v_exp_f32 v41, v41\n"
"v_exp_f32 v42, v42\n"
"v_mfma_f32_32x32x16_bf16 v[56:71], v[148:151], v[248:251], v[56:71]\n"
"buffer_load_dwordx4 v2, s[28:31], s80 offen offset:2048 sc1 lds\n"
"v_exp_f32 v43, v43\n"
"v_exp_f32 v44, v44\n"
"v_exp_f32 v45, v45\n"
"v_smfmac_f32_32x32x32_bf16 v[168:183], v[112:115], v[18:25], v17\n"
"buffer_load_dwordx4 v2, s[28:31], s80 offen offset:3072 sc1 lds\n"
"v_exp_f32 v46, v46\n"
"v_exp_f32 v47, v47\n"
"v_exp_f32 v48, v48\n"
"v_smfmac_f32_16x16x64_bf16 v[232:235], v[224:227], v[18:25], v17\n"
"ds_read_b128 v[144:147], v7 offset:40960\n"
"v_exp_f32 v49, v49\n"
"v_exp_f32 v50, v50\n"
"v_smfmac_f32_32x32x32_bf16 v[168:183], v[116:119], v[26:33], v17\n"
"s_bfe_u32 s64, s68, 262144\n"
"v_exp_f32 v51, v51\n"
"v_exp_f32 v52, v52\n"
"v_exp_f32 v53, v53\n"
"v_smfmac_f32_16x16x64_bf16 v[232:235], v[224:227], v[26:33], v17\n"
"ds_read_b128 v[148:151], v7 offset:41984\n"
"v_exp_f32 v54, v54\n"
"v_exp_f32 v55, v55\n"
"v_mfma_f32_32x32x16_bf16 v[18:33], v[128:131], v[236:239], v[192:207]\n"
"s_mul_i32 s64, s64, 8192\n"
"v_exp_f32 v56, v56\n"
"v_exp_f32 v57, v57\n"
"v_exp_f32 v58, v58\n"
"v_mfma_f32_32x32x16_bf16 v[18:33], v[132:135], v[240:243], v[18:33]\n"
"s_waitcnt lgkmcnt(2)\n"
"v_exp_f32 v59, v59\n"
"v_exp_f32 v60, v60\n"
"v_exp_f32 v61, v61\n"
"v_smfmac_f32_32x32x32_bf16 v[152:167], v[120:123], v[40:47], v17\n"
"s_add_u32 s64, s64, s66\n"
"v_exp_f32 v62, v62\n"
"v_exp_f32 v63, v63\n"
"v_exp_f32 v64, v64\n"
"v_smfmac_f32_16x16x64_bf16 v[228:231], v[224:227], v[40:47], v17\n"
"ds_read_b128 v[112:115], v7 offset:38912\n"
"v_exp_f32 v65, v65\n"
"v_exp_f32 v66, v66\n"
"v_smfmac_f32_32x32x32_bf16 v[152:167], v[124:127], v[48:55], v17\n"
"buffer_load_dwordx4 a[0:3], v2, s[28:31], s64 offen offset:0 sc1\n"
"v_exp_f32 v67, v67\n"
"v_exp_f32 v68, v68\n"
"v_exp_f32 v69, v69\n"
"v_smfmac_f32_16x16x64_bf16 v[228:231], v[224:227], v[48:55], v17\n"
"ds_read_b128 v[116:119], v7 offset:39936\n"
"v_exp_f32 v70, v70\n"
"v_exp_f32 v71, v71\n"
"v_mfma_f32_32x32x16_bf16 v[40:55], v[128:131], v[244:247], v[208:223]\n"
"v_exp_f32 v18, v18\n"
"v_exp_f32 v19, v19\n"
"v_exp_f32 v20, v20\n"
"v_mfma_f32_32x32x16_bf16 v[40:55], v[132:135], v[248:251], v[40:55]\n"
"v_exp_f32 v21, v21\n"
"v_exp_f32 v22, v22\n"
"v_exp_f32 v23, v23\n"
"v_smfmac_f32_32x32x32_bf16 v[168:183], v[120:123], v[56:63], v17\n"
"buffer_load_dwordx4 a[4:7], v2, s[28:31], s64 offen offset:1024 sc1\n"
"v_exp_f32 v24, v24\n"
"v_exp_f32 v25, v25\n"
"v_exp_f32 v26, v26\n"
"v_smfmac_f32_16x16x64_bf16 v[232:235], v[224:227], v[56:63], v17\n"
"ds_read_b128 v[128:131], v7 offset:45056\n"
"v_exp_f32 v27, v27\n"
"v_exp_f32 v28, v28\n"
"v_smfmac_f32_32x32x32_bf16 v[168:183], v[124:127], v[64:71], v17\n"
"buffer_load_dwordx4 a[8:11], v2, s[28:31], s64 offen offset:2048 sc1\n"
"v_exp_f32 v29, v29\n"
"v_exp_f32 v30, v30\n"
"v_exp_f32 v31, v31\n"
"v_smfmac_f32_16x16x64_bf16 v[232:235], v[224:227], v[64:71], v17\n"
"ds_read_b128 v[132:135], v7 offset:46080\n"
"v_exp_f32 v32, v32\n"
"v_exp_f32 v33, v33\n"
"v_mfma_f32_32x32x16_bf16 v[56:71], v[136:139], v[236:239], v[192:207]\n"
"buffer_load_dwordx4 a[12:15], v2, s[28:31], s64 offen offset:3072 sc1\n"
"v_exp_f32 v40, v40\n"
"v_exp_f32 v41, v41\n"
"v_exp_f32 v42, v42\n"
"v_mfma_f32_32x32x16_bf16 v[56:71], v[140:143], v[240:243], v[56:71]\n"
"s_waitcnt lgkmcnt(2)\n"
"v_exp_f32 v43, v43\n"
"v_exp_f32 v44, v44\n"
"v_exp_f32 v45, v45\n"
"v_smfmac_f32_32x32x32_bf16 v[152:167], v[104:107], v[18:25], v17\n"
"buffer_load_dwordx4 a[16:19], v3, s[28:31], s64 offen offset:0 sc1\n"
"v_exp_f32 v46, v46\n"
"v_exp_f32 v47, v47\n"
"v_exp_f32 v48, v48\n"
"v_smfmac_f32_16x16x64_bf16 v[228:231], v[224:227], v[18:25], v17\n"
"ds_read_b128 v[120:123], v7 offset:43008\n"
"v_exp_f32 v49, v49\n"
"v_exp_f32 v50, v50\n"
"v_smfmac_f32_32x32x32_bf16 v[152:167], v[108:111], v[26:33], v17\n"
"buffer_load_dwordx4 a[20:23], v3, s[28:31], s64 offen offset:1024 sc1\n"
"v_exp_f32 v51, v51\n"
"v_exp_f32 v52, v52\n"
"v_exp_f32 v53, v53\n"
"v_smfmac_f32_16x16x64_bf16 v[228:231], v[224:227], v[26:33], v17\n"
"ds_read_b128 v[124:127], v7 offset:44032\n"
"v_exp_f32 v54, v54\n"
"v_exp_f32 v55, v55\n"
"v_mfma_f32_32x32x16_bf16 v[18:33], v[136:139], v[244:247], v[208:223]\n"
"buffer_load_dwordx4 a[24:27], v3, s[28:31], s64 offen offset:2048 sc1\n"
"v_exp_f32 v56, v56\n"
"v_exp_f32 v57, v57\n"
"v_exp_f32 v58, v58\n"
"v_mfma_f32_32x32x16_bf16 v[18:33], v[140:143], v[248:251], v[18:33]\n"
"v_exp_f32 v59, v59\n"
"v_exp_f32 v60, v60\n"
"v_exp_f32 v61, v61\n"
"v_smfmac_f32_32x32x32_bf16 v[168:183], v[104:107], v[40:47], v17\n"
"buffer_load_dwordx4 a[28:31], v3, s[28:31], s64 offen offset:3072 sc1\n"
"v_exp_f32 v62, v62\n"
"v_exp_f32 v63, v63\n"
"v_exp_f32 v64, v64\n"
"v_smfmac_f32_16x16x64_bf16 v[232:235], v[224:227], v[40:47], v17\n"
"ds_read_b128 v[136:139], v7 offset:49152\n"
"v_exp_f32 v65, v65\n"
"v_exp_f32 v66, v66\n"
"v_smfmac_f32_32x32x32_bf16 v[168:183], v[108:111], v[48:55], v17\n"
"s_bfe_u32 s64, s68, 262148\n"
"v_exp_f32 v67, v67\n"
"v_exp_f32 v68, v68\n"
"v_exp_f32 v69, v69\n"
"v_smfmac_f32_16x16x64_bf16 v[232:235], v[224:227], v[48:55], v17\n"
"ds_read_b128 v[140:143], v7 offset:50176\n"
"v_exp_f32 v70, v70\n"
"v_exp_f32 v71, v71\n"
"v_mfma_f32_32x32x16_bf16 v[40:55], v[144:147], v[236:239], v[192:207]\n"
"s_mul_i32 s64, s64, 8192\n"
"v_exp_f32 v18, v18\n"
"v_exp_f32 v19, v19\n"
"v_exp_f32 v20, v20\n"
"v_mfma_f32_32x32x16_bf16 v[40:55], v[148:151], v[240:243], v[40:55]\n"
"s_waitcnt lgkmcnt(2)\n"
"v_exp_f32 v21, v21\n"
"v_exp_f32 v22, v22\n"
"v_exp_f32 v23, v23\n"
"v_smfmac_f32_32x32x32_bf16 v[152:167], v[112:115], v[56:63], v17\n"
"s_add_u32 s64, s64, s66\n"
"v_exp_f32 v24, v24\n"
"v_exp_f32 v25, v25\n"
"v_exp_f32 v26, v26\n"
"v_smfmac_f32_16x16x64_bf16 v[228:231], v[224:227], v[56:63], v17\n"
"ds_read_b128 v[104:107], v7 offset:47104\n"
"v_exp_f32 v27, v27\n"
"v_exp_f32 v28, v28\n"
"v_smfmac_f32_32x32x32_bf16 v[152:167], v[116:119], v[64:71], v17\n"
"buffer_load_dwordx4 a[32:35], v2, s[28:31], s64 offen offset:0 sc1\n"
"v_exp_f32 v29, v29\n"
"v_exp_f32 v30, v30\n"
"v_exp_f32 v31, v31\n"
"v_smfmac_f32_16x16x64_bf16 v[228:231], v[224:227], v[64:71], v17\n"
"ds_read_b128 v[108:111], v7 offset:48128\n"
"v_exp_f32 v32, v32\n"
"v_exp_f32 v33, v33\n"
"v_mfma_f32_32x32x16_bf16 v[56:71], v[144:147], v[244:247], v[208:223]\n"
"buffer_load_dwordx4 a[36:39], v2, s[28:31], s64 offen offset:1024 sc1\n"
"v_exp_f32 v40, v40\n"
"v_exp_f32 v41, v41\n"
"v_exp_f32 v42, v42\n"
"v_mfma_f32_32x32x16_bf16 v[56:71], v[148:151], v[248:251], v[56:71]\n"
"v_exp_f32 v43, v43\n"
"v_exp_f32 v44, v44\n"
"v_exp_f32 v45, v45\n"
"v_smfmac_f32_32x32x32_bf16 v[168:183], v[112:115], v[18:25], v17\n"
"buffer_load_dwordx4 a[40:43], v2, s[28:31], s64 offen offset:2048 sc1\n"
"v_exp_f32 v46, v46\n"
"v_exp_f32 v47, v47\n"
"v_exp_f32 v48, v48\n"
"v_smfmac_f32_16x16x64_bf16 v[232:235], v[224:227], v[18:25], v17\n"
"ds_read_b128 v[144:147], v7 offset:53248\n"
"v_exp_f32 v49, v49\n"
"v_exp_f32 v50, v50\n"
"v_smfmac_f32_32x32x32_bf16 v[168:183], v[116:119], v[26:33], v17\n"
"buffer_load_dwordx4 a[44:47], v2, s[28:31], s64 offen offset:3072 sc1\n"
"v_exp_f32 v51, v51\n"
"v_exp_f32 v52, v52\n"
"v_exp_f32 v53, v53\n"
"v_smfmac_f32_16x16x64_bf16 v[232:235], v[224:227], v[26:33], v17\n"
"ds_read_b128 v[148:151], v7 offset:54272\n"
"v_exp_f32 v54, v54\n"
"v_exp_f32 v55, v55\n"
"v_mfma_f32_32x32x16_bf16 v[18:33], v[128:131], v[236:239], v[192:207]\n"
"buffer_load_dwordx4 a[48:51], v3, s[28:31], s64 offen offset:0 sc1\n"
"v_exp_f32 v56, v56\n"
"v_exp_f32 v57, v57\n"
"v_exp_f32 v58, v58\n"
"v_mfma_f32_32x32x16_bf16 v[18:33], v[132:135], v[240:243], v[18:33]\n"
"s_waitcnt lgkmcnt(2)\n"
"v_exp_f32 v59, v59\n"
"v_exp_f32 v60, v60\n"
"v_exp_f32 v61, v61\n"
"v_smfmac_f32_32x32x32_bf16 v[152:167], v[120:123], v[40:47], v17\n"
"buffer_load_dwordx4 a[52:55], v3, s[28:31], s64 offen offset:1024 sc1\n"
"v_exp_f32 v62, v62\n"
"v_exp_f32 v63, v63\n"
"v_exp_f32 v64, v64\n"
"v_smfmac_f32_16x16x64_bf16 v[228:231], v[224:227], v[40:47], v17\n"
"ds_read_b128 v[112:115], v7 offset:51200\n"
"v_exp_f32 v65, v65\n"
"v_exp_f32 v66, v66\n"
"v_smfmac_f32_32x32x32_bf16 v[152:167], v[124:127], v[48:55], v17\n"
"buffer_load_dwordx4 a[56:59], v3, s[28:31], s64 offen offset:2048 sc1\n"
"v_exp_f32 v67, v67\n"
"v_exp_f32 v68, v68\n"
"v_exp_f32 v69, v69\n"
"v_smfmac_f32_16x16x64_bf16 v[228:231], v[224:227], v[48:55], v17\n"
"ds_read_b128 v[116:119], v7 offset:52224\n"
"v_exp_f32 v70, v70\n"
"v_exp_f32 v71, v71\n"
"v_mfma_f32_32x32x16_bf16 v[40:55], v[128:131], v[244:247], v[208:223]\n"
"buffer_load_dwordx4 a[60:63], v3, s[28:31], s64 offen offset:3072 sc1\n"
"v_exp_f32 v18, v18\n"
"v_exp_f32 v19, v19\n"
"v_exp_f32 v20, v20\n"
"v_mfma_f32_32x32x16_bf16 v[40:55], v[132:135], v[248:251], v[40:55]\n"
"s_waitcnt vmcnt(28)\n"
"v_exp_f32 v21, v21\n"
"v_exp_f32 v22, v22\n"
"v_exp_f32 v23, v23\n"
"v_smfmac_f32_32x32x32_bf16 v[168:183], v[120:123], v[56:63], v17\n"
"ds_read_b64_tr_b16 a[240:241], v186 offset:0\n"
"ds_read_b64_tr_b16 a[242:243], v186 offset:2048\n"
"v_exp_f32 v24, v24\n"
"v_exp_f32 v25, v25\n"
"v_exp_f32 v26, v26\n"
"v_smfmac_f32_16x16x64_bf16 v[232:235], v[224:227], v[56:63], v17\n"
"ds_read_b128 v[128:131], v7 offset:57344\n"
"v_exp_f32 v27, v27\n"
"v_exp_f32 v28, v28\n"
"v_smfmac_f32_32x32x32_bf16 v[168:183], v[124:127], v[64:71], v17\n"
"ds_read_b64_tr_b16 a[244:245], v186 offset:8\n"
"ds_read_b64_tr_b16 a[246:247], v186 offset:2056\n"
"v_exp_f32 v29, v29\n"
"v_exp_f32 v30, v30\n"
"v_exp_f32 v31, v31\n"
"v_smfmac_f32_16x16x64_bf16 v[232:235], v[224:227], v[64:71], v17\n"
"ds_read_b128 v[132:135], v7 offset:58368\n"
"v_exp_f32 v32, v32\n"
"v_exp_f32 v33, v33\n"
"v_mfma_f32_32x32x16_bf16 v[56:71], v[136:139], v[236:239], v[192:207]\n"
"v_exp_f32 v40, v40\n"
"v_exp_f32 v41, v41\n"
"v_exp_f32 v42, v42\n"
"v_mfma_f32_32x32x16_bf16 v[56:71], v[140:143], v[240:243], v[56:71]\n"
"s_waitcnt lgkmcnt(6)\n"
"v_exp_f32 v43, v43\n"
"v_exp_f32 v44, v44\n"
"v_exp_f32 v45, v45\n"
"v_smfmac_f32_32x32x32_bf16 v[152:167], v[104:107], v[18:25], v17\n"
"ds_read_b64_tr_b16 a[248:249], v186 offset:64\n"
"ds_read_b64_tr_b16 a[250:251], v186 offset:2112\n"
"v_exp_f32 v46, v46\n"
"v_exp_f32 v47, v47\n"
"v_exp_f32 v48, v48\n"
"v_smfmac_f32_16x16x64_bf16 v[228:231], v[224:227], v[18:25], v17\n"
"ds_read_b128 v[120:123], v7 offset:55296\n"
"v_exp_f32 v49, v49\n"
"v_exp_f32 v50, v50\n"
"v_smfmac_f32_32x32x32_bf16 v[152:167], v[108:111], v[26:33], v17\n"
"ds_read_b64_tr_b16 a[252:253], v186 offset:72\n"
"ds_read_b64_tr_b16 a[254:255], v186 offset:2120\n"
"v_exp_f32 v51, v51\n"
"v_exp_f32 v52, v52\n"
"v_exp_f32 v53, v53\n"
"v_smfmac_f32_16x16x64_bf16 v[228:231], v[224:227], v[26:33], v17\n"
"ds_read_b128 v[124:127], v7 offset:56320\n"
"v_exp_f32 v54, v54\n"
"v_exp_f32 v55, v55\n"
"v_mfma_f32_32x32x16_bf16 v[18:33], v[136:139], v[244:247], v[208:223]\n"
"s_add_u32 s57, s24, 114688\n"
"v_exp_f32 v56, v56\n"
"v_exp_f32 v57, v57\n"
"v_exp_f32 v58, v58\n"
"v_mfma_f32_32x32x16_bf16 v[18:33], v[140:143], v[248:251], v[18:33]\n"
"s_waitcnt lgkmcnt(1)\n"
"v_exp_f32 v59, v59\n"
"v_exp_f32 v60, v60\n"
"v_exp_f32 v61, v61\n"
"v_smfmac_f32_32x32x32_bf16 v[168:183], v[104:107], v[40:47], v17\n"
"buffer_store_dwordx4 a[240:243], v2, s[28:31], s24 offen offset:0 sc1\n"
"v_exp_f32 v62, v62\n"
"v_exp_f32 v63, v63\n"
"v_exp_f32 v64, v64\n"
"v_smfmac_f32_16x16x64_bf16 v[232:235], v[224:227], v[40:47], v17\n"
"ds_read_b128 v[136:139], v7 offset:61440\n"
"v_exp_f32 v65, v65\n"
"v_exp_f32 v66, v66\n"
"v_smfmac_f32_32x32x32_bf16 v[168:183], v[108:111], v[48:55], v17\n"
"buffer_store_dwordx4 a[244:247], v2, s[28:31], s24 offen offset:1024 sc1\n"
"v_exp_f32 v67, v67\n"
"v_exp_f32 v68, v68\n"
"v_exp_f32 v69, v69\n"
"v_smfmac_f32_16x16x64_bf16 v[232:235], v[224:227], v[48:55], v17\n"
"ds_read_b128 v[140:143], v7 offset:62464\n"
"v_exp_f32 v70, v70\n"
"v_exp_f32 v71, v71\n"
"v_mfma_f32_32x32x16_bf16 v[40:55], v[144:147], v[236:239], v[192:207]\n"
"v_exp_f32 v18, v18\n"
"v_exp_f32 v19, v19\n"
"v_exp_f32 v20, v20\n"
"v_mfma_f32_32x32x16_bf16 v[40:55], v[148:151], v[240:243], v[40:55]\n"
"s_waitcnt lgkmcnt(2)\n"
"v_exp_f32 v21, v21\n"
"v_exp_f32 v22, v22\n"
"v_exp_f32 v23, v23\n"
"v_smfmac_f32_32x32x32_bf16 v[152:167], v[112:115], v[56:63], v17\n"
"buffer_store_dwordx4 a[248:251], v2, s[28:31], s57 offen offset:0 sc1\n"
"v_exp_f32 v24, v24\n"
"v_exp_f32 v25, v25\n"
"v_exp_f32 v26, v26\n"
"v_smfmac_f32_16x16x64_bf16 v[228:231], v[224:227], v[56:63], v17\n"
"ds_read_b128 v[104:107], v7 offset:59392\n"
"v_exp_f32 v27, v27\n"
"v_exp_f32 v28, v28\n"
"v_smfmac_f32_32x32x32_bf16 v[152:167], v[116:119], v[64:71], v17\n"
"buffer_store_dwordx4 a[252:255], v2, s[28:31], s57 offen offset:1024 sc1\n"
"v_exp_f32 v29, v29\n"
"v_exp_f32 v30, v30\n"
"v_exp_f32 v31, v31\n"
"v_smfmac_f32_16x16x64_bf16 v[228:231], v[224:227], v[64:71], v17\n"
"ds_read_b128 v[108:111], v7 offset:60416\n"
"v_exp_f32 v32, v32\n"
"v_exp_f32 v33, v33\n"
"v_mfma_f32_32x32x16_bf16 v[56:71], v[144:147], v[244:247], v[208:223]\n"
"s_waitcnt vmcnt(20)\n"
"v_exp_f32 v40, v40\n"
"v_exp_f32 v41, v41\n"
"v_exp_f32 v42, v42\n"
"v_mfma_f32_32x32x16_bf16 v[56:71], v[148:151], v[248:251], v[56:71]\n"
"s_barrier\n"
"v_exp_f32 v43, v43\n"
"v_exp_f32 v44, v44\n"
"v_exp_f32 v45, v45\n"
"v_smfmac_f32_32x32x32_bf16 v[168:183], v[112:115], v[18:25], v17\n"
"s_bfe_u32 s64, s68, 262152\n"
"v_exp_f32 v46, v46\n"
"v_exp_f32 v47, v47\n"
"v_exp_f32 v48, v48\n"
"v_smfmac_f32_16x16x64_bf16 v[232:235], v[224:227], v[18:25], v17\n"
"ds_read_b128 v[144:147], v6 offset:0\n"
"v_exp_f32 v49, v49\n"
"v_exp_f32 v50, v50\n"
"v_smfmac_f32_32x32x32_bf16 v[168:183], v[116:119], v[26:33], v17\n"
"s_mul_i32 s64, s64, 8192\n"
"v_exp_f32 v51, v51\n"
"v_exp_f32 v52, v52\n"
"v_exp_f32 v53, v53\n"
"v_smfmac_f32_16x16x64_bf16 v[232:235], v[224:227], v[26:33], v17\n"
"ds_read_b128 v[148:151], v6 offset:1024\n"
"v_exp_f32 v54, v54\n"
"v_exp_f32 v55, v55\n"
"v_mfma_f32_32x32x16_bf16 v[18:33], v[128:131], v[236:239], v[192:207]\n"
"s_add_u32 s64, s64, s66\n"
"v_exp_f32 v56, v56\n"
"v_exp_f32 v57, v57\n"
"v_exp_f32 v58, v58\n"
"v_mfma_f32_32x32x16_bf16 v[18:33], v[132:135], v[240:243], v[18:33]\n"
"s_waitcnt lgkmcnt(2)\n"
"v_exp_f32 v59, v59\n"
"v_exp_f32 v60, v60\n"
"v_exp_f32 v61, v61\n"
"v_smfmac_f32_32x32x32_bf16 v[152:167], v[120:123], v[40:47], v17\n"
"buffer_load_dwordx4 a[64:67], v2, s[28:31], s64 offen offset:0 sc1\n"
"v_exp_f32 v62, v62\n"
"v_exp_f32 v63, v63\n"
"v_exp_f32 v64, v64\n"
"v_smfmac_f32_16x16x64_bf16 v[228:231], v[224:227], v[40:47], v17\n"
"ds_read_b128 v[112:115], v7 offset:63488\n"
"v_exp_f32 v65, v65\n"
"v_exp_f32 v66, v66\n"
"v_smfmac_f32_32x32x32_bf16 v[152:167], v[124:127], v[48:55], v17\n"
"buffer_load_dwordx4 a[68:71], v2, s[28:31], s64 offen offset:1024 sc1\n"
"v_exp_f32 v67, v67\n"
"v_exp_f32 v68, v68\n"
"v_exp_f32 v69, v69\n"
"v_smfmac_f32_16x16x64_bf16 v[228:231], v[224:227], v[48:55], v17\n"
"ds_read_b128 v[116:119], v7 offset:64512\n"
"v_exp_f32 v70, v70\n"
"v_exp_f32 v71, v71\n"
"v_mfma_f32_32x32x16_bf16 v[40:55], v[128:131], v[244:247], v[208:223]\n"
"v_exp_f32 v18, v18\n"
"v_exp_f32 v19, v19\n"
"v_exp_f32 v20, v20\n"
"v_mfma_f32_32x32x16_bf16 v[40:55], v[132:135], v[248:251], v[40:55]\n"
"v_exp_f32 v21, v21\n"
"v_exp_f32 v22, v22\n"
"v_exp_f32 v23, v23\n"
"v_smfmac_f32_32x32x32_bf16 v[168:183], v[120:123], v[56:63], v17\n"
"buffer_load_dwordx4 a[72:75], v2, s[28:31], s64 offen offset:2048 sc1\n"
"v_exp_f32 v24, v24\n"
"v_exp_f32 v25, v25\n"
"v_exp_f32 v26, v26\n"
"v_smfmac_f32_16x16x64_bf16 v[232:235], v[224:227], v[56:63], v17\n"
"ds_read_b128 v[128:131], v6 offset:4096\n"
"v_exp_f32 v27, v27\n"
"v_exp_f32 v28, v28\n"
"v_smfmac_f32_32x32x32_bf16 v[168:183], v[124:127], v[64:71], v17\n"
"buffer_load_dwordx4 a[76:79], v2, s[28:31], s64 offen offset:3072 sc1\n"
"v_exp_f32 v29, v29\n"
"v_exp_f32 v30, v30\n"
"v_exp_f32 v31, v31\n"
"v_smfmac_f32_16x16x64_bf16 v[232:235], v[224:227], v[64:71], v17\n"
"ds_read_b128 v[132:135], v6 offset:5120\n"
"v_exp_f32 v32, v32\n"
"v_exp_f32 v33, v33\n"
"v_mfma_f32_32x32x16_bf16 v[56:71], v[136:139], v[236:239], v[192:207]\n"
"buffer_load_dwordx4 a[80:83], v3, s[28:31], s64 offen offset:0 sc1\n"
"v_exp_f32 v40, v40\n"
"v_exp_f32 v41, v41\n"
"v_exp_f32 v42, v42\n"
"v_mfma_f32_32x32x16_bf16 v[56:71], v[140:143], v[240:243], v[56:71]\n"
"s_waitcnt lgkmcnt(2)\n"
"v_exp_f32 v43, v43\n"
"v_exp_f32 v44, v44\n"
"v_exp_f32 v45, v45\n"
"v_smfmac_f32_32x32x32_bf16 v[152:167], v[104:107], v[18:25], v17\n"
"buffer_load_dwordx4 a[84:87], v3, s[28:31], s64 offen offset:1024 sc1\n"
"v_exp_f32 v46, v46\n"
"v_exp_f32 v47, v47\n"
"v_exp_f32 v48, v48\n"
"v_smfmac_f32_16x16x64_bf16 v[228:231], v[224:227], v[18:25], v17\n"
"ds_read_b128 v[120:123], v6 offset:2048\n"
"v_exp_f32 v49, v49\n"
"v_exp_f32 v50, v50\n"
"v_smfmac_f32_32x32x32_bf16 v[152:167], v[108:111], v[26:33], v17\n"
"buffer_load_dwordx4 a[88:91], v3, s[28:31], s64 offen offset:2048 sc1\n"
"v_exp_f32 v51, v51\n"
"v_exp_f32 v52, v52\n"
"v_exp_f32 v53, v53\n"
"v_smfmac_f32_16x16x64_bf16 v[228:231], v[224:227], v[26:33], v17\n"
"ds_read_b128 v[124:127], v6 offset:3072\n"
"v_exp_f32 v54, v54\n"
"v_exp_f32 v55, v55\n"
"v_mfma_f32_32x32x16_bf16 v[18:33], v[136:139], v[244:247], v[208:223]\n"
"buffer_load_dwordx4 a[92:95], v3, s[28:31], s64 offen offset:3072 sc1\n"
"v_exp_f32 v56, v56\n"
"v_exp_f32 v57, v57\n"
"v_exp_f32 v58, v58\n"
"v_mfma_f32_32x32x16_bf16 v[18:33], v[140:143], v[248:251], v[18:33]\n"
"s_bfe_u32 s64, s68, 262156\n"
".Lmk_pg_7:\n"
"s_cbranch_execz .Lmk_ret_1\n"
"v_exp_f32 v59, v59\n"
"v_exp_f32 v60, v60\n"
"v_exp_f32 v61, v61\n"
"v_smfmac_f32_32x32x32_bf16 v[168:183], v[104:107], v[40:47], v17\n"
"s_mul_i32 s64, s64, 8192\n"
"v_exp_f32 v62, v62\n"
"v_exp_f32 v63, v63\n"
"v_exp_f32 v64, v64\n"
"v_smfmac_f32_16x16x64_bf16 v[232:235], v[224:227], v[40:47], v17\n"
"ds_read_b128 v[136:139], v6 offset:8192\n"
"v_exp_f32 v65, v65\n"
"v_exp_f32 v66, v66\n"
"v_smfmac_f32_32x32x32_bf16 v[168:183], v[108:111], v[48:55], v17\n"
"s_add_u32 s64, s64, s66\n"
"v_exp_f32 v67, v67\n"
"v_exp_f32 v68, v68\n"
"v_exp_f32 v69, v69\n"
"v_smfmac_f32_16x16x64_bf16 v[232:235], v[224:227], v[48:55], v17\n"
"ds_read_b128 v[140:143], v6 offset:9216\n"
"v_exp_f32 v70, v70\n"
"v_exp_f32 v71, v71\n"
"v_mfma_f32_32x32x16_bf16 v[40:55], v[144:147], v[236:239], v[192:207]\n"
"buffer_load_dwordx4 a[96:99], v2, s[28:31], s64 offen offset:0 sc1\n"
"v_exp_f32 v18, v18\n"
"v_exp_f32 v19, v19\n"
"v_exp_f32 v20, v20\n"
"v_mfma_f32_32x32x16_bf16 v[40:55], v[148:151], v[240:243], v[40:55]\n"
"s_waitcnt lgkmcnt(2)\n"
"v_exp_f32 v21, v21\n"
"v_exp_f32 v22, v22\n"
"v_exp_f32 v23, v23\n"
"v_smfmac_f32_32x32x32_bf16 v[152:167], v[112:115], v[56:63], v17\n"
"buffer_load_dwordx4 a[100:103], v2, s[28:31], s64 offen offset:1024 sc1\n"
"v_exp_f32 v24, v24\n"
"v_exp_f32 v25, v25\n"
"v_exp_f32 v26, v26\n"
"v_smfmac_f32_16x16x64_bf16 v[228:231], v[224:227], v[56:63], v17\n"
"ds_read_b128 v[104:107], v6 offset:6144\n"
"v_exp_f32 v27, v27\n"
"v_exp_f32 v28, v28\n"
"v_smfmac_f32_32x32x32_bf16 v[152:167], v[116:119], v[64:71], v17\n"
"buffer_load_dwordx4 a[104:107], v2, s[28:31], s64 offen offset:2048 sc1\n"
"v_exp_f32 v29, v29\n"
"v_exp_f32 v30, v30\n"
"v_exp_f32 v31, v31\n"
"v_smfmac_f32_16x16x64_bf16 v[228:231], v[224:227], v[64:71], v17\n"
"ds_read_b128 v[108:111], v6 offset:7168\n"
"v_exp_f32 v32, v32\n"
"v_exp_f32 v33, v33\n"
"v_mfma_f32_32x32x16_bf16 v[56:71], v[144:147], v[244:247], v[208:223]\n"
"buffer_load_dwordx4 a[108:111], v2, s[28:31], s64 offen offset:3072 sc1\n"
"v_exp_f32 v40, v40\n"
"v_exp_f32 v41, v41\n"
"v_exp_f32 v42, v42\n"
"v_mfma_f32_32x32x16_bf16 v[56:71], v[148:151], v[248:251], v[56:71]\n"
"buffer_load_dwordx4 a[112:115], v3, s[28:31], s64 offen offset:0 sc1\n"
"v_exp_f32 v43, v43\n"
"v_exp_f32 v44, v44\n"
"v_exp_f32 v45, v45\n"
"v_smfmac_f32_32x32x32_bf16 v[168:183], v[112:115], v[18:25], v17\n"
"buffer_load_dwordx4 a[116:119], v3, s[28:31], s64 offen offset:1024 sc1\n"
"v_exp_f32 v46, v46\n"
"v_exp_f32 v47, v47\n"
"v_exp_f32 v48, v48\n"
"v_smfmac_f32_16x16x64_bf16 v[232:235], v[224:227], v[18:25], v17\n"
"ds_read_b128 v[144:147], v6 offset:12288\n"
"v_exp_f32 v49, v49\n"
"v_exp_f32 v50, v50\n"
"v_smfmac_f32_32x32x32_bf16 v[168:183], v[116:119], v[26:33], v17\n"
"buffer_load_dwordx4 a[120:123], v3, s[28:31], s64 offen offset:2048 sc1\n"
"v_exp_f32 v51, v51\n"
"v_exp_f32 v52, v52\n"
"v_exp_f32 v53, v53\n"
"v_smfmac_f32_16x16x64_bf16 v[232:235], v[224:227], v[26:33], v17\n"
"ds_read_b128 v[148:151], v6 offset:13312\n"
"v_exp_f32 v54, v54\n"
"v_exp_f32 v55, v55\n"
"v_mfma_f32_32x32x16_bf16 v[18:33], v[128:131], v[236:239], v[192:207]\n"
"buffer_load_dwordx4 a[124:127], v3, s[28:31], s64 offen offset:3072 sc1\n"
"v_exp_f32 v56, v56\n"
"v_exp_f32 v57, v57\n"
"v_exp_f32 v58, v58\n"
"v_mfma_f32_32x32x16_bf16 v[18:33], v[132:135], v[240:243], v[18:33]\n"
"s_waitcnt lgkmcnt(2)\n"
"v_exp_f32 v59, v59\n"
"v_exp_f32 v60, v60\n"
"v_exp_f32 v61, v61\n"
"v_smfmac_f32_32x32x32_bf16 v[152:167], v[120:123], v[40:47], v17\n"
"s_bfe_u32 s64, s68, 262160\n"
"v_exp_f32 v62, v62\n"
"v_exp_f32 v63, v63\n"
"v_exp_f32 v64, v64\n"
"v_smfmac_f32_16x16x64_bf16 v[228:231], v[224:227], v[40:47], v17\n"
"ds_read_b128 v[112:115], v6 offset:10240\n"
"v_exp_f32 v65, v65\n"
"v_exp_f32 v66, v66\n"
"v_smfmac_f32_32x32x32_bf16 v[152:167], v[124:127], v[48:55], v17\n"
"s_mul_i32 s64, s64, 8192\n"
"v_exp_f32 v67, v67\n"
"v_exp_f32 v68, v68\n"
"v_exp_f32 v69, v69\n"
"v_smfmac_f32_16x16x64_bf16 v[228:231], v[224:227], v[48:55], v17\n"
"ds_read_b128 v[116:119], v6 offset:11264\n"
"v_exp_f32 v70, v70\n"
"v_exp_f32 v71, v71\n"
"v_mfma_f32_32x32x16_bf16 v[40:55], v[128:131], v[244:247], v[208:223]\n"
"s_add_u32 s64, s64, s66\n"
"v_exp_f32 v18, v18\n"
"v_exp_f32 v19, v19\n"
"v_exp_f32 v20, v20\n"
"v_mfma_f32_32x32x16_bf16 v[40:55], v[132:135], v[248:251], v[40:55]\n"
"buffer_load_dwordx4 a[128:131], v2, s[28:31], s64 offen offset:0 sc1\n"
"v_exp_f32 v21, v21\n"
"v_exp_f32 v22, v22\n"
"v_exp_f32 v23, v23\n"
"v_smfmac_f32_32x32x32_bf16 v[168:183], v[120:123], v[56:63], v17\n"
"buffer_load_dwordx4 a[132:135], v2, s[28:31], s64 offen offset:1024 sc1\n"
"v_exp_f32 v24, v24\n"
"v_exp_f32 v25, v25\n"
"v_exp_f32 v26, v26\n"
"v_smfmac_f32_16x16x64_bf16 v[232:235], v[224:227], v[56:63], v17\n"
"ds_read_b128 v[128:131], v6 offset:16384\n"
"v_exp_f32 v27, v27\n"
"v_exp_f32 v28, v28\n"
"v_smfmac_f32_32x32x32_bf16 v[168:183], v[124:127], v[64:71], v17\n"
"buffer_load_dwordx4 a[136:139], v2, s[28:31], s64 offen offset:2048 sc1\n"
"v_exp_f32 v29, v29\n"
"v_exp_f32 v30, v30\n"
"v_exp_f32 v31, v31\n"
"v_smfmac_f32_16x16x64_bf16 v[232:235], v[224:227], v[64:71], v17\n"
"ds_read_b128 v[132:135], v6 offset:17408\n"
"v_exp_f32 v32, v32\n"
"v_exp_f32 v33, v33\n"
"v_mfma_f32_32x32x16_bf16 v[56:71], v[136:139], v[236:239], v[192:207]\n"
"buffer_load_dwordx4 a[140:143], v2, s[28:31], s64 offen offset:3072 sc1\n"
"v_exp_f32 v40, v40\n"
"v_exp_f32 v41, v41\n"
"v_exp_f32 v42, v42\n"
"v_mfma_f32_32x32x16_bf16 v[56:71], v[140:143], v[240:243], v[56:71]\n"
"s_waitcnt lgkmcnt(2)\n"
"v_exp_f32 v43, v43\n"
"v_exp_f32 v44, v44\n"
"v_exp_f32 v45, v45\n"
"v_smfmac_f32_32x32x32_bf16 v[152:167], v[104:107], v[18:25], v17\n"
"buffer_load_dwordx4 a[144:147], v3, s[28:31], s64 offen offset:0 sc1\n"
"v_exp_f32 v46, v46\n"
"v_exp_f32 v47, v47\n"
"v_exp_f32 v48, v48\n"
"v_smfmac_f32_16x16x64_bf16 v[228:231], v[224:227], v[18:25], v17\n"
"ds_read_b128 v[120:123], v6 offset:14336\n"
"v_exp_f32 v49, v49\n"
"v_exp_f32 v50, v50\n"
"v_smfmac_f32_32x32x32_bf16 v[152:167], v[108:111], v[26:33], v17\n"
"buffer_load_dwordx4 a[148:151], v3, s[28:31], s64 offen offset:1024 sc1\n"
"v_exp_f32 v51, v51\n"
"v_exp_f32 v52, v52\n"
"v_exp_f32 v53, v53\n"
"v_smfmac_f32_16x16x64_bf16 v[228:231], v[224:227], v[26:33], v17\n"
"ds_read_b128 v[124:127], v6 offset:15360\n"
"v_exp_f32 v54, v54\n"
"v_exp_f32 v55, v55\n"
"v_mfma_f32_32x32x16_bf16 v[18:33], v[136:139], v[244:247], v[208:223]\n"
"buffer_load_dwordx4 a[152:155], v3, s[28:31], s64 offen offset:2048 sc1\n"
"v_exp_f32 v56, v56\n"
"v_exp_f32 v57, v57\n"
"v_exp_f32 v58, v58\n"
"v_mfma_f32_32x32x16_bf16 v[18:33], v[140:143], v[248:251], v[18:33]\n"
"buffer_load_dwordx4 a[156:159], v3, s[28:31], s64 offen offset:3072 sc1\n"
"s_lshl_b32 s61, s43, 2\n"
"s_add_u32 s61, s61, s41\n"
"s_lshl_b32 s61, s61, 10\n"
"v_lshl_add_u32 v0, v1, 7, s61\n"
"s_mov_b64 exec, 0xff\n"
"buffer_load_dword v0, v0, s[36:39], 0 offen\n"
"s_mov_b64 exec, -1\n"
"v_exp_f32 v59, v59\n"
"v_exp_f32 v60, v60\n"
"v_exp_f32 v61, v61\n"
"v_smfmac_f32_32x32x32_bf16 v[168:183], v[104:107], v[40:47], v17\n"
"s_mov_b32 s82, 77824\n"
"v_exp_f32 v62, v62\n"
"v_exp_f32 v63, v63\n"
"v_exp_f32 v64, v64\n"
"v_smfmac_f32_16x16x64_bf16 v[232:235], v[224:227], v[40:47], v17\n"
"ds_read_b128 v[136:139], v6 offset:20480\n"
"v_exp_f32 v65, v65\n"
"v_exp_f32 v66, v66\n"
"v_smfmac_f32_32x32x32_bf16 v[168:183], v[108:111], v[48:55], v17\n"
"buffer_load_dwordx4 v[34:37], v2, s[28:31], s82 offen sc1\n"
"v_exp_f32 v67, v67\n"
"v_exp_f32 v68, v68\n"
"v_exp_f32 v69, v69\n"
"v_smfmac_f32_16x16x64_bf16 v[232:235], v[224:227], v[48:55], v17\n"
"ds_read_b128 v[140:143], v6 offset:21504\n"
"v_exp_f32 v70, v70\n"
"v_exp_f32 v71, v71\n"
"v_mfma_f32_32x32x16_bf16 v[40:55], v[144:147], v[236:239], v[192:207]\n"
"s_bfe_u32 s64, s68, 262164\n"
"v_exp_f32 v18, v18\n"
"v_exp_f32 v19, v19\n"
"v_exp_f32 v20, v20\n"
"v_mfma_f32_32x32x16_bf16 v[40:55], v[148:151], v[240:243], v[40:55]\n"
"s_waitcnt lgkmcnt(2)\n"
"v_exp_f32 v21, v21\n"
"v_exp_f32 v22, v22\n"
"v_exp_f32 v23, v23\n"
"v_smfmac_f32_32x32x32_bf16 v[152:167], v[112:115], v[56:63], v17\n"
"s_mul_i32 s64, s64, 8192\n"
"v_exp_f32 v24, v24\n"
"v_exp_f32 v25, v25\n"
"v_exp_f32 v26, v26\n"
"v_smfmac_f32_16x16x64_bf16 v[228:231], v[224:227], v[56:63], v17\n"
"ds_read_b128 v[104:107], v6 offset:18432\n"
"v_exp_f32 v27, v27\n"
"v_exp_f32 v28, v28\n"
"v_smfmac_f32_32x32x32_bf16 v[152:167], v[116:119], v[64:71], v17\n"
"s_add_u32 s64, s64, s66\n"
"v_exp_f32 v29, v29\n"
"v_exp_f32 v30, v30\n"
"v_exp_f32 v31, v31\n"
"v_smfmac_f32_16x16x64_bf16 v[228:231], v[224:227], v[64:71], v17\n"
"ds_read_b128 v[108:111], v6 offset:19456\n"
"v_exp_f32 v32, v32\n"
"v_exp_f32 v33, v33\n"
"v_mfma_f32_32x32x16_bf16 v[56:71], v[144:147], v[244:247], v[208:223]\n"
"v_exp_f32 v40, v40\n"
"v_exp_f32 v41, v41\n"
"v_exp_f32 v42, v42\n"
"v_mfma_f32_32x32x16_bf16 v[56:71], v[148:151], v[248:251], v[56:71]\n"
"buffer_load_dwordx4 a[160:163], v2, s[28:31], s64 offen offset:0 sc1\n"
"v_exp_f32 v43, v43\n"
"v_exp_f32 v44, v44\n"
"v_exp_f32 v45, v45\n"
"v_smfmac_f32_32x32x32_bf16 v[168:183], v[112:115], v[18:25], v17\n"
"buffer_load_dwordx4 a[164:167], v2, s[28:31], s64 offen offset:1024 sc1\n"
"v_exp_f32 v46, v46\n"
"v_exp_f32 v47, v47\n"
"v_exp_f32 v48, v48\n"
"v_smfmac_f32_16x16x64_bf16 v[232:235], v[224:227], v[18:25], v17\n"
"ds_read_b128 v[144:147], v6 offset:24576\n"
"v_exp_f32 v49, v49\n"
"v_exp_f32 v50, v50\n"
"v_smfmac_f32_32x32x32_bf16 v[168:183], v[116:119], v[26:33], v17\n"
"buffer_load_dwordx4 a[168:171], v2, s[28:31], s64 offen offset:2048 sc1\n"
"v_exp_f32 v51, v51\n"
"v_exp_f32 v52, v52\n"
"v_exp_f32 v53, v53\n"
"v_smfmac_f32_16x16x64_bf16 v[232:235], v[224:227], v[26:33], v17\n"
"ds_read_b128 v[148:151], v6 offset:25600\n"
"v_exp_f32 v54, v54\n"
"v_exp_f32 v55, v55\n"
"v_mfma_f32_32x32x16_bf16 v[18:33], v[128:131], v[236:239], v[192:207]\n"
"buffer_load_dwordx4 a[172:175], v2, s[28:31], s64 offen offset:3072 sc1\n"
"v_exp_f32 v56, v56\n"
"v_exp_f32 v57, v57\n"
"v_exp_f32 v58, v58\n"
"v_mfma_f32_32x32x16_bf16 v[18:33], v[132:135], v[240:243], v[18:33]\n"
"s_waitcnt lgkmcnt(2)\n"
"v_exp_f32 v59, v59\n"
"v_exp_f32 v60, v60\n"
"v_exp_f32 v61, v61\n"
"v_smfmac_f32_32x32x32_bf16 v[152:167], v[120:123], v[40:47], v17\n"
"buffer_load_dwordx4 a[176:179], v3, s[28:31], s64 offen offset:0 sc1\n"
"v_exp_f32 v62, v62\n"
"v_exp_f32 v63, v63\n"
"v_exp_f32 v64, v64\n"
"v_smfmac_f32_16x16x64_bf16 v[228:231], v[224:227], v[40:47], v17\n"
"ds_read_b128 v[112:115], v6 offset:22528\n"
"v_exp_f32 v65, v65\n"
"v_exp_f32 v66, v66\n"
"v_smfmac_f32_32x32x32_bf16 v[152:167], v[124:127], v[48:55], v17\n"
"buffer_load_dwordx4 a[180:183], v3, s[28:31], s64 offen offset:1024 sc1\n"
"v_exp_f32 v67, v67\n"
"v_exp_f32 v68, v68\n"
"v_exp_f32 v69, v69\n"
"v_smfmac_f32_16x16x64_bf16 v[228:231], v[224:227], v[48:55], v17\n"
"ds_read_b128 v[116:119], v6 offset:23552\n"
"v_exp_f32 v70, v70\n"
"v_exp_f32 v71, v71\n"
"v_mfma_f32_32x32x16_bf16 v[40:55], v[128:131], v[244:247], v[208:223]\n"
"buffer_load_dwordx4 a[184:187], v3, s[28:31], s64 offen offset:2048 sc1\n"
"v_exp_f32 v18, v18\n"
"v_exp_f32 v19, v19\n"
"v_exp_f32 v20, v20\n"
"v_mfma_f32_32x32x16_bf16 v[40:55], v[132:135], v[248:251], v[40:55]\n"
"buffer_load_dwordx4 a[188:191], v3, s[28:31], s64 offen offset:3072 sc1\n"
"v_exp_f32 v21, v21\n"
"v_exp_f32 v22, v22\n"
"v_exp_f32 v23, v23\n"
"v_smfmac_f32_32x32x32_bf16 v[168:183], v[120:123], v[56:63], v17\n"
"s_bfe_u32 s64, s68, 262168\n"
"v_exp_f32 v24, v24\n"
"v_exp_f32 v25, v25\n"
"v_exp_f32 v26, v26\n"
"v_smfmac_f32_16x16x64_bf16 v[232:235], v[224:227], v[56:63], v17\n"
"ds_read_b128 v[128:131], v6 offset:28672\n"
"v_exp_f32 v27, v27\n"
"v_exp_f32 v28, v28\n"
"v_smfmac_f32_32x32x32_bf16 v[168:183], v[124:127], v[64:71], v17\n"
"s_mul_i32 s64, s64, 8192\n"
"v_exp_f32 v29, v29\n"
"v_exp_f32 v30, v30\n"
"v_exp_f32 v31, v31\n"
"v_smfmac_f32_16x16x64_bf16 v[232:235], v[224:227], v[64:71], v17\n"
"ds_read_b128 v[132:135], v6 offset:29696\n"
"v_exp_f32 v32, v32\n"
"v_exp_f32 v33, v33\n"
"v_mfma_f32_32x32x16_bf16 v[56:71], v[136:139], v[236:239], v[192:207]\n"
"s_add_u32 s64, s64, s66\n"
"v_exp_f32 v40, v40\n"
"v_exp_f32 v41, v41\n"
"v_exp_f32 v42, v42\n"
"v_mfma_f32_32x32x16_bf16 v[56:71], v[140:143], v[240:243], v[56:71]\n"
"s_waitcnt lgkmcnt(2)\n"
"v_exp_f32 v43, v43\n"
"v_exp_f32 v44, v44\n"
"v_exp_f32 v45, v45\n"
"v_smfmac_f32_32x32x32_bf16 v[152:167], v[104:107], v[18:25], v17\n"
"buffer_load_dwordx4 a[192:195], v2, s[28:31], s64 offen offset:0 sc1\n"
"v_exp_f32 v46, v46\n"
"v_exp_f32 v47, v47\n"
"v_exp_f32 v48, v48\n"
"v_smfmac_f32_16x16x64_bf16 v[228:231], v[224:227], v[18:25], v17\n"
"ds_read_b128 v[120:123], v6 offset:26624\n"
"v_exp_f32 v49, v49\n"
"v_exp_f32 v50, v50\n"
"v_smfmac_f32_32x32x32_bf16 v[152:167], v[108:111], v[26:33], v17\n"
"buffer_load_dwordx4 a[196:199], v2, s[28:31], s64 offen offset:1024 sc1\n"
"v_exp_f32 v51, v51\n"
"v_exp_f32 v52, v52\n"
"v_exp_f32 v53, v53\n"
"v_smfmac_f32_16x16x64_bf16 v[228:231], v[224:227], v[26:33], v17\n"
"ds_read_b128 v[124:127], v6 offset:27648\n"
"v_exp_f32 v54, v54\n"
"v_exp_f32 v55, v55\n"
"v_mfma_f32_32x32x16_bf16 v[18:33], v[136:139], v[244:247], v[208:223]\n"
"buffer_load_dwordx4 a[200:203], v2, s[28:31], s64 offen offset:2048 sc1\n"
"v_exp_f32 v56, v56\n"
"v_exp_f32 v57, v57\n"
"v_exp_f32 v58, v58\n"
"v_mfma_f32_32x32x16_bf16 v[18:33], v[140:143], v[248:251], v[18:33]\n"
"buffer_load_dwordx4 a[204:207], v2, s[28:31], s64 offen offset:3072 sc1\n"
"v_exp_f32 v59, v59\n"
"v_exp_f32 v60, v60\n"
"v_exp_f32 v61, v61\n"
"v_smfmac_f32_32x32x32_bf16 v[168:183], v[104:107], v[40:47], v17\n"
"buffer_load_dwordx4 a[208:211], v3, s[28:31], s64 offen offset:0 sc1\n"
"v_exp_f32 v62, v62\n"
"v_exp_f32 v63, v63\n"
"v_exp_f32 v64, v64\n"
"v_smfmac_f32_16x16x64_bf16 v[232:235], v[224:227], v[40:47], v17\n"
"buffer_load_dwordx4 a[212:215], v3, s[28:31], s64 offen offset:1024 sc1\n"
"v_exp_f32 v65, v65\n"
"v_exp_f32 v66, v66\n"
"v_smfmac_f32_32x32x32_bf16 v[168:183], v[108:111], v[48:55], v17\n"
"buffer_load_dwordx4 a[216:219], v3, s[28:31], s64 offen offset:2048 sc1\n"
"v_exp_f32 v67, v67\n"
"v_exp_f32 v68, v68\n"
"v_exp_f32 v69, v69\n"
"v_smfmac_f32_16x16x64_bf16 v[232:235], v[224:227], v[48:55], v17\n"
"buffer_load_dwordx4 a[220:223], v3, s[28:31], s64 offen offset:3072 sc1\n"
"v_exp_f32 v70, v70\n"
"v_exp_f32 v71, v71\n"
"v_mfma_f32_32x32x16_bf16 v[40:55], v[144:147], v[236:239], v[192:207]\n"
"s_bfe_u32 s64, s68, 262172\n"
"v_exp_f32 v18, v18\n"
"v_exp_f32 v19, v19\n"
"v_exp_f32 v20, v20\n"
"v_mfma_f32_32x32x16_bf16 v[40:55], v[148:151], v[240:243], v[40:55]\n"
"s_waitcnt lgkmcnt(0)\n"
"v_exp_f32 v21, v21\n"
"v_exp_f32 v22, v22\n"
"v_exp_f32 v23, v23\n"
"v_smfmac_f32_32x32x32_bf16 v[152:167], v[112:115], v[56:63], v17\n"
"s_mul_i32 s64, s64, 8192\n"
"v_exp_f32 v24, v24\n"
"v_exp_f32 v25, v25\n"
"v_exp_f32 v26, v26\n"
"v_smfmac_f32_16x16x64_bf16 v[228:231], v[224:227], v[56:63], v17\n"
"ds_read_b128 v[104:107], v6 offset:30720\n"
"v_exp_f32 v27, v27\n"
"v_exp_f32 v28, v28\n"
"v_smfmac_f32_32x32x32_bf16 v[152:167], v[116:119], v[64:71], v17\n"
"s_add_u32 s64, s64, s66\n"
"v_exp_f32 v29, v29\n"
"v_exp_f32 v30, v30\n"
"v_exp_f32 v31, v31\n"
"v_smfmac_f32_16x16x64_bf16 v[228:231], v[224:227], v[64:71], v17\n"
"ds_read_b128 v[108:111], v6 offset:31744\n"
"v_exp_f32 v32, v32\n"
"v_exp_f32 v33, v33\n"
"v_mfma_f32_32x32x16_bf16 v[56:71], v[144:147], v[244:247], v[208:223]\n"
"v_exp_f32 v40, v40\n"
"v_exp_f32 v41, v41\n"
"v_exp_f32 v42, v42\n"
"v_mfma_f32_32x32x16_bf16 v[56:71], v[148:151], v[248:251], v[56:71]\n"
"s_waitcnt vmcnt(54)\n"
"buffer_load_dwordx4 a[224:227], v2, s[28:31], s64 offen offset:0 sc1\n"
"v_exp_f32 v43, v43\n"
"v_exp_f32 v44, v44\n"
"v_exp_f32 v45, v45\n"
"v_smfmac_f32_32x32x32_bf16 v[168:183], v[112:115], v[18:25], v17\n"
"buffer_load_dwordx4 a[228:231], v2, s[28:31], s64 offen offset:1024 sc1\n"
"v_exp_f32 v46, v46\n"
"v_exp_f32 v47, v47\n"
"v_exp_f32 v48, v48\n"
"v_smfmac_f32_16x16x64_bf16 v[232:235], v[224:227], v[18:25], v17\n"
"buffer_load_dwordx4 a[232:235], v2, s[28:31], s64 offen offset:2048 sc1\n"
"v_exp_f32 v49, v49\n"
"v_exp_f32 v50, v50\n"
"v_smfmac_f32_32x32x32_bf16 v[168:183], v[116:119], v[26:33], v17\n"
"buffer_load_dwordx4 a[236:239], v2, s[28:31], s64 offen offset:3072 sc1\n"
"v_exp_f32 v51, v51\n"
"v_exp_f32 v52, v52\n"
"v_exp_f32 v53, v53\n"
"v_smfmac_f32_16x16x64_bf16 v[232:235], v[224:227], v[26:33], v17\n"
"buffer_load_dwordx4 a[240:243], v3, s[28:31], s64 offen offset:0 sc1\n"
"v_exp_f32 v54, v54\n"
"v_exp_f32 v55, v55\n"
"v_mfma_f32_32x32x16_bf16 v[18:33], v[128:131], v[236:239], v[192:207]\n"
"v_exp_f32 v56, v56\n"
"v_exp_f32 v57, v57\n"
"v_exp_f32 v58, v58\n"
"v_mfma_f32_32x32x16_bf16 v[18:33], v[132:135], v[240:243], v[18:33]\n"
"s_waitcnt lgkmcnt(0)\n"
"v_exp_f32 v59, v59\n"
"v_exp_f32 v60, v60\n"
"v_exp_f32 v61, v61\n"
"v_smfmac_f32_32x32x32_bf16 v[152:167], v[120:123], v[40:47], v17\n"
"buffer_load_dwordx4 a[244:247], v3, s[28:31], s64 offen offset:1024 sc1\n"
"v_exp_f32 v62, v62\n"
"v_exp_f32 v63, v63\n"
"v_exp_f32 v64, v64\n"
"v_smfmac_f32_16x16x64_bf16 v[228:231], v[224:227], v[40:47], v17\n"
"buffer_load_dwordx4 a[248:251], v3, s[28:31], s64 offen offset:2048 sc1\n"
"v_exp_f32 v65, v65\n"
"v_exp_f32 v66, v66\n"
"v_smfmac_f32_32x32x32_bf16 v[152:167], v[124:127], v[48:55], v17\n"
"buffer_load_dwordx4 a[252:255], v3, s[28:31], s64 offen offset:3072 sc1\n"
"v_exp_f32 v67, v67\n"
"v_exp_f32 v68, v68\n"
"v_exp_f32 v69, v69\n"
"v_smfmac_f32_16x16x64_bf16 v[228:231], v[224:227], v[48:55], v17\n"
"s_waitcnt vmcnt(8)\n"
"v_bitop3_b32 v34, v34, v35, s26 bitop3:0x7e\n"
"v_exp_f32 v70, v70\n"
"v_exp_f32 v71, v71\n"
"v_mfma_f32_32x32x16_bf16 v[40:55], v[128:131], v[244:247], v[208:223]\n"
"v_bitop3_b32 v36, v36, v37, s26 bitop3:0x7e\n"
"v_exp_f32 v18, v18\n"
"v_exp_f32 v19, v19\n"
"v_exp_f32 v20, v20\n"
"v_mfma_f32_32x32x16_bf16 v[40:55], v[132:135], v[248:251], v[40:55]\n"
"v_exp_f32 v21, v21\n"
"v_exp_f32 v22, v22\n"
"v_exp_f32 v23, v23\n"
"v_smfmac_f32_32x32x32_bf16 v[168:183], v[120:123], v[56:63], v17\n"
"v_or_b32 v34, v34, v36\n"
"v_exp_f32 v24, v24\n"
"v_exp_f32 v25, v25\n"
"v_exp_f32 v26, v26\n"
"v_smfmac_f32_16x16x64_bf16 v[232:235], v[224:227], v[56:63], v17\n"
"v_exp_f32 v27, v27\n"
"v_exp_f32 v28, v28\n"
"v_smfmac_f32_32x32x32_bf16 v[168:183], v[124:127], v[64:71], v17\n"
"v_cmp_ne_u32 vcc, 0, v34\n"
"s_nop 0\n"
"s_cbranch_vccnz .Lmk_fspin_16\n"
".Lmk_fback_17:\n"
"v_exp_f32 v29, v29\n"
"v_exp_f32 v30, v30\n"
"v_exp_f32 v31, v31\n"
"v_smfmac_f32_16x16x64_bf16 v[232:235], v[224:227], v[64:71], v17\n"
"v_exp_f32 v32, v32\n"
"v_exp_f32 v33, v33\n"
"v_exp_f32 v40, v40\n"
"v_exp_f32 v41, v41\n"
"v_exp_f32 v42, v42\n"
"v_exp_f32 v43, v43\n"
"v_exp_f32 v44, v44\n"
"v_exp_f32 v45, v45\n"
"v_smfmac_f32_32x32x32_bf16 v[152:167], v[104:107], v[18:25], v17\n"
"v_exp_f32 v46, v46\n"
"v_exp_f32 v47, v47\n"
"v_exp_f32 v48, v48\n"
"v_smfmac_f32_16x16x64_bf16 v[228:231], v[224:227], v[18:25], v17\n"
"v_exp_f32 v49, v49\n"
"v_exp_f32 v50, v50\n"
"v_smfmac_f32_32x32x32_bf16 v[152:167], v[108:111], v[26:33], v17\n"
"v_exp_f32 v51, v51\n"
"v_exp_f32 v52, v52\n"
"v_exp_f32 v53, v53\n"
"v_smfmac_f32_16x16x64_bf16 v[228:231], v[224:227], v[26:33], v17\n"
"v_exp_f32 v54, v54\n"
"v_exp_f32 v55, v55\n"
"v_smfmac_f32_32x32x32_bf16 v[168:183], v[104:107], v[40:47], v17\n"
"v_smfmac_f32_16x16x64_bf16 v[232:235], v[224:227], v[40:47], v17\n"
"v_smfmac_f32_32x32x32_bf16 v[168:183], v[108:111], v[48:55], v17\n"
"v_smfmac_f32_16x16x64_bf16 v[232:235], v[224:227], v[48:55], v17\n"
"s_nop 5\n"
"v_rcp_f32 v12, v228\n"
"s_nop 0\n"
"s_nop 0\n"
"v_pk_mul_f32 v[152:153], v[152:153], v[12:13] op_sel_hi:[1,0]\n"
"v_pk_mul_f32 v[154:155], v[154:155], v[12:13] op_sel_hi:[1,0]\n"
"v_pk_mul_f32 v[156:157], v[156:157], v[12:13] op_sel_hi:[1,0]\n"
"v_pk_mul_f32 v[158:159], v[158:159], v[12:13] op_sel_hi:[1,0]\n"
"v_pk_mul_f32 v[160:161], v[160:161], v[12:13] op_sel_hi:[1,0]\n"
"v_pk_mul_f32 v[162:163], v[162:163], v[12:13] op_sel_hi:[1,0]\n"
"v_pk_mul_f32 v[164:165], v[164:165], v[12:13] op_sel_hi:[1,0]\n"
"v_pk_mul_f32 v[166:167], v[166:167], v[12:13] op_sel_hi:[1,0]\n"
"v_cvt_pk_bf16_f32 v18, v152, v153\n"
"v_cvt_pk_bf16_f32 v19, v154, v155\n"
"v_cvt_pk_bf16_f32 v20, v156, v157\n"
"v_cvt_pk_bf16_f32 v21, v158, v159\n"
"v_cvt_pk_bf16_f32 v22, v160, v161\n"
"v_cvt_pk_bf16_f32 v23, v162, v163\n"
"v_cvt_pk_bf16_f32 v24, v164, v165\n"
"v_cvt_pk_bf16_f32 v25, v166, v167\n"
"buffer_store_dwordx4 v[18:21], v14, s[28:31], s65 offen offset:0 sc1\n"
"buffer_store_dwordx4 v[22:25], v14, s[28:31], s65 offen offset:256 sc1\n"
"v_rcp_f32 v12, v232\n"
"s_nop 0\n"
"v_pk_mul_f32 v[168:169], v[168:169], v[12:13] op_sel_hi:[1,0]\n"
"v_pk_mul_f32 v[170:171], v[170:171], v[12:13] op_sel_hi:[1,0]\n"
"v_pk_mul_f32 v[172:173], v[172:173], v[12:13] op_sel_hi:[1,0]\n"
"v_pk_mul_f32 v[174:175], v[174:175], v[12:13] op_sel_hi:[1,0]\n"
"v_pk_mul_f32 v[176:177], v[176:177], v[12:13] op_sel_hi:[1,0]\n"
"v_pk_mul_f32 v[178:179], v[178:179], v[12:13] op_sel_hi:[1,0]\n"
"v_pk_mul_f32 v[180:181], v[180:181], v[12:13] op_sel_hi:[1,0]\n"
"v_pk_mul_f32 v[182:183], v[182:183], v[12:13] op_sel_hi:[1,0]\n"
"v_cvt_pk_bf16_f32 v26, v168, v169\n"
"v_cvt_pk_bf16_f32 v27, v170, v171\n"
"v_cvt_pk_bf16_f32 v28, v172, v173\n"
"v_cvt_pk_bf16_f32 v29, v174, v175\n"
"v_cvt_pk_bf16_f32 v30, v176, v177\n"
"v_cvt_pk_bf16_f32 v31, v178, v179\n"
"v_cvt_pk_bf16_f32 v32, v180, v181\n"
"v_cvt_pk_bf16_f32 v33, v182, v183\n"
"buffer_store_dwordx4 v[26:29], v14, s[28:31], s65 offen offset:2048 sc1\n"
"buffer_store_dwordx4 v[30:33], v14, s[28:31], s65 offen offset:2304 sc1\n"
"s_add_u32 s57, s53, 77824\n"
"v_add_u32 v13, s57, v14\n"
"s_nop 0\n"
"ds_write_b128 v13, v[18:21] offset:0\n"
"ds_write_b128 v13, v[22:25] offset:256\n"
"ds_write_b128 v13, v[26:29] offset:2048\n"
"ds_write_b128 v13, v[30:33] offset:2304\n"
"s_cmp_lg_u32 s40, 0\n"
"s_cbranch_scc1 .Lmk_ep_19\n"
"v_mov_b32 v11, s26\n"
"s_mov_b32 s58, 65536\n"
"s_mov_b64 exec, 1\n"
"buffer_store_dword v11, v9, s[28:31], s58 offen sc1\n"
"s_mov_b64 exec, -1\n"
".Lmk_ep_19:\n"
"s_mul_i32 s70, s44, 512\n"
"s_mul_i32 s56, s41, 128\n"
"s_add_u32 s70, s70, s56\n"
"s_add_u32 s70, s70, 0\n"
"s_mul_i32 s71, s46, 512\n"
"s_mul_i32 s56, s41, 128\n"
"s_add_u32 s71, s71, s56\n"
"s_add_u32 s71, s71, 0\n"
"s_mov_b64 exec, 0\n"
"s_cmp_eq_u32 s41, 0\n"
"s_cbranch_scc1 .Lmk_pg_8\n"
"s_cmp_eq_u32 s41, 1\n"
"s_cbranch_scc1 .Lmk_pg_9\n"
"s_cmp_eq_u32 s41, 2\n"
"s_cbranch_scc1 .Lmk_pg_10\n"
"s_branch .Lmk_pg_11\n"
".Lmk_ret_2:\n"
"s_mov_b64 exec, -1\n"
"s_nop 0\n"
"v_lshlrev_b32 v40, 16, v72\n"
"v_and_b32 v41, 0xffff0000, v72\n"
"v_lshlrev_b32 v42, 16, v73\n"
"v_and_b32 v43, 0xffff0000, v73\n"
"v_lshlrev_b32 v56, 16, v74\n"
"v_and_b32 v57, 0xffff0000, v74\n"
"v_lshlrev_b32 v58, 16, v75\n"
"v_and_b32 v59, 0xffff0000, v75\n"
"v_lshlrev_b32 v44, 16, v76\n"
"v_and_b32 v45, 0xffff0000, v76\n"
"v_lshlrev_b32 v46, 16, v77\n"
"v_and_b32 v47, 0xffff0000, v77\n"
"v_lshlrev_b32 v60, 16, v78\n"
"v_and_b32 v61, 0xffff0000, v78\n"
"v_lshlrev_b32 v62, 16, v79\n"
"v_and_b32 v63, 0xffff0000, v79\n"
"v_lshlrev_b32 v48, 16, v80\n"
"v_and_b32 v49, 0xffff0000, v80\n"
"v_lshlrev_b32 v50, 16, v81\n"
"v_and_b32 v51, 0xffff0000, v81\n"
"v_lshlrev_b32 v64, 16, v82\n"
"v_and_b32 v65, 0xffff0000, v82\n"
"v_lshlrev_b32 v66, 16, v83\n"
"v_and_b32 v67, 0xffff0000, v83\n"
"v_lshlrev_b32 v52, 16, v84\n"
"v_and_b32 v53, 0xffff0000, v84\n"
"v_lshlrev_b32 v54, 16, v85\n"
"v_and_b32 v55, 0xffff0000, v85\n"
"v_lshlrev_b32 v68, 16, v86\n"
"v_and_b32 v69, 0xffff0000, v86\n"
"v_lshlrev_b32 v70, 16, v87\n"
"v_and_b32 v71, 0xffff0000, v87\n"
"s_waitcnt vmcnt(0)\n"
"s_mov_b64 exec, 1\n"
"buffer_atomic_add v10, v9, s[28:31], s70 offen sc1\n"
"s_mov_b64 exec, -1\n"
"s_waitcnt lgkmcnt(0)\n"
"s_barrier\n"
"ds_read_b128 v[190:193], v7 offset:12288\n"
"ds_read_b128 v[194:197], v7 offset:13312\n"
"ds_read_b128 v[198:201], v7 offset:14336\n"
"ds_read_b128 v[202:205], v7 offset:15360\n"
"ds_read_b128 v[206:209], v7 offset:16384\n"
"ds_read_b128 v[210:213], v7 offset:17408\n"
"ds_read_b128 v[214:217], v7 offset:18432\n"
"ds_read_b128 v[218:221], v7 offset:19456\n"
"s_waitcnt lgkmcnt(7)\n"
"v_mfma_f32_16x16x32_bf16 v[40:43], a[0:3], v[190:193], v[40:43]\n"
"ds_read_b128 v[222:225], v7 offset:20480\n"
"v_mfma_f32_16x16x32_bf16 v[56:59], a[4:7], v[190:193], v[56:59]\n"
"s_waitcnt lgkmcnt(7)\n"
"v_mfma_f32_16x16x32_bf16 v[44:47], a[0:3], v[194:197], v[44:47]\n"
"ds_read_b128 v[226:229], v7 offset:21504\n"
"v_mfma_f32_16x16x32_bf16 v[60:63], a[4:7], v[194:197], v[60:63]\n"
"s_waitcnt lgkmcnt(7)\n"
"v_mfma_f32_16x16x32_bf16 v[48:51], a[0:3], v[198:201], v[48:51]\n"
"ds_read_b128 v[190:193], v7 offset:22528\n"
"v_mfma_f32_16x16x32_bf16 v[64:67], a[4:7], v[198:201], v[64:67]\n"
"s_waitcnt lgkmcnt(7)\n"
"v_mfma_f32_16x16x32_bf16 v[52:55], a[0:3], v[202:205], v[52:55]\n"
"ds_read_b128 v[194:197], v7 offset:23552\n"
"v_mfma_f32_16x16x32_bf16 v[68:71], a[4:7], v[202:205], v[68:71]\n"
"s_waitcnt lgkmcnt(4)\n"
"v_mfma_f32_16x16x32_bf16 v[40:43], a[8:11], v[206:209], v[40:43]\n"
"ds_read_b128 v[198:201], v7 offset:24576\n"
"v_mov_b32 v11, 0\n"
"s_mov_b64 exec, 1\n"
"buffer_atomic_add v11, v9, s[28:31], s71 offen sc0 sc1\n"
"s_mov_b64 exec, -1\n"
"s_nop 1\n"
"v_mfma_f32_16x16x32_bf16 v[56:59], a[12:15], v[206:209], v[56:59]\n"
"s_and_b32 s59, s41, 1\n"
"s_lshr_b32 s60, s41, 1\n"
"v_mfma_f32_16x16x32_bf16 v[44:47], a[8:11], v[210:213], v[44:47]\n"
"ds_read_b128 v[202:205], v7 offset:25600\n"
"s_mul_i32 s58, s49, 112\n"
"v_mfma_f32_16x16x32_bf16 v[60:63], a[12:15], v[210:213], v[60:63]\n"
"s_mul_i32 s56, s59, 56\n"
"s_add_u32 s58, s58, s56\n"
"v_mfma_f32_16x16x32_bf16 v[48:51], a[8:11], v[214:217], v[48:51]\n"
"ds_read_b128 v[206:209], v7 offset:26624\n"
"s_mul_i32 s56, s60, -1792\n"
"v_mfma_f32_16x16x32_bf16 v[64:67], a[12:15], v[214:217], v[64:67]\n"
"s_add_u32 s58, s58, s56\n"
"s_add_u32 s58, s58, 1792\n"
"v_mfma_f32_16x16x32_bf16 v[52:55], a[8:11], v[218:221], v[52:55]\n"
"ds_read_b128 v[210:213], v7 offset:27648\n"
"v_add_u32 v14, s58, v1\n"
"v_mfma_f32_16x16x32_bf16 v[68:71], a[12:15], v[218:221], v[68:71]\n"
"s_waitcnt lgkmcnt(4)\n"
"v_and_b32 v15, 63, v14\n"
"v_mfma_f32_16x16x32_bf16 v[40:43], a[16:19], v[222:225], v[40:43]\n"
"v_lshrrev_b32 v14, 6, v14\n"
"v_lshlrev_b32 v14, 12, v14\n"
"v_mfma_f32_16x16x32_bf16 v[56:59], a[20:23], v[222:225], v[56:59]\n"
"v_lshl_add_u32 v14, v15, 2, v14\n"
"s_mov_b32 s58, 786432\n"
"v_mfma_f32_16x16x32_bf16 v[44:47], a[16:19], v[226:229], v[44:47]\n"
"buffer_load_dword v236, v14, s[28:31], s58 offen offset:0 sc1\n"
"v_mfma_f32_16x16x32_bf16 v[60:63], a[20:23], v[226:229], v[60:63]\n"
"buffer_load_dword v237, v14, s[28:31], s58 offen offset:256 sc1\n"
"v_mfma_f32_16x16x32_bf16 v[48:51], a[16:19], v[190:193], v[48:51]\n"
"buffer_load_dword v238, v14, s[28:31], s58 offen offset:512 sc1\n"
"v_mfma_f32_16x16x32_bf16 v[64:67], a[20:23], v[190:193], v[64:67]\n"
"buffer_load_dword v239, v14, s[28:31], s58 offen offset:768 sc1\n"
"v_mfma_f32_16x16x32_bf16 v[52:55], a[16:19], v[194:197], v[52:55]\n"
"buffer_load_dword v240, v14, s[28:31], s58 offen offset:1024 sc1\n"
"v_mfma_f32_16x16x32_bf16 v[68:71], a[20:23], v[194:197], v[68:71]\n"
"s_waitcnt lgkmcnt(0)\n"
"v_mfma_f32_16x16x32_bf16 v[40:43], a[24:27], v[198:201], v[40:43]\n"
"buffer_load_dword v241, v14, s[28:31], s58 offen offset:1280 sc1\n"
"v_mfma_f32_16x16x32_bf16 v[56:59], a[28:31], v[198:201], v[56:59]\n"
"buffer_load_dword v242, v14, s[28:31], s58 offen offset:1536 sc1\n"
"v_mfma_f32_16x16x32_bf16 v[44:47], a[24:27], v[202:205], v[44:47]\n"
"buffer_load_dword v243, v14, s[28:31], s58 offen offset:1792 sc1\n"
"v_mfma_f32_16x16x32_bf16 v[60:63], a[28:31], v[202:205], v[60:63]\n"
"buffer_load_dword v244, v14, s[28:31], s58 offen offset:2048 sc1\n"
"v_mfma_f32_16x16x32_bf16 v[48:51], a[24:27], v[206:209], v[48:51]\n"
"buffer_load_dword v245, v14, s[28:31], s58 offen offset:2304 sc1\n"
"v_mfma_f32_16x16x32_bf16 v[64:67], a[28:31], v[206:209], v[64:67]\n"
"buffer_load_dword v246, v14, s[28:31], s58 offen offset:2560 sc1\n"
"v_mfma_f32_16x16x32_bf16 v[52:55], a[24:27], v[210:213], v[52:55]\n"
"buffer_load_dword v247, v14, s[28:31], s58 offen offset:2816 sc1\n"
"buffer_load_dword v248, v14, s[28:31], s58 offen offset:3072 sc1\n"
"buffer_load_dword v249, v14, s[28:31], s58 offen offset:3328 sc1\n"
"buffer_load_dword v250, v14, s[28:31], s58 offen offset:3584 sc1\n"
"buffer_load_dword v251, v14, s[28:31], s58 offen offset:3840 sc1\n"
".Lmk_pg_8:\n"
"s_cbranch_execz .Lmk_ret_2\n"
"s_mul_i32 s67, s46, 131072\n"
"s_mul_i32 s56, s41, 4096\n"
"s_add_u32 s67, s67, s56\n"
"s_add_u32 s67, s67, 5242880\n"
"s_mul_i32 s69, s49, 229376\n"
"s_mul_i32 s56, s41, 7168\n"
"s_add_u32 s69, s69, s56\n"
"s_add_u32 s69, s69, 22020096\n"
"s_mov_b32 s81, 1985167888\n"
"s_mul_i32 s62, s48, 1\n"
"s_mul_i32 s56, s49, 1\n"
"s_add_u32 s62, s62, s56\n"
"s_add_u32 s62, s62, 0\n"
"s_and_b32 s62, s62, 3\n"
"s_cmp_eq_u32 s62, 1\n"
"s_cselect_b32 s81, 1984246273, s81\n"
"s_cmp_eq_u32 s62, 2\n"
"s_cselect_b32 s81, 1969500448, s81\n"
"s_cmp_eq_u32 s62, 3\n"
"s_cselect_b32 s81, 1733567248, s81\n"
"v_mfma_f32_16x16x32_bf16 v[68:71], a[28:31], v[210:213], v[68:71]\n"
"s_waitcnt vmcnt(16)\n"
"v_readfirstlane_b32 s61, v11\n"
"s_nop 0\n"
"s_cmp_ge_u32 s61, 8\n"
"s_cbranch_scc0 .Lmk_spin_21\n"
".Lmk_back_22:\n"
"s_bfe_u32 s65, s68, 262148\n"
"s_mul_i32 s65, s65, 16384\n"
"s_add_u32 s65, s65, s67\n"
"s_add_u32 m0, s53, 28672\n"
"s_nop 0\n"
"buffer_load_dwordx4 v2, s[28:31], s65 offen offset:0 sc1 lds\n"
"buffer_load_dwordx4 v2, s[28:31], s65 offen offset:1024 sc1 lds\n"
"buffer_load_dwordx4 v2, s[28:31], s65 offen offset:2048 sc1 lds\n"
"buffer_load_dwordx4 v2, s[28:31], s65 offen offset:3072 sc1 lds\n"
"s_bfe_u32 s65, s68, 262152\n"
"s_mul_i32 s65, s65, 16384\n"
"s_add_u32 s65, s65, s67\n"
"s_add_u32 m0, s53, 45056\n"
"s_nop 0\n"
"buffer_load_dwordx4 v2, s[28:31], s65 offen offset:0 sc1 lds\n"
"buffer_load_dwordx4 v2, s[28:31], s65 offen offset:1024 sc1 lds\n"
"buffer_load_dwordx4 v2, s[28:31], s65 offen offset:2048 sc1 lds\n"
"buffer_load_dwordx4 v2, s[28:31], s65 offen offset:3072 sc1 lds\n"
"s_waitcnt vmcnt(15)\n"
"v_add_f32 v236, v236, v244\n"
"s_waitcnt vmcnt(14)\n"
"v_add_f32 v237, v237, v245\n"
"s_waitcnt vmcnt(13)\n"
"v_add_f32 v238, v238, v246\n"
"s_waitcnt vmcnt(12)\n"
"v_add_f32 v239, v239, v247\n"
"s_waitcnt vmcnt(11)\n"
"v_add_f32 v240, v240, v248\n"
"s_waitcnt vmcnt(10)\n"
"v_add_f32 v241, v241, v249\n"
"s_waitcnt vmcnt(9)\n"
"v_add_f32 v242, v242, v250\n"
"s_waitcnt vmcnt(8)\n"
"v_add_f32 v243, v243, v251\n"
"v_add_f32 v236, v236, v240\n"
"v_add_f32 v237, v237, v241\n"
"v_add_f32 v238, v238, v242\n"
"v_add_f32 v239, v239, v243\n"
"v_add_f32 v236, v236, v238\n"
"v_add_f32 v237, v237, v239\n"
"v_add_f32 v236, v236, v237\n"
"s_mul_i32 s58, s41, 224\n"
"s_add_u32 s58, s58, 162816\n"
"v_lshl_add_u32 v14, v1, 2, s58\n"
"s_mov_b32 exec_hi, 0xffffff\n"
"ds_write_b32 v14, v236\n"
"s_mov_b64 exec, -1\n"
"v_bfe_u32 v15, v1, 0, 4\n"
"v_mul_u32_u24 v230, 4, v15\n"
"v_add_u32 v230, 162816, v230\n"
"v_bfe_u32 v15, v1, 4, 2\n"
"v_mul_u32_u24 v231, 16, v15\n"
"v_add_u32 v231, 163264, v231\n"
"s_mul_i32 s57, s41, 256\n"
"s_add_u32 s57, s57, 161792\n"
"v_lshl_add_u32 v232, v1, 2, s57\n"
"v_bfe_u32 v15, v1, 4, 2\n"
"v_mul_u32_u24 v233, 32, v15\n"
"v_add_u32 v233, s57, v233\n"
"v_bfe_u32 v15, v1, 0, 2\n"
"v_mul_u32_u24 v234, 4, v15\n"
"v_bfe_u32 v15, v1, 2, 2\n"
"v_lshl_add_u32 v234, v15, 5, v234\n"
"v_add_u32 v234, s57, v234\n"
"s_bfe_u32 s65, s68, 262156\n"
"s_mul_i32 s65, s65, 16384\n"
"s_add_u32 s65, s65, s67\n"
"s_add_u32 m0, s53, 61440\n"
"s_nop 0\n"
"buffer_load_dwordx4 v2, s[28:31], s65 offen offset:0 sc1 lds\n"
"buffer_load_dwordx4 v2, s[28:31], s65 offen offset:1024 sc1 lds\n"
"buffer_load_dwordx4 v2, s[28:31], s65 offen offset:2048 sc1 lds\n"
"buffer_load_dwordx4 v2, s[28:31], s65 offen offset:3072 sc1 lds\n"
"s_waitcnt vmcnt(8)\n"
"s_barrier\n"
"ds_read_b128 v[214:217], v6 offset:28672\n"
"ds_read_b128 v[218:221], v6 offset:29696\n"
"ds_read_b128 v[222:225], v6 offset:30720\n"
"ds_read_b128 v[226:229], v6 offset:31744\n"
"ds_read_b128 v[190:193], v6 offset:32768\n"
"ds_read_b128 v[194:197], v6 offset:33792\n"
"ds_read_b128 v[198:201], v6 offset:34816\n"
"ds_read_b128 v[202:205], v6 offset:35840\n"
"s_waitcnt lgkmcnt(7)\n"
"v_mfma_f32_16x16x32_bf16 v[40:43], a[32:35], v[214:217], v[40:43]\n"
"ds_read_b128 v[206:209], v6 offset:36864\n"
"v_mfma_f32_16x16x32_bf16 v[56:59], a[36:39], v[214:217], v[56:59]\n"
"s_bfe_u32 s65, s68, 262160\n"
"s_mul_i32 s65, s65, 16384\n"
"s_waitcnt lgkmcnt(7)\n"
"v_mfma_f32_16x16x32_bf16 v[44:47], a[32:35], v[218:221], v[44:47]\n"
"ds_read_b128 v[210:213], v6 offset:37888\n"
"v_mfma_f32_16x16x32_bf16 v[60:63], a[36:39], v[218:221], v[60:63]\n"
"s_add_u32 s65, s65, s67\n"
"s_waitcnt lgkmcnt(7)\n"
"v_mfma_f32_16x16x32_bf16 v[48:51], a[32:35], v[222:225], v[48:51]\n"
"ds_read_b128 v[214:217], v6 offset:38912\n"
"v_mfma_f32_16x16x32_bf16 v[64:67], a[36:39], v[222:225], v[64:67]\n"
"s_waitcnt lgkmcnt(7)\n"
"v_mfma_f32_16x16x32_bf16 v[52:55], a[32:35], v[226:229], v[52:55]\n"
"ds_read_b128 v[218:221], v6 offset:39936\n"
"s_add_u32 m0, s53, 94208\n"
"v_mfma_f32_16x16x32_bf16 v[68:71], a[36:39], v[226:229], v[68:71]\n"
"s_waitcnt lgkmcnt(4)\n"
"v_mfma_f32_16x16x32_bf16 v[40:43], a[40:43], v[190:193], v[40:43]\n"
"ds_read_b128 v[222:225], v6 offset:40960\n"
"v_mfma_f32_16x16x32_bf16 v[56:59], a[44:47], v[190:193], v[56:59]\n"
"buffer_load_dwordx4 v2, s[28:31], s65 offen offset:0 sc1 lds\n"
"v_mfma_f32_16x16x32_bf16 v[44:47], a[40:43], v[194:197], v[44:47]\n"
"ds_read_b128 v[226:229], v6 offset:41984\n"
"s_mul_i32 s64, s46, 131072\n"
"v_mfma_f32_16x16x32_bf16 v[60:63], a[44:47], v[194:197], v[60:63]\n"
"v_mfma_f32_16x16x32_bf16 v[48:51], a[40:43], v[198:201], v[48:51]\n"
"ds_read_b128 v[190:193], v6 offset:43008\n"
"v_mfma_f32_16x16x32_bf16 v[64:67], a[44:47], v[198:201], v[64:67]\n"
"buffer_load_dwordx4 v2, s[28:31], s65 offen offset:1024 sc1 lds\n"
"v_mfma_f32_16x16x32_bf16 v[52:55], a[40:43], v[202:205], v[52:55]\n"
"ds_read_b128 v[194:197], v6 offset:44032\n"
"s_mul_i32 s56, s47, 8192\n"
"v_mfma_f32_16x16x32_bf16 v[68:71], a[44:47], v[202:205], v[68:71]\n"
"s_waitcnt lgkmcnt(4)\n"
"v_mfma_f32_16x16x32_bf16 v[40:43], a[48:51], v[206:209], v[40:43]\n"
"s_waitcnt vmcnt(6)\n"
"s_barrier\n"
"ds_read_b128 v[198:201], v6 offset:45056\n"
"v_mfma_f32_16x16x32_bf16 v[56:59], a[52:55], v[206:209], v[56:59]\n"
"buffer_load_dwordx4 v2, s[28:31], s65 offen offset:2048 sc1 lds\n"
"v_mfma_f32_16x16x32_bf16 v[44:47], a[48:51], v[210:213], v[44:47]\n"
"ds_read_b128 v[202:205], v6 offset:46080\n"
"s_add_u32 s64, s64, s56\n"
"v_mfma_f32_16x16x32_bf16 v[60:63], a[52:55], v[210:213], v[60:63]\n"
"buffer_load_dwordx4 v2, s[28:31], s65 offen offset:3072 sc1 lds\n"
"v_mfma_f32_16x16x32_bf16 v[48:51], a[48:51], v[214:217], v[48:51]\n"
"ds_read_b128 v[206:209], v6 offset:47104\n"
"s_mul_i32 s56, s41, 2048\n"
"v_mfma_f32_16x16x32_bf16 v[64:67], a[52:55], v[214:217], v[64:67]\n"
"v_mfma_f32_16x16x32_bf16 v[52:55], a[48:51], v[218:221], v[52:55]\n"
"ds_read_b128 v[210:213], v6 offset:48128\n"
"v_mfma_f32_16x16x32_bf16 v[68:71], a[52:55], v[218:221], v[68:71]\n"
"s_waitcnt lgkmcnt(4)\n"
"v_mfma_f32_16x16x32_bf16 v[40:43], a[56:59], v[222:225], v[40:43]\n"
"ds_read_b128 v[214:217], v6 offset:49152\n"
"s_add_u32 s64, s64, s56\n"
"v_mfma_f32_16x16x32_bf16 v[56:59], a[60:63], v[222:225], v[56:59]\n"
"s_bfe_u32 s65, s68, 262164\n"
"s_mul_i32 s65, s65, 16384\n"
"v_mfma_f32_16x16x32_bf16 v[44:47], a[56:59], v[226:229], v[44:47]\n"
"ds_read_b128 v[218:221], v6 offset:50176\n"
"s_add_u32 s65, s65, s67\n"
"v_mfma_f32_16x16x32_bf16 v[60:63], a[60:63], v[226:229], v[60:63]\n"
"s_add_u32 s64, s64, 9437184\n"
"v_mfma_f32_16x16x32_bf16 v[48:51], a[56:59], v[190:193], v[48:51]\n"
"ds_read_b128 v[222:225], v6 offset:51200\n"
"s_add_u32 m0, s53, 110592\n"
"v_mfma_f32_16x16x32_bf16 v[64:67], a[60:63], v[190:193], v[64:67]\n"
"v_mfma_f32_16x16x32_bf16 v[52:55], a[56:59], v[194:197], v[52:55]\n"
"ds_read_b128 v[226:229], v6 offset:52224\n"
"v_mfma_f32_16x16x32_bf16 v[68:71], a[60:63], v[194:197], v[68:71]\n"
"s_waitcnt lgkmcnt(4)\n"
"s_mul_i32 s57, s41, 512\n"
"v_mfma_f32_16x16x32_bf16 v[40:43], a[64:67], v[198:201], v[40:43]\n"
"ds_read_b128 v[190:193], v6 offset:53248\n"
"v_mfma_f32_16x16x32_bf16 v[56:59], a[68:71], v[198:201], v[56:59]\n"
"buffer_load_dwordx4 v2, s[28:31], s65 offen offset:0 sc1 lds\n"
"v_mfma_f32_16x16x32_bf16 v[44:47], a[64:67], v[202:205], v[44:47]\n"
"ds_read_b128 v[194:197], v6 offset:54272\n"
"s_add_u32 s57, s57, 159744\n"
"v_mfma_f32_16x16x32_bf16 v[60:63], a[68:71], v[202:205], v[60:63]\n"
"buffer_load_dwordx4 v2, s[28:31], s65 offen offset:1024 sc1 lds\n"
"v_mfma_f32_16x16x32_bf16 v[48:51], a[64:67], v[206:209], v[48:51]\n"
"ds_read_b128 v[198:201], v6 offset:55296\n"
"v_mfma_f32_16x16x32_bf16 v[64:67], a[68:71], v[206:209], v[64:67]\n"
"buffer_load_dwordx4 v2, s[28:31], s65 offen offset:2048 sc1 lds\n"
"v_mfma_f32_16x16x32_bf16 v[52:55], a[64:67], v[210:213], v[52:55]\n"
"ds_read_b128 v[202:205], v6 offset:56320\n"
"v_bfe_u32 v15, v1, 0, 4\n"
"v_mfma_f32_16x16x32_bf16 v[68:71], a[68:71], v[210:213], v[68:71]\n"
"s_waitcnt lgkmcnt(4)\n"
"v_mfma_f32_16x16x32_bf16 v[40:43], a[72:75], v[214:217], v[40:43]\n"
"ds_read_b128 v[206:209], v6 offset:57344\n"
"v_mfma_f32_16x16x32_bf16 v[56:59], a[76:79], v[214:217], v[56:59]\n"
"buffer_load_dwordx4 v2, s[28:31], s65 offen offset:3072 sc1 lds\n"
"v_mfma_f32_16x16x32_bf16 v[44:47], a[72:75], v[218:221], v[44:47]\n"
"ds_read_b128 v[210:213], v6 offset:58368\n"
"v_mul_u32_u24 v34, 16, v15\n"
"v_mfma_f32_16x16x32_bf16 v[60:63], a[76:79], v[218:221], v[60:63]\n"
"v_mfma_f32_16x16x32_bf16 v[48:51], a[72:75], v[222:225], v[48:51]\n"
"ds_read_b128 v[214:217], v6 offset:59392\n"
"v_mfma_f32_16x16x32_bf16 v[64:67], a[76:79], v[222:225], v[64:67]\n"
"s_bfe_u32 s65, s68, 262168\n"
"s_mul_i32 s65, s65, 16384\n"
"v_mfma_f32_16x16x32_bf16 v[52:55], a[72:75], v[226:229], v[52:55]\n"
"ds_read_b128 v[218:221], v6 offset:60416\n"
"v_add_u32 v34, s57, v34\n"
"v_mfma_f32_16x16x32_bf16 v[68:71], a[76:79], v[226:229], v[68:71]\n"
"s_waitcnt lgkmcnt(4)\n"
"s_add_u32 s65, s65, s67\n"
"v_mfma_f32_16x16x32_bf16 v[40:43], a[80:83], v[190:193], v[40:43]\n"
"s_waitcnt vmcnt(8)\n"
"s_barrier\n"
"ds_read_b128 v[222:225], v6 offset:61440\n"
"v_mfma_f32_16x16x32_bf16 v[56:59], a[84:87], v[190:193], v[56:59]\n"
"s_mul_i32 s57, s41, 4\n"
"v_mfma_f32_16x16x32_bf16 v[44:47], a[80:83], v[194:197], v[44:47]\n"
"ds_read_b128 v[226:229], v6 offset:62464\n"
"s_add_u32 m0, s53, 126976\n"
"v_mfma_f32_16x16x32_bf16 v[60:63], a[84:87], v[194:197], v[60:63]\n"
"v_mfma_f32_16x16x32_bf16 v[48:51], a[80:83], v[198:201], v[48:51]\n"
"ds_read_b128 v[190:193], v6 offset:63488\n"
"s_add_u32 s57, s57, 159744\n"
"v_mfma_f32_16x16x32_bf16 v[64:67], a[84:87], v[198:201], v[64:67]\n"
"buffer_load_dwordx4 v2, s[28:31], s65 offen offset:0 sc1 lds\n"
"v_mfma_f32_16x16x32_bf16 v[52:55], a[80:83], v[202:205], v[52:55]\n"
"ds_read_b128 v[194:197], v6 offset:64512\n"
"v_mfma_f32_16x16x32_bf16 v[68:71], a[84:87], v[202:205], v[68:71]\n"
"s_waitcnt lgkmcnt(4)\n"
"v_bfe_u32 v15, v1, 0, 4\n"
"v_mfma_f32_16x16x32_bf16 v[40:43], a[88:91], v[206:209], v[40:43]\n"
"ds_read_b128 v[198:201], v7 offset:0\n"
"v_mfma_f32_16x16x32_bf16 v[56:59], a[92:95], v[206:209], v[56:59]\n"
"buffer_load_dwordx4 v2, s[28:31], s65 offen offset:1024 sc1 lds\n"
"v_mfma_f32_16x16x32_bf16 v[44:47], a[88:91], v[210:213], v[44:47]\n"
"ds_read_b128 v[202:205], v7 offset:1024\n"
"v_mfma_f32_16x16x32_bf16 v[60:63], a[92:95], v[210:213], v[60:63]\n"
"buffer_load_dwordx4 v2, s[28:31], s65 offen offset:2048 sc1 lds\n"
"v_mfma_f32_16x16x32_bf16 v[48:51], a[88:91], v[214:217], v[48:51]\n"
"ds_read_b128 v[206:209], v7 offset:2048\n"
"v_mul_u32_u24 v35, 16, v15\n"
"v_mfma_f32_16x16x32_bf16 v[64:67], a[92:95], v[214:217], v[64:67]\n"
"v_mfma_f32_16x16x32_bf16 v[52:55], a[88:91], v[218:221], v[52:55]\n"
"ds_read_b128 v[210:213], v7 offset:3072\n"
"buffer_load_dwordx4 v2, s[28:31], s65 offen offset:3072 sc1 lds\n"
"v_mfma_f32_16x16x32_bf16 v[68:71], a[92:95], v[218:221], v[68:71]\n"
"s_waitcnt lgkmcnt(4)\n"
"v_bfe_u32 v15, v1, 4, 1\n"
"v_mfma_f32_16x16x32_bf16 v[40:43], a[96:99], v[222:225], v[40:43]\n"
"ds_read_b128 v[214:217], v7 offset:4096\n"
"v_mfma_f32_16x16x32_bf16 v[56:59], a[100:103], v[222:225], v[56:59]\n"
"v_mfma_f32_16x16x32_bf16 v[44:47], a[96:99], v[226:229], v[44:47]\n"
"ds_read_b128 v[218:221], v7 offset:5120\n"
"v_lshl_add_u32 v35, v15, 8, v35\n"
"v_mfma_f32_16x16x32_bf16 v[60:63], a[100:103], v[226:229], v[60:63]\n"
"s_bfe_u32 s65, s68, 262172\n"
"s_mul_i32 s65, s65, 16384\n"
"v_mfma_f32_16x16x32_bf16 v[48:51], a[96:99], v[190:193], v[48:51]\n"
"ds_read_b128 v[222:225], v7 offset:6144\n"
"v_mfma_f32_16x16x32_bf16 v[64:67], a[100:103], v[190:193], v[64:67]\n"
"s_add_u32 s65, s65, s67\n"
"v_add_u32 v35, s57, v35\n"
"v_mfma_f32_16x16x32_bf16 v[52:55], a[96:99], v[194:197], v[52:55]\n"
"ds_read_b128 v[226:229], v7 offset:7168\n"
"v_mfma_f32_16x16x32_bf16 v[68:71], a[100:103], v[194:197], v[68:71]\n"
"s_waitcnt lgkmcnt(4)\n"
"v_mfma_f32_16x16x32_bf16 v[40:43], a[104:107], v[198:201], v[40:43]\n"
"ds_read_b128 v[190:193], v7 offset:8192\n"
"s_add_u32 m0, s53, 143360\n"
"v_mfma_f32_16x16x32_bf16 v[56:59], a[108:111], v[198:201], v[56:59]\n"
"v_bfe_u32 v15, v1, 0, 2\n"
"v_mfma_f32_16x16x32_bf16 v[44:47], a[104:107], v[202:205], v[44:47]\n"
"ds_read_b128 v[194:197], v7 offset:9216\n"
"v_mfma_f32_16x16x32_bf16 v[60:63], a[108:111], v[202:205], v[60:63]\n"
"buffer_load_dwordx4 v2, s[28:31], s65 offen offset:0 sc1 lds\n"
"v_mfma_f32_16x16x32_bf16 v[48:51], a[104:107], v[206:209], v[48:51]\n"
"ds_read_b128 v[198:201], v7 offset:10240\n"
"v_mul_u32_u24 v36, 16, v15\n"
"v_mfma_f32_16x16x32_bf16 v[64:67], a[108:111], v[206:209], v[64:67]\n"
"v_mfma_f32_16x16x32_bf16 v[52:55], a[104:107], v[210:213], v[52:55]\n"
"ds_read_b128 v[202:205], v7 offset:11264\n"
"v_mfma_f32_16x16x32_bf16 v[68:71], a[108:111], v[210:213], v[68:71]\n"
"s_waitcnt lgkmcnt(4)\n"
"v_mfma_f32_16x16x32_bf16 v[40:43], a[112:115], v[214:217], v[40:43]\n"
"s_waitcnt vmcnt(9)\n"
"s_barrier\n"
"ds_read_b128 v[206:209], v7 offset:28672\n"
"v_mfma_f32_16x16x32_bf16 v[56:59], a[116:119], v[214:217], v[56:59]\n"
"buffer_load_dwordx4 v2, s[28:31], s65 offen offset:1024 sc1 lds\n"
"v_mfma_f32_16x16x32_bf16 v[44:47], a[112:115], v[218:221], v[44:47]\n"
"ds_read_b128 v[210:213], v7 offset:29696\n"
"v_bfe_u32 v15, v1, 2, 2\n"
"v_mfma_f32_16x16x32_bf16 v[60:63], a[116:119], v[218:221], v[60:63]\n"
"buffer_load_dwordx4 v2, s[28:31], s65 offen offset:2048 sc1 lds\n"
"v_mfma_f32_16x16x32_bf16 v[48:51], a[112:115], v[222:225], v[48:51]\n"
"ds_read_b128 v[214:217], v7 offset:30720\n"
"v_lshl_add_u32 v36, v15, 7, v36\n"
"v_mfma_f32_16x16x32_bf16 v[64:67], a[116:119], v[222:225], v[64:67]\n"
"buffer_load_dwordx4 v2, s[28:31], s65 offen offset:3072 sc1 lds\n"
"v_mfma_f32_16x16x32_bf16 v[52:55], a[112:115], v[226:229], v[52:55]\n"
"ds_read_b128 v[218:221], v7 offset:31744\n"
"v_mfma_f32_16x16x32_bf16 v[68:71], a[116:119], v[226:229], v[68:71]\n"
"s_waitcnt lgkmcnt(4)\n"
"v_bfe_u32 v15, v1, 4, 1\n"
"v_mfma_f32_16x16x32_bf16 v[40:43], a[120:123], v[190:193], v[40:43]\n"
"ds_read_b128 v[222:225], v7 offset:32768\n"
"v_mfma_f32_16x16x32_bf16 v[56:59], a[124:127], v[190:193], v[56:59]\n"
"s_bfe_u32 s65, s81, 262144\n"
"s_mul_i32 s65, s65, 28672\n"
"v_mfma_f32_16x16x32_bf16 v[44:47], a[120:123], v[194:197], v[44:47]\n"
"ds_read_b128 v[226:229], v7 offset:33792\n"
"s_add_u32 s65, s65, s69\n"
"v_mfma_f32_16x16x32_bf16 v[60:63], a[124:127], v[194:197], v[60:63]\n"
"v_lshl_add_u32 v36, v15, 3, v36\n"
"v_mfma_f32_16x16x32_bf16 v[48:51], a[120:123], v[198:201], v[48:51]\n"
"ds_read_b128 v[190:193], v7 offset:34816\n"
"v_mfma_f32_16x16x32_bf16 v[64:67], a[124:127], v[198:201], v[64:67]\n"
"s_add_u32 m0, s54, 0\n"
"v_mfma_f32_16x16x32_bf16 v[52:55], a[120:123], v[202:205], v[52:55]\n"
"ds_read_b128 v[194:197], v7 offset:35840\n"
"s_lshr_b32 s59, s41, 1\n"
"v_mfma_f32_16x16x32_bf16 v[68:71], a[124:127], v[202:205], v[68:71]\n"
"s_waitcnt lgkmcnt(4)\n"
"v_mfma_f32_16x16x32_bf16 v[40:43], a[128:131], v[206:209], v[40:43]\n"
"ds_read_b128 v[198:201], v7 offset:36864\n"
"v_mfma_f32_16x16x32_bf16 v[56:59], a[132:135], v[206:209], v[56:59]\n"
"buffer_load_dwordx4 v2, s[28:31], s65 offen offset:0 sc1 lds\n"
"v_mfma_f32_16x16x32_bf16 v[44:47], a[128:131], v[210:213], v[44:47]\n"
"ds_read_b128 v[202:205], v7 offset:37888\n"
"s_and_b32 s60, s41, 1\n"
"v_mfma_f32_16x16x32_bf16 v[60:63], a[132:135], v[210:213], v[60:63]\n"
"buffer_load_dwordx4 v2, s[28:31], s65 offen offset:1024 sc1 lds\n"
"v_mfma_f32_16x16x32_bf16 v[48:51], a[128:131], v[214:217], v[48:51]\n"
"ds_read_b128 v[206:209], v7 offset:38912\n"
"v_mfma_f32_16x16x32_bf16 v[64:67], a[132:135], v[214:217], v[64:67]\n"
"s_mul_i32 s80, s46, 8192\n"
"v_mfma_f32_16x16x32_bf16 v[52:55], a[128:131], v[218:221], v[52:55]\n"
"ds_read_b128 v[210:213], v7 offset:39936\n"
"v_mfma_f32_16x16x32_bf16 v[68:71], a[132:135], v[218:221], v[68:71]\n"
"s_waitcnt lgkmcnt(4)\n"
"v_mfma_f32_16x16x32_bf16 v[40:43], a[136:139], v[222:225], v[40:43]\n"
"ds_read_b128 v[214:217], v7 offset:40960\n"
"s_mul_i32 s56, s59, 4096\n"
"v_mfma_f32_16x16x32_bf16 v[56:59], a[140:143], v[222:225], v[56:59]\n"
"buffer_load_dwordx4 v2, s[28:31], s65 offen offset:2048 sc1 lds\n"
"v_mfma_f32_16x16x32_bf16 v[44:47], a[136:139], v[226:229], v[44:47]\n"
"ds_read_b128 v[218:221], v7 offset:41984\n"
"v_mfma_f32_16x16x32_bf16 v[60:63], a[140:143], v[226:229], v[60:63]\n"
"buffer_load_dwordx4 v2, s[28:31], s65 offen offset:3072 sc1 lds\n"
"v_mfma_f32_16x16x32_bf16 v[48:51], a[136:139], v[190:193], v[48:51]\n"
"ds_read_b128 v[222:225], v7 offset:43008\n"
"s_add_u32 m0, s54, 4096\n"
"v_mfma_f32_16x16x32_bf16 v[64:67], a[140:143], v[190:193], v[64:67]\n"
"s_add_u32 s80, s80, s56\n"
"v_mfma_f32_16x16x32_bf16 v[52:55], a[136:139], v[194:197], v[52:55]\n"
"ds_read_b128 v[226:229], v7 offset:44032\n"
"v_mfma_f32_16x16x32_bf16 v[68:71], a[140:143], v[194:197], v[68:71]\n"
"s_waitcnt lgkmcnt(4)\n"
"s_mul_i32 s56, s47, 512\n"
"v_mfma_f32_16x16x32_bf16 v[40:43], a[144:147], v[198:201], v[40:43]\n"
"s_waitcnt vmcnt(12)\n"
"s_barrier\n"
"ds_read_b128 v[190:193], v7 offset:45056\n"
"v_mfma_f32_16x16x32_bf16 v[56:59], a[148:151], v[198:201], v[56:59]\n"
"buffer_load_dwordx4 v3, s[28:31], s65 offen offset:0 sc1 lds\n"
"v_mfma_f32_16x16x32_bf16 v[44:47], a[144:147], v[202:205], v[44:47]\n"
"ds_read_b128 v[194:197], v7 offset:46080\n"
"v_mfma_f32_16x16x32_bf16 v[60:63], a[148:151], v[202:205], v[60:63]\n"
"buffer_load_dwordx4 v3, s[28:31], s65 offen offset:1024 sc1 lds\n"
"v_mfma_f32_16x16x32_bf16 v[48:51], a[144:147], v[206:209], v[48:51]\n"
"ds_read_b128 v[198:201], v7 offset:47104\n"
"s_add_u32 s80, s80, s56\n"
"v_mfma_f32_16x16x32_bf16 v[64:67], a[148:151], v[206:209], v[64:67]\n"
"buffer_load_dwordx4 v3, s[28:31], s65 offen offset:2048 sc1 lds\n"
"v_mfma_f32_16x16x32_bf16 v[52:55], a[144:147], v[210:213], v[52:55]\n"
"ds_read_b128 v[202:205], v7 offset:48128\n"
"s_mul_i32 s56, s60, 64\n"
"v_mfma_f32_16x16x32_bf16 v[68:71], a[148:151], v[210:213], v[68:71]\n"
"s_waitcnt lgkmcnt(4)\n"
"v_mfma_f32_16x16x32_bf16 v[40:43], a[152:155], v[214:217], v[40:43]\n"
"ds_read_b128 v[206:209], v7 offset:49152\n"
"v_mfma_f32_16x16x32_bf16 v[56:59], a[156:159], v[214:217], v[56:59]\n"
"s_bfe_u32 s65, s81, 262148\n"
"s_mul_i32 s65, s65, 28672\n"
"v_mfma_f32_16x16x32_bf16 v[44:47], a[152:155], v[218:221], v[44:47]\n"
"ds_read_b128 v[210:213], v7 offset:50176\n"
"s_add_u32 s80, s80, s56\n"
"v_mfma_f32_16x16x32_bf16 v[60:63], a[156:159], v[218:221], v[60:63]\n"
"s_add_u32 s65, s65, s69\n"
"v_mfma_f32_16x16x32_bf16 v[48:51], a[152:155], v[222:225], v[48:51]\n"
"ds_read_b128 v[214:217], v7 offset:51200\n"
"v_mfma_f32_16x16x32_bf16 v[64:67], a[156:159], v[222:225], v[64:67]\n"
"s_add_u32 s80, s80, 262144\n"
"v_mfma_f32_16x16x32_bf16 v[52:55], a[152:155], v[226:229], v[52:55]\n"
"ds_read_b128 v[218:221], v7 offset:52224\n"
"s_add_u32 m0, s54, 28672\n"
"v_mfma_f32_16x16x32_bf16 v[68:71], a[156:159], v[226:229], v[68:71]\n"
"s_waitcnt lgkmcnt(4)\n"
"v_mfma_f32_16x16x32_bf16 v[40:43], a[160:163], v[190:193], v[40:43]\n"
"ds_read_b128 v[222:225], v7 offset:53248\n"
"v_mfma_f32_16x16x32_bf16 v[56:59], a[164:167], v[190:193], v[56:59]\n"
"buffer_load_dwordx4 v2, s[28:31], s65 offen offset:0 sc1 lds\n"
"v_mfma_f32_16x16x32_bf16 v[44:47], a[160:163], v[194:197], v[44:47]\n"
"ds_read_b128 v[226:229], v7 offset:54272\n"
"s_mul_i32 s70, s46, 512\n"
"v_mfma_f32_16x16x32_bf16 v[60:63], a[164:167], v[194:197], v[60:63]\n"
"v_mfma_f32_16x16x32_bf16 v[48:51], a[160:163], v[198:201], v[48:51]\n"
"ds_read_b128 v[190:193], v7 offset:55296\n"
"s_add_u32 s70, s70, 16384\n"
"v_mfma_f32_16x16x32_bf16 v[64:67], a[164:167], v[198:201], v[64:67]\n"
"buffer_load_dwordx4 v2, s[28:31], s65 offen offset:1024 sc1 lds\n"
"v_mfma_f32_16x16x32_bf16 v[52:55], a[160:163], v[202:205], v[52:55]\n"
"ds_read_b128 v[194:197], v7 offset:56320\n"
"v_mfma_f32_16x16x32_bf16 v[68:71], a[164:167], v[202:205], v[68:71]\n"
"s_waitcnt lgkmcnt(4)\n"
"v_mfma_f32_16x16x32_bf16 v[40:43], a[168:171], v[206:209], v[40:43]\n"
"ds_read_b128 v[198:201], v7 offset:57344\n"
"s_lshr_b32 s59, s50, 1\n"
"v_mfma_f32_16x16x32_bf16 v[56:59], a[172:175], v[206:209], v[56:59]\n"
"buffer_load_dwordx4 v2, s[28:31], s65 offen offset:2048 sc1 lds\n"
"v_mfma_f32_16x16x32_bf16 v[44:47], a[168:171], v[210:213], v[44:47]\n"
"ds_read_b128 v[202:205], v7 offset:58368\n"
"v_mfma_f32_16x16x32_bf16 v[60:63], a[172:175], v[210:213], v[60:63]\n"
"buffer_load_dwordx4 v2, s[28:31], s65 offen offset:3072 sc1 lds\n"
"v_mfma_f32_16x16x32_bf16 v[48:51], a[168:171], v[214:217], v[48:51]\n"
"ds_read_b128 v[206:209], v7 offset:59392\n"
"s_and_b32 s60, s49, 3\n"
"v_mfma_f32_16x16x32_bf16 v[64:67], a[172:175], v[214:217], v[64:67]\n"
"s_add_u32 m0, s54, 32768\n"
"v_mfma_f32_16x16x32_bf16 v[52:55], a[168:171], v[218:221], v[52:55]\n"
"ds_read_b128 v[210:213], v7 offset:60416\n"
"v_mfma_f32_16x16x32_bf16 v[68:71], a[172:175], v[218:221], v[68:71]\n"
"s_waitcnt lgkmcnt(4)\n"
"s_mul_i32 s71, s59, 512\n"
"v_mfma_f32_16x16x32_bf16 v[40:43], a[176:179], v[222:225], v[40:43]\n"
"s_waitcnt vmcnt(15)\n"
"s_barrier\n"
"ds_read_b128 v[214:217], v7 offset:61440\n"
"v_mfma_f32_16x16x32_bf16 v[56:59], a[180:183], v[222:225], v[56:59]\n"
"buffer_load_dwordx4 v3, s[28:31], s65 offen offset:0 sc1 lds\n"
"v_mfma_f32_16x16x32_bf16 v[44:47], a[176:179], v[226:229], v[44:47]\n"
"ds_read_b128 v[218:221], v7 offset:62464\n"
"s_mul_i32 s56, s60, 128\n"
"v_mfma_f32_16x16x32_bf16 v[60:63], a[180:183], v[226:229], v[60:63]\n"
"buffer_load_dwordx4 v3, s[28:31], s65 offen offset:1024 sc1 lds\n"
"v_mfma_f32_16x16x32_bf16 v[48:51], a[176:179], v[190:193], v[48:51]\n"
"ds_read_b128 v[222:225], v8 offset:0\n"
"v_mfma_f32_16x16x32_bf16 v[64:67], a[180:183], v[190:193], v[64:67]\n"
"v_mfma_f32_16x16x32_bf16 v[52:55], a[176:179], v[194:197], v[52:55]\n"
"ds_read_b128 v[226:229], v8 offset:1024\n"
"s_add_u32 s71, s71, s56\n"
"v_mfma_f32_16x16x32_bf16 v[68:71], a[180:183], v[194:197], v[68:71]\n"
"s_waitcnt lgkmcnt(4)\n"
"v_mfma_f32_16x16x32_bf16 v[40:43], a[184:187], v[198:201], v[40:43]\n"
"ds_read_b128 v[190:193], v8 offset:4096\n"
"v_mfma_f32_16x16x32_bf16 v[56:59], a[188:191], v[198:201], v[56:59]\n"
"buffer_load_dwordx4 v3, s[28:31], s65 offen offset:2048 sc1 lds\n"
"v_mfma_f32_16x16x32_bf16 v[44:47], a[184:187], v[202:205], v[44:47]\n"
"ds_read_b128 v[194:197], v8 offset:5120\n"
"s_add_u32 s71, s71, 16384\n"
"v_mfma_f32_16x16x32_bf16 v[60:63], a[188:191], v[202:205], v[60:63]\n"
"s_bfe_u32 s65, s81, 262152\n"
"s_mul_i32 s65, s65, 28672\n"
"v_mfma_f32_16x16x32_bf16 v[48:51], a[184:187], v[206:209], v[48:51]\n"
"ds_read_b128 v[198:201], v8 offset:8192\n"
"v_mfma_f32_16x16x32_bf16 v[64:67], a[188:191], v[206:209], v[64:67]\n"
"s_mul_i32 s66, s50, 65536\n"
"v_mfma_f32_16x16x32_bf16 v[52:55], a[184:187], v[210:213], v[52:55]\n"
"ds_read_b128 v[202:205], v8 offset:9216\n"
"s_add_u32 s65, s65, s69\n"
"v_mfma_f32_16x16x32_bf16 v[68:71], a[188:191], v[210:213], v[68:71]\n"
"s_waitcnt lgkmcnt(4)\n"
"v_mfma_f32_16x16x32_bf16 v[40:43], a[192:195], v[214:217], v[40:43]\n"
"s_waitcnt vmcnt(14)\n"
"s_barrier\n"
"ds_read_b128 v[206:209], v8 offset:12288\n"
"v_mfma_f32_16x16x32_bf16 v[56:59], a[196:199], v[214:217], v[56:59]\n"
"s_add_u32 m0, s54, 57344\n"
"s_add_u32 s66, s66, 9437184\n"
"v_mfma_f32_16x16x32_bf16 v[44:47], a[192:195], v[218:221], v[44:47]\n"
"ds_read_b128 v[210:213], v8 offset:13312\n"
"v_mfma_f32_16x16x32_bf16 v[60:63], a[196:199], v[218:221], v[60:63]\n"
"v_mfma_f32_16x16x32_bf16 v[40:43], a[200:203], v[222:225], v[40:43]\n"
"ds_read_b128 v[214:217], v8 offset:16384\n"
"v_lshlrev_b32 v12, 7, v1\n"
"v_mfma_f32_16x16x32_bf16 v[56:59], a[204:207], v[222:225], v[56:59]\n"
"buffer_load_dwordx4 v2, s[28:31], s65 offen offset:0 sc1 lds\n"
"v_mfma_f32_16x16x32_bf16 v[44:47], a[200:203], v[226:229], v[44:47]\n"
"ds_read_b128 v[218:221], v8 offset:17408\n"
"v_mfma_f32_16x16x32_bf16 v[60:63], a[204:207], v[226:229], v[60:63]\n"
"s_waitcnt lgkmcnt(4)\n"
"v_mfma_f32_16x16x32_bf16 v[40:43], a[208:211], v[190:193], v[40:43]\n"
"ds_read_b128 v[222:225], v8 offset:20480\n"
"v_mfma_f32_16x16x32_bf16 v[56:59], a[212:215], v[190:193], v[56:59]\n"
"buffer_load_dwordx4 v2, s[28:31], s65 offen offset:1024 sc1 lds\n"
"v_mfma_f32_16x16x32_bf16 v[44:47], a[208:211], v[194:197], v[44:47]\n"
"ds_read_b128 v[226:229], v8 offset:21504\n"
"v_mfma_f32_16x16x32_bf16 v[60:63], a[212:215], v[194:197], v[60:63]\n"
"buffer_load_dwordx4 v2, s[28:31], s65 offen offset:2048 sc1 lds\n"
"v_mfma_f32_16x16x32_bf16 v[40:43], a[216:219], v[198:201], v[40:43]\n"
"ds_read_b128 v[190:193], v8 offset:24576\n"
"v_mfma_f32_16x16x32_bf16 v[56:59], a[220:223], v[198:201], v[56:59]\n"
"buffer_load_dwordx4 v2, s[28:31], s65 offen offset:3072 sc1 lds\n"
"v_mfma_f32_16x16x32_bf16 v[44:47], a[216:219], v[202:205], v[44:47]\n"
"ds_read_b128 v[194:197], v8 offset:25600\n"
"v_mfma_f32_16x16x32_bf16 v[60:63], a[220:223], v[202:205], v[60:63]\n"
"s_waitcnt lgkmcnt(4)\n"
"v_mfma_f32_16x16x32_bf16 v[40:43], a[224:227], v[206:209], v[40:43]\n"
"ds_read_b128 v[198:201], v7 offset:63488\n"
"s_add_u32 m0, s54, 61440\n"
"v_mfma_f32_16x16x32_bf16 v[56:59], a[228:231], v[206:209], v[56:59]\n"
"v_mfma_f32_16x16x32_bf16 v[44:47], a[224:227], v[210:213], v[44:47]\n"
"ds_read_b128 v[202:205], v7 offset:64512\n"
"v_mfma_f32_16x16x32_bf16 v[60:63], a[228:231], v[210:213], v[60:63]\n"
"buffer_load_dwordx4 v3, s[28:31], s65 offen offset:0 sc1 lds\n"
"v_mfma_f32_16x16x32_bf16 v[40:43], a[232:235], v[214:217], v[40:43]\n"
"ds_read_b128 v[206:209], v8 offset:2048\n"
"v_mfma_f32_16x16x32_bf16 v[56:59], a[236:239], v[214:217], v[56:59]\n"
"v_mfma_f32_16x16x32_bf16 v[44:47], a[232:235], v[218:221], v[44:47]\n"
"ds_read_b128 v[210:213], v8 offset:3072\n"
"v_mfma_f32_16x16x32_bf16 v[60:63], a[236:239], v[218:221], v[60:63]\n"
"s_waitcnt lgkmcnt(4)\n"
"v_mfma_f32_16x16x32_bf16 v[40:43], a[240:243], v[222:225], v[40:43]\n"
"ds_read_b128 v[214:217], v8 offset:6144\n"
"v_mfma_f32_16x16x32_bf16 v[56:59], a[244:247], v[222:225], v[56:59]\n"
"buffer_load_dwordx4 v3, s[28:31], s65 offen offset:1024 sc1 lds\n"
"v_mfma_f32_16x16x32_bf16 v[44:47], a[240:243], v[226:229], v[44:47]\n"
"ds_read_b128 v[218:221], v8 offset:7168\n"
"v_mfma_f32_16x16x32_bf16 v[60:63], a[244:247], v[226:229], v[60:63]\n"
"buffer_load_dwordx4 v3, s[28:31], s65 offen offset:2048 sc1 lds\n"
"v_mfma_f32_16x16x32_bf16 v[40:43], a[248:251], v[190:193], v[40:43]\n"
"ds_read_b128 v[222:225], v8 offset:10240\n"
"v_mfma_f32_16x16x32_bf16 v[56:59], a[252:255], v[190:193], v[56:59]\n"
"v_mfma_f32_16x16x32_bf16 v[44:47], a[248:251], v[194:197], v[44:47]\n"
"ds_read_b128 v[226:229], v8 offset:11264\n"
"v_mfma_f32_16x16x32_bf16 v[60:63], a[252:255], v[194:197], v[60:63]\n"
"s_waitcnt lgkmcnt(4)\n"
"v_mfma_f32_16x16x32_bf16 v[48:51], a[192:195], v[198:201], v[48:51]\n"
"ds_read_b128 v[190:193], v8 offset:14336\n"
"v_mfma_f32_16x16x32_bf16 v[64:67], a[196:199], v[198:201], v[64:67]\n"
"v_mfma_f32_16x16x32_bf16 v[52:55], a[192:195], v[202:205], v[52:55]\n"
"ds_read_b128 v[194:197], v8 offset:15360\n"
"v_mfma_f32_16x16x32_bf16 v[68:71], a[196:199], v[202:205], v[68:71]\n"
"v_mfma_f32_16x16x32_bf16 v[48:51], a[200:203], v[206:209], v[48:51]\n"
"ds_read_b128 v[198:201], v8 offset:18432\n"
"v_cvt_pk_bf16_f32 v72, v40, v41\n"
"v_mfma_f32_16x16x32_bf16 v[64:67], a[204:207], v[206:209], v[64:67]\n"
"v_cvt_pk_bf16_f32 v73, v42, v43\n"
"v_mfma_f32_16x16x32_bf16 v[52:55], a[200:203], v[210:213], v[52:55]\n"
"ds_read_b128 v[202:205], v8 offset:19456\n"
"v_cvt_pk_bf16_f32 v74, v56, v57\n"
"v_mfma_f32_16x16x32_bf16 v[68:71], a[204:207], v[210:213], v[68:71]\n"
"s_waitcnt lgkmcnt(4)\n"
"v_mfma_f32_16x16x32_bf16 v[48:51], a[208:211], v[214:217], v[48:51]\n"
"ds_read_b128 v[206:209], v8 offset:22528\n"
"v_cvt_pk_bf16_f32 v75, v58, v59\n"
"v_mfma_f32_16x16x32_bf16 v[64:67], a[212:215], v[214:217], v[64:67]\n"
"buffer_store_dwordx4 v[72:75], v2, s[28:31], s64 offen offset:0 sc1\n"
"v_mfma_f32_16x16x32_bf16 v[52:55], a[208:211], v[218:221], v[52:55]\n"
".Lmk_pg_9:\n"
"s_cbranch_execz .Lmk_ret_2\n"
"ds_read_b128 v[210:213], v8 offset:23552\n"
"v_cvt_pk_bf16_f32 v76, v44, v45\n"
"v_mfma_f32_16x16x32_bf16 v[68:71], a[212:215], v[218:221], v[68:71]\n"
"v_mfma_f32_16x16x32_bf16 v[48:51], a[216:219], v[222:225], v[48:51]\n"
"ds_read_b128 v[214:217], v8 offset:26624\n"
"v_cvt_pk_bf16_f32 v77, v46, v47\n"
"v_mfma_f32_16x16x32_bf16 v[64:67], a[220:223], v[222:225], v[64:67]\n"
"v_cvt_pk_bf16_f32 v78, v60, v61\n"
"v_mfma_f32_16x16x32_bf16 v[52:55], a[216:219], v[226:229], v[52:55]\n"
"ds_read_b128 v[218:221], v8 offset:27648\n"
"v_cvt_pk_bf16_f32 v79, v62, v63\n"
"v_mfma_f32_16x16x32_bf16 v[68:71], a[220:223], v[226:229], v[68:71]\n"
"s_waitcnt lgkmcnt(4)\n"
"v_mfma_f32_16x16x32_bf16 v[48:51], a[224:227], v[190:193], v[48:51]\n"
"buffer_store_dwordx4 v[76:79], v2, s[28:31], s64 offen offset:1024 sc1\n"
"v_mfma_f32_16x16x32_bf16 v[64:67], a[228:231], v[190:193], v[64:67]\n"
"s_add_u32 s64, s64, 65536\n"
"v_mfma_f32_16x16x32_bf16 v[52:55], a[224:227], v[194:197], v[52:55]\n"
"v_mfma_f32_16x16x32_bf16 v[68:71], a[228:231], v[194:197], v[68:71]\n"
"v_mfma_f32_16x16x32_bf16 v[48:51], a[232:235], v[198:201], v[48:51]\n"
"v_mfma_f32_16x16x32_bf16 v[64:67], a[236:239], v[198:201], v[64:67]\n"
"v_mfma_f32_16x16x32_bf16 v[52:55], a[232:235], v[202:205], v[52:55]\n"
"v_mfma_f32_16x16x32_bf16 v[68:71], a[236:239], v[202:205], v[68:71]\n"
"s_waitcnt lgkmcnt(0)\n"
"v_mfma_f32_16x16x32_bf16 v[48:51], a[240:243], v[206:209], v[48:51]\n"
"v_mfma_f32_16x16x32_bf16 v[64:67], a[244:247], v[206:209], v[64:67]\n"
"v_mfma_f32_16x16x32_bf16 v[52:55], a[240:243], v[210:213], v[52:55]\n"
"v_mfma_f32_16x16x32_bf16 v[68:71], a[244:247], v[210:213], v[68:71]\n"
"v_mfma_f32_16x16x32_bf16 v[48:51], a[248:251], v[214:217], v[48:51]\n"
"v_mfma_f32_16x16x32_bf16 v[64:67], a[252:255], v[214:217], v[64:67]\n"
"v_mfma_f32_16x16x32_bf16 v[52:55], a[248:251], v[218:221], v[52:55]\n"
"v_mfma_f32_16x16x32_bf16 v[68:71], a[252:255], v[218:221], v[68:71]\n"
"v_mfma_f32_16x16x32_bf16 v[40:43], v[72:75], v[72:75], 0\n"
"v_mfma_f32_16x16x32_bf16 v[56:59], v[252:255], v[72:75], 0\n"
"v_mfma_f32_16x16x32_bf16 v[44:47], v[76:79], v[76:79], 0\n"
"v_mfma_f32_16x16x32_bf16 v[60:63], v[252:255], v[76:79], 0\n"
"s_nop 0\n"
"v_cvt_pk_bf16_f32 v80, v48, v49\n"
"v_cvt_pk_bf16_f32 v81, v50, v51\n"
"v_cvt_pk_bf16_f32 v82, v64, v65\n"
"v_cvt_pk_bf16_f32 v83, v66, v67\n"
"buffer_store_dwordx4 v[80:83], v2, s[28:31], s64 offen offset:0 sc1\n"
"v_cvt_pk_bf16_f32 v84, v52, v53\n"
"v_cvt_pk_bf16_f32 v85, v54, v55\n"
"v_cvt_pk_bf16_f32 v86, v68, v69\n"
"v_cvt_pk_bf16_f32 v87, v70, v71\n"
"buffer_store_dwordx4 v[84:87], v2, s[28:31], s64 offen offset:1024 sc1\n"
"v_mfma_f32_16x16x32_bf16 v[48:51], v[80:83], v[80:83], 0\n"
"v_mfma_f32_16x16x32_bf16 v[64:67], v[252:255], v[80:83], 0\n"
"v_mfma_f32_16x16x32_bf16 v[52:55], v[84:87], v[84:87], 0\n"
"v_mfma_f32_16x16x32_bf16 v[68:71], v[252:255], v[84:87], 0\n"
"v_cndmask_b32 v240, v40, v41, s[74:75]\n"
"v_cndmask_b32 v240, v240, v42, s[76:77]\n"
"v_cndmask_b32 v240, v240, v43, s[78:79]\n"
"v_mov_b32 v236, v56\n"
"v_cndmask_b32 v241, v44, v45, s[74:75]\n"
"v_cndmask_b32 v241, v241, v46, s[76:77]\n"
"v_cndmask_b32 v241, v241, v47, s[78:79]\n"
"v_mov_b32 v237, v60\n"
"v_cndmask_b32 v242, v48, v49, s[74:75]\n"
"v_cndmask_b32 v242, v242, v50, s[76:77]\n"
"v_cndmask_b32 v242, v242, v51, s[78:79]\n"
"v_mov_b32 v238, v64\n"
"v_cndmask_b32 v243, v52, v53, s[74:75]\n"
"v_cndmask_b32 v243, v243, v54, s[76:77]\n"
"v_cndmask_b32 v243, v243, v55, s[78:79]\n"
"v_mov_b32 v239, v68\n"
"s_mov_b32 exec_lo, 0x00f0000f\n"
"s_mov_b32 exec_hi, 0xf0000f00\n"
"ds_write_b128 v34, v[236:239]\n"
"ds_write_b128 v34, v[240:243] offset:256\n"
"s_mov_b64 exec, -1\n"
"s_waitcnt lgkmcnt(0)\n"
"s_waitcnt vmcnt(0)\n"
"s_barrier\n"
"s_cmp_lg_u32 s41, 0\n"
"s_cbranch_scc1 .Lmk_sig_23\n"
"s_mov_b64 exec, 15\n"
"buffer_atomic_add v10, v12, s[28:31], s70 offen sc1\n"
"s_mov_b64 exec, -1\n"
".Lmk_sig_23:\n"
"ds_read_b32 v236, v35 offset:0\n"
"ds_read_b32 v237, v35 offset:512\n"
"ds_read_b32 v238, v35 offset:1024\n"
"ds_read_b32 v239, v35 offset:1536\n"
"s_waitcnt lgkmcnt(2)\n"
"v_add_f32 v236, v236, v237\n"
"s_waitcnt lgkmcnt(0)\n"
"v_add_f32 v238, v238, v239\n"
"v_add_f32 v236, v236, v238\n"
"v_mov_b32 v237, s26\n"
"v_mov_b32 v11, 0\n"
"s_mov_b64 exec, 1\n"
"buffer_atomic_add v11, v9, s[28:31], s71 offen sc0 sc1\n"
"s_mov_b64 exec, -1\n"
"s_mov_b32 exec_hi, 0\n"
"buffer_store_dwordx2 v[236:237], v36, s[28:31], s80 offen sc1\n"
"s_mov_b64 exec, -1\n"
"v_add_u32 v16, s53, v8\n"
"ds_write_b128 v16, v[72:75] offset:12288\n"
"ds_write_b128 v16, v[76:79] offset:13312\n"
"ds_write_b128 v16, v[80:83] offset:14336\n"
"ds_write_b128 v16, v[84:87] offset:15360\n"
"s_bfe_u32 s65, s81, 262156\n"
"s_mul_i32 s65, s65, 28672\n"
"s_add_u32 s65, s65, s69\n"
"s_add_u32 m0, s54, 86016\n"
"s_nop 0\n"
"buffer_load_dwordx4 v2, s[28:31], s65 offen offset:0 sc1 lds\n"
"buffer_load_dwordx4 v2, s[28:31], s65 offen offset:1024 sc1 lds\n"
"buffer_load_dwordx4 v2, s[28:31], s65 offen offset:2048 sc1 lds\n"
"buffer_load_dwordx4 v2, s[28:31], s65 offen offset:3072 sc1 lds\n"
"s_add_u32 m0, s54, 90112\n"
"s_nop 0\n"
"buffer_load_dwordx4 v3, s[28:31], s65 offen offset:0 sc1 lds\n"
"buffer_load_dwordx4 v3, s[28:31], s65 offen offset:1024 sc1 lds\n"
"buffer_load_dwordx4 v3, s[28:31], s65 offen offset:2048 sc1 lds\n"
"s_mov_b32 s82, 2253603344\n"
"s_mov_b32 s83, 4275817111\n"
"s_mul_i32 s62, s48, 1\n"
"s_mul_i32 s56, s49, 1\n"
"s_add_u32 s62, s62, s56\n"
"s_add_u32 s62, s62, 0\n"
"s_and_b32 s62, s62, 3\n"
"s_cmp_eq_u32 s62, 1\n"
"s_cselect_b32 s82, 1984246273, s82\n"
"s_cselect_b32 s83, 4274895497, s83\n"
"s_cmp_eq_u32 s62, 2\n"
"s_cselect_b32 s82, 1969500448, s82\n"
"s_cselect_b32 s83, 4260149672, s83\n"
"s_cmp_eq_u32 s62, 3\n"
"s_cselect_b32 s82, 1733567248, s82\n"
"s_cselect_b32 s83, 4024216472, s83\n"
"s_mov_b32 s72, 2253603344\n"
"s_mov_b32 s73, 4275817111\n"
"s_mul_i32 s62, s48, 1\n"
"s_mul_i32 s56, s49, 1\n"
"s_add_u32 s62, s62, s56\n"
"s_add_u32 s62, s62, 0\n"
"s_and_b32 s62, s62, 3\n"
"s_cmp_eq_u32 s62, 1\n"
"s_cselect_b32 s72, 1984246273, s72\n"
"s_cselect_b32 s73, 4274895497, s73\n"
"s_cmp_eq_u32 s62, 2\n"
"s_cselect_b32 s72, 1969500448, s72\n"
"s_cselect_b32 s73, 4260149672, s73\n"
"s_cmp_eq_u32 s62, 3\n"
"s_cselect_b32 s72, 1733567248, s72\n"
"s_cselect_b32 s73, 4024216472, s73\n"
"s_lshl_b32 s62, s48, 2\n"
"s_lshr_b64 s[72:73], s[72:73], s62\n"
"s_and_b32 s62, s72, 15\n"
"s_lshl_b32 s62, s62, 2\n"
"s_bfm_b64 s[72:73], s62, 0\n"
"s_and_b64 s[56:57], s[82:83], s[72:73]\n"
"s_lshl_b64 s[56:57], s[56:57], 4\n"
"s_lshl_b64 s[72:73], s[72:73], 4\n"
"s_or_b32 s72, s72, 15\n"
"s_andn2_b64 s[82:83], s[82:83], s[72:73]\n"
"s_or_b64 s[82:83], s[82:83], s[56:57]\n"
"s_or_b32 s82, s82, s48\n"
"s_mov_b32 s84, 1985167888\n"
"s_mul_i32 s62, s51, 1\n"
"s_mul_i32 s56, s52, 1\n"
"s_add_u32 s62, s62, s56\n"
"s_add_u32 s62, s62, 0\n"
"s_and_b32 s62, s62, 3\n"
"s_cmp_eq_u32 s62, 1\n"
"s_cselect_b32 s84, 1984246273, s84\n"
"s_cmp_eq_u32 s62, 2\n"
"s_cselect_b32 s84, 1969500448, s84\n"
"s_cmp_eq_u32 s62, 3\n"
"s_cselect_b32 s84, 1733567248, s84\n"
"s_mov_b64 exec, 0\n"
"s_cmp_eq_u32 s41, 0\n"
"s_cbranch_scc1 .Lmk_pg_12\n"
"s_cmp_eq_u32 s41, 1\n"
"s_cbranch_scc1 .Lmk_pg_13\n"
"s_cmp_eq_u32 s41, 2\n"
"s_cbranch_scc1 .Lmk_pg_14\n"
"s_branch .Lmk_pg_15\n"
".Lmk_ret_3:\n"
"s_mov_b64 exec, -1\n"
"s_nop 0\n"
"s_waitcnt vmcnt(8)\n"
"v_readfirstlane_b32 s61, v11\n"
"s_nop 0\n"
"s_cmp_ge_u32 s61, 8\n"
"s_cbranch_scc0 .Lmk_spin_24\n"
".Lmk_back_25:\n"
"s_bfe_u32 s64, s81, 262144\n"
"s_mul_i32 s64, s64, 8192\n"
"s_add_u32 s64, s64, s66\n"
"buffer_load_dwordx4 a[0:3], v2, s[28:31], s64 offen offset:0 sc1\n"
"buffer_load_dwordx4 a[4:7], v2, s[28:31], s64 offen offset:1024 sc1\n"
"buffer_load_dwordx4 a[8:11], v2, s[28:31], s64 offen offset:2048 sc1\n"
"buffer_load_dwordx4 a[12:15], v2, s[28:31], s64 offen offset:3072 sc1\n"
"buffer_load_dwordx4 a[16:19], v3, s[28:31], s64 offen offset:0 sc1\n"
"buffer_load_dwordx4 a[20:23], v3, s[28:31], s64 offen offset:1024 sc1\n"
"buffer_load_dwordx4 a[24:27], v3, s[28:31], s64 offen offset:2048 sc1\n"
"buffer_load_dwordx4 a[28:31], v3, s[28:31], s64 offen offset:3072 sc1\n"
"s_barrier\n"
"ds_read_b128 v[190:193], v6 offset:0\n"
"ds_read_b128 v[194:197], v6 offset:1024\n"
"ds_read_b128 v[198:201], v6 offset:2048\n"
"ds_read_b128 v[202:205], v6 offset:3072\n"
"ds_read_b128 v[206:209], v6 offset:4096\n"
"ds_read_b128 v[210:213], v6 offset:5120\n"
"ds_read_b128 v[214:217], v6 offset:6144\n"
"ds_read_b128 v[218:221], v6 offset:7168\n"
"s_waitcnt lgkmcnt(7)\n"
"v_max3_f32 v22, abs(v190), abs(v191), abs(v192)\n"
"v_max_f32 v22, v22, abs(v193)\n"
"v_mul_f32 v22, 0x42c00000, v22\n"
"s_waitcnt vmcnt(7)\n"
"v_accvgpr_read_b32 v18, a0\n"
"v_accvgpr_read_b32 v19, a1\n"
"v_accvgpr_read_b32 v20, a2\n"
"v_accvgpr_read_b32 v21, a3\n"
"v_max3_f32 v19, abs(v18), abs(v19), abs(v20)\n"
"v_max_f32 v23, v19, abs(v21)\n"
"v_mul_f32 v18, v22, v23\n"
"v_mul_f32 v19, v22, v23\n"
"v_mul_f32 v20, v22, v23\n"
"v_mul_f32 v21, v22, v23\n"
"s_nop 1\n"
"v_mfma_f32_16x16x32_bf16 v[30:33], a[0:3], v[190:193], v[18:21]\n"
"ds_read_b128 v[222:225], v6 offset:8192\n"
"s_waitcnt vmcnt(6)\n"
"v_mfma_f32_16x16x32_bf16 v[34:37], a[4:7], v[190:193], v[18:21]\n"
"s_bfe_u32 s64, s81, 262148\n"
"s_mul_i32 s64, s64, 8192\n"
"s_waitcnt lgkmcnt(7)\n"
"v_mfma_f32_16x16x32_bf16 v[38:41], a[0:3], v[194:197], v[18:21]\n"
"ds_read_b128 v[226:229], v6 offset:9216\n"
"s_add_u32 s64, s64, s66\n"
"v_mfma_f32_16x16x32_bf16 v[42:45], a[4:7], v[194:197], v[18:21]\n"
"buffer_load_dwordx4 a[32:35], v2, s[28:31], s64 offen offset:0 sc1\n"
"s_waitcnt lgkmcnt(6)\n"
"v_mfma_f32_16x16x32_bf16 v[46:49], a[0:3], v[198:201], v[18:21]\n"
"ds_read_b128 v[190:193], v6 offset:10240\n"
"v_mfma_f32_16x16x32_bf16 v[50:53], a[4:7], v[198:201], v[18:21]\n"
"buffer_load_dwordx4 a[36:39], v2, s[28:31], s64 offen offset:1024 sc1\n"
"v_mfma_f32_16x16x32_bf16 v[54:57], a[0:3], v[202:205], v[18:21]\n"
"ds_read_b128 v[194:197], v6 offset:11264\n"
"v_mfma_f32_16x16x32_bf16 v[58:61], a[4:7], v[202:205], v[18:21]\n"
"s_waitcnt lgkmcnt(4)\n"
"v_mfma_f32_16x16x32_bf16 v[62:65], a[0:3], v[206:209], v[18:21]\n"
"ds_read_b128 v[198:201], v6 offset:12288\n"
"v_mfma_f32_16x16x32_bf16 v[66:69], a[4:7], v[206:209], v[18:21]\n"
"buffer_load_dwordx4 a[40:43], v2, s[28:31], s64 offen offset:2048 sc1\n"
"v_mfma_f32_16x16x32_bf16 v[70:73], a[0:3], v[210:213], v[18:21]\n"
"ds_read_b128 v[202:205], v6 offset:13312\n"
"v_mfma_f32_16x16x32_bf16 v[74:77], a[4:7], v[210:213], v[18:21]\n"
"v_mfma_f32_16x16x32_bf16 v[78:81], a[0:3], v[214:217], v[18:21]\n"
"ds_read_b128 v[206:209], v6 offset:14336\n"
"v_mfma_f32_16x16x32_bf16 v[82:85], a[4:7], v[214:217], v[18:21]\n"
"buffer_load_dwordx4 a[44:47], v2, s[28:31], s64 offen offset:3072 sc1\n"
"s_waitcnt vmcnt(9)\n"
"v_mfma_f32_16x16x32_bf16 v[30:33], a[8:11], v[218:221], v[30:33]\n"
"ds_read_b128 v[210:213], v6 offset:15360\n"
"s_waitcnt vmcnt(8)\n"
"v_mfma_f32_16x16x32_bf16 v[34:37], a[12:15], v[218:221], v[34:37]\n"
"s_waitcnt lgkmcnt(4)\n"
"v_mfma_f32_16x16x32_bf16 v[38:41], a[8:11], v[222:225], v[38:41]\n"
"ds_read_b128 v[214:217], v6 offset:16384\n"
"v_mfma_f32_16x16x32_bf16 v[42:45], a[12:15], v[222:225], v[42:45]\n"
"v_mfma_f32_16x16x32_bf16 v[46:49], a[8:11], v[226:229], v[46:49]\n"
"ds_read_b128 v[218:221], v6 offset:17408\n"
"v_mfma_f32_16x16x32_bf16 v[50:53], a[12:15], v[226:229], v[50:53]\n"
"buffer_load_dwordx4 a[48:51], v3, s[28:31], s64 offen offset:0 sc1\n"
"v_mfma_f32_16x16x32_bf16 v[54:57], a[8:11], v[190:193], v[54:57]\n"
"ds_read_b128 v[222:225], v6 offset:18432\n"
"v_mfma_f32_16x16x32_bf16 v[58:61], a[12:15], v[190:193], v[58:61]\n"
"v_mfma_f32_16x16x32_bf16 v[62:65], a[8:11], v[194:197], v[62:65]\n"
"ds_read_b128 v[226:229], v6 offset:19456\n"
"v_mfma_f32_16x16x32_bf16 v[66:69], a[12:15], v[194:197], v[66:69]\n"
"s_waitcnt lgkmcnt(4)\n"
"v_mfma_f32_16x16x32_bf16 v[70:73], a[8:11], v[198:201], v[70:73]\n"
"ds_read_b128 v[190:193], v6 offset:20480\n"
"v_mfma_f32_16x16x32_bf16 v[74:77], a[12:15], v[198:201], v[74:77]\n"
"buffer_load_dwordx4 a[52:55], v3, s[28:31], s64 offen offset:1024 sc1\n"
"v_mfma_f32_16x16x32_bf16 v[78:81], a[8:11], v[202:205], v[78:81]\n"
"ds_read_b128 v[194:197], v6 offset:21504\n"
"v_mfma_f32_16x16x32_bf16 v[82:85], a[12:15], v[202:205], v[82:85]\n"
"buffer_load_dwordx4 a[56:59], v3, s[28:31], s64 offen offset:2048 sc1\n"
"s_waitcnt vmcnt(10)\n"
"v_mfma_f32_16x16x32_bf16 v[30:33], a[16:19], v[206:209], v[30:33]\n"
"ds_read_b128 v[198:201], v6 offset:22528\n"
"s_waitcnt vmcnt(9)\n"
"v_mfma_f32_16x16x32_bf16 v[34:37], a[20:23], v[206:209], v[34:37]\n"
"v_mfma_f32_16x16x32_bf16 v[38:41], a[16:19], v[210:213], v[38:41]\n"
"ds_read_b128 v[202:205], v6 offset:23552\n"
"v_mfma_f32_16x16x32_bf16 v[42:45], a[20:23], v[210:213], v[42:45]\n"
"s_waitcnt lgkmcnt(4)\n"
"v_mfma_f32_16x16x32_bf16 v[46:49], a[16:19], v[214:217], v[46:49]\n"
"ds_read_b128 v[206:209], v6 offset:24576\n"
"v_mfma_f32_16x16x32_bf16 v[50:53], a[20:23], v[214:217], v[50:53]\n"
"buffer_load_dwordx4 a[60:63], v3, s[28:31], s64 offen offset:3072 sc1\n"
"v_mfma_f32_16x16x32_bf16 v[54:57], a[16:19], v[218:221], v[54:57]\n"
"ds_read_b128 v[210:213], v6 offset:25600\n"
"v_mfma_f32_16x16x32_bf16 v[58:61], a[20:23], v[218:221], v[58:61]\n"
"s_bfe_u32 s64, s81, 262152\n"
"s_mul_i32 s64, s64, 8192\n"
"v_mfma_f32_16x16x32_bf16 v[62:65], a[16:19], v[222:225], v[62:65]\n"
"ds_read_b128 v[214:217], v6 offset:26624\n"
"s_add_u32 s64, s64, s66\n"
"v_mfma_f32_16x16x32_bf16 v[66:69], a[20:23], v[222:225], v[66:69]\n"
"buffer_load_dwordx4 a[64:67], v2, s[28:31], s64 offen offset:0 sc1\n"
"v_mfma_f32_16x16x32_bf16 v[70:73], a[16:19], v[226:229], v[70:73]\n"
"ds_read_b128 v[218:221], v6 offset:27648\n"
"v_mfma_f32_16x16x32_bf16 v[74:77], a[20:23], v[226:229], v[74:77]\n"
"s_waitcnt lgkmcnt(4)\n"
"v_mfma_f32_16x16x32_bf16 v[78:81], a[16:19], v[190:193], v[78:81]\n"
"s_barrier\n"
"ds_read_b128 v[222:225], v6 offset:28672\n"
"v_mfma_f32_16x16x32_bf16 v[82:85], a[20:23], v[190:193], v[82:85]\n"
"buffer_load_dwordx4 a[68:71], v2, s[28:31], s64 offen offset:1024 sc1\n"
"s_waitcnt vmcnt(10)\n"
"v_mfma_f32_16x16x32_bf16 v[30:33], a[24:27], v[194:197], v[30:33]\n"
"ds_read_b128 v[226:229], v6 offset:29696\n"
"v_mfma_f32_16x16x32_bf16 v[34:37], a[28:31], v[194:197], v[34:37]\n"
"v_mfma_f32_16x16x32_bf16 v[38:41], a[24:27], v[198:201], v[38:41]\n"
"ds_read_b128 v[190:193], v6 offset:30720\n"
"v_mfma_f32_16x16x32_bf16 v[42:45], a[28:31], v[198:201], v[42:45]\n"
"buffer_load_dwordx4 a[72:75], v2, s[28:31], s64 offen offset:2048 sc1\n"
"v_mfma_f32_16x16x32_bf16 v[46:49], a[24:27], v[202:205], v[46:49]\n"
"ds_read_b128 v[194:197], v6 offset:31744\n"
"v_mfma_f32_16x16x32_bf16 v[50:53], a[28:31], v[202:205], v[50:53]\n"
"s_waitcnt lgkmcnt(4)\n"
"v_mfma_f32_16x16x32_bf16 v[54:57], a[24:27], v[206:209], v[54:57]\n"
"ds_read_b128 v[198:201], v6 offset:32768\n"
"v_mfma_f32_16x16x32_bf16 v[58:61], a[28:31], v[206:209], v[58:61]\n"
"buffer_load_dwordx4 a[76:79], v2, s[28:31], s64 offen offset:3072 sc1\n"
"v_mfma_f32_16x16x32_bf16 v[62:65], a[24:27], v[210:213], v[62:65]\n"
"ds_read_b128 v[202:205], v6 offset:33792\n"
"v_mfma_f32_16x16x32_bf16 v[66:69], a[28:31], v[210:213], v[66:69]\n"
"v_mfma_f32_16x16x32_bf16 v[70:73], a[24:27], v[214:217], v[70:73]\n"
"ds_read_b128 v[206:209], v6 offset:34816\n"
"v_mfma_f32_16x16x32_bf16 v[74:77], a[28:31], v[214:217], v[74:77]\n"
"buffer_load_dwordx4 a[80:83], v3, s[28:31], s64 offen offset:0 sc1\n"
"v_mfma_f32_16x16x32_bf16 v[78:81], a[24:27], v[218:221], v[78:81]\n"
"ds_read_b128 v[210:213], v6 offset:35840\n"
"v_mfma_f32_16x16x32_bf16 v[82:85], a[28:31], v[218:221], v[82:85]\n"
"s_waitcnt lgkmcnt(4)\n"
"s_waitcnt vmcnt(11)\n"
"v_mfma_f32_16x16x32_bf16 v[30:33], a[32:35], v[222:225], v[30:33]\n"
"ds_read_b128 v[214:217], v6 offset:36864\n"
"v_mfma_f32_16x16x32_bf16 v[34:37], a[36:39], v[222:225], v[34:37]\n"
"buffer_load_dwordx4 a[84:87], v3, s[28:31], s64 offen offset:1024 sc1\n"
"v_mfma_f32_16x16x32_bf16 v[38:41], a[32:35], v[226:229], v[38:41]\n"
"ds_read_b128 v[218:221], v6 offset:37888\n"
"v_mfma_f32_16x16x32_bf16 v[42:45], a[36:39], v[226:229], v[42:45]\n"
"v_mfma_f32_16x16x32_bf16 v[46:49], a[32:35], v[190:193], v[46:49]\n"
"ds_read_b128 v[222:225], v6 offset:38912\n"
"s_mul_i32 s80, s50, 4096\n"
"v_mfma_f32_16x16x32_bf16 v[50:53], a[36:39], v[190:193], v[50:53]\n"
"buffer_load_dwordx4 a[88:91], v3, s[28:31], s64 offen offset:2048 sc1\n"
"v_mfma_f32_16x16x32_bf16 v[54:57], a[32:35], v[194:197], v[54:57]\n"
"ds_read_b128 v[226:229], v6 offset:39936\n"
"v_mfma_f32_16x16x32_bf16 v[58:61], a[36:39], v[194:197], v[58:61]\n"
"s_waitcnt lgkmcnt(4)\n"
"s_add_u32 s80, s80, 262144\n"
"v_mfma_f32_16x16x32_bf16 v[62:65], a[32:35], v[198:201], v[62:65]\n"
"ds_read_b128 v[190:193], v6 offset:40960\n"
"v_mfma_f32_16x16x32_bf16 v[66:69], a[36:39], v[198:201], v[66:69]\n"
"buffer_load_dwordx4 a[92:95], v3, s[28:31], s64 offen offset:3072 sc1\n"
"v_mfma_f32_16x16x32_bf16 v[70:73], a[32:35], v[202:205], v[70:73]\n"
"ds_read_b128 v[194:197], v6 offset:41984\n"
"v_mfma_f32_16x16x32_bf16 v[74:77], a[36:39], v[202:205], v[74:77]\n"
"s_mov_b32 s86, 0xbdd2d3e8\n"
"v_mfma_f32_16x16x32_bf16 v[78:81], a[32:35], v[206:209], v[78:81]\n"
"ds_read_b128 v[198:201], v6 offset:43008\n"
"v_mfma_f32_16x16x32_bf16 v[82:85], a[36:39], v[206:209], v[82:85]\n"
"s_bfe_u32 s64, s81, 262156\n"
"s_mul_i32 s64, s64, 8192\n"
"s_waitcnt vmcnt(11)\n"
"v_mfma_f32_16x16x32_bf16 v[30:33], a[40:43], v[210:213], v[30:33]\n"
"ds_read_b128 v[202:205], v6 offset:44032\n"
"s_add_u32 s64, s64, s66\n"
"v_mfma_f32_16x16x32_bf16 v[34:37], a[44:47], v[210:213], v[34:37]\n"
"s_waitcnt lgkmcnt(4)\n"
"s_mov_b32 s87, s86\n"
"v_mfma_f32_16x16x32_bf16 v[38:41], a[40:43], v[214:217], v[38:41]\n"
"ds_read_b128 v[206:209], v6 offset:45056\n"
"v_mfma_f32_16x16x32_bf16 v[42:45], a[44:47], v[214:217], v[42:45]\n"
"buffer_load_dwordx4 a[96:99], v2, s[28:31], s64 offen offset:0 sc1\n"
"v_mfma_f32_16x16x32_bf16 v[46:49], a[40:43], v[218:221], v[46:49]\n"
"ds_read_b128 v[210:213], v6 offset:46080\n"
"v_mfma_f32_16x16x32_bf16 v[50:53], a[44:47], v[218:221], v[50:53]\n"
"buffer_load_dwordx4 a[100:103], v2, s[28:31], s64 offen offset:1024 sc1\n"
"v_mfma_f32_16x16x32_bf16 v[54:57], a[40:43], v[222:225], v[54:57]\n"
"ds_read_b128 v[214:217], v6 offset:47104\n"
"v_mov_b32 v178, 0xc0135761\n"
"v_mfma_f32_16x16x32_bf16 v[58:61], a[44:47], v[222:225], v[58:61]\n"
"v_mfma_f32_16x16x32_bf16 v[62:65], a[40:43], v[226:229], v[62:65]\n"
"ds_read_b128 v[218:221], v6 offset:48128\n"
"v_mov_b32 v179, 0xc0135761\n"
"v_mfma_f32_16x16x32_bf16 v[66:69], a[44:47], v[226:229], v[66:69]\n"
"s_waitcnt lgkmcnt(4)\n"
"v_mfma_f32_16x16x32_bf16 v[70:73], a[40:43], v[190:193], v[70:73]\n"
"ds_read_b128 v[222:225], v6 offset:49152\n"
"v_mfma_f32_16x16x32_bf16 v[74:77], a[44:47], v[190:193], v[74:77]\n"
"buffer_load_dwordx4 a[104:107], v2, s[28:31], s64 offen offset:2048 sc1\n"
"v_mfma_f32_16x16x32_bf16 v[78:81], a[40:43], v[194:197], v[78:81]\n"
"ds_read_b128 v[226:229], v6 offset:50176\n"
"ds_read_b32 v162, v230 offset:0\n"
"v_mfma_f32_16x16x32_bf16 v[82:85], a[44:47], v[194:197], v[82:85]\n"
"s_bfe_u32 s65, s81, 262160\n"
"s_mul_i32 s65, s65, 28672\n"
"v_mfma_f32_16x16x32_bf16 v[30:33], a[48:51], v[198:201], v[30:33]\n"
"ds_read_b128 v[190:193], v6 offset:51200\n"
"s_add_u32 s65, s65, s69\n"
"s_waitcnt vmcnt(10)\n"
"v_mfma_f32_16x16x32_bf16 v[34:37], a[52:55], v[198:201], v[34:37]\n"
"s_add_u32 m0, s54, 114688\n"
"s_nop 0\n"
"buffer_load_dwordx4 v2, s[28:31], s65 offen offset:0 sc1 lds\n"
"v_mfma_f32_16x16x32_bf16 v[38:41], a[48:51], v[202:205], v[38:41]\n"
"ds_read_b128 v[194:197], v6 offset:52224\n"
"ds_read_b32 v163, v230 offset:64\n"
"v_mfma_f32_16x16x32_bf16 v[42:45], a[52:55], v[202:205], v[42:45]\n"
"s_waitcnt lgkmcnt(6)\n"
"v_mfma_f32_16x16x32_bf16 v[46:49], a[48:51], v[206:209], v[46:49]\n"
"ds_read_b128 v[198:201], v6 offset:53248\n"
"v_mfma_f32_16x16x32_bf16 v[50:53], a[52:55], v[206:209], v[50:53]\n"
"buffer_load_dwordx4 v2, s[28:31], s65 offen offset:1024 sc1 lds\n"
"v_mfma_f32_16x16x32_bf16 v[54:57], a[48:51], v[210:213], v[54:57]\n"
"ds_read_b128 v[202:205], v6 offset:54272\n"
"ds_read_b32 v164, v230 offset:128\n"
"v_mfma_f32_16x16x32_bf16 v[58:61], a[52:55], v[210:213], v[58:61]\n"
"buffer_load_dwordx4 a[108:111], v2, s[28:31], s64 offen offset:3072 sc1\n"
"v_mfma_f32_16x16x32_bf16 v[62:65], a[48:51], v[214:217], v[62:65]\n"
"ds_read_b128 v[206:209], v6 offset:55296\n"
"v_mfma_f32_16x16x32_bf16 v[66:69], a[52:55], v[214:217], v[66:69]\n"
"v_mfma_f32_16x16x32_bf16 v[70:73], a[48:51], v[218:221], v[70:73]\n"
"ds_read_b128 v[210:213], v6 offset:56320\n"
"ds_read_b32 v165, v230 offset:192\n"
"v_mfma_f32_16x16x32_bf16 v[74:77], a[52:55], v[218:221], v[74:77]\n"
"s_waitcnt lgkmcnt(7)\n"
"v_mfma_f32_16x16x32_bf16 v[78:81], a[48:51], v[222:225], v[78:81]\n"
"s_barrier\n"
"ds_read_b128 v[214:217], v6 offset:57344\n"
"v_mfma_f32_16x16x32_bf16 v[82:85], a[52:55], v[222:225], v[82:85]\n"
"buffer_load_dwordx4 v2, s[28:31], s65 offen offset:2048 sc1 lds\n"
"v_mfma_f32_16x16x32_bf16 v[30:33], a[56:59], v[226:229], v[30:33]\n"
"ds_read_b128 v[218:221], v6 offset:58368\n"
"ds_read_b32 v166, v230 offset:256\n"
"v_mfma_f32_16x16x32_bf16 v[34:37], a[60:63], v[226:229], v[34:37]\n"
"buffer_load_dwordx4 v2, s[28:31], s65 offen offset:3072 sc1 lds\n"
"v_mfma_f32_16x16x32_bf16 v[38:41], a[56:59], v[190:193], v[38:41]\n"
"ds_read_b128 v[222:225], v6 offset:59392\n"
"v_mfma_f32_16x16x32_bf16 v[42:45], a[60:63], v[190:193], v[42:45]\n"
"ds_read_b32 v167, v230 offset:320\n"
"v_mfma_f32_16x16x32_bf16 v[46:49], a[56:59], v[194:197], v[46:49]\n"
"ds_read_b128 v[226:229], v6 offset:60416\n"
"v_mfma_f32_16x16x32_bf16 v[50:53], a[60:63], v[194:197], v[50:53]\n"
"s_waitcnt lgkmcnt(7)\n"
"v_mfma_f32_16x16x32_bf16 v[54:57], a[56:59], v[198:201], v[54:57]\n"
"ds_read_b128 v[190:193], v6 offset:61440\n"
"v_mfma_f32_16x16x32_bf16 v[58:61], a[60:63], v[198:201], v[58:61]\n"
"buffer_load_dwordx4 a[112:115], v3, s[28:31], s64 offen offset:0 sc1\n"
"v_mfma_f32_16x16x32_bf16 v[62:65], a[56:59], v[202:205], v[62:65]\n"
"ds_read_b128 v[194:197], v6 offset:62464\n"
"ds_read_b32 v168, v230 offset:384\n"
"v_mfma_f32_16x16x32_bf16 v[66:69], a[60:63], v[202:205], v[66:69]\n"
"v_mfma_f32_16x16x32_bf16 v[70:73], a[56:59], v[206:209], v[70:73]\n"
"ds_read_b128 v[198:201], v6 offset:63488\n"
"s_add_u32 m0, s54, 118784\n"
"v_mfma_f32_16x16x32_bf16 v[74:77], a[60:63], v[206:209], v[74:77]\n"
"v_bfe_u32 v15, v1, 0, 3\n"
"v_mfma_f32_16x16x32_bf16 v[78:81], a[56:59], v[210:213], v[78:81]\n"
"ds_read_b128 v[202:205], v6 offset:64512\n"
"buffer_load_dwordx4 v3, s[28:31], s65 offen offset:0 sc1 lds\n"
"v_mfma_f32_16x16x32_bf16 v[82:85], a[60:63], v[210:213], v[82:85]\n"
"s_waitcnt lgkmcnt(5)\n"
"v_mfma_f32_16x16x32_bf16 v[30:33], a[64:67], v[214:217], v[30:33]\n"
"ds_read_b128 v[206:209], v7 offset:0\n"
"v_mul_u32_u24 v14, 16, v15\n"
"s_waitcnt vmcnt(10)\n"
"v_mfma_f32_16x16x32_bf16 v[34:37], a[68:71], v[214:217], v[34:37]\n"
"buffer_load_dwordx4 a[116:119], v3, s[28:31], s64 offen offset:1024 sc1\n"
"v_mfma_f32_16x16x32_bf16 v[38:41], a[64:67], v[218:221], v[38:41]\n"
"ds_read_b128 v[210:213], v7 offset:1024\n"
"v_mfma_f32_16x16x32_bf16 v[42:45], a[68:71], v[218:221], v[42:45]\n"
"v_mfma_f32_16x16x32_bf16 v[46:49], a[64:67], v[222:225], v[46:49]\n"
"ds_read_b128 v[214:217], v7 offset:2048\n"
"v_bfe_u32 v15, v1, 3, 1\n"
"v_mfma_f32_16x16x32_bf16 v[50:53], a[68:71], v[222:225], v[50:53]\n"
"buffer_load_dwordx4 v3, s[28:31], s65 offen offset:1024 sc1 lds\n"
"v_mfma_f32_16x16x32_bf16 v[54:57], a[64:67], v[226:229], v[54:57]\n"
"ds_read_b128 v[218:221], v7 offset:3072\n"
"v_mfma_f32_16x16x32_bf16 v[58:61], a[68:71], v[226:229], v[58:61]\n"
"s_waitcnt lgkmcnt(4)\n"
"v_mfma_f32_16x16x32_bf16 v[62:65], a[64:67], v[190:193], v[62:65]\n"
"ds_read_b128 v[222:225], v7 offset:4096\n"
"v_lshl_add_u32 v14, v15, 3, v14\n"
"v_mfma_f32_16x16x32_bf16 v[66:69], a[68:71], v[190:193], v[66:69]\n"
"buffer_load_dwordx4 v3, s[28:31], s65 offen offset:2048 sc1 lds\n"
"v_mfma_f32_16x16x32_bf16 v[70:73], a[64:67], v[194:197], v[70:73]\n"
"ds_read_b128 v[226:229], v7 offset:5120\n"
"v_mfma_f32_16x16x32_bf16 v[74:77], a[68:71], v[194:197], v[74:77]\n"
"v_mfma_f32_16x16x32_bf16 v[78:81], a[64:67], v[198:201], v[78:81]\n"
"ds_read_b128 v[190:193], v7 offset:6144\n"
"v_bfe_u32 v15, v1, 4, 2\n"
"v_mfma_f32_16x16x32_bf16 v[82:85], a[68:71], v[198:201], v[82:85]\n"
"buffer_load_dwordx4 a[120:123], v3, s[28:31], s64 offen offset:2048 sc1\n"
"v_mfma_f32_16x16x32_bf16 v[30:33], a[72:75], v[202:205], v[30:33]\n"
"ds_read_b128 v[194:197], v7 offset:7168\n"
"v_mfma_f32_16x16x32_bf16 v[34:37], a[76:79], v[202:205], v[34:37]\n"
"s_waitcnt lgkmcnt(4)\n"
"v_mfma_f32_16x16x32_bf16 v[38:41], a[72:75], v[206:209], v[38:41]\n"
"ds_read_b128 v[198:201], v7 offset:8192\n"
"v_lshl_add_u32 v14, v15, 7, v14\n"
"v_mfma_f32_16x16x32_bf16 v[42:45], a[76:79], v[206:209], v[42:45]\n"
"v_mfma_f32_16x16x32_bf16 v[46:49], a[72:75], v[210:213], v[46:49]\n"
"ds_read_b128 v[202:205], v7 offset:9216\n"
"v_mfma_f32_16x16x32_bf16 v[50:53], a[76:79], v[210:213], v[50:53]\n"
"buffer_load_dwordx4 a[124:127], v3, s[28:31], s64 offen offset:3072 sc1\n"
"v_mfma_f32_16x16x32_bf16 v[54:57], a[72:75], v[214:217], v[54:57]\n"
"ds_read_b128 v[206:209], v7 offset:10240\n"
"s_mul_i32 s89, s50, 8192\n"
"v_mfma_f32_16x16x32_bf16 v[58:61], a[76:79], v[214:217], v[58:61]\n"
"v_mfma_f32_16x16x32_bf16 v[62:65], a[72:75], v[218:221], v[62:65]\n"
"ds_read_b128 v[210:213], v7 offset:11264\n"
"v_mfma_f32_16x16x32_bf16 v[66:69], a[76:79], v[218:221], v[66:69]\n"
"s_waitcnt lgkmcnt(4)\n"
"s_mul_i32 s56, s49, 512\n"
"v_mfma_f32_16x16x32_bf16 v[70:73], a[72:75], v[222:225], v[70:73]\n"
"ds_read_b128 v[214:217], v7 offset:12288\n"
"v_mfma_f32_16x16x32_bf16 v[74:77], a[76:79], v[222:225], v[74:77]\n"
"s_bfe_u32 s64, s81, 262160\n"
"s_mul_i32 s64, s64, 8192\n"
"s_add_u32 s64, s64, s66\n"
"buffer_load_dwordx4 a[128:131], v2, s[28:31], s64 offen offset:0 sc1\n"
"v_mfma_f32_16x16x32_bf16 v[78:81], a[72:75], v[226:229], v[78:81]\n"
"ds_read_b128 v[218:221], v7 offset:13312\n"
"v_mfma_f32_16x16x32_bf16 v[82:85], a[76:79], v[226:229], v[82:85]\n"
"buffer_load_dwordx4 a[132:135], v2, s[28:31], s64 offen offset:1024 sc1\n"
"v_mfma_f32_16x16x32_bf16 v[30:33], a[80:83], v[190:193], v[30:33]\n"
"ds_read_b128 v[222:225], v7 offset:14336\n"
"s_add_u32 s89, s89, s56\n"
"v_mfma_f32_16x16x32_bf16 v[34:37], a[84:87], v[190:193], v[34:37]\n"
"v_mfma_f32_16x16x32_bf16 v[38:41], a[80:83], v[194:197], v[38:41]\n"
"ds_read_b128 v[226:229], v7 offset:15360\n"
"v_mfma_f32_16x16x32_bf16 v[42:45], a[84:87], v[194:197], v[42:45]\n"
"s_waitcnt lgkmcnt(4)\n"
"s_add_u32 s89, s89, 1048576\n"
"v_mfma_f32_16x16x32_bf16 v[46:49], a[80:83], v[198:201], v[46:49]\n"
"ds_read_b128 v[190:193], v7 offset:16384\n"
"v_mfma_f32_16x16x32_bf16 v[50:53], a[84:87], v[198:201], v[50:53]\n"
"buffer_load_dwordx4 a[136:139], v2, s[28:31], s64 offen offset:2048 sc1\n"
"v_mfma_f32_16x16x32_bf16 v[54:57], a[80:83], v[202:205], v[54:57]\n"
"ds_read_b128 v[194:197], v7 offset:17408\n"
"v_mfma_f32_16x16x32_bf16 v[58:61], a[84:87], v[202:205], v[58:61]\n"
"s_bfe_u32 s65, s81, 262164\n"
"s_mul_i32 s65, s65, 28672\n"
"v_mfma_f32_16x16x32_bf16 v[62:65], a[80:83], v[206:209], v[62:65]\n"
"ds_read_b128 v[198:201], v7 offset:18432\n"
"s_add_u32 s65, s65, s69\n"
"v_mfma_f32_16x16x32_bf16 v[66:69], a[84:87], v[206:209], v[66:69]\n"
"s_add_u32 m0, s54, 0\n"
"s_mul_i32 s70, s50, 128\n"
"v_mfma_f32_16x16x32_bf16 v[70:73], a[80:83], v[210:213], v[70:73]\n"
"ds_read_b128 v[202:205], v7 offset:19456\n"
"buffer_load_dwordx4 v2, s[28:31], s65 offen offset:0 sc1 lds\n"
"v_mfma_f32_16x16x32_bf16 v[74:77], a[84:87], v[210:213], v[74:77]\n"
"s_waitcnt lgkmcnt(4)\n"
"s_add_u32 s70, s70, 32768\n"
"v_mfma_f32_16x16x32_bf16 v[78:81], a[80:83], v[214:217], v[78:81]\n"
"s_barrier\n"
"ds_read_b128 v[206:209], v7 offset:20480\n"
"v_mfma_f32_16x16x32_bf16 v[82:85], a[84:87], v[214:217], v[82:85]\n"
"buffer_load_dwordx4 v2, s[28:31], s65 offen offset:1024 sc1 lds\n"
"v_mfma_f32_16x16x32_bf16 v[30:33], a[88:91], v[218:221], v[30:33]\n"
"ds_read_b128 v[210:213], v7 offset:21504\n"
"s_mul_i32 s85, s49, 512\n"
"v_mfma_f32_16x16x32_bf16 v[34:37], a[92:95], v[218:221], v[34:37]\n"
"buffer_load_dwordx4 a[140:143], v2, s[28:31], s64 offen offset:3072 sc1\n"
"v_mfma_f32_16x16x32_bf16 v[38:41], a[88:91], v[222:225], v[38:41]\n"
"ds_read_b128 v[214:217], v7 offset:22528\n"
"v_mfma_f32_16x16x32_bf16 v[42:45], a[92:95], v[222:225], v[42:45]\n"
".Lmk_pg_10:\n"
"s_cbranch_execz .Lmk_ret_2\n"
"v_mfma_f32_16x16x32_bf16 v[46:49], a[88:91], v[226:229], v[46:49]\n"
"ds_read_b128 v[218:221], v7 offset:23552\n"
"s_mul_i32 s56, s41, 128\n"
"v_mfma_f32_16x16x32_bf16 v[50:53], a[92:95], v[226:229], v[50:53]\n"
"s_waitcnt lgkmcnt(4)\n"
"v_mfma_f32_16x16x32_bf16 v[54:57], a[88:91], v[190:193], v[54:57]\n"
"ds_read_b128 v[222:225], v7 offset:24576\n"
"v_mfma_f32_16x16x32_bf16 v[58:61], a[92:95], v[190:193], v[58:61]\n"
"buffer_load_dwordx4 v2, s[28:31], s65 offen offset:2048 sc1 lds\n"
"v_mfma_f32_16x16x32_bf16 v[62:65], a[88:91], v[194:197], v[62:65]\n"
"ds_read_b128 v[226:229], v7 offset:25600\n"
"s_add_u32 s85, s85, s56\n"
"v_mfma_f32_16x16x32_bf16 v[66:69], a[92:95], v[194:197], v[66:69]\n"
"v_mfma_f32_16x16x32_bf16 v[70:73], a[88:91], v[198:201], v[70:73]\n"
"ds_read_b128 v[190:193], v7 offset:26624\n"
"v_mfma_f32_16x16x32_bf16 v[74:77], a[92:95], v[198:201], v[74:77]\n"
"buffer_load_dwordx4 v2, s[28:31], s65 offen offset:3072 sc1 lds\n"
"v_mfma_f32_16x16x32_bf16 v[78:81], a[88:91], v[202:205], v[78:81]\n"
"ds_read_b128 v[194:197], v7 offset:27648\n"
"s_add_u32 s85, s85, 40960\n"
"v_mfma_f32_16x16x32_bf16 v[82:85], a[92:95], v[202:205], v[82:85]\n"
"s_waitcnt lgkmcnt(4)\n"
"s_waitcnt vmcnt(20)\n"
"v_mfma_f32_16x16x32_bf16 v[30:33], a[96:99], v[206:209], v[30:33]\n"
"ds_read_b128 v[198:201], v7 offset:28672\n"
"v_mfma_f32_16x16x32_bf16 v[34:37], a[100:103], v[206:209], v[34:37]\n"
"buffer_load_dwordx4 a[144:147], v3, s[28:31], s64 offen offset:0 sc1\n"
"v_mfma_f32_16x16x32_bf16 v[38:41], a[96:99], v[210:213], v[38:41]\n"
"ds_read_b128 v[202:205], v7 offset:29696\n"
"s_mul_i32 s88, s49, 458752\n"
"v_mfma_f32_16x16x32_bf16 v[42:45], a[100:103], v[210:213], v[42:45]\n"
"v_mfma_f32_16x16x32_bf16 v[46:49], a[96:99], v[214:217], v[46:49]\n"
"ds_read_b128 v[206:209], v7 offset:30720\n"
"v_mfma_f32_16x16x32_bf16 v[50:53], a[100:103], v[214:217], v[50:53]\n"
"s_add_u32 m0, s54, 4096\n"
"s_mul_i32 s56, s50, 7168\n"
"v_mfma_f32_16x16x32_bf16 v[54:57], a[96:99], v[218:221], v[54:57]\n"
"ds_read_b128 v[210:213], v7 offset:31744\n"
"v_mfma_f32_16x16x32_bf16 v[58:61], a[100:103], v[218:221], v[58:61]\n"
"s_waitcnt lgkmcnt(4)\n"
"buffer_load_dwordx4 v3, s[28:31], s65 offen offset:0 sc1 lds\n"
"v_mfma_f32_16x16x32_bf16 v[62:65], a[96:99], v[222:225], v[62:65]\n"
"ds_read_b128 v[214:217], v7 offset:32768\n"
"v_mfma_f32_16x16x32_bf16 v[66:69], a[100:103], v[222:225], v[66:69]\n"
"buffer_load_dwordx4 a[148:151], v3, s[28:31], s64 offen offset:1024 sc1\n"
"v_mfma_f32_16x16x32_bf16 v[70:73], a[96:99], v[226:229], v[70:73]\n"
"ds_read_b128 v[218:221], v7 offset:33792\n"
"s_add_u32 s88, s88, s56\n"
"v_mfma_f32_16x16x32_bf16 v[74:77], a[100:103], v[226:229], v[74:77]\n"
"v_mfma_f32_16x16x32_bf16 v[78:81], a[96:99], v[190:193], v[78:81]\n"
"ds_read_b128 v[222:225], v7 offset:34816\n"
"v_mfma_f32_16x16x32_bf16 v[82:85], a[100:103], v[190:193], v[82:85]\n"
"buffer_load_dwordx4 v3, s[28:31], s65 offen offset:1024 sc1 lds\n"
"v_mfma_f32_16x16x32_bf16 v[30:33], a[104:107], v[194:197], v[30:33]\n"
"ds_read_b128 v[226:229], v7 offset:35840\n"
"s_add_u32 s88, s88, 37748736\n"
"s_waitcnt vmcnt(21)\n"
"v_mfma_f32_16x16x32_bf16 v[34:37], a[108:111], v[194:197], v[34:37]\n"
"s_waitcnt lgkmcnt(4)\n"
"v_mfma_f32_16x16x32_bf16 v[38:41], a[104:107], v[198:201], v[38:41]\n"
"ds_read_b128 v[190:193], v7 offset:36864\n"
"s_mul_i32 s67, s49, 458752\n"
"v_mfma_f32_16x16x32_bf16 v[42:45], a[108:111], v[198:201], v[42:45]\n"
"buffer_load_dwordx4 v3, s[28:31], s65 offen offset:2048 sc1 lds\n"
"v_mfma_f32_16x16x32_bf16 v[46:49], a[104:107], v[202:205], v[46:49]\n"
"ds_read_b128 v[194:197], v7 offset:37888\n"
"v_mfma_f32_16x16x32_bf16 v[50:53], a[108:111], v[202:205], v[50:53]\n"
"v_mfma_f32_16x16x32_bf16 v[54:57], a[104:107], v[206:209], v[54:57]\n"
"ds_read_b128 v[198:201], v7 offset:38912\n"
"s_mul_i32 s56, s41, 7168\n"
"v_mfma_f32_16x16x32_bf16 v[58:61], a[108:111], v[206:209], v[58:61]\n"
"v_mfma_f32_16x16x32_bf16 v[62:65], a[104:107], v[210:213], v[62:65]\n"
"ds_read_b128 v[202:205], v7 offset:39936\n"
"v_mfma_f32_16x16x32_bf16 v[66:69], a[108:111], v[210:213], v[66:69]\n"
"s_waitcnt lgkmcnt(4)\n"
"v_mfma_f32_16x16x32_bf16 v[70:73], a[104:107], v[214:217], v[70:73]\n"
"ds_read_b128 v[206:209], v7 offset:40960\n"
"s_add_u32 s67, s67, s56\n"
"v_mfma_f32_16x16x32_bf16 v[74:77], a[108:111], v[214:217], v[74:77]\n"
"buffer_load_dwordx4 a[152:155], v3, s[28:31], s64 offen offset:2048 sc1\n"
"v_mfma_f32_16x16x32_bf16 v[78:81], a[104:107], v[218:221], v[78:81]\n"
"ds_read_b128 v[210:213], v7 offset:41984\n"
"v_mfma_f32_16x16x32_bf16 v[82:85], a[108:111], v[218:221], v[82:85]\n"
"buffer_load_dwordx4 a[156:159], v3, s[28:31], s64 offen offset:3072 sc1\n"
"s_waitcnt vmcnt(21)\n"
"v_mfma_f32_16x16x32_bf16 v[30:33], a[112:115], v[222:225], v[30:33]\n"
"ds_read_b128 v[214:217], v7 offset:43008\n"
"s_add_u32 s67, s67, 37748736\n"
"s_waitcnt vmcnt(19)\n"
"v_mfma_f32_16x16x32_bf16 v[34:37], a[116:119], v[222:225], v[34:37]\n"
"v_mfma_f32_16x16x32_bf16 v[38:41], a[112:115], v[226:229], v[38:41]\n"
"ds_read_b128 v[218:221], v7 offset:44032\n"
"v_mfma_f32_16x16x32_bf16 v[42:45], a[116:119], v[226:229], v[42:45]\n"
"s_waitcnt lgkmcnt(4)\n"
"v_mfma_f32_16x16x32_bf16 v[46:49], a[112:115], v[190:193], v[46:49]\n"
"ds_read_b128 v[222:225], v7 offset:45056\n"
"v_mfma_f32_16x16x32_bf16 v[50:53], a[116:119], v[190:193], v[50:53]\n"
"s_bfe_u32 s64, s81, 262164\n"
"s_mul_i32 s64, s64, 8192\n"
"v_mfma_f32_16x16x32_bf16 v[54:57], a[112:115], v[194:197], v[54:57]\n"
"ds_read_b128 v[226:229], v7 offset:46080\n"
"s_add_u32 s64, s64, s66\n"
"buffer_load_dwordx4 a[160:163], v2, s[28:31], s64 offen offset:0 sc1\n"
"v_mfma_f32_16x16x32_bf16 v[58:61], a[116:119], v[194:197], v[58:61]\n"
"buffer_load_dwordx4 a[164:167], v2, s[28:31], s64 offen offset:1024 sc1\n"
"v_mfma_f32_16x16x32_bf16 v[62:65], a[112:115], v[198:201], v[62:65]\n"
"ds_read_b128 v[190:193], v7 offset:47104\n"
"v_mfma_f32_16x16x32_bf16 v[66:69], a[116:119], v[198:201], v[66:69]\n"
"v_mfma_f32_16x16x32_bf16 v[70:73], a[112:115], v[202:205], v[70:73]\n"
"ds_read_b128 v[194:197], v7 offset:48128\n"
"v_mfma_f32_16x16x32_bf16 v[74:77], a[116:119], v[202:205], v[74:77]\n"
"s_waitcnt lgkmcnt(4)\n"
"v_mfma_f32_16x16x32_bf16 v[78:81], a[112:115], v[206:209], v[78:81]\n"
"s_waitcnt vmcnt(19)\n"
"s_barrier\n"
"ds_read_b128 v[198:201], v7 offset:49152\n"
"v_mfma_f32_16x16x32_bf16 v[82:85], a[116:119], v[206:209], v[82:85]\n"
"buffer_load_dwordx4 a[168:171], v2, s[28:31], s64 offen offset:2048 sc1\n"
"s_waitcnt vmcnt(15)\n"
"v_mfma_f32_16x16x32_bf16 v[30:33], a[120:123], v[210:213], v[30:33]\n"
"ds_read_b128 v[202:205], v7 offset:50176\n"
"v_mfma_f32_16x16x32_bf16 v[34:37], a[124:127], v[210:213], v[34:37]\n"
"buffer_load_dwordx4 v[236:239], v2, s[28:31], s80 offen offset:0 sc1\n"
"v_mfma_f32_16x16x32_bf16 v[38:41], a[120:123], v[214:217], v[38:41]\n"
"ds_read_b128 v[206:209], v7 offset:51200\n"
"v_mfma_f32_16x16x32_bf16 v[42:45], a[124:127], v[214:217], v[42:45]\n"
"v_mfma_f32_16x16x32_bf16 v[46:49], a[120:123], v[218:221], v[46:49]\n"
"ds_read_b128 v[210:213], v7 offset:52224\n"
"v_mfma_f32_16x16x32_bf16 v[50:53], a[124:127], v[218:221], v[50:53]\n"
"s_waitcnt lgkmcnt(4)\n"
"v_mfma_f32_16x16x32_bf16 v[54:57], a[120:123], v[222:225], v[54:57]\n"
"ds_read_b128 v[214:217], v7 offset:53248\n"
"v_mfma_f32_16x16x32_bf16 v[58:61], a[124:127], v[222:225], v[58:61]\n"
"buffer_load_dwordx4 v[240:243], v2, s[28:31], s80 offen offset:1024 sc1\n"
"v_mfma_f32_16x16x32_bf16 v[62:65], a[120:123], v[226:229], v[62:65]\n"
"ds_read_b128 v[218:221], v7 offset:54272\n"
"v_mfma_f32_16x16x32_bf16 v[66:69], a[124:127], v[226:229], v[66:69]\n"
"v_mfma_f32_16x16x32_bf16 v[70:73], a[120:123], v[190:193], v[70:73]\n"
"ds_read_b128 v[222:225], v7 offset:55296\n"
"v_mfma_f32_16x16x32_bf16 v[74:77], a[124:127], v[190:193], v[74:77]\n"
"buffer_load_dwordx4 v[244:247], v2, s[28:31], s80 offen offset:2048 sc1\n"
"v_mfma_f32_16x16x32_bf16 v[78:81], a[120:123], v[194:197], v[78:81]\n"
"ds_read_b128 v[226:229], v7 offset:56320\n"
"v_mfma_f32_16x16x32_bf16 v[82:85], a[124:127], v[194:197], v[82:85]\n"
"s_waitcnt lgkmcnt(4)\n"
"v_mfma_f32_16x16x32_bf16 v[30:33], a[128:131], v[198:201], v[30:33]\n"
"ds_read_b128 v[190:193], v7 offset:57344\n"
"v_mfma_f32_16x16x32_bf16 v[34:37], a[132:135], v[198:201], v[34:37]\n"
"buffer_load_dwordx4 v[248:251], v2, s[28:31], s80 offen offset:3072 sc1\n"
"v_mfma_f32_16x16x32_bf16 v[38:41], a[128:131], v[202:205], v[38:41]\n"
"ds_read_b128 v[194:197], v7 offset:58368\n"
"v_mfma_f32_16x16x32_bf16 v[42:45], a[132:135], v[202:205], v[42:45]\n"
"v_mfma_f32_16x16x32_bf16 v[46:49], a[128:131], v[206:209], v[46:49]\n"
"ds_read_b128 v[198:201], v7 offset:59392\n"
"v_mfma_f32_16x16x32_bf16 v[50:53], a[132:135], v[206:209], v[50:53]\n"
"s_bfe_u32 s65, s81, 262168\n"
"s_mul_i32 s65, s65, 28672\n"
"v_mfma_f32_16x16x32_bf16 v[54:57], a[128:131], v[210:213], v[54:57]\n"
"ds_read_b128 v[202:205], v7 offset:60416\n"
"s_add_u32 s65, s65, s69\n"
"v_mfma_f32_16x16x32_bf16 v[58:61], a[132:135], v[210:213], v[58:61]\n"
"s_waitcnt lgkmcnt(4)\n"
"s_add_u32 m0, s54, 28672\n"
"v_mfma_f32_16x16x32_bf16 v[62:65], a[128:131], v[214:217], v[62:65]\n"
"ds_read_b128 v[206:209], v7 offset:61440\n"
"buffer_load_dwordx4 v2, s[28:31], s65 offen offset:0 sc1 lds\n"
"v_mfma_f32_16x16x32_bf16 v[66:69], a[132:135], v[214:217], v[66:69]\n"
"buffer_load_dwordx4 v2, s[28:31], s65 offen offset:1024 sc1 lds\n"
"v_mfma_f32_16x16x32_bf16 v[70:73], a[128:131], v[218:221], v[70:73]\n"
"ds_read_b128 v[210:213], v7 offset:62464\n"
"v_mfma_f32_16x16x32_bf16 v[74:77], a[132:135], v[218:221], v[74:77]\n"
"v_mfma_f32_16x16x32_bf16 v[78:81], a[128:131], v[222:225], v[78:81]\n"
"ds_read_b128 v[214:217], v7 offset:63488\n"
"v_mfma_f32_16x16x32_bf16 v[82:85], a[132:135], v[222:225], v[82:85]\n"
"buffer_load_dwordx4 a[172:175], v2, s[28:31], s64 offen offset:3072 sc1\n"
"v_mfma_f32_16x16x32_bf16 v[30:33], a[136:139], v[226:229], v[30:33]\n"
"ds_read_b128 v[218:221], v7 offset:64512\n"
"s_waitcnt vmcnt(19)\n"
"v_mfma_f32_16x16x32_bf16 v[34:37], a[140:143], v[226:229], v[34:37]\n"
"s_waitcnt lgkmcnt(4)\n"
"v_mfma_f32_16x16x32_bf16 v[38:41], a[136:139], v[190:193], v[38:41]\n"
"ds_read_b128 v[222:225], v8 offset:0\n"
"v_mfma_f32_16x16x32_bf16 v[42:45], a[140:143], v[190:193], v[42:45]\n"
"buffer_load_dwordx4 v2, s[28:31], s65 offen offset:2048 sc1 lds\n"
"v_mfma_f32_16x16x32_bf16 v[46:49], a[136:139], v[194:197], v[46:49]\n"
"ds_read_b128 v[226:229], v8 offset:1024\n"
"v_mfma_f32_16x16x32_bf16 v[50:53], a[140:143], v[194:197], v[50:53]\n"
"v_mfma_f32_16x16x32_bf16 v[54:57], a[136:139], v[198:201], v[54:57]\n"
"ds_read_b128 v[190:193], v8 offset:2048\n"
"v_mfma_f32_16x16x32_bf16 v[58:61], a[140:143], v[198:201], v[58:61]\n"
"v_mfma_f32_16x16x32_bf16 v[62:65], a[136:139], v[202:205], v[62:65]\n"
"ds_read_b128 v[194:197], v8 offset:3072\n"
"v_mfma_f32_16x16x32_bf16 v[66:69], a[140:143], v[202:205], v[66:69]\n"
"s_waitcnt lgkmcnt(4)\n"
"v_mfma_f32_16x16x32_bf16 v[70:73], a[136:139], v[206:209], v[70:73]\n"
"ds_read_b128 v[198:201], v8 offset:4096\n"
"v_mfma_f32_16x16x32_bf16 v[74:77], a[140:143], v[206:209], v[74:77]\n"
"buffer_load_dwordx4 v2, s[28:31], s65 offen offset:3072 sc1 lds\n"
"v_mfma_f32_16x16x32_bf16 v[78:81], a[136:139], v[210:213], v[78:81]\n"
"ds_read_b128 v[202:205], v8 offset:5120\n"
"v_mfma_f32_16x16x32_bf16 v[82:85], a[140:143], v[210:213], v[82:85]\n"
"buffer_load_dwordx4 a[176:179], v3, s[28:31], s64 offen offset:0 sc1\n"
"s_waitcnt vmcnt(19)\n"
"v_mfma_f32_16x16x32_bf16 v[30:33], a[144:147], v[214:217], v[30:33]\n"
"ds_read_b128 v[206:209], v8 offset:6144\n"
"s_waitcnt vmcnt(17)\n"
"v_mfma_f32_16x16x32_bf16 v[34:37], a[148:151], v[214:217], v[34:37]\n"
"v_mfma_f32_16x16x32_bf16 v[38:41], a[144:147], v[218:221], v[38:41]\n"
"ds_read_b128 v[210:213], v8 offset:7168\n"
"s_add_u32 m0, s54, 32768\n"
"v_mfma_f32_16x16x32_bf16 v[42:45], a[148:151], v[218:221], v[42:45]\n"
"s_waitcnt lgkmcnt(4)\n"
"v_mfma_f32_16x16x32_bf16 v[46:49], a[144:147], v[222:225], v[46:49]\n"
"ds_read_b128 v[214:217], v8 offset:8192\n"
"v_mfma_f32_16x16x32_bf16 v[50:53], a[148:151], v[222:225], v[50:53]\n"
"buffer_load_dwordx4 v3, s[28:31], s65 offen offset:0 sc1 lds\n"
"v_mfma_f32_16x16x32_bf16 v[54:57], a[144:147], v[226:229], v[54:57]\n"
"ds_read_b128 v[218:221], v8 offset:9216\n"
"v_mfma_f32_16x16x32_bf16 v[58:61], a[148:151], v[226:229], v[58:61]\n"
"buffer_load_dwordx4 a[180:183], v3, s[28:31], s64 offen offset:1024 sc1\n"
"v_mfma_f32_16x16x32_bf16 v[62:65], a[144:147], v[190:193], v[62:65]\n"
"ds_read_b128 v[222:225], v8 offset:10240\n"
"v_mfma_f32_16x16x32_bf16 v[66:69], a[148:151], v[190:193], v[66:69]\n"
"v_mfma_f32_16x16x32_bf16 v[70:73], a[144:147], v[194:197], v[70:73]\n"
"ds_read_b128 v[226:229], v8 offset:11264\n"
"v_mfma_f32_16x16x32_bf16 v[74:77], a[148:151], v[194:197], v[74:77]\n"
"s_waitcnt lgkmcnt(4)\n"
"v_mfma_f32_16x16x32_bf16 v[78:81], a[144:147], v[198:201], v[78:81]\n"
"s_waitcnt vmcnt(17)\n"
"s_barrier\n"
"ds_read_b128 v[190:193], v6 offset:0\n"
"v_mfma_f32_16x16x32_bf16 v[82:85], a[148:151], v[198:201], v[82:85]\n"
"buffer_load_dwordx4 v3, s[28:31], s65 offen offset:1024 sc1 lds\n"
"s_waitcnt vmcnt(12)\n"
"v_mfma_f32_16x16x32_bf16 v[30:33], a[152:155], v[202:205], v[30:33]\n"
"ds_read_b128 v[194:197], v6 offset:1024\n"
"v_mfma_f32_16x16x32_bf16 v[34:37], a[156:159], v[202:205], v[34:37]\n"
"buffer_load_dwordx4 v3, s[28:31], s65 offen offset:2048 sc1 lds\n"
"v_mfma_f32_16x16x32_bf16 v[38:41], a[152:155], v[206:209], v[38:41]\n"
"ds_read_b128 v[198:201], v6 offset:2048\n"
"v_mfma_f32_16x16x32_bf16 v[42:45], a[156:159], v[206:209], v[42:45]\n"
"v_mfma_f32_16x16x32_bf16 v[46:49], a[152:155], v[210:213], v[46:49]\n"
"ds_read_b128 v[202:205], v6 offset:3072\n"
"v_mfma_f32_16x16x32_bf16 v[50:53], a[156:159], v[210:213], v[50:53]\n"
"s_waitcnt lgkmcnt(4)\n"
"v_mfma_f32_16x16x32_bf16 v[54:57], a[152:155], v[214:217], v[54:57]\n"
"ds_read_b128 v[206:209], v6 offset:4096\n"
"v_mfma_f32_16x16x32_bf16 v[58:61], a[156:159], v[214:217], v[58:61]\n"
"buffer_load_dwordx4 a[184:187], v3, s[28:31], s64 offen offset:2048 sc1\n"
"v_mfma_f32_16x16x32_bf16 v[62:65], a[152:155], v[218:221], v[62:65]\n"
"ds_read_b128 v[210:213], v6 offset:5120\n"
"v_mfma_f32_16x16x32_bf16 v[66:69], a[156:159], v[218:221], v[66:69]\n"
"v_mfma_f32_16x16x32_bf16 v[70:73], a[152:155], v[222:225], v[70:73]\n"
"ds_read_b128 v[214:217], v6 offset:6144\n"
"v_mfma_f32_16x16x32_bf16 v[74:77], a[156:159], v[222:225], v[74:77]\n"
"buffer_load_dwordx4 a[188:191], v3, s[28:31], s64 offen offset:3072 sc1\n"
"v_mfma_f32_16x16x32_bf16 v[78:81], a[152:155], v[226:229], v[78:81]\n"
"ds_read_b128 v[218:221], v6 offset:7168\n"
"v_mfma_f32_16x16x32_bf16 v[82:85], a[156:159], v[226:229], v[82:85]\n"
"s_waitcnt lgkmcnt(4)\n"
"v_mfma_f32_16x16x32_bf16 v[30:33], a[160:163], v[190:193], v[30:33]\n"
"ds_read_b128 v[222:225], v6 offset:8192\n"
"v_mfma_f32_16x16x32_bf16 v[34:37], a[164:167], v[190:193], v[34:37]\n"
"s_bfe_u32 s64, s81, 262168\n"
"s_mul_i32 s64, s64, 8192\n"
"v_mfma_f32_16x16x32_bf16 v[38:41], a[160:163], v[194:197], v[38:41]\n"
"ds_read_b128 v[226:229], v6 offset:9216\n"
"s_add_u32 s64, s64, s66\n"
"v_mfma_f32_16x16x32_bf16 v[42:45], a[164:167], v[194:197], v[42:45]\n"
"buffer_load_dwordx4 a[192:195], v2, s[28:31], s64 offen offset:0 sc1\n"
"v_mfma_f32_16x16x32_bf16 v[46:49], a[160:163], v[198:201], v[46:49]\n"
"ds_read_b128 v[190:193], v6 offset:10240\n"
"v_mfma_f32_16x16x32_bf16 v[50:53], a[164:167], v[198:201], v[50:53]\n"
"buffer_load_dwordx4 a[196:199], v2, s[28:31], s64 offen offset:1024 sc1\n"
"v_mfma_f32_16x16x32_bf16 v[54:57], a[160:163], v[202:205], v[54:57]\n"
"ds_read_b128 v[194:197], v6 offset:11264\n"
"v_mfma_f32_16x16x32_bf16 v[58:61], a[164:167], v[202:205], v[58:61]\n"
"s_waitcnt lgkmcnt(4)\n"
"v_mfma_f32_16x16x32_bf16 v[62:65], a[160:163], v[206:209], v[62:65]\n"
"ds_read_b128 v[198:201], v6 offset:12288\n"
"v_mfma_f32_16x16x32_bf16 v[66:69], a[164:167], v[206:209], v[66:69]\n"
"buffer_load_dwordx4 a[200:203], v2, s[28:31], s64 offen offset:2048 sc1\n"
"v_mfma_f32_16x16x32_bf16 v[70:73], a[160:163], v[210:213], v[70:73]\n"
"ds_read_b128 v[202:205], v6 offset:13312\n"
"v_mfma_f32_16x16x32_bf16 v[74:77], a[164:167], v[210:213], v[74:77]\n"
"v_mfma_f32_16x16x32_bf16 v[78:81], a[160:163], v[214:217], v[78:81]\n"
"ds_read_b128 v[206:209], v6 offset:14336\n"
"v_mfma_f32_16x16x32_bf16 v[82:85], a[164:167], v[214:217], v[82:85]\n"
"s_bfe_u32 s65, s81, 262172\n"
"s_mul_i32 s65, s65, 28672\n"
"v_mfma_f32_16x16x32_bf16 v[30:33], a[168:171], v[218:221], v[30:33]\n"
"ds_read_b128 v[210:213], v6 offset:15360\n"
"s_add_u32 s65, s65, s69\n"
"s_waitcnt vmcnt(12)\n"
"v_mfma_f32_16x16x32_bf16 v[34:37], a[172:175], v[218:221], v[34:37]\n"
"s_waitcnt lgkmcnt(4)\n"
"s_add_u32 m0, s54, 57344\n"
"v_mfma_f32_16x16x32_bf16 v[38:41], a[168:171], v[222:225], v[38:41]\n"
"ds_read_b128 v[214:217], v6 offset:16384\n"
"buffer_load_dwordx4 v2, s[28:31], s65 offen offset:0 sc1 lds\n"
"v_mfma_f32_16x16x32_bf16 v[42:45], a[172:175], v[222:225], v[42:45]\n"
"buffer_load_dwordx4 v2, s[28:31], s65 offen offset:1024 sc1 lds\n"
"v_mfma_f32_16x16x32_bf16 v[46:49], a[168:171], v[226:229], v[46:49]\n"
"ds_read_b128 v[218:221], v6 offset:17408\n"
"v_mfma_f32_16x16x32_bf16 v[50:53], a[172:175], v[226:229], v[50:53]\n"
"v_mfma_f32_16x16x32_bf16 v[54:57], a[168:171], v[190:193], v[54:57]\n"
"ds_read_b128 v[222:225], v6 offset:18432\n"
"v_mfma_f32_16x16x32_bf16 v[58:61], a[172:175], v[190:193], v[58:61]\n"
"v_mfma_f32_16x16x32_bf16 v[62:65], a[168:171], v[194:197], v[62:65]\n"
"ds_read_b128 v[226:229], v6 offset:19456\n"
"v_mfma_f32_16x16x32_bf16 v[66:69], a[172:175], v[194:197], v[66:69]\n"
"s_waitcnt lgkmcnt(4)\n"
"v_mfma_f32_16x16x32_bf16 v[70:73], a[168:171], v[198:201], v[70:73]\n"
"ds_read_b128 v[190:193], v6 offset:20480\n"
"v_mfma_f32_16x16x32_bf16 v[74:77], a[172:175], v[198:201], v[74:77]\n"
"buffer_load_dwordx4 a[204:207], v2, s[28:31], s64 offen offset:3072 sc1\n"
"v_mfma_f32_16x16x32_bf16 v[78:81], a[168:171], v[202:205], v[78:81]\n"
"ds_read_b128 v[194:197], v6 offset:21504\n"
"v_mfma_f32_16x16x32_bf16 v[82:85], a[172:175], v[202:205], v[82:85]\n"
"buffer_load_dwordx4 v2, s[28:31], s65 offen offset:2048 sc1 lds\n"
"s_waitcnt vmcnt(13)\n"
"v_mfma_f32_16x16x32_bf16 v[30:33], a[176:179], v[206:209], v[30:33]\n"
"ds_read_b128 v[198:201], v6 offset:22528\n"
"s_waitcnt vmcnt(11)\n"
"v_mfma_f32_16x16x32_bf16 v[34:37], a[180:183], v[206:209], v[34:37]\n"
"v_mfma_f32_16x16x32_bf16 v[38:41], a[176:179], v[210:213], v[38:41]\n"
"ds_read_b128 v[202:205], v6 offset:23552\n"
"v_mfma_f32_16x16x32_bf16 v[42:45], a[180:183], v[210:213], v[42:45]\n"
"s_waitcnt lgkmcnt(4)\n"
"v_mfma_f32_16x16x32_bf16 v[46:49], a[176:179], v[214:217], v[46:49]\n"
"ds_read_b128 v[206:209], v6 offset:24576\n"
"v_mfma_f32_16x16x32_bf16 v[50:53], a[180:183], v[214:217], v[50:53]\n"
"buffer_load_dwordx4 v2, s[28:31], s65 offen offset:3072 sc1 lds\n"
"v_mfma_f32_16x16x32_bf16 v[54:57], a[176:179], v[218:221], v[54:57]\n"
"ds_read_b128 v[210:213], v6 offset:25600\n"
"v_mfma_f32_16x16x32_bf16 v[58:61], a[180:183], v[218:221], v[58:61]\n"
"buffer_load_dwordx4 a[208:211], v3, s[28:31], s64 offen offset:0 sc1\n"
"v_mfma_f32_16x16x32_bf16 v[62:65], a[176:179], v[222:225], v[62:65]\n"
"ds_read_b128 v[214:217], v6 offset:26624\n"
"v_mfma_f32_16x16x32_bf16 v[66:69], a[180:183], v[222:225], v[66:69]\n"
"v_mfma_f32_16x16x32_bf16 v[70:73], a[176:179], v[226:229], v[70:73]\n"
"ds_read_b128 v[218:221], v6 offset:27648\n"
"v_mfma_f32_16x16x32_bf16 v[74:77], a[180:183], v[226:229], v[74:77]\n"
"s_waitcnt lgkmcnt(4)\n"
"s_add_u32 m0, s54, 61440\n"
"v_mfma_f32_16x16x32_bf16 v[78:81], a[176:179], v[190:193], v[78:81]\n"
"s_waitcnt vmcnt(11)\n"
"s_barrier\n"
"ds_read_b128 v[222:225], v6 offset:28672\n"
"v_mfma_f32_16x16x32_bf16 v[82:85], a[180:183], v[190:193], v[82:85]\n"
"buffer_load_dwordx4 v3, s[28:31], s65 offen offset:0 sc1 lds\n"
"s_waitcnt vmcnt(11)\n"
"v_mfma_f32_16x16x32_bf16 v[30:33], a[184:187], v[194:197], v[30:33]\n"
"ds_read_b128 v[226:229], v6 offset:29696\n"
"s_waitcnt vmcnt(10)\n"
"v_mfma_f32_16x16x32_bf16 v[34:37], a[188:191], v[194:197], v[34:37]\n"
"buffer_load_dwordx4 a[212:215], v3, s[28:31], s64 offen offset:1024 sc1\n"
"v_mfma_f32_16x16x32_bf16 v[38:41], a[184:187], v[198:201], v[38:41]\n"
"ds_read_b128 v[190:193], v6 offset:30720\n"
"v_mfma_f32_16x16x32_bf16 v[42:45], a[188:191], v[198:201], v[42:45]\n"
"v_mfma_f32_16x16x32_bf16 v[46:49], a[184:187], v[202:205], v[46:49]\n"
"ds_read_b128 v[194:197], v6 offset:31744\n"
"v_mfma_f32_16x16x32_bf16 v[50:53], a[188:191], v[202:205], v[50:53]\n"
"s_waitcnt lgkmcnt(4)\n"
"v_mfma_f32_16x16x32_bf16 v[54:57], a[184:187], v[206:209], v[54:57]\n"
"ds_read_b128 v[198:201], v6 offset:32768\n"
"v_mfma_f32_16x16x32_bf16 v[58:61], a[188:191], v[206:209], v[58:61]\n"
"buffer_load_dwordx4 v3, s[28:31], s65 offen offset:1024 sc1 lds\n"
"v_mfma_f32_16x16x32_bf16 v[62:65], a[184:187], v[210:213], v[62:65]\n"
"ds_read_b128 v[202:205], v6 offset:33792\n"
"v_bitop3_b32 v237, v237, v239, s26 bitop3:0x7e\n"
"v_mfma_f32_16x16x32_bf16 v[66:69], a[188:191], v[210:213], v[66:69]\n"
"v_mfma_f32_16x16x32_bf16 v[70:73], a[184:187], v[214:217], v[70:73]\n"
"ds_read_b128 v[206:209], v6 offset:34816\n"
"v_bitop3_b32 v241, v241, v243, s26 bitop3:0x7e\n"
"v_mfma_f32_16x16x32_bf16 v[74:77], a[188:191], v[214:217], v[74:77]\n"
"buffer_load_dwordx4 v3, s[28:31], s65 offen offset:2048 sc1 lds\n"
"v_mfma_f32_16x16x32_bf16 v[78:81], a[184:187], v[218:221], v[78:81]\n"
"ds_read_b128 v[210:213], v6 offset:35840\n"
"v_bitop3_b32 v245, v245, v247, s26 bitop3:0x7e\n"
"v_mfma_f32_16x16x32_bf16 v[82:85], a[188:191], v[218:221], v[82:85]\n"
"s_waitcnt lgkmcnt(4)\n"
"s_waitcnt vmcnt(11)\n"
"v_mfma_f32_16x16x32_bf16 v[30:33], a[192:195], v[222:225], v[30:33]\n"
"ds_read_b128 v[214:217], v6 offset:36864\n"
"v_bitop3_b32 v249, v249, v251, s26 bitop3:0x7e\n"
"v_mfma_f32_16x16x32_bf16 v[34:37], a[196:199], v[222:225], v[34:37]\n"
"buffer_load_dwordx4 a[216:219], v3, s[28:31], s64 offen offset:2048 sc1\n"
"v_mfma_f32_16x16x32_bf16 v[38:41], a[192:195], v[226:229], v[38:41]\n"
"ds_read_b128 v[218:221], v6 offset:37888\n"
"v_mfma_f32_16x16x32_bf16 v[42:45], a[196:199], v[226:229], v[42:45]\n"
"v_or3_b32 v237, v237, v249, v245\n"
"v_mfma_f32_16x16x32_bf16 v[46:49], a[192:195], v[190:193], v[46:49]\n"
"ds_read_b128 v[222:225], v6 offset:38912\n"
"v_mfma_f32_16x16x32_bf16 v[50:53], a[196:199], v[190:193], v[50:53]\n"
"buffer_load_dwordx4 a[220:223], v3, s[28:31], s64 offen offset:3072 sc1\n"
"v_mfma_f32_16x16x32_bf16 v[54:57], a[192:195], v[194:197], v[54:57]\n"
"ds_read_b128 v[226:229], v6 offset:39936\n"
"v_or_b32 v237, v237, v241\n"
"v_mfma_f32_16x16x32_bf16 v[58:61], a[196:199], v[194:197], v[58:61]\n"
"s_waitcnt lgkmcnt(4)\n"
"v_cmp_ne_u32 vcc, 0, v237\n"
"s_nop 0\n"
"s_cbranch_vccnz .Lmk_fspin_26\n"
".Lmk_fback_27:\n"
"v_mfma_f32_16x16x32_bf16 v[62:65], a[192:195], v[198:201], v[62:65]\n"
"ds_read_b128 v[190:193], v6 offset:40960\n"
"v_mfma_f32_16x16x32_bf16 v[66:69], a[196:199], v[198:201], v[66:69]\n"
"s_bfe_u32 s64, s81, 262172\n"
"s_mul_i32 s64, s64, 8192\n"
"v_mfma_f32_16x16x32_bf16 v[70:73], a[192:195], v[202:205], v[70:73]\n"
"ds_read_b128 v[194:197], v6 offset:41984\n"
"s_add_u32 s64, s64, s66\n"
"v_mfma_f32_16x16x32_bf16 v[74:77], a[196:199], v[202:205], v[74:77]\n"
"buffer_load_dwordx4 a[224:227], v2, s[28:31], s64 offen offset:0 sc1\n"
"v_mfma_f32_16x16x32_bf16 v[78:81], a[192:195], v[206:209], v[78:81]\n"
"ds_read_b128 v[198:201], v6 offset:43008\n"
"v_add_f32 v236, v236, v244\n"
"v_mfma_f32_16x16x32_bf16 v[82:85], a[196:199], v[206:209], v[82:85]\n"
"buffer_load_dwordx4 a[228:231], v2, s[28:31], s64 offen offset:1024 sc1\n"
"s_waitcnt vmcnt(14)\n"
"v_mfma_f32_16x16x32_bf16 v[30:33], a[200:203], v[210:213], v[30:33]\n"
"ds_read_b128 v[202:205], v6 offset:44032\n"
"v_add_f32 v238, v238, v246\n"
"s_waitcnt vmcnt(11)\n"
"v_mfma_f32_16x16x32_bf16 v[34:37], a[204:207], v[210:213], v[34:37]\n"
"s_waitcnt lgkmcnt(4)\n"
"v_add_f32 v240, v240, v248\n"
"v_mfma_f32_16x16x32_bf16 v[38:41], a[200:203], v[214:217], v[38:41]\n"
"ds_read_b128 v[206:209], v6 offset:45056\n"
"v_add_f32 v242, v242, v250\n"
"v_mfma_f32_16x16x32_bf16 v[42:45], a[204:207], v[214:217], v[42:45]\n"
"buffer_load_dwordx4 a[232:235], v2, s[28:31], s64 offen offset:2048 sc1\n"
"v_mfma_f32_16x16x32_bf16 v[46:49], a[200:203], v[218:221], v[46:49]\n"
"ds_read_b128 v[210:213], v6 offset:46080\n"
"v_mfma_f32_16x16x32_bf16 v[50:53], a[204:207], v[218:221], v[50:53]\n"
"v_add_f32 v236, v236, v240\n"
"v_mfma_f32_16x16x32_bf16 v[54:57], a[200:203], v[222:225], v[54:57]\n"
"ds_read_b128 v[214:217], v6 offset:47104\n"
"v_mfma_f32_16x16x32_bf16 v[58:61], a[204:207], v[222:225], v[58:61]\n"
"s_bfe_u32 s65, s81, 262144\n"
"s_mul_i32 s65, s65, 28672\n"
"v_mfma_f32_16x16x32_bf16 v[62:65], a[200:203], v[226:229], v[62:65]\n"
"ds_read_b128 v[218:221], v6 offset:48128\n"
"v_add_f32 v238, v238, v242\n"
"v_mfma_f32_16x16x32_bf16 v[66:69], a[204:207], v[226:229], v[66:69]\n"
"s_waitcnt lgkmcnt(4)\n"
"v_permlane32_swap_b32 v236, v238\n"
"v_mfma_f32_16x16x32_bf16 v[70:73], a[200:203], v[190:193], v[70:73]\n"
"ds_read_b128 v[222:225], v6 offset:49152\n"
"v_mfma_f32_16x16x32_bf16 v[74:77], a[204:207], v[190:193], v[74:77]\n"
"s_add_u32 s65, s65, s69\n"
"s_add_u32 s65, s65, 3670016\n"
"s_add_u32 m0, s54, 86016\n"
"s_nop 0\n"
"buffer_load_dwordx4 v2, s[28:31], s65 offen offset:0 sc1 lds\n"
"v_mfma_f32_16x16x32_bf16 v[78:81], a[200:203], v[194:197], v[78:81]\n"
"ds_read_b128 v[226:229], v6 offset:50176\n"
"v_add_f32 v236, v236, v238\n"
"v_mfma_f32_16x16x32_bf16 v[82:85], a[204:207], v[194:197], v[82:85]\n"
"buffer_load_dwordx4 v2, s[28:31], s65 offen offset:1024 sc1 lds\n"
"s_waitcnt vmcnt(11)\n"
"v_mfma_f32_16x16x32_bf16 v[30:33], a[208:211], v[198:201], v[30:33]\n"
"ds_read_b128 v[190:193], v6 offset:51200\n"
"v_mul_f32 v236, 0x3a800000, v236\n"
"s_waitcnt vmcnt(9)\n"
"v_mfma_f32_16x16x32_bf16 v[34:37], a[212:215], v[198:201], v[34:37]\n"
"v_mfma_f32_16x16x32_bf16 v[38:41], a[208:211], v[202:205], v[38:41]\n"
"ds_read_b128 v[194:197], v6 offset:52224\n"
"v_mov_b32 v238, v236\n"
"v_mfma_f32_16x16x32_bf16 v[42:45], a[212:215], v[202:205], v[42:45]\n"
"s_waitcnt lgkmcnt(4)\n"
"v_mfma_f32_16x16x32_bf16 v[46:49], a[208:211], v[206:209], v[46:49]\n"
"ds_read_b128 v[198:201], v6 offset:53248\n"
"v_permlane32_swap_b32 v236, v238\n"
"v_mfma_f32_16x16x32_bf16 v[50:53], a[212:215], v[206:209], v[50:53]\n"
"buffer_load_dwordx4 a[236:239], v2, s[28:31], s64 offen offset:3072 sc1\n"
"v_mfma_f32_16x16x32_bf16 v[54:57], a[208:211], v[210:213], v[54:57]\n"
"ds_read_b128 v[202:205], v6 offset:54272\n"
"v_fma_f32 v238, -v236, v236, v238\n"
"v_mfma_f32_16x16x32_bf16 v[58:61], a[212:215], v[210:213], v[58:61]\n"
"buffer_load_dwordx4 v2, s[28:31], s65 offen offset:2048 sc1 lds\n"
"v_mfma_f32_16x16x32_bf16 v[62:65], a[208:211], v[214:217], v[62:65]\n"
"ds_read_b128 v[206:209], v6 offset:55296\n"
"v_mfma_f32_16x16x32_bf16 v[66:69], a[212:215], v[214:217], v[66:69]\n"
"v_max_f32 v238, 0, v238\n"
"v_mfma_f32_16x16x32_bf16 v[70:73], a[208:211], v[218:221], v[70:73]\n"
"ds_read_b128 v[210:213], v6 offset:56320\n"
"v_mfma_f32_16x16x32_bf16 v[74:77], a[212:215], v[218:221], v[74:77]\n"
"s_waitcnt lgkmcnt(4)\n"
"v_add_f32 v238, 0x3727c5ac, v238\n"
"v_mfma_f32_16x16x32_bf16 v[78:81], a[208:211], v[222:225], v[78:81]\n"
"s_waitcnt vmcnt(9)\n"
"s_barrier\n"
"ds_read_b128 v[214:217], v6 offset:57344\n"
"v_mfma_f32_16x16x32_bf16 v[82:85], a[212:215], v[222:225], v[82:85]\n"
"buffer_load_dwordx4 v2, s[28:31], s65 offen offset:3072 sc1 lds\n"
"s_waitcnt vmcnt(9)\n"
"v_mfma_f32_16x16x32_bf16 v[30:33], a[216:219], v[226:229], v[30:33]\n"
"ds_read_b128 v[218:221], v6 offset:58368\n"
"s_waitcnt vmcnt(8)\n"
"v_mfma_f32_16x16x32_bf16 v[34:37], a[220:223], v[226:229], v[34:37]\n"
"buffer_load_dwordx4 a[240:243], v3, s[28:31], s64 offen offset:0 sc1\n"
"v_mfma_f32_16x16x32_bf16 v[38:41], a[216:219], v[190:193], v[38:41]\n"
"ds_read_b128 v[222:225], v6 offset:59392\n"
"v_mfma_f32_16x16x32_bf16 v[42:45], a[220:223], v[190:193], v[42:45]\n"
"v_rsq_f32 v238, v238\n"
"v_mfma_f32_16x16x32_bf16 v[46:49], a[216:219], v[194:197], v[46:49]\n"
"ds_read_b128 v[226:229], v6 offset:60416\n"
"v_mul_f32 v236, v236, v238\n"
"v_mfma_f32_16x16x32_bf16 v[50:53], a[220:223], v[194:197], v[50:53]\n"
"s_waitcnt lgkmcnt(4)\n"
"s_add_u32 m0, s54, 90112\n"
"v_mfma_f32_16x16x32_bf16 v[54:57], a[216:219], v[198:201], v[54:57]\n"
"ds_read_b128 v[190:193], v6 offset:61440\n"
"v_sub_f32 v236, 0, v236\n"
"v_mfma_f32_16x16x32_bf16 v[58:61], a[220:223], v[198:201], v[58:61]\n"
"buffer_load_dwordx4 v3, s[28:31], s65 offen offset:0 sc1 lds\n"
"v_mfma_f32_16x16x32_bf16 v[62:65], a[216:219], v[202:205], v[62:65]\n"
"ds_read_b128 v[194:197], v6 offset:62464\n"
"v_permlane32_swap_b32 v238, v236\n"
"v_mfma_f32_16x16x32_bf16 v[66:69], a[220:223], v[202:205], v[66:69]\n"
"ds_write_b32 v232, v238\n"
"v_mfma_f32_16x16x32_bf16 v[70:73], a[216:219], v[206:209], v[70:73]\n"
"ds_read_b128 v[198:201], v6 offset:63488\n"
"v_mfma_f32_16x16x32_bf16 v[74:77], a[220:223], v[206:209], v[74:77]\n"
"buffer_load_dwordx4 a[244:247], v3, s[28:31], s64 offen offset:1024 sc1\n"
"v_mfma_f32_16x16x32_bf16 v[78:81], a[216:219], v[210:213], v[78:81]\n"
"ds_read_b128 v[202:205], v6 offset:64512\n"
"ds_read_b128 v[142:145], v233 offset:0\n"
"v_mfma_f32_16x16x32_bf16 v[82:85], a[220:223], v[210:213], v[82:85]\n"
"s_waitcnt lgkmcnt(6)\n"
"ds_read_b128 v[146:149], v233 offset:16\n"
"s_waitcnt vmcnt(9)\n"
"v_mfma_f32_16x16x32_bf16 v[30:33], a[224:227], v[214:217], v[30:33]\n"
"ds_read_b128 v[206:209], v7 offset:0\n"
"v_mfma_f32_16x16x32_bf16 v[34:37], a[228:231], v[214:217], v[34:37]\n"
"buffer_load_dwordx4 v3, s[28:31], s65 offen offset:1024 sc1 lds\n"
"v_mfma_f32_16x16x32_bf16 v[38:41], a[224:227], v[218:221], v[38:41]\n"
"ds_read_b128 v[210:213], v7 offset:1024\n"
"ds_read_b128 v[150:153], v233 offset:128\n"
"v_mfma_f32_16x16x32_bf16 v[42:45], a[228:231], v[218:221], v[42:45]\n"
"ds_read_b128 v[154:157], v233 offset:144\n"
"v_mfma_f32_16x16x32_bf16 v[46:49], a[224:227], v[222:225], v[46:49]\n"
"ds_read_b128 v[214:217], v7 offset:2048\n"
"v_mfma_f32_16x16x32_bf16 v[50:53], a[228:231], v[222:225], v[50:53]\n"
"buffer_load_dwordx4 v3, s[28:31], s65 offen offset:2048 sc1 lds\n"
"v_mfma_f32_16x16x32_bf16 v[54:57], a[224:227], v[226:229], v[54:57]\n"
"ds_read_b128 v[218:221], v7 offset:3072\n"
"ds_read_b32 v158, v234 offset:0\n"
"v_mfma_f32_16x16x32_bf16 v[58:61], a[228:231], v[226:229], v[58:61]\n"
"s_waitcnt lgkmcnt(9)\n"
"v_mfma_f32_16x16x32_bf16 v[62:65], a[224:227], v[190:193], v[62:65]\n"
"ds_read_b128 v[222:225], v7 offset:4096\n"
"ds_read_b32 v159, v234 offset:128\n"
"v_mfma_f32_16x16x32_bf16 v[66:69], a[228:231], v[190:193], v[66:69]\n"
"buffer_load_dwordx4 a[248:251], v3, s[28:31], s64 offen offset:2048 sc1\n"
".Lmk_pg_11:\n"
"s_cbranch_execz .Lmk_ret_2\n"
"v_mfma_f32_16x16x32_bf16 v[70:73], a[224:227], v[194:197], v[70:73]\n"
"ds_read_b128 v[226:229], v7 offset:5120\n"
"ds_read_b32 v160, v234 offset:16\n"
"v_mfma_f32_16x16x32_bf16 v[74:77], a[228:231], v[194:197], v[74:77]\n"
"v_mfma_f32_16x16x32_bf16 v[78:81], a[224:227], v[198:201], v[78:81]\n"
"ds_read_b128 v[190:193], v7 offset:6144\n"
"v_mfma_f32_16x16x32_bf16 v[82:85], a[228:231], v[198:201], v[82:85]\n"
"buffer_load_dwordx4 a[252:255], v3, s[28:31], s64 offen offset:3072 sc1\n"
"s_waitcnt vmcnt(12)\n"
"v_mfma_f32_16x16x32_bf16 v[30:33], a[232:235], v[202:205], v[30:33]\n"
"ds_read_b128 v[194:197], v7 offset:7168\n"
"ds_read_b32 v161, v234 offset:144\n"
"s_waitcnt vmcnt(9)\n"
"v_mfma_f32_16x16x32_bf16 v[34:37], a[236:239], v[202:205], v[34:37]\n"
"s_waitcnt lgkmcnt(8)\n"
"s_mul_i32 s80, s50, 8192\n"
"v_mfma_f32_16x16x32_bf16 v[38:41], a[232:235], v[206:209], v[38:41]\n"
"ds_read_b128 v[198:201], v7 offset:8192\n"
"v_mfma_f32_16x16x32_bf16 v[42:45], a[236:239], v[206:209], v[42:45]\n"
"s_bfe_u32 s65, s81, 262148\n"
"s_mul_i32 s65, s65, 28672\n"
"v_mfma_f32_16x16x32_bf16 v[46:49], a[232:235], v[210:213], v[46:49]\n"
"ds_read_b128 v[202:205], v7 offset:9216\n"
"s_add_u32 s80, s80, 1048576\n"
"v_mfma_f32_16x16x32_bf16 v[50:53], a[236:239], v[210:213], v[50:53]\n"
"s_add_u32 s65, s65, s69\n"
"s_add_u32 s65, s65, 3670016\n"
"v_mfma_f32_16x16x32_bf16 v[54:57], a[232:235], v[214:217], v[54:57]\n"
"ds_read_b128 v[206:209], v7 offset:10240\n"
"s_add_u32 m0, s54, 114688\n"
"s_nop 0\n"
"buffer_load_dwordx4 v2, s[28:31], s65 offen offset:0 sc1 lds\n"
"v_mfma_f32_16x16x32_bf16 v[58:61], a[236:239], v[214:217], v[58:61]\n"
"buffer_load_dwordx4 v2, s[28:31], s65 offen offset:1024 sc1 lds\n"
"v_mfma_f32_16x16x32_bf16 v[62:65], a[232:235], v[218:221], v[62:65]\n"
"ds_read_b128 v[210:213], v7 offset:11264\n"
"v_mul_f32 v22, v18, v142\n"
"v_mfma_f32_16x16x32_bf16 v[66:69], a[236:239], v[218:221], v[66:69]\n"
"s_waitcnt lgkmcnt(5)\n"
"v_mul_f32 v23, v18, v143\n"
"v_mfma_f32_16x16x32_bf16 v[70:73], a[232:235], v[222:225], v[70:73]\n"
"ds_read_b128 v[214:217], v7 offset:12288\n"
"v_mul_f32 v24, v18, v144\n"
"v_mfma_f32_16x16x32_bf16 v[74:77], a[236:239], v[222:225], v[74:77]\n"
"v_mfma_f32_16x16x32_bf16 v[78:81], a[232:235], v[226:229], v[78:81]\n"
"ds_read_b128 v[218:221], v7 offset:13312\n"
"v_mul_f32 v25, v18, v145\n"
"v_mfma_f32_16x16x32_bf16 v[82:85], a[236:239], v[226:229], v[82:85]\n"
"buffer_load_dwordx4 v2, s[28:31], s65 offen offset:2048 sc1 lds\n"
"s_waitcnt vmcnt(9)\n"
"v_mfma_f32_16x16x32_bf16 v[30:33], a[240:243], v[190:193], v[30:33]\n"
"ds_read_b128 v[222:225], v7 offset:14336\n"
"v_mul_f32 v26, v18, v146\n"
"s_waitcnt vmcnt(7)\n"
"v_mfma_f32_16x16x32_bf16 v[34:37], a[244:247], v[190:193], v[34:37]\n"
"v_mfma_f32_16x16x32_bf16 v[38:41], a[240:243], v[194:197], v[38:41]\n"
"ds_read_b128 v[226:229], v7 offset:15360\n"
"v_mfma_f32_16x16x32_bf16 v[42:45], a[244:247], v[194:197], v[42:45]\n"
"s_waitcnt lgkmcnt(4)\n"
"v_mul_f32 v27, v18, v147\n"
"v_mfma_f32_16x16x32_bf16 v[46:49], a[240:243], v[198:201], v[46:49]\n"
"ds_read_b128 v[190:193], v7 offset:16384\n"
"v_mfma_f32_16x16x32_bf16 v[50:53], a[244:247], v[198:201], v[50:53]\n"
"buffer_load_dwordx4 v2, s[28:31], s65 offen offset:3072 sc1 lds\n"
"v_mfma_f32_16x16x32_bf16 v[54:57], a[240:243], v[202:205], v[54:57]\n"
"ds_read_b128 v[194:197], v7 offset:17408\n"
"s_add_u32 m0, s54, 118784\n"
"v_mfma_f32_16x16x32_bf16 v[58:61], a[244:247], v[202:205], v[58:61]\n"
"v_mul_f32 v28, v18, v148\n"
"v_mul_f32 v29, v18, v149\n"
"v_mfma_f32_16x16x32_bf16 v[62:65], a[240:243], v[206:209], v[62:65]\n"
"ds_read_b128 v[198:201], v7 offset:18432\n"
"v_mfma_f32_16x16x32_bf16 v[66:69], a[244:247], v[206:209], v[66:69]\n"
"buffer_load_dwordx4 v3, s[28:31], s65 offen offset:0 sc1 lds\n"
"v_mfma_f32_16x16x32_bf16 v[70:73], a[240:243], v[210:213], v[70:73]\n"
"ds_read_b128 v[202:205], v7 offset:19456\n"
"v_mfma_f32_16x16x32_bf16 v[74:77], a[244:247], v[210:213], v[74:77]\n"
"s_waitcnt lgkmcnt(4)\n"
"v_mfma_f32_16x16x32_bf16 v[78:81], a[240:243], v[214:217], v[78:81]\n"
"s_waitcnt vmcnt(7)\n"
"s_barrier\n"
"ds_read_b128 v[206:209], v7 offset:20480\n"
"v_mfma_f32_16x16x32_bf16 v[82:85], a[244:247], v[214:217], v[82:85]\n"
"buffer_load_dwordx4 v3, s[28:31], s65 offen offset:1024 sc1 lds\n"
"s_waitcnt vmcnt(7)\n"
"v_mfma_f32_16x16x32_bf16 v[30:33], a[248:251], v[218:221], v[30:33]\n"
"ds_read_b128 v[210:213], v7 offset:21504\n"
"s_waitcnt vmcnt(6)\n"
"v_mfma_f32_16x16x32_bf16 v[34:37], a[252:255], v[218:221], v[34:37]\n"
"buffer_load_dwordx4 v3, s[28:31], s65 offen offset:2048 sc1 lds\n"
"v_mfma_f32_16x16x32_bf16 v[38:41], a[248:251], v[222:225], v[38:41]\n"
"ds_read_b128 v[214:217], v7 offset:22528\n"
"v_mfma_f32_16x16x32_bf16 v[42:45], a[252:255], v[222:225], v[42:45]\n"
"v_mfma_f32_16x16x32_bf16 v[46:49], a[248:251], v[226:229], v[46:49]\n"
"ds_read_b128 v[218:221], v7 offset:23552\n"
"v_mfma_f32_16x16x32_bf16 v[50:53], a[252:255], v[226:229], v[50:53]\n"
"s_waitcnt lgkmcnt(4)\n"
"v_mfma_f32_16x16x32_bf16 v[54:57], a[248:251], v[190:193], v[54:57]\n"
"ds_read_b128 v[222:225], v7 offset:24576\n"
"v_mfma_f32_16x16x32_bf16 v[58:61], a[252:255], v[190:193], v[58:61]\n"
"s_bfe_u32 s65, s81, 262152\n"
"s_mul_i32 s65, s65, 28672\n"
"v_mfma_f32_16x16x32_bf16 v[62:65], a[248:251], v[194:197], v[62:65]\n"
"ds_read_b128 v[226:229], v7 offset:25600\n"
"s_add_u32 s65, s65, s69\n"
"s_add_u32 s65, s65, 3670016\n"
"s_add_u32 m0, s54, 0\n"
"s_nop 0\n"
"buffer_load_dwordx4 v2, s[28:31], s65 offen offset:0 sc1 lds\n"
"v_mfma_f32_16x16x32_bf16 v[66:69], a[252:255], v[194:197], v[66:69]\n"
"buffer_load_dwordx4 v2, s[28:31], s65 offen offset:1024 sc1 lds\n"
"v_mfma_f32_16x16x32_bf16 v[70:73], a[248:251], v[198:201], v[70:73]\n"
"ds_read_b128 v[190:193], v7 offset:26624\n"
"v_mfma_f32_16x16x32_bf16 v[74:77], a[252:255], v[198:201], v[74:77]\n"
"v_mfma_f32_16x16x32_bf16 v[78:81], a[248:251], v[202:205], v[78:81]\n"
"ds_read_b128 v[194:197], v7 offset:27648\n"
"v_mfma_f32_16x16x32_bf16 v[82:85], a[252:255], v[202:205], v[82:85]\n"
"s_nop 0\n"
"s_mul_i32 s66, s50, 131072\n"
"s_add_u32 s66, s66, 29360128\n"
"v_pk_fma_f32 v[170:171], v[150:151], v[162:163], v[22:23] op_sel:[0,0,0] op_sel_hi:[1,0,1] neg_lo:[0,0,1] neg_hi:[0,0,1]\n"
"v_pk_fma_f32 v[172:173], v[152:153], v[162:163], v[24:25] op_sel:[0,0,0] op_sel_hi:[1,0,1] neg_lo:[0,0,1] neg_hi:[0,0,1]\n"
"v_pk_fma_f32 v[174:175], v[154:155], v[162:163], v[26:27] op_sel:[0,0,0] op_sel_hi:[1,0,1] neg_lo:[0,0,1] neg_hi:[0,0,1]\n"
"v_pk_fma_f32 v[176:177], v[156:157], v[162:163], v[28:29] op_sel:[0,0,0] op_sel_hi:[1,0,1] neg_lo:[0,0,1] neg_hi:[0,0,1]\n"
"v_pk_fma_f32 v[30:31], v[30:31], v[142:143], v[170:171]\n"
"v_pk_fma_f32 v[32:33], v[32:33], v[144:145], v[172:173]\n"
"v_pk_fma_f32 v[34:35], v[34:35], v[146:147], v[174:175]\n"
"v_pk_fma_f32 v[36:37], v[36:37], v[148:149], v[176:177]\n"
"v_pk_mul_f32 v[170:171], v[30:31], v[30:31]\n"
"v_pk_mul_f32 v[172:173], v[32:33], v[32:33]\n"
"v_pk_mul_f32 v[174:175], v[34:35], v[34:35]\n"
"v_pk_mul_f32 v[176:177], v[36:37], v[36:37]\n"
"v_pk_fma_f32 v[170:171], v[170:171], s[86:87], v[178:179]\n"
"v_pk_fma_f32 v[172:173], v[172:173], s[86:87], v[178:179]\n"
"v_pk_fma_f32 v[174:175], v[174:175], s[86:87], v[178:179]\n"
"v_pk_fma_f32 v[176:177], v[176:177], s[86:87], v[178:179]\n"
"v_pk_mul_f32 v[170:171], v[170:171], v[30:31]\n"
"v_pk_mul_f32 v[172:173], v[172:173], v[32:33]\n"
"v_pk_mul_f32 v[174:175], v[174:175], v[34:35]\n"
"v_pk_mul_f32 v[176:177], v[176:177], v[36:37]\n"
"v_exp_f32 v170, v170\n"
"v_exp_f32 v171, v171\n"
"v_exp_f32 v172, v172\n"
"v_exp_f32 v173, v173\n"
"v_exp_f32 v174, v174\n"
"v_exp_f32 v175, v175\n"
"v_exp_f32 v176, v176\n"
"v_exp_f32 v177, v177\n"
"v_pk_add_f32 v[170:171], v[170:171], 1.0 op_sel_hi:[1,0]\n"
"v_pk_add_f32 v[172:173], v[172:173], 1.0 op_sel_hi:[1,0]\n"
"v_pk_add_f32 v[174:175], v[174:175], 1.0 op_sel_hi:[1,0]\n"
"v_pk_add_f32 v[176:177], v[176:177], 1.0 op_sel_hi:[1,0]\n"
"v_rcp_f32 v170, v170\n"
"v_rcp_f32 v171, v171\n"
"v_rcp_f32 v172, v172\n"
"v_rcp_f32 v173, v173\n"
"v_rcp_f32 v174, v174\n"
"v_rcp_f32 v175, v175\n"
"v_rcp_f32 v176, v176\n"
"v_rcp_f32 v177, v177\n"
"v_pk_mul_f32 v[30:31], v[30:31], v[170:171]\n"
"v_pk_mul_f32 v[32:33], v[32:33], v[172:173]\n"
"v_pk_mul_f32 v[34:35], v[34:35], v[174:175]\n"
"v_pk_mul_f32 v[36:37], v[36:37], v[176:177]\n"
"v_pk_fma_f32 v[170:171], v[150:151], v[162:163], v[22:23] op_sel:[0,1,0] op_sel_hi:[1,1,1] neg_lo:[0,0,1] neg_hi:[0,0,1]\n"
"v_pk_fma_f32 v[172:173], v[152:153], v[162:163], v[24:25] op_sel:[0,1,0] op_sel_hi:[1,1,1] neg_lo:[0,0,1] neg_hi:[0,0,1]\n"
"v_pk_fma_f32 v[174:175], v[154:155], v[162:163], v[26:27] op_sel:[0,1,0] op_sel_hi:[1,1,1] neg_lo:[0,0,1] neg_hi:[0,0,1]\n"
"v_pk_fma_f32 v[176:177], v[156:157], v[162:163], v[28:29] op_sel:[0,1,0] op_sel_hi:[1,1,1] neg_lo:[0,0,1] neg_hi:[0,0,1]\n"
"v_pk_fma_f32 v[38:39], v[38:39], v[142:143], v[170:171]\n"
"v_pk_fma_f32 v[40:41], v[40:41], v[144:145], v[172:173]\n"
"v_pk_fma_f32 v[42:43], v[42:43], v[146:147], v[174:175]\n"
"v_pk_fma_f32 v[44:45], v[44:45], v[148:149], v[176:177]\n"
"v_pk_mul_f32 v[170:171], v[38:39], v[38:39]\n"
"v_pk_mul_f32 v[172:173], v[40:41], v[40:41]\n"
"v_pk_mul_f32 v[174:175], v[42:43], v[42:43]\n"
"v_pk_mul_f32 v[176:177], v[44:45], v[44:45]\n"
"v_pk_fma_f32 v[170:171], v[170:171], s[86:87], v[178:179]\n"
"v_pk_fma_f32 v[172:173], v[172:173], s[86:87], v[178:179]\n"
"v_pk_fma_f32 v[174:175], v[174:175], s[86:87], v[178:179]\n"
"v_pk_fma_f32 v[176:177], v[176:177], s[86:87], v[178:179]\n"
"v_pk_mul_f32 v[170:171], v[170:171], v[38:39]\n"
"v_pk_mul_f32 v[172:173], v[172:173], v[40:41]\n"
"v_pk_mul_f32 v[174:175], v[174:175], v[42:43]\n"
"v_pk_mul_f32 v[176:177], v[176:177], v[44:45]\n"
"v_exp_f32 v170, v170\n"
"v_exp_f32 v171, v171\n"
"v_exp_f32 v172, v172\n"
"v_exp_f32 v173, v173\n"
"v_exp_f32 v174, v174\n"
"v_exp_f32 v175, v175\n"
"v_exp_f32 v176, v176\n"
"v_exp_f32 v177, v177\n"
"v_pk_add_f32 v[170:171], v[170:171], 1.0 op_sel_hi:[1,0]\n"
"v_pk_add_f32 v[172:173], v[172:173], 1.0 op_sel_hi:[1,0]\n"
"v_pk_add_f32 v[174:175], v[174:175], 1.0 op_sel_hi:[1,0]\n"
"v_pk_add_f32 v[176:177], v[176:177], 1.0 op_sel_hi:[1,0]\n"
"v_rcp_f32 v170, v170\n"
"v_rcp_f32 v171, v171\n"
"v_rcp_f32 v172, v172\n"
"v_rcp_f32 v173, v173\n"
"v_rcp_f32 v174, v174\n"
"v_rcp_f32 v175, v175\n"
"v_rcp_f32 v176, v176\n"
"v_rcp_f32 v177, v177\n"
"v_pk_mul_f32 v[38:39], v[38:39], v[170:171]\n"
"v_pk_mul_f32 v[40:41], v[40:41], v[172:173]\n"
"v_pk_mul_f32 v[42:43], v[42:43], v[174:175]\n"
"v_pk_mul_f32 v[44:45], v[44:45], v[176:177]\n"
"v_pk_fma_f32 v[170:171], v[150:151], v[164:165], v[22:23] op_sel:[0,0,0] op_sel_hi:[1,0,1] neg_lo:[0,0,1] neg_hi:[0,0,1]\n"
"v_pk_fma_f32 v[172:173], v[152:153], v[164:165], v[24:25] op_sel:[0,0,0] op_sel_hi:[1,0,1] neg_lo:[0,0,1] neg_hi:[0,0,1]\n"
"v_pk_fma_f32 v[174:175], v[154:155], v[164:165], v[26:27] op_sel:[0,0,0] op_sel_hi:[1,0,1] neg_lo:[0,0,1] neg_hi:[0,0,1]\n"
"v_pk_fma_f32 v[176:177], v[156:157], v[164:165], v[28:29] op_sel:[0,0,0] op_sel_hi:[1,0,1] neg_lo:[0,0,1] neg_hi:[0,0,1]\n"
"v_pk_fma_f32 v[46:47], v[46:47], v[142:143], v[170:171]\n"
"v_pk_fma_f32 v[48:49], v[48:49], v[144:145], v[172:173]\n"
"v_pk_fma_f32 v[50:51], v[50:51], v[146:147], v[174:175]\n"
"v_pk_fma_f32 v[52:53], v[52:53], v[148:149], v[176:177]\n"
"v_pk_mul_f32 v[170:171], v[46:47], v[46:47]\n"
"v_pk_mul_f32 v[172:173], v[48:49], v[48:49]\n"
"v_pk_mul_f32 v[174:175], v[50:51], v[50:51]\n"
"v_pk_mul_f32 v[176:177], v[52:53], v[52:53]\n"
"v_pk_fma_f32 v[170:171], v[170:171], s[86:87], v[178:179]\n"
"v_pk_fma_f32 v[172:173], v[172:173], s[86:87], v[178:179]\n"
"v_pk_fma_f32 v[174:175], v[174:175], s[86:87], v[178:179]\n"
"v_pk_fma_f32 v[176:177], v[176:177], s[86:87], v[178:179]\n"
"v_pk_mul_f32 v[170:171], v[170:171], v[46:47]\n"
"v_pk_mul_f32 v[172:173], v[172:173], v[48:49]\n"
"v_pk_mul_f32 v[174:175], v[174:175], v[50:51]\n"
"v_pk_mul_f32 v[176:177], v[176:177], v[52:53]\n"
"v_exp_f32 v170, v170\n"
"v_exp_f32 v171, v171\n"
"v_exp_f32 v172, v172\n"
"v_exp_f32 v173, v173\n"
"v_exp_f32 v174, v174\n"
"v_exp_f32 v175, v175\n"
"v_exp_f32 v176, v176\n"
"v_exp_f32 v177, v177\n"
"v_pk_add_f32 v[170:171], v[170:171], 1.0 op_sel_hi:[1,0]\n"
"v_pk_add_f32 v[172:173], v[172:173], 1.0 op_sel_hi:[1,0]\n"
"v_pk_add_f32 v[174:175], v[174:175], 1.0 op_sel_hi:[1,0]\n"
"v_pk_add_f32 v[176:177], v[176:177], 1.0 op_sel_hi:[1,0]\n"
"v_rcp_f32 v170, v170\n"
"v_rcp_f32 v171, v171\n"
"v_rcp_f32 v172, v172\n"
"v_rcp_f32 v173, v173\n"
"v_rcp_f32 v174, v174\n"
"v_rcp_f32 v175, v175\n"
"v_rcp_f32 v176, v176\n"
"v_rcp_f32 v177, v177\n"
"v_pk_mul_f32 v[46:47], v[46:47], v[170:171]\n"
"v_pk_mul_f32 v[48:49], v[48:49], v[172:173]\n"
"v_pk_mul_f32 v[50:51], v[50:51], v[174:175]\n"
"v_pk_mul_f32 v[52:53], v[52:53], v[176:177]\n"
"v_pk_fma_f32 v[170:171], v[150:151], v[164:165], v[22:23] op_sel:[0,1,0] op_sel_hi:[1,1,1] neg_lo:[0,0,1] neg_hi:[0,0,1]\n"
"v_pk_fma_f32 v[172:173], v[152:153], v[164:165], v[24:25] op_sel:[0,1,0] op_sel_hi:[1,1,1] neg_lo:[0,0,1] neg_hi:[0,0,1]\n"
"v_pk_fma_f32 v[174:175], v[154:155], v[164:165], v[26:27] op_sel:[0,1,0] op_sel_hi:[1,1,1] neg_lo:[0,0,1] neg_hi:[0,0,1]\n"
"v_pk_fma_f32 v[176:177], v[156:157], v[164:165], v[28:29] op_sel:[0,1,0] op_sel_hi:[1,1,1] neg_lo:[0,0,1] neg_hi:[0,0,1]\n"
"v_pk_fma_f32 v[54:55], v[54:55], v[142:143], v[170:171]\n"
"v_pk_fma_f32 v[56:57], v[56:57], v[144:145], v[172:173]\n"
"v_pk_fma_f32 v[58:59], v[58:59], v[146:147], v[174:175]\n"
"v_pk_fma_f32 v[60:61], v[60:61], v[148:149], v[176:177]\n"
"v_pk_mul_f32 v[170:171], v[54:55], v[54:55]\n"
"v_pk_mul_f32 v[172:173], v[56:57], v[56:57]\n"
"v_pk_mul_f32 v[174:175], v[58:59], v[58:59]\n"
"v_pk_mul_f32 v[176:177], v[60:61], v[60:61]\n"
"v_pk_fma_f32 v[170:171], v[170:171], s[86:87], v[178:179]\n"
"v_pk_fma_f32 v[172:173], v[172:173], s[86:87], v[178:179]\n"
"v_pk_fma_f32 v[174:175], v[174:175], s[86:87], v[178:179]\n"
"v_pk_fma_f32 v[176:177], v[176:177], s[86:87], v[178:179]\n"
"v_pk_mul_f32 v[170:171], v[170:171], v[54:55]\n"
"v_pk_mul_f32 v[172:173], v[172:173], v[56:57]\n"
"v_pk_mul_f32 v[174:175], v[174:175], v[58:59]\n"
"v_pk_mul_f32 v[176:177], v[176:177], v[60:61]\n"
"v_exp_f32 v170, v170\n"
"v_exp_f32 v171, v171\n"
"v_exp_f32 v172, v172\n"
"v_exp_f32 v173, v173\n"
"v_exp_f32 v174, v174\n"
"v_exp_f32 v175, v175\n"
"v_exp_f32 v176, v176\n"
"v_exp_f32 v177, v177\n"
"v_pk_add_f32 v[170:171], v[170:171], 1.0 op_sel_hi:[1,0]\n"
"v_pk_add_f32 v[172:173], v[172:173], 1.0 op_sel_hi:[1,0]\n"
"v_pk_add_f32 v[174:175], v[174:175], 1.0 op_sel_hi:[1,0]\n"
"v_pk_add_f32 v[176:177], v[176:177], 1.0 op_sel_hi:[1,0]\n"
"v_rcp_f32 v170, v170\n"
"v_rcp_f32 v171, v171\n"
"v_rcp_f32 v172, v172\n"
"v_rcp_f32 v173, v173\n"
"v_rcp_f32 v174, v174\n"
"v_rcp_f32 v175, v175\n"
"v_rcp_f32 v176, v176\n"
"v_rcp_f32 v177, v177\n"
"v_pk_mul_f32 v[54:55], v[54:55], v[170:171]\n"
"v_pk_mul_f32 v[56:57], v[56:57], v[172:173]\n"
"v_pk_mul_f32 v[58:59], v[58:59], v[174:175]\n"
"v_pk_mul_f32 v[60:61], v[60:61], v[176:177]\n"
"v_pk_fma_f32 v[170:171], v[150:151], v[166:167], v[22:23] op_sel:[0,0,0] op_sel_hi:[1,0,1] neg_lo:[0,0,1] neg_hi:[0,0,1]\n"
"v_pk_fma_f32 v[172:173], v[152:153], v[166:167], v[24:25] op_sel:[0,0,0] op_sel_hi:[1,0,1] neg_lo:[0,0,1] neg_hi:[0,0,1]\n"
"v_pk_fma_f32 v[174:175], v[154:155], v[166:167], v[26:27] op_sel:[0,0,0] op_sel_hi:[1,0,1] neg_lo:[0,0,1] neg_hi:[0,0,1]\n"
"v_pk_fma_f32 v[176:177], v[156:157], v[166:167], v[28:29] op_sel:[0,0,0] op_sel_hi:[1,0,1] neg_lo:[0,0,1] neg_hi:[0,0,1]\n"
"v_pk_fma_f32 v[62:63], v[62:63], v[142:143], v[170:171]\n"
"v_pk_fma_f32 v[64:65], v[64:65], v[144:145], v[172:173]\n"
"v_pk_fma_f32 v[66:67], v[66:67], v[146:147], v[174:175]\n"
"v_pk_fma_f32 v[68:69], v[68:69], v[148:149], v[176:177]\n"
"v_pk_mul_f32 v[170:171], v[62:63], v[62:63]\n"
"v_pk_mul_f32 v[172:173], v[64:65], v[64:65]\n"
"v_pk_mul_f32 v[174:175], v[66:67], v[66:67]\n"
"v_pk_mul_f32 v[176:177], v[68:69], v[68:69]\n"
"v_pk_fma_f32 v[170:171], v[170:171], s[86:87], v[178:179]\n"
"v_pk_fma_f32 v[172:173], v[172:173], s[86:87], v[178:179]\n"
"v_pk_fma_f32 v[174:175], v[174:175], s[86:87], v[178:179]\n"
"v_pk_fma_f32 v[176:177], v[176:177], s[86:87], v[178:179]\n"
"v_pk_mul_f32 v[170:171], v[170:171], v[62:63]\n"
"v_pk_mul_f32 v[172:173], v[172:173], v[64:65]\n"
"v_pk_mul_f32 v[174:175], v[174:175], v[66:67]\n"
"v_pk_mul_f32 v[176:177], v[176:177], v[68:69]\n"
"v_exp_f32 v170, v170\n"
"v_exp_f32 v171, v171\n"
"v_exp_f32 v172, v172\n"
"v_exp_f32 v173, v173\n"
"v_exp_f32 v174, v174\n"
"v_exp_f32 v175, v175\n"
"v_exp_f32 v176, v176\n"
"v_exp_f32 v177, v177\n"
"v_pk_add_f32 v[170:171], v[170:171], 1.0 op_sel_hi:[1,0]\n"
"v_pk_add_f32 v[172:173], v[172:173], 1.0 op_sel_hi:[1,0]\n"
"v_pk_add_f32 v[174:175], v[174:175], 1.0 op_sel_hi:[1,0]\n"
"v_pk_add_f32 v[176:177], v[176:177], 1.0 op_sel_hi:[1,0]\n"
"v_rcp_f32 v170, v170\n"
"v_rcp_f32 v171, v171\n"
"v_rcp_f32 v172, v172\n"
"v_rcp_f32 v173, v173\n"
"v_rcp_f32 v174, v174\n"
"v_rcp_f32 v175, v175\n"
"v_rcp_f32 v176, v176\n"
"v_rcp_f32 v177, v177\n"
"v_pk_mul_f32 v[62:63], v[62:63], v[170:171]\n"
"v_pk_mul_f32 v[64:65], v[64:65], v[172:173]\n"
"v_pk_mul_f32 v[66:67], v[66:67], v[174:175]\n"
"v_pk_mul_f32 v[68:69], v[68:69], v[176:177]\n"
"v_pk_fma_f32 v[170:171], v[150:151], v[166:167], v[22:23] op_sel:[0,1,0] op_sel_hi:[1,1,1] neg_lo:[0,0,1] neg_hi:[0,0,1]\n"
"v_pk_fma_f32 v[172:173], v[152:153], v[166:167], v[24:25] op_sel:[0,1,0] op_sel_hi:[1,1,1] neg_lo:[0,0,1] neg_hi:[0,0,1]\n"
"v_pk_fma_f32 v[174:175], v[154:155], v[166:167], v[26:27] op_sel:[0,1,0] op_sel_hi:[1,1,1] neg_lo:[0,0,1] neg_hi:[0,0,1]\n"
"v_pk_fma_f32 v[176:177], v[156:157], v[166:167], v[28:29] op_sel:[0,1,0] op_sel_hi:[1,1,1] neg_lo:[0,0,1] neg_hi:[0,0,1]\n"
"v_pk_fma_f32 v[70:71], v[70:71], v[142:143], v[170:171]\n"
"v_pk_fma_f32 v[72:73], v[72:73], v[144:145], v[172:173]\n"
"v_pk_fma_f32 v[74:75], v[74:75], v[146:147], v[174:175]\n"
"v_pk_fma_f32 v[76:77], v[76:77], v[148:149], v[176:177]\n"
"v_pk_mul_f32 v[170:171], v[70:71], v[70:71]\n"
"v_pk_mul_f32 v[172:173], v[72:73], v[72:73]\n"
"v_pk_mul_f32 v[174:175], v[74:75], v[74:75]\n"
"v_pk_mul_f32 v[176:177], v[76:77], v[76:77]\n"
"v_pk_fma_f32 v[170:171], v[170:171], s[86:87], v[178:179]\n"
"v_pk_fma_f32 v[172:173], v[172:173], s[86:87], v[178:179]\n"
"v_pk_fma_f32 v[174:175], v[174:175], s[86:87], v[178:179]\n"
"v_pk_fma_f32 v[176:177], v[176:177], s[86:87], v[178:179]\n"
"v_pk_mul_f32 v[170:171], v[170:171], v[70:71]\n"
"v_pk_mul_f32 v[172:173], v[172:173], v[72:73]\n"
"v_pk_mul_f32 v[174:175], v[174:175], v[74:75]\n"
"v_pk_mul_f32 v[176:177], v[176:177], v[76:77]\n"
"v_exp_f32 v170, v170\n"
"v_exp_f32 v171, v171\n"
"v_exp_f32 v172, v172\n"
"v_exp_f32 v173, v173\n"
"v_exp_f32 v174, v174\n"
"v_exp_f32 v175, v175\n"
"v_exp_f32 v176, v176\n"
"v_exp_f32 v177, v177\n"
"v_pk_add_f32 v[170:171], v[170:171], 1.0 op_sel_hi:[1,0]\n"
"v_pk_add_f32 v[172:173], v[172:173], 1.0 op_sel_hi:[1,0]\n"
"v_pk_add_f32 v[174:175], v[174:175], 1.0 op_sel_hi:[1,0]\n"
"v_pk_add_f32 v[176:177], v[176:177], 1.0 op_sel_hi:[1,0]\n"
"v_rcp_f32 v170, v170\n"
"v_rcp_f32 v171, v171\n"
"v_rcp_f32 v172, v172\n"
"v_rcp_f32 v173, v173\n"
"v_rcp_f32 v174, v174\n"
"v_rcp_f32 v175, v175\n"
"v_rcp_f32 v176, v176\n"
"v_rcp_f32 v177, v177\n"
"v_pk_mul_f32 v[70:71], v[70:71], v[170:171]\n"
"v_pk_mul_f32 v[72:73], v[72:73], v[172:173]\n"
"v_pk_mul_f32 v[74:75], v[74:75], v[174:175]\n"
"v_pk_mul_f32 v[76:77], v[76:77], v[176:177]\n"
"v_pk_fma_f32 v[170:171], v[150:151], v[168:169], v[22:23] op_sel:[0,0,0] op_sel_hi:[1,0,1] neg_lo:[0,0,1] neg_hi:[0,0,1]\n"
"v_pk_fma_f32 v[172:173], v[152:153], v[168:169], v[24:25] op_sel:[0,0,0] op_sel_hi:[1,0,1] neg_lo:[0,0,1] neg_hi:[0,0,1]\n"
"v_pk_fma_f32 v[174:175], v[154:155], v[168:169], v[26:27] op_sel:[0,0,0] op_sel_hi:[1,0,1] neg_lo:[0,0,1] neg_hi:[0,0,1]\n"
"v_pk_fma_f32 v[176:177], v[156:157], v[168:169], v[28:29] op_sel:[0,0,0] op_sel_hi:[1,0,1] neg_lo:[0,0,1] neg_hi:[0,0,1]\n"
"v_pk_fma_f32 v[78:79], v[78:79], v[142:143], v[170:171]\n"
"v_pk_fma_f32 v[80:81], v[80:81], v[144:145], v[172:173]\n"
"v_pk_fma_f32 v[82:83], v[82:83], v[146:147], v[174:175]\n"
"v_pk_fma_f32 v[84:85], v[84:85], v[148:149], v[176:177]\n"
"v_pk_mul_f32 v[170:171], v[78:79], v[78:79]\n"
"v_pk_mul_f32 v[172:173], v[80:81], v[80:81]\n"
"v_pk_mul_f32 v[174:175], v[82:83], v[82:83]\n"
"v_pk_mul_f32 v[176:177], v[84:85], v[84:85]\n"
"v_pk_fma_f32 v[170:171], v[170:171], s[86:87], v[178:179]\n"
"v_pk_fma_f32 v[172:173], v[172:173], s[86:87], v[178:179]\n"
"v_pk_fma_f32 v[174:175], v[174:175], s[86:87], v[178:179]\n"
"v_pk_fma_f32 v[176:177], v[176:177], s[86:87], v[178:179]\n"
"v_pk_mul_f32 v[170:171], v[170:171], v[78:79]\n"
"v_pk_mul_f32 v[172:173], v[172:173], v[80:81]\n"
"v_pk_mul_f32 v[174:175], v[174:175], v[82:83]\n"
"v_pk_mul_f32 v[176:177], v[176:177], v[84:85]\n"
"v_exp_f32 v170, v170\n"
"v_exp_f32 v171, v171\n"
"v_exp_f32 v172, v172\n"
"v_exp_f32 v173, v173\n"
"v_exp_f32 v174, v174\n"
"v_exp_f32 v175, v175\n"
"v_exp_f32 v176, v176\n"
"v_exp_f32 v177, v177\n"
"v_pk_add_f32 v[170:171], v[170:171], 1.0 op_sel_hi:[1,0]\n"
"v_pk_add_f32 v[172:173], v[172:173], 1.0 op_sel_hi:[1,0]\n"
"v_pk_add_f32 v[174:175], v[174:175], 1.0 op_sel_hi:[1,0]\n"
"v_pk_add_f32 v[176:177], v[176:177], 1.0 op_sel_hi:[1,0]\n"
"v_rcp_f32 v170, v170\n"
"v_rcp_f32 v171, v171\n"
"v_rcp_f32 v172, v172\n"
"v_rcp_f32 v173, v173\n"
"v_rcp_f32 v174, v174\n"
"v_rcp_f32 v175, v175\n"
"v_rcp_f32 v176, v176\n"
"v_rcp_f32 v177, v177\n"
"v_pk_mul_f32 v[78:79], v[78:79], v[170:171]\n"
"v_pk_mul_f32 v[80:81], v[80:81], v[172:173]\n"
"v_pk_mul_f32 v[82:83], v[82:83], v[174:175]\n"
"v_pk_mul_f32 v[84:85], v[84:85], v[176:177]\n"
"v_pk_add_f32 v[236:237], v[30:31], v[38:39]\n"
"v_pk_mul_f32 v[244:245], v[30:31], v[30:31]\n"
"v_pk_add_f32 v[238:239], v[32:33], v[40:41]\n"
"v_pk_mul_f32 v[246:247], v[32:33], v[32:33]\n"
"v_pk_add_f32 v[240:241], v[34:35], v[42:43]\n"
"v_pk_mul_f32 v[248:249], v[34:35], v[34:35]\n"
"v_pk_add_f32 v[242:243], v[36:37], v[44:45]\n"
"v_pk_mul_f32 v[250:251], v[36:37], v[36:37]\n"
"v_pk_fma_f32 v[244:245], v[38:39], v[38:39], v[244:245]\n"
"v_pk_fma_f32 v[246:247], v[40:41], v[40:41], v[246:247]\n"
"v_pk_fma_f32 v[248:249], v[42:43], v[42:43], v[248:249]\n"
"v_pk_fma_f32 v[250:251], v[44:45], v[44:45], v[250:251]\n"
"v_pk_add_f32 v[236:237], v[236:237], v[46:47]\n"
"v_pk_fma_f32 v[244:245], v[46:47], v[46:47], v[244:245]\n"
"v_pk_add_f32 v[238:239], v[238:239], v[48:49]\n"
"v_pk_fma_f32 v[246:247], v[48:49], v[48:49], v[246:247]\n"
"v_pk_add_f32 v[240:241], v[240:241], v[50:51]\n"
"v_pk_fma_f32 v[248:249], v[50:51], v[50:51], v[248:249]\n"
"v_pk_add_f32 v[242:243], v[242:243], v[52:53]\n"
"v_pk_fma_f32 v[250:251], v[52:53], v[52:53], v[250:251]\n"
"v_pk_add_f32 v[236:237], v[236:237], v[54:55]\n"
"v_pk_fma_f32 v[244:245], v[54:55], v[54:55], v[244:245]\n"
"v_pk_add_f32 v[238:239], v[238:239], v[56:57]\n"
"v_pk_fma_f32 v[246:247], v[56:57], v[56:57], v[246:247]\n"
"v_pk_add_f32 v[240:241], v[240:241], v[58:59]\n"
"v_pk_fma_f32 v[248:249], v[58:59], v[58:59], v[248:249]\n"
"v_pk_add_f32 v[242:243], v[242:243], v[60:61]\n"
"v_pk_fma_f32 v[250:251], v[60:61], v[60:61], v[250:251]\n"
"v_pk_add_f32 v[236:237], v[236:237], v[62:63]\n"
"v_pk_fma_f32 v[244:245], v[62:63], v[62:63], v[244:245]\n"
"v_pk_add_f32 v[238:239], v[238:239], v[64:65]\n"
"v_pk_fma_f32 v[246:247], v[64:65], v[64:65], v[246:247]\n"
"v_pk_add_f32 v[240:241], v[240:241], v[66:67]\n"
"v_pk_fma_f32 v[248:249], v[66:67], v[66:67], v[248:249]\n"
"v_pk_add_f32 v[242:243], v[242:243], v[68:69]\n"
"v_pk_fma_f32 v[250:251], v[68:69], v[68:69], v[250:251]\n"
"v_pk_add_f32 v[236:237], v[236:237], v[70:71]\n"
"v_pk_fma_f32 v[244:245], v[70:71], v[70:71], v[244:245]\n"
"v_pk_add_f32 v[238:239], v[238:239], v[72:73]\n"
"v_pk_fma_f32 v[246:247], v[72:73], v[72:73], v[246:247]\n"
"v_pk_add_f32 v[240:241], v[240:241], v[74:75]\n"
"v_pk_fma_f32 v[248:249], v[74:75], v[74:75], v[248:249]\n"
"v_pk_add_f32 v[242:243], v[242:243], v[76:77]\n"
"v_pk_fma_f32 v[250:251], v[76:77], v[76:77], v[250:251]\n"
"v_pk_add_f32 v[236:237], v[236:237], v[78:79]\n"
"v_pk_fma_f32 v[244:245], v[78:79], v[78:79], v[244:245]\n"
"v_pk_add_f32 v[238:239], v[238:239], v[80:81]\n"
"v_pk_fma_f32 v[246:247], v[80:81], v[80:81], v[246:247]\n"
"v_pk_add_f32 v[240:241], v[240:241], v[82:83]\n"
"v_pk_fma_f32 v[248:249], v[82:83], v[82:83], v[248:249]\n"
"v_pk_add_f32 v[242:243], v[242:243], v[84:85]\n"
"v_pk_fma_f32 v[250:251], v[84:85], v[84:85], v[250:251]\n"
"s_waitcnt lgkmcnt(0)\n"
"v_mfma_f32_16x16x32_bf16 v[86:89], v[206:209], a[0:3], v[18:21]\n"
"ds_read_b128 v[198:201], v7 offset:28672\n"
"v_add_f32_dpp v170, v236, v236 row_ror:8 row_mask:0xf bank_mask:0x3\n"
"v_mfma_f32_16x16x32_bf16 v[90:93], v[206:209], a[4:7], v[18:21]\n"
"buffer_load_dwordx4 v2, s[28:31], s65 offen offset:2048 sc1 lds\n"
"v_mfma_f32_16x16x32_bf16 v[94:97], v[210:213], a[0:3], v[18:21]\n"
"ds_read_b128 v[202:205], v7 offset:29696\n"
"v_add_f32_dpp v171, v237, v237 row_ror:8 row_mask:0xf bank_mask:0x3\n"
"v_mfma_f32_16x16x32_bf16 v[98:101], v[210:213], a[4:7], v[18:21]\n"
"v_add_f32_dpp v172, v238, v238 row_ror:8 row_mask:0xf bank_mask:0x3\n"
"v_add_f32_dpp v173, v239, v239 row_ror:8 row_mask:0xf bank_mask:0x3\n"
"v_mfma_f32_16x16x32_bf16 v[102:105], v[214:217], a[0:3], v[18:21]\n"
"ds_read_b128 v[206:209], v7 offset:30720\n"
"v_add_f32_dpp v174, v240, v240 row_ror:8 row_mask:0xf bank_mask:0x3\n"
"v_mfma_f32_16x16x32_bf16 v[106:109], v[214:217], a[4:7], v[18:21]\n"
"buffer_load_dwordx4 v2, s[28:31], s65 offen offset:3072 sc1 lds\n"
"v_mfma_f32_16x16x32_bf16 v[110:113], v[218:221], a[0:3], v[18:21]\n"
"ds_read_b128 v[210:213], v7 offset:31744\n"
"v_add_f32_dpp v175, v241, v241 row_ror:8 row_mask:0xf bank_mask:0x3\n"
"v_mfma_f32_16x16x32_bf16 v[114:117], v[218:221], a[4:7], v[18:21]\n"
"v_add_f32_dpp v176, v242, v242 row_ror:8 row_mask:0xf bank_mask:0x3\n"
"v_mfma_f32_16x16x32_bf16 v[118:121], v[222:225], a[0:3], v[18:21]\n"
"ds_read_b128 v[214:217], v7 offset:32768\n"
"v_add_f32_dpp v177, v243, v243 row_ror:8 row_mask:0xf bank_mask:0x3\n"
"v_mfma_f32_16x16x32_bf16 v[122:125], v[222:225], a[4:7], v[18:21]\n"
"s_add_u32 m0, s54, 4096\n"
"v_add_f32_dpp v170, v244, v244 row_ror:8 row_mask:0xf bank_mask:0xc\n"
"v_mfma_f32_16x16x32_bf16 v[126:129], v[226:229], a[0:3], v[18:21]\n"
"ds_read_b128 v[218:221], v7 offset:33792\n"
"v_add_f32_dpp v171, v245, v245 row_ror:8 row_mask:0xf bank_mask:0xc\n"
"v_mfma_f32_16x16x32_bf16 v[130:133], v[226:229], a[4:7], v[18:21]\n"
"buffer_load_dwordx4 v3, s[28:31], s65 offen offset:0 sc1 lds\n"
"v_mfma_f32_16x16x32_bf16 v[134:137], v[190:193], a[0:3], v[18:21]\n"
"ds_read_b128 v[222:225], v7 offset:34816\n"
"v_add_f32_dpp v172, v246, v246 row_ror:8 row_mask:0xf bank_mask:0xc\n"
"v_mfma_f32_16x16x32_bf16 v[138:141], v[190:193], a[4:7], v[18:21]\n"
"buffer_load_dwordx4 v3, s[28:31], s65 offen offset:1024 sc1 lds\n"
"v_mfma_f32_16x16x32_bf16 v[86:89], v[194:197], a[8:11], v[86:89]\n"
"ds_read_b128 v[226:229], v7 offset:35840\n"
"v_add_f32_dpp v173, v247, v247 row_ror:8 row_mask:0xf bank_mask:0xc\n"
"v_mfma_f32_16x16x32_bf16 v[90:93], v[194:197], a[12:15], v[90:93]\n"
"s_waitcnt lgkmcnt(4)\n"
"v_add_f32_dpp v174, v248, v248 row_ror:8 row_mask:0xf bank_mask:0xc\n"
"v_mfma_f32_16x16x32_bf16 v[94:97], v[198:201], a[8:11], v[94:97]\n"
"ds_read_b128 v[190:193], v7 offset:36864\n"
"v_add_f32_dpp v175, v249, v249 row_ror:8 row_mask:0xf bank_mask:0xc\n"
"v_mfma_f32_16x16x32_bf16 v[98:101], v[198:201], a[12:15], v[98:101]\n"
"buffer_load_dwordx4 v3, s[28:31], s65 offen offset:2048 sc1 lds\n"
"v_mfma_f32_16x16x32_bf16 v[102:105], v[202:205], a[8:11], v[102:105]\n"
"ds_read_b128 v[194:197], v7 offset:37888\n"
"v_add_f32_dpp v176, v250, v250 row_ror:8 row_mask:0xf bank_mask:0xc\n"
"v_mfma_f32_16x16x32_bf16 v[106:109], v[202:205], a[12:15], v[106:109]\n"
"v_add_f32_dpp v177, v251, v251 row_ror:8 row_mask:0xf bank_mask:0xc\n"
"v_add_f32_dpp v236, v170, v170 row_half_mirror row_mask:0xf bank_mask:0x5\n"
"v_mfma_f32_16x16x32_bf16 v[110:113], v[206:209], a[8:11], v[110:113]\n"
"ds_read_b128 v[198:201], v7 offset:38912\n"
"v_add_f32_dpp v237, v171, v171 row_half_mirror row_mask:0xf bank_mask:0x5\n"
"v_add_f32_dpp v238, v172, v172 row_half_mirror row_mask:0xf bank_mask:0x5\n"
"v_mfma_f32_16x16x32_bf16 v[114:117], v[206:209], a[12:15], v[114:117]\n"
"s_bfe_u32 s65, s81, 262156\n"
"s_mul_i32 s65, s65, 28672\n"
"v_add_f32_dpp v239, v173, v173 row_half_mirror row_mask:0xf bank_mask:0x5\n"
"v_mfma_f32_16x16x32_bf16 v[118:121], v[210:213], a[8:11], v[118:121]\n"
"ds_read_b128 v[202:205], v7 offset:39936\n"
"s_add_u32 s65, s65, s69\n"
"s_add_u32 s65, s65, 3670016\n"
"v_mfma_f32_16x16x32_bf16 v[122:125], v[210:213], a[12:15], v[122:125]\n"
"s_waitcnt lgkmcnt(4)\n"
"s_add_u32 m0, s54, 28672\n"
"v_mfma_f32_16x16x32_bf16 v[126:129], v[214:217], a[8:11], v[126:129]\n"
"ds_read_b128 v[206:209], v7 offset:40960\n"
"buffer_load_dwordx4 v2, s[28:31], s65 offen offset:0 sc1 lds\n"
"v_mfma_f32_16x16x32_bf16 v[130:133], v[214:217], a[12:15], v[130:133]\n"
"buffer_load_dwordx4 v2, s[28:31], s65 offen offset:1024 sc1 lds\n"
"v_mfma_f32_16x16x32_bf16 v[134:137], v[218:221], a[8:11], v[134:137]\n"
"ds_read_b128 v[210:213], v7 offset:41984\n"
"v_add_f32_dpp v236, v174, v174 row_half_mirror row_mask:0xf bank_mask:0xa\n"
"v_mfma_f32_16x16x32_bf16 v[138:141], v[218:221], a[12:15], v[138:141]\n"
"v_add_f32_dpp v237, v175, v175 row_half_mirror row_mask:0xf bank_mask:0xa\n"
"v_add_f32_dpp v238, v176, v176 row_half_mirror row_mask:0xf bank_mask:0xa\n"
"v_mfma_f32_16x16x32_bf16 v[86:89], v[222:225], a[16:19], v[86:89]\n"
"ds_read_b128 v[214:217], v7 offset:43008\n"
"v_add_f32_dpp v239, v177, v177 row_half_mirror row_mask:0xf bank_mask:0xa\n"
"v_mfma_f32_16x16x32_bf16 v[90:93], v[222:225], a[20:23], v[90:93]\n"
"buffer_load_dwordx4 v2, s[28:31], s65 offen offset:2048 sc1 lds\n"
"v_mfma_f32_16x16x32_bf16 v[94:97], v[226:229], a[16:19], v[94:97]\n"
"ds_read_b128 v[218:221], v7 offset:44032\n"
"v_add_f32_dpp v236, v236, v236 quad_perm:[1,0,3,2] row_mask:0xf bank_mask:0xf\n"
"v_mfma_f32_16x16x32_bf16 v[98:101], v[226:229], a[20:23], v[98:101]\n"
"s_waitcnt lgkmcnt(4)\n"
"v_add_f32_dpp v237, v237, v237 quad_perm:[1,0,3,2] row_mask:0xf bank_mask:0xf\n"
"v_mfma_f32_16x16x32_bf16 v[102:105], v[190:193], a[16:19], v[102:105]\n"
"ds_read_b128 v[222:225], v7 offset:45056\n"
"v_add_f32_dpp v238, v238, v238 quad_perm:[1,0,3,2] row_mask:0xf bank_mask:0xf\n"
"v_add_f32_dpp v239, v239, v239 quad_perm:[1,0,3,2] row_mask:0xf bank_mask:0xf\n"
"v_mfma_f32_16x16x32_bf16 v[106:109], v[190:193], a[20:23], v[106:109]\n"
"buffer_load_dwordx4 v2, s[28:31], s65 offen offset:3072 sc1 lds\n"
"v_add_f32_dpp v236, v236, v236 quad_perm:[2,3,0,1] row_mask:0xf bank_mask:0xf\n"
"v_add_f32_dpp v237, v237, v237 quad_perm:[2,3,0,1] row_mask:0xf bank_mask:0xf\n"
"v_add_f32_dpp v238, v238, v238 quad_perm:[2,3,0,1] row_mask:0xf bank_mask:0xf\n"
"v_add_f32_dpp v239, v239, v239 quad_perm:[2,3,0,1] row_mask:0xf bank_mask:0xf\n"
"v_cndmask_b32 v240, v236, v237, s[74:75]\n"
"v_cndmask_b32 v240, v240, v238, s[76:77]\n"
".Lmk_pg_12:\n"
"s_cbranch_execz .Lmk_pg_16\n"
"v_cndmask_b32 v240, v240, v239, s[78:79]\n"
"v_mov_b32 v241, s26\n"
"buffer_store_dwordx2 v[240:241], v14, s[28:31], s89 offen sc1\n"
"s_mov_b64 exec, 1\n"
"buffer_atomic_add v10, v9, s[28:31], s70 offen sc1\n"
"s_mov_b64 exec, -1\n"
"s_nop 1\n"
"v_mfma_f32_16x16x32_bf16 v[110:113], v[194:197], a[16:19], v[110:113]\n"
"ds_read_b128 v[226:229], v7 offset:46080\n"
"v_mfma_f32_16x16x32_bf16 v[114:117], v[194:197], a[20:23], v[114:117]\n"
"v_mfma_f32_16x16x32_bf16 v[118:121], v[198:201], a[16:19], v[118:121]\n"
"ds_read_b128 v[190:193], v7 offset:47104\n"
"v_mfma_f32_16x16x32_bf16 v[122:125], v[198:201], a[20:23], v[122:125]\n"
"s_add_u32 m0, s54, 32768\n"
"v_mfma_f32_16x16x32_bf16 v[126:129], v[202:205], a[16:19], v[126:129]\n"
"ds_read_b128 v[194:197], v7 offset:48128\n"
"v_mfma_f32_16x16x32_bf16 v[130:133], v[202:205], a[20:23], v[130:133]\n"
"s_waitcnt lgkmcnt(4)\n"
"v_mfma_f32_16x16x32_bf16 v[134:137], v[206:209], a[16:19], v[134:137]\n"
"s_waitcnt vmcnt(13)\n"
"s_barrier\n"
"ds_read_b128 v[198:201], v7 offset:49152\n"
"v_mfma_f32_16x16x32_bf16 v[138:141], v[206:209], a[20:23], v[138:141]\n"
"buffer_load_dwordx4 v3, s[28:31], s65 offen offset:0 sc1 lds\n"
"v_mfma_f32_16x16x32_bf16 v[86:89], v[210:213], a[24:27], v[86:89]\n"
"ds_read_b128 v[202:205], v7 offset:50176\n"
"v_mfma_f32_16x16x32_bf16 v[90:93], v[210:213], a[28:31], v[90:93]\n"
"buffer_load_dwordx4 v3, s[28:31], s65 offen offset:1024 sc1 lds\n"
"v_mfma_f32_16x16x32_bf16 v[94:97], v[214:217], a[24:27], v[94:97]\n"
"ds_read_b128 v[206:209], v7 offset:51200\n"
"v_mfma_f32_16x16x32_bf16 v[98:101], v[214:217], a[28:31], v[98:101]\n"
"v_mfma_f32_16x16x32_bf16 v[102:105], v[218:221], a[24:27], v[102:105]\n"
"ds_read_b128 v[210:213], v7 offset:52224\n"
"v_mfma_f32_16x16x32_bf16 v[106:109], v[218:221], a[28:31], v[106:109]\n"
"s_waitcnt lgkmcnt(4)\n"
"v_mfma_f32_16x16x32_bf16 v[110:113], v[222:225], a[24:27], v[110:113]\n"
"ds_read_b128 v[214:217], v7 offset:53248\n"
"v_mfma_f32_16x16x32_bf16 v[114:117], v[222:225], a[28:31], v[114:117]\n"
"buffer_load_dwordx4 v3, s[28:31], s65 offen offset:2048 sc1 lds\n"
"v_mfma_f32_16x16x32_bf16 v[118:121], v[226:229], a[24:27], v[118:121]\n"
"ds_read_b128 v[218:221], v7 offset:54272\n"
"v_mfma_f32_16x16x32_bf16 v[122:125], v[226:229], a[28:31], v[122:125]\n"
"s_bfe_u32 s65, s81, 262160\n"
"s_mul_i32 s65, s65, 28672\n"
"v_mfma_f32_16x16x32_bf16 v[126:129], v[190:193], a[24:27], v[126:129]\n"
"ds_read_b128 v[222:225], v7 offset:55296\n"
"v_mfma_f32_16x16x32_bf16 v[130:133], v[190:193], a[28:31], v[130:133]\n"
"s_add_u32 s65, s65, s69\n"
"s_add_u32 s65, s65, 3670016\n"
"s_add_u32 m0, s54, 57344\n"
"s_nop 0\n"
"buffer_load_dwordx4 v2, s[28:31], s65 offen offset:0 sc1 lds\n"
"v_mfma_f32_16x16x32_bf16 v[134:137], v[194:197], a[24:27], v[134:137]\n"
"ds_read_b128 v[226:229], v7 offset:56320\n"
"v_mfma_f32_16x16x32_bf16 v[138:141], v[194:197], a[28:31], v[138:141]\n"
"s_waitcnt lgkmcnt(4)\n"
"v_mfma_f32_16x16x32_bf16 v[86:89], v[198:201], a[32:35], v[86:89]\n"
"ds_read_b128 v[190:193], v7 offset:57344\n"
"v_mfma_f32_16x16x32_bf16 v[90:93], v[198:201], a[36:39], v[90:93]\n"
"buffer_load_dwordx4 v2, s[28:31], s65 offen offset:1024 sc1 lds\n"
"v_mfma_f32_16x16x32_bf16 v[94:97], v[202:205], a[32:35], v[94:97]\n"
"ds_read_b128 v[194:197], v7 offset:58368\n"
"v_mfma_f32_16x16x32_bf16 v[98:101], v[202:205], a[36:39], v[98:101]\n"
"buffer_load_dwordx4 v2, s[28:31], s65 offen offset:2048 sc1 lds\n"
"v_mfma_f32_16x16x32_bf16 v[102:105], v[206:209], a[32:35], v[102:105]\n"
"ds_read_b128 v[198:201], v7 offset:59392\n"
"v_mov_b32 v11, 0\n"
"s_mov_b64 exec, 1\n"
"buffer_atomic_add v11, v9, s[28:31], s70 offen sc0 sc1\n"
"s_mov_b64 exec, -1\n"
"s_nop 1\n"
"v_mfma_f32_16x16x32_bf16 v[106:109], v[206:209], a[36:39], v[106:109]\n"
"v_mfma_f32_16x16x32_bf16 v[110:113], v[210:213], a[32:35], v[110:113]\n"
"ds_read_b128 v[202:205], v7 offset:60416\n"
"v_mfma_f32_16x16x32_bf16 v[114:117], v[210:213], a[36:39], v[114:117]\n"
"s_waitcnt lgkmcnt(4)\n"
"v_mfma_f32_16x16x32_bf16 v[118:121], v[214:217], a[32:35], v[118:121]\n"
"ds_read_b128 v[206:209], v7 offset:61440\n"
"v_mfma_f32_16x16x32_bf16 v[122:125], v[214:217], a[36:39], v[122:125]\n"
"buffer_load_dwordx4 v2, s[28:31], s65 offen offset:3072 sc1 lds\n"
"v_mfma_f32_16x16x32_bf16 v[126:129], v[218:221], a[32:35], v[126:129]\n"
"ds_read_b128 v[210:213], v7 offset:62464\n"
"s_add_u32 m0, s54, 61440\n"
"v_mfma_f32_16x16x32_bf16 v[130:133], v[218:221], a[36:39], v[130:133]\n"
"v_mfma_f32_16x16x32_bf16 v[134:137], v[222:225], a[32:35], v[134:137]\n"
"ds_read_b128 v[214:217], v7 offset:63488\n"
"v_mfma_f32_16x16x32_bf16 v[138:141], v[222:225], a[36:39], v[138:141]\n"
"buffer_load_dwordx4 v3, s[28:31], s65 offen offset:0 sc1 lds\n"
"v_mfma_f32_16x16x32_bf16 v[86:89], v[226:229], a[40:43], v[86:89]\n"
"ds_read_b128 v[218:221], v7 offset:64512\n"
"v_mfma_f32_16x16x32_bf16 v[90:93], v[226:229], a[44:47], v[90:93]\n"
"s_waitcnt lgkmcnt(4)\n"
"v_mfma_f32_16x16x32_bf16 v[94:97], v[190:193], a[40:43], v[94:97]\n"
"ds_read_b128 v[222:225], v8 offset:0\n"
"v_mfma_f32_16x16x32_bf16 v[98:101], v[190:193], a[44:47], v[98:101]\n"
"buffer_load_dwordx4 v3, s[28:31], s65 offen offset:1024 sc1 lds\n"
"v_mfma_f32_16x16x32_bf16 v[102:105], v[194:197], a[40:43], v[102:105]\n"
"ds_read_b128 v[226:229], v8 offset:1024\n"
"v_mfma_f32_16x16x32_bf16 v[106:109], v[194:197], a[44:47], v[106:109]\n"
"buffer_load_dwordx4 v3, s[28:31], s65 offen offset:2048 sc1 lds\n"
"v_mfma_f32_16x16x32_bf16 v[110:113], v[198:201], a[40:43], v[110:113]\n"
"ds_read_b128 v[190:193], v8 offset:2048\n"
"v_mfma_f32_16x16x32_bf16 v[114:117], v[198:201], a[44:47], v[114:117]\n"
"v_mfma_f32_16x16x32_bf16 v[118:121], v[202:205], a[40:43], v[118:121]\n"
"ds_read_b128 v[194:197], v8 offset:3072\n"
"v_mfma_f32_16x16x32_bf16 v[122:125], v[202:205], a[44:47], v[122:125]\n"
"s_waitcnt lgkmcnt(4)\n"
"v_mfma_f32_16x16x32_bf16 v[126:129], v[206:209], a[40:43], v[126:129]\n"
"ds_read_b128 v[198:201], v8 offset:4096\n"
"v_mfma_f32_16x16x32_bf16 v[130:133], v[206:209], a[44:47], v[130:133]\n"
"s_bfe_u32 s64, s82, 262144\n"
"s_mul_i32 s64, s64, 8192\n"
"v_mfma_f32_16x16x32_bf16 v[134:137], v[210:213], a[40:43], v[134:137]\n"
"ds_read_b128 v[202:205], v8 offset:5120\n"
"s_add_u32 s64, s64, s66\n"
"buffer_load_dwordx4 a[0:3], v2, s[28:31], s64 offen offset:0 sc1\n"
"v_mfma_f32_16x16x32_bf16 v[138:141], v[210:213], a[44:47], v[138:141]\n"
"buffer_load_dwordx4 a[4:7], v2, s[28:31], s64 offen offset:1024 sc1\n"
"v_mfma_f32_16x16x32_bf16 v[86:89], v[214:217], a[48:51], v[86:89]\n"
"ds_read_b128 v[206:209], v8 offset:6144\n"
"v_mfma_f32_16x16x32_bf16 v[90:93], v[214:217], a[52:55], v[90:93]\n"
"v_mfma_f32_16x16x32_bf16 v[94:97], v[218:221], a[48:51], v[94:97]\n"
"ds_read_b128 v[210:213], v8 offset:7168\n"
"v_mfma_f32_16x16x32_bf16 v[98:101], v[218:221], a[52:55], v[98:101]\n"
"s_waitcnt lgkmcnt(4)\n"
"v_mfma_f32_16x16x32_bf16 v[102:105], v[222:225], a[48:51], v[102:105]\n"
"ds_read_b128 v[214:217], v8 offset:8192\n"
"v_mfma_f32_16x16x32_bf16 v[106:109], v[222:225], a[52:55], v[106:109]\n"
"buffer_load_dwordx4 a[8:11], v2, s[28:31], s64 offen offset:2048 sc1\n"
"v_mfma_f32_16x16x32_bf16 v[110:113], v[226:229], a[48:51], v[110:113]\n"
"ds_read_b128 v[218:221], v8 offset:9216\n"
"v_mfma_f32_16x16x32_bf16 v[114:117], v[226:229], a[52:55], v[114:117]\n"
"buffer_load_dwordx4 a[12:15], v2, s[28:31], s64 offen offset:3072 sc1\n"
"v_mfma_f32_16x16x32_bf16 v[118:121], v[190:193], a[48:51], v[118:121]\n"
"ds_read_b128 v[222:225], v8 offset:10240\n"
"v_mfma_f32_16x16x32_bf16 v[122:125], v[190:193], a[52:55], v[122:125]\n"
"v_mfma_f32_16x16x32_bf16 v[126:129], v[194:197], a[48:51], v[126:129]\n"
"ds_read_b128 v[226:229], v8 offset:11264\n"
"v_mfma_f32_16x16x32_bf16 v[130:133], v[194:197], a[52:55], v[130:133]\n"
"s_waitcnt lgkmcnt(4)\n"
"v_mfma_f32_16x16x32_bf16 v[134:137], v[198:201], a[48:51], v[134:137]\n"
"s_waitcnt vmcnt(21)\n"
"s_barrier\n"
"ds_read_b128 v[190:193], v6 offset:0\n"
"v_mfma_f32_16x16x32_bf16 v[138:141], v[198:201], a[52:55], v[138:141]\n"
"buffer_load_dwordx4 a[16:19], v3, s[28:31], s64 offen offset:0 sc1\n"
"v_mfma_f32_16x16x32_bf16 v[86:89], v[202:205], a[56:59], v[86:89]\n"
"ds_read_b128 v[194:197], v6 offset:1024\n"
"v_mfma_f32_16x16x32_bf16 v[90:93], v[202:205], a[60:63], v[90:93]\n"
"s_bfe_u32 s65, s81, 262164\n"
"s_mul_i32 s65, s65, 28672\n"
"s_waitcnt vmcnt(9)\n"
"v_readfirstlane_b32 s61, v11\n"
"s_nop 0\n"
"s_cmp_ge_u32 s61, 16\n"
"s_cbranch_scc0 .Lmk_spin_30\n"
".Lmk_back_31:\n"
"v_mfma_f32_16x16x32_bf16 v[94:97], v[206:209], a[56:59], v[94:97]\n"
"ds_read_b128 v[198:201], v6 offset:2048\n"
"v_mfma_f32_16x16x32_bf16 v[98:101], v[206:209], a[60:63], v[98:101]\n"
"s_add_u32 s65, s65, s69\n"
"s_add_u32 s65, s65, 3670016\n"
"v_mfma_f32_16x16x32_bf16 v[102:105], v[210:213], a[56:59], v[102:105]\n"
"ds_read_b128 v[202:205], v6 offset:3072\n"
"s_add_u32 m0, s54, 86016\n"
"s_nop 0\n"
"buffer_load_dwordx4 v2, s[28:31], s65 offen offset:0 sc1 lds\n"
"v_mfma_f32_16x16x32_bf16 v[106:109], v[210:213], a[60:63], v[106:109]\n"
"s_waitcnt lgkmcnt(4)\n"
"v_mfma_f32_16x16x32_bf16 v[110:113], v[214:217], a[56:59], v[110:113]\n"
"ds_read_b128 v[206:209], v6 offset:4096\n"
"v_mfma_f32_16x16x32_bf16 v[114:117], v[214:217], a[60:63], v[114:117]\n"
"buffer_load_dwordx4 v[236:239], v2, s[28:31], s80 offen offset:0 sc1\n"
"v_mfma_f32_16x16x32_bf16 v[118:121], v[218:221], a[56:59], v[118:121]\n"
"ds_read_b128 v[210:213], v6 offset:5120\n"
"v_mfma_f32_16x16x32_bf16 v[122:125], v[218:221], a[60:63], v[122:125]\n"
"buffer_load_dwordx4 v[240:243], v2, s[28:31], s80 offen offset:1024 sc1\n"
"v_mfma_f32_16x16x32_bf16 v[126:129], v[222:225], a[56:59], v[126:129]\n"
"ds_read_b128 v[214:217], v6 offset:6144\n"
"v_mfma_f32_16x16x32_bf16 v[130:133], v[222:225], a[60:63], v[130:133]\n"
"v_mfma_f32_16x16x32_bf16 v[134:137], v[226:229], a[56:59], v[134:137]\n"
"ds_read_b128 v[218:221], v6 offset:7168\n"
"v_mfma_f32_16x16x32_bf16 v[138:141], v[226:229], a[60:63], v[138:141]\n"
"s_waitcnt lgkmcnt(4)\n"
"v_mfma_f32_16x16x32_bf16 v[86:89], v[190:193], a[64:67], v[86:89]\n"
"ds_read_b128 v[222:225], v6 offset:8192\n"
"v_mfma_f32_16x16x32_bf16 v[90:93], v[190:193], a[68:71], v[90:93]\n"
"buffer_load_dwordx4 v[244:247], v2, s[28:31], s80 offen offset:2048 sc1\n"
"v_mfma_f32_16x16x32_bf16 v[94:97], v[194:197], a[64:67], v[94:97]\n"
"ds_read_b128 v[226:229], v6 offset:9216\n"
"v_mfma_f32_16x16x32_bf16 v[98:101], v[194:197], a[68:71], v[98:101]\n"
"buffer_load_dwordx4 v[248:251], v2, s[28:31], s80 offen offset:3072 sc1\n"
"v_mfma_f32_16x16x32_bf16 v[102:105], v[198:201], a[64:67], v[102:105]\n"
"ds_read_b128 v[190:193], v6 offset:10240\n"
"v_mfma_f32_16x16x32_bf16 v[106:109], v[198:201], a[68:71], v[106:109]\n"
"v_mfma_f32_16x16x32_bf16 v[110:113], v[202:205], a[64:67], v[110:113]\n"
"ds_read_b128 v[194:197], v6 offset:11264\n"
"v_mfma_f32_16x16x32_bf16 v[114:117], v[202:205], a[68:71], v[114:117]\n"
"s_waitcnt lgkmcnt(4)\n"
"v_mfma_f32_16x16x32_bf16 v[118:121], v[206:209], a[64:67], v[118:121]\n"
"ds_read_b128 v[198:201], v6 offset:12288\n"
"v_mfma_f32_16x16x32_bf16 v[122:125], v[206:209], a[68:71], v[122:125]\n"
"buffer_load_dwordx4 v[170:173], v3, s[28:31], s80 offen offset:0 sc1\n"
"v_mfma_f32_16x16x32_bf16 v[126:129], v[210:213], a[64:67], v[126:129]\n"
"ds_read_b128 v[202:205], v6 offset:13312\n"
"v_mfma_f32_16x16x32_bf16 v[130:133], v[210:213], a[68:71], v[130:133]\n"
"v_mfma_f32_16x16x32_bf16 v[134:137], v[214:217], a[64:67], v[134:137]\n"
"ds_read_b128 v[206:209], v6 offset:14336\n"
"v_mfma_f32_16x16x32_bf16 v[138:141], v[214:217], a[68:71], v[138:141]\n"
"buffer_load_dwordx4 v[174:177], v3, s[28:31], s80 offen offset:1024 sc1\n"
"v_mfma_f32_16x16x32_bf16 v[86:89], v[218:221], a[72:75], v[86:89]\n"
"ds_read_b128 v[210:213], v6 offset:15360\n"
"v_mfma_f32_16x16x32_bf16 v[90:93], v[218:221], a[76:79], v[90:93]\n"
"s_waitcnt lgkmcnt(4)\n"
"v_mfma_f32_16x16x32_bf16 v[94:97], v[222:225], a[72:75], v[94:97]\n"
"ds_read_b128 v[214:217], v6 offset:16384\n"
"v_mfma_f32_16x16x32_bf16 v[98:101], v[222:225], a[76:79], v[98:101]\n"
"buffer_load_dwordx4 v[162:165], v3, s[28:31], s80 offen offset:2048 sc1\n"
"v_mfma_f32_16x16x32_bf16 v[102:105], v[226:229], a[72:75], v[102:105]\n"
"ds_read_b128 v[218:221], v6 offset:17408\n"
"v_mfma_f32_16x16x32_bf16 v[106:109], v[226:229], a[76:79], v[106:109]\n"
"v_mfma_f32_16x16x32_bf16 v[110:113], v[190:193], a[72:75], v[110:113]\n"
"ds_read_b128 v[222:225], v6 offset:18432\n"
"v_mfma_f32_16x16x32_bf16 v[114:117], v[190:193], a[76:79], v[114:117]\n"
"buffer_load_dwordx4 v[166:169], v3, s[28:31], s80 offen offset:3072 sc1\n"
"v_mfma_f32_16x16x32_bf16 v[118:121], v[194:197], a[72:75], v[118:121]\n"
"ds_read_b128 v[226:229], v6 offset:19456\n"
"v_mfma_f32_16x16x32_bf16 v[122:125], v[194:197], a[76:79], v[122:125]\n"
"s_waitcnt lgkmcnt(4)\n"
"v_mfma_f32_16x16x32_bf16 v[126:129], v[198:201], a[72:75], v[126:129]\n"
"ds_read_b128 v[190:193], v6 offset:20480\n"
"v_mfma_f32_16x16x32_bf16 v[130:133], v[198:201], a[76:79], v[130:133]\n"
"buffer_load_dwordx4 v2, s[28:31], s65 offen offset:1024 sc1 lds\n"
"v_mfma_f32_16x16x32_bf16 v[134:137], v[202:205], a[72:75], v[134:137]\n"
"ds_read_b128 v[194:197], v6 offset:21504\n"
"v_mfma_f32_16x16x32_bf16 v[138:141], v[202:205], a[76:79], v[138:141]\n"
"v_mfma_f32_16x16x32_bf16 v[86:89], v[206:209], a[80:83], v[86:89]\n"
"ds_read_b128 v[198:201], v6 offset:22528\n"
"v_mfma_f32_16x16x32_bf16 v[90:93], v[206:209], a[84:87], v[90:93]\n"
"buffer_load_dwordx4 v2, s[28:31], s65 offen offset:2048 sc1 lds\n"
"v_mfma_f32_16x16x32_bf16 v[94:97], v[210:213], a[80:83], v[94:97]\n"
"ds_read_b128 v[202:205], v6 offset:23552\n"
"v_mfma_f32_16x16x32_bf16 v[98:101], v[210:213], a[84:87], v[98:101]\n"
"s_waitcnt lgkmcnt(4)\n"
"v_mfma_f32_16x16x32_bf16 v[102:105], v[214:217], a[80:83], v[102:105]\n"
"ds_read_b128 v[206:209], v6 offset:24576\n"
"v_mfma_f32_16x16x32_bf16 v[106:109], v[214:217], a[84:87], v[106:109]\n"
"buffer_load_dwordx4 v2, s[28:31], s65 offen offset:3072 sc1 lds\n"
"v_mfma_f32_16x16x32_bf16 v[110:113], v[218:221], a[80:83], v[110:113]\n"
"ds_read_b128 v[210:213], v6 offset:25600\n"
"v_mfma_f32_16x16x32_bf16 v[114:117], v[218:221], a[84:87], v[114:117]\n"
"v_mfma_f32_16x16x32_bf16 v[118:121], v[222:225], a[80:83], v[118:121]\n"
"ds_read_b128 v[214:217], v6 offset:26624\n"
"s_add_u32 m0, s54, 90112\n"
"v_mfma_f32_16x16x32_bf16 v[122:125], v[222:225], a[84:87], v[122:125]\n"
"v_mfma_f32_16x16x32_bf16 v[126:129], v[226:229], a[80:83], v[126:129]\n"
"ds_read_b128 v[218:221], v6 offset:27648\n"
"buffer_load_dwordx4 v3, s[28:31], s65 offen offset:0 sc1 lds\n"
"v_mfma_f32_16x16x32_bf16 v[130:133], v[226:229], a[84:87], v[130:133]\n"
"s_waitcnt lgkmcnt(4)\n"
"v_mfma_f32_16x16x32_bf16 v[134:137], v[190:193], a[80:83], v[134:137]\n"
"s_barrier\n"
"ds_read_b128 v[222:225], v6 offset:28672\n"
"v_mfma_f32_16x16x32_bf16 v[138:141], v[190:193], a[84:87], v[138:141]\n"
"buffer_load_dwordx4 v3, s[28:31], s65 offen offset:1024 sc1 lds\n"
"v_mfma_f32_16x16x32_bf16 v[86:89], v[194:197], a[88:91], v[86:89]\n"
"ds_read_b128 v[226:229], v6 offset:29696\n"
"v_mfma_f32_16x16x32_bf16 v[90:93], v[194:197], a[92:95], v[90:93]\n"
"v_mfma_f32_16x16x32_bf16 v[94:97], v[198:201], a[88:91], v[94:97]\n"
"ds_read_b128 v[190:193], v6 offset:30720\n"
"v_mfma_f32_16x16x32_bf16 v[98:101], v[198:201], a[92:95], v[98:101]\n"
"buffer_load_dwordx4 v3, s[28:31], s65 offen offset:2048 sc1 lds\n"
"v_mfma_f32_16x16x32_bf16 v[102:105], v[202:205], a[88:91], v[102:105]\n"
"ds_read_b128 v[194:197], v6 offset:31744\n"
"v_mfma_f32_16x16x32_bf16 v[106:109], v[202:205], a[92:95], v[106:109]\n"
"s_waitcnt lgkmcnt(4)\n"
"v_mfma_f32_16x16x32_bf16 v[110:113], v[206:209], a[88:91], v[110:113]\n"
"ds_read_b128 v[198:201], v6 offset:32768\n"
"v_mfma_f32_16x16x32_bf16 v[114:117], v[206:209], a[92:95], v[114:117]\n"
"s_bfe_u32 s65, s81, 262168\n"
"s_mul_i32 s65, s65, 28672\n"
"v_mfma_f32_16x16x32_bf16 v[118:121], v[210:213], a[88:91], v[118:121]\n"
"ds_read_b128 v[202:205], v6 offset:33792\n"
"v_mfma_f32_16x16x32_bf16 v[122:125], v[210:213], a[92:95], v[122:125]\n"
"s_add_u32 s65, s65, s69\n"
"s_add_u32 s65, s65, 3670016\n"
"v_mfma_f32_16x16x32_bf16 v[126:129], v[214:217], a[88:91], v[126:129]\n"
"ds_read_b128 v[206:209], v6 offset:34816\n"
"s_add_u32 m0, s54, 114688\n"
"s_nop 0\n"
"buffer_load_dwordx4 v2, s[28:31], s65 offen offset:0 sc1 lds\n"
"v_mfma_f32_16x16x32_bf16 v[130:133], v[214:217], a[92:95], v[130:133]\n"
"buffer_load_dwordx4 v2, s[28:31], s65 offen offset:1024 sc1 lds\n"
"v_mfma_f32_16x16x32_bf16 v[134:137], v[218:221], a[88:91], v[134:137]\n"
"ds_read_b128 v[210:213], v6 offset:35840\n"
"s_waitcnt vmcnt(12)\n"
"v_bitop3_b32 v237, v237, v239, s26 bitop3:0x7e\n"
"v_mfma_f32_16x16x32_bf16 v[138:141], v[218:221], a[92:95], v[138:141]\n"
"s_waitcnt lgkmcnt(4)\n"
"v_bitop3_b32 v241, v241, v243, s26 bitop3:0x7e\n"
"v_mfma_f32_16x16x32_bf16 v[86:89], v[222:225], a[96:99], v[86:89]\n"
"ds_read_b128 v[214:217], v6 offset:36864\n"
"v_bitop3_b32 v245, v245, v247, s26 bitop3:0x7e\n"
"v_mfma_f32_16x16x32_bf16 v[90:93], v[222:225], a[100:103], v[90:93]\n"
"buffer_load_dwordx4 v2, s[28:31], s65 offen offset:2048 sc1 lds\n"
"v_mfma_f32_16x16x32_bf16 v[94:97], v[226:229], a[96:99], v[94:97]\n"
"ds_read_b128 v[218:221], v6 offset:37888\n"
"v_bitop3_b32 v249, v249, v251, s26 bitop3:0x7e\n"
"v_mfma_f32_16x16x32_bf16 v[98:101], v[226:229], a[100:103], v[98:101]\n"
"s_waitcnt vmcnt(12)\n"
"v_bitop3_b32 v171, v171, v173, s26 bitop3:0x7e\n"
"s_waitcnt vmcnt(11)\n"
"v_bitop3_b32 v175, v175, v177, s26 bitop3:0x7e\n"
"v_mfma_f32_16x16x32_bf16 v[102:105], v[190:193], a[96:99], v[102:105]\n"
"ds_read_b128 v[222:225], v6 offset:38912\n"
"s_waitcnt vmcnt(10)\n"
"v_bitop3_b32 v163, v163, v165, s26 bitop3:0x7e\n"
"v_mfma_f32_16x16x32_bf16 v[106:109], v[190:193], a[100:103], v[106:109]\n"
"buffer_load_dwordx4 v2, s[28:31], s65 offen offset:3072 sc1 lds\n"
"v_mfma_f32_16x16x32_bf16 v[110:113], v[194:197], a[96:99], v[110:113]\n"
"ds_read_b128 v[226:229], v6 offset:39936\n"
"s_waitcnt vmcnt(10)\n"
"v_bitop3_b32 v167, v167, v169, s26 bitop3:0x7e\n"
"v_mfma_f32_16x16x32_bf16 v[114:117], v[194:197], a[100:103], v[114:117]\n"
"s_waitcnt lgkmcnt(4)\n"
"v_or3_b32 v237, v237, v167, v163\n"
"v_mfma_f32_16x16x32_bf16 v[118:121], v[198:201], a[96:99], v[118:121]\n"
"ds_read_b128 v[190:193], v6 offset:40960\n"
"v_or3_b32 v237, v237, v175, v171\n"
"v_mfma_f32_16x16x32_bf16 v[122:125], v[198:201], a[100:103], v[122:125]\n"
"s_add_u32 m0, s54, 118784\n"
"v_or3_b32 v237, v237, v249, v245\n"
"v_mfma_f32_16x16x32_bf16 v[126:129], v[202:205], a[96:99], v[126:129]\n"
"ds_read_b128 v[194:197], v6 offset:41984\n"
"v_or_b32 v237, v237, v241\n"
"v_mfma_f32_16x16x32_bf16 v[130:133], v[202:205], a[100:103], v[130:133]\n"
"buffer_load_dwordx4 v3, s[28:31], s65 offen offset:0 sc1 lds\n"
"v_mfma_f32_16x16x32_bf16 v[134:137], v[206:209], a[96:99], v[134:137]\n"
"ds_read_b128 v[198:201], v6 offset:43008\n"
"v_cmp_ne_u32 vcc, 0, v237\n"
"s_nop 0\n"
"s_cbranch_vccnz .Lmk_fspin_32\n"
".Lmk_fback_33:\n"
"v_mfma_f32_16x16x32_bf16 v[138:141], v[206:209], a[100:103], v[138:141]\n"
"buffer_load_dwordx4 v3, s[28:31], s65 offen offset:1024 sc1 lds\n"
"v_mfma_f32_16x16x32_bf16 v[86:89], v[210:213], a[104:107], v[86:89]\n"
"ds_read_b128 v[202:205], v6 offset:44032\n"
"v_add_f32 v236, v236, v170\n"
"v_mfma_f32_16x16x32_bf16 v[90:93], v[210:213], a[108:111], v[90:93]\n"
"s_waitcnt lgkmcnt(4)\n"
"v_add_f32 v238, v238, v172\n"
"v_mfma_f32_16x16x32_bf16 v[94:97], v[214:217], a[104:107], v[94:97]\n"
"ds_read_b128 v[206:209], v6 offset:45056\n"
"v_add_f32 v240, v240, v174\n"
"v_mfma_f32_16x16x32_bf16 v[98:101], v[214:217], a[108:111], v[98:101]\n"
"buffer_load_dwordx4 v3, s[28:31], s65 offen offset:2048 sc1 lds\n"
"v_mfma_f32_16x16x32_bf16 v[102:105], v[218:221], a[104:107], v[102:105]\n"
"ds_read_b128 v[210:213], v6 offset:46080\n"
"v_add_f32 v242, v242, v176\n"
"v_mfma_f32_16x16x32_bf16 v[106:109], v[218:221], a[108:111], v[106:109]\n"
"v_add_f32 v244, v244, v162\n"
"v_add_f32 v246, v246, v164\n"
"v_mfma_f32_16x16x32_bf16 v[110:113], v[222:225], a[104:107], v[110:113]\n"
"ds_read_b128 v[214:217], v6 offset:47104\n"
"v_add_f32 v248, v248, v166\n"
"v_mfma_f32_16x16x32_bf16 v[114:117], v[222:225], a[108:111], v[114:117]\n"
"buffer_load_dwordx4 a[20:23], v3, s[28:31], s64 offen offset:1024 sc1\n"
"v_mfma_f32_16x16x32_bf16 v[118:121], v[226:229], a[104:107], v[118:121]\n"
"ds_read_b128 v[218:221], v6 offset:48128\n"
"v_add_f32 v250, v250, v168\n"
"v_mfma_f32_16x16x32_bf16 v[122:125], v[226:229], a[108:111], v[122:125]\n"
"s_waitcnt lgkmcnt(4)\n"
"v_add_f32 v236, v236, v244\n"
"v_mfma_f32_16x16x32_bf16 v[126:129], v[190:193], a[104:107], v[126:129]\n"
"ds_read_b128 v[222:225], v6 offset:49152\n"
"v_add_f32 v238, v238, v246\n"
"v_mfma_f32_16x16x32_bf16 v[130:133], v[190:193], a[108:111], v[130:133]\n"
"buffer_load_dwordx4 a[24:27], v3, s[28:31], s64 offen offset:2048 sc1\n"
"v_mfma_f32_16x16x32_bf16 v[134:137], v[194:197], a[104:107], v[134:137]\n"
"ds_read_b128 v[226:229], v6 offset:50176\n"
"v_add_f32 v240, v240, v248\n"
"v_mfma_f32_16x16x32_bf16 v[138:141], v[194:197], a[108:111], v[138:141]\n"
"v_add_f32 v242, v242, v250\n"
"v_add_f32 v236, v236, v240\n"
"v_mfma_f32_16x16x32_bf16 v[86:89], v[198:201], a[112:115], v[86:89]\n"
"ds_read_b128 v[190:193], v6 offset:51200\n"
"v_add_f32 v238, v238, v242\n"
"v_mfma_f32_16x16x32_bf16 v[90:93], v[198:201], a[116:119], v[90:93]\n"
"buffer_load_dwordx4 a[28:31], v3, s[28:31], s64 offen offset:3072 sc1\n"
"v_mfma_f32_16x16x32_bf16 v[94:97], v[202:205], a[112:115], v[94:97]\n"
"ds_read_b128 v[194:197], v6 offset:52224\n"
"v_permlane32_swap_b32 v236, v238\n"
"v_mfma_f32_16x16x32_bf16 v[98:101], v[202:205], a[116:119], v[98:101]\n"
"s_waitcnt lgkmcnt(4)\n"
"v_add_f32 v236, v236, v238\n"
"v_mfma_f32_16x16x32_bf16 v[102:105], v[206:209], a[112:115], v[102:105]\n"
"ds_read_b128 v[198:201], v6 offset:53248\n"
"v_mul_f32 v236, 0x3a124925, v236\n"
"v_mov_b32 v238, v236\n"
"v_mfma_f32_16x16x32_bf16 v[106:109], v[206:209], a[116:119], v[106:109]\n"
"s_bfe_u32 s64, s82, 262148\n"
"s_mul_i32 s64, s64, 8192\n"
"v_permlane32_swap_b32 v236, v238\n"
"v_fma_f32 v238, -v236, v236, v238\n"
"v_max_f32 v238, 0, v238\n"
"v_add_f32 v238, 0x3727c5ac, v238\n"
"v_rsq_f32 v238, v238\n"
"s_nop 0\n"
"v_mul_f32 v236, v236, v238\n"
"v_sub_f32 v236, 0, v236\n"
"s_nop 1\n"
"v_permlane32_swap_b32 v238, v236\n"
"ds_write_b32 v232, v238\n"
"ds_read_b128 v[142:145], v233 offset:0\n"
"ds_read_b128 v[146:149], v233 offset:16\n"
"ds_read_b128 v[150:153], v233 offset:128\n"
"ds_read_b128 v[154:157], v233 offset:144\n"
"v_mfma_f32_16x16x32_bf16 v[110:113], v[210:213], a[112:115], v[110:113]\n"
"ds_read_b128 v[202:205], v6 offset:54272\n"
"s_add_u32 s64, s64, s66\n"
"v_mfma_f32_16x16x32_bf16 v[114:117], v[210:213], a[116:119], v[114:117]\n"
"v_mfma_f32_16x16x32_bf16 v[118:121], v[214:217], a[112:115], v[118:121]\n"
"ds_read_b128 v[206:209], v6 offset:55296\n"
"v_mfma_f32_16x16x32_bf16 v[122:125], v[214:217], a[116:119], v[122:125]\n"
"buffer_load_dwordx4 a[32:35], v2, s[28:31], s64 offen offset:0 sc1\n"
"v_mfma_f32_16x16x32_bf16 v[126:129], v[218:221], a[112:115], v[126:129]\n"
"ds_read_b128 v[210:213], v6 offset:56320\n"
"v_mfma_f32_16x16x32_bf16 v[130:133], v[218:221], a[116:119], v[130:133]\n"
"s_waitcnt lgkmcnt(9)\n"
"v_mfma_f32_16x16x32_bf16 v[134:137], v[222:225], a[112:115], v[134:137]\n"
"s_barrier\n"
"ds_read_b128 v[214:217], v6 offset:57344\n"
"v_mfma_f32_16x16x32_bf16 v[138:141], v[222:225], a[116:119], v[138:141]\n"
"buffer_load_dwordx4 a[36:39], v2, s[28:31], s64 offen offset:1024 sc1\n"
"v_mfma_f32_16x16x32_bf16 v[86:89], v[226:229], a[120:123], v[86:89]\n"
"ds_read_b128 v[218:221], v6 offset:58368\n"
"s_waitcnt lgkmcnt(6)\n"
"v_fma_f32 v30, v30, v142, v150\n"
"v_mfma_f32_16x16x32_bf16 v[90:93], v[226:229], a[124:127], v[90:93]\n"
"s_bfe_u32 s65, s81, 262172\n"
"s_mul_i32 s65, s65, 28672\n"
"v_mfma_f32_16x16x32_bf16 v[94:97], v[190:193], a[120:123], v[94:97]\n"
"ds_read_b128 v[222:225], v6 offset:59392\n"
"v_fma_f32 v31, v31, v143, v151\n"
"v_mfma_f32_16x16x32_bf16 v[98:101], v[190:193], a[124:127], v[98:101]\n"
"s_add_u32 s65, s65, s69\n"
"s_add_u32 s65, s65, 3670016\n"
"s_add_u32 m0, s54, 0\n"
"s_nop 0\n"
"buffer_load_dwordx4 v2, s[28:31], s65 offen offset:0 sc1 lds\n"
"v_mfma_f32_16x16x32_bf16 v[102:105], v[194:197], a[120:123], v[102:105]\n"
"ds_read_b128 v[226:229], v6 offset:60416\n"
"v_cvt_pk_bf16_f32 v30, v30, v31\n"
"v_mfma_f32_16x16x32_bf16 v[106:109], v[194:197], a[124:127], v[106:109]\n"
"s_waitcnt lgkmcnt(4)\n"
"v_fma_f32 v32, v32, v144, v152\n"
"v_mfma_f32_16x16x32_bf16 v[110:113], v[198:201], a[120:123], v[110:113]\n"
"ds_read_b128 v[190:193], v6 offset:61440\n"
"v_fma_f32 v33, v33, v145, v153\n"
"v_mfma_f32_16x16x32_bf16 v[114:117], v[198:201], a[124:127], v[114:117]\n"
"buffer_load_dwordx4 v2, s[28:31], s65 offen offset:1024 sc1 lds\n"
"v_mfma_f32_16x16x32_bf16 v[118:121], v[202:205], a[120:123], v[118:121]\n"
"ds_read_b128 v[194:197], v6 offset:62464\n"
"v_cvt_pk_bf16_f32 v31, v32, v33\n"
"v_mfma_f32_16x16x32_bf16 v[122:125], v[202:205], a[124:127], v[122:125]\n"
"buffer_load_dwordx4 v2, s[28:31], s65 offen offset:2048 sc1 lds\n"
"v_mfma_f32_16x16x32_bf16 v[126:129], v[206:209], a[120:123], v[126:129]\n"
"ds_read_b128 v[198:201], v6 offset:63488\n"
"v_fma_f32 v34, v34, v146, v154\n"
"v_fma_f32 v35, v35, v147, v155\n"
"v_cvt_pk_bf16_f32 v32, v34, v35\n"
"v_fma_f32 v36, v36, v148, v156\n"
"v_fma_f32 v37, v37, v149, v157\n"
"v_cvt_pk_bf16_f32 v33, v36, v37\n"
"v_mfma_f32_16x16x32_bf16 v[130:133], v[206:209], a[124:127], v[130:133]\n"
"v_fma_f32 v38, v38, v142, v150\n"
"v_fma_f32 v39, v39, v143, v151\n"
"v_mfma_f32_16x16x32_bf16 v[134:137], v[210:213], a[120:123], v[134:137]\n"
"ds_read_b128 v[202:205], v6 offset:64512\n"
"v_cvt_pk_bf16_f32 v38, v38, v39\n"
"v_mfma_f32_16x16x32_bf16 v[138:141], v[210:213], a[124:127], v[138:141]\n"
"s_waitcnt lgkmcnt(4)\n"
"v_fma_f32 v40, v40, v144, v152\n"
"v_mfma_f32_16x16x32_bf16 v[86:89], v[214:217], a[128:131], v[86:89]\n"
"ds_read_b128 v[206:209], v7 offset:0\n"
"v_fma_f32 v41, v41, v145, v153\n"
"v_mfma_f32_16x16x32_bf16 v[90:93], v[214:217], a[132:135], v[90:93]\n"
"buffer_load_dwordx4 v2, s[28:31], s65 offen offset:3072 sc1 lds\n"
"v_mfma_f32_16x16x32_bf16 v[94:97], v[218:221], a[128:131], v[94:97]\n"
"ds_read_b128 v[210:213], v7 offset:1024\n"
"v_cvt_pk_bf16_f32 v39, v40, v41\n"
"v_mfma_f32_16x16x32_bf16 v[98:101], v[218:221], a[132:135], v[98:101]\n"
"buffer_store_dwordx4 v[30:33], v2, s[28:31], s88 offen offset:0 sc1\n"
"v_mfma_f32_16x16x32_bf16 v[102:105], v[222:225], a[128:131], v[102:105]\n"
"ds_read_b128 v[214:217], v7 offset:2048\n"
"v_fma_f32 v42, v42, v146, v154\n"
"v_mfma_f32_16x16x32_bf16 v[106:109], v[222:225], a[132:135], v[106:109]\n"
"v_fma_f32 v43, v43, v147, v155\n"
"v_cvt_pk_bf16_f32 v40, v42, v43\n"
"v_mfma_f32_16x16x32_bf16 v[110:113], v[226:229], a[128:131], v[110:113]\n"
"ds_read_b128 v[218:221], v7 offset:3072\n"
"s_add_u32 m0, s54, 4096\n"
"v_mfma_f32_16x16x32_bf16 v[114:117], v[226:229], a[132:135], v[114:117]\n"
"s_waitcnt lgkmcnt(4)\n"
"v_fma_f32 v44, v44, v148, v156\n"
"v_fma_f32 v45, v45, v149, v157\n"
"v_cvt_pk_bf16_f32 v41, v44, v45\n"
"v_mfma_f32_16x16x32_bf16 v[118:121], v[190:193], a[128:131], v[118:121]\n"
"ds_read_b128 v[222:225], v7 offset:4096\n"
"v_fma_f32 v46, v46, v142, v150\n"
"v_mfma_f32_16x16x32_bf16 v[122:125], v[190:193], a[132:135], v[122:125]\n"
"buffer_load_dwordx4 v3, s[28:31], s65 offen offset:0 sc1 lds\n"
"v_mfma_f32_16x16x32_bf16 v[126:129], v[194:197], a[128:131], v[126:129]\n"
"ds_read_b128 v[226:229], v7 offset:5120\n"
"v_fma_f32 v47, v47, v143, v151\n"
"v_mfma_f32_16x16x32_bf16 v[130:133], v[194:197], a[132:135], v[130:133]\n"
"buffer_store_dwordx4 v[38:41], v2, s[28:31], s88 offen offset:1024 sc1\n"
"v_mfma_f32_16x16x32_bf16 v[134:137], v[198:201], a[128:131], v[134:137]\n"
"ds_read_b128 v[190:193], v7 offset:6144\n"
"v_cvt_pk_bf16_f32 v46, v46, v47\n"
"v_mfma_f32_16x16x32_bf16 v[138:141], v[198:201], a[132:135], v[138:141]\n"
"v_fma_f32 v48, v48, v144, v152\n"
"v_fma_f32 v49, v49, v145, v153\n"
"v_mfma_f32_16x16x32_bf16 v[86:89], v[202:205], a[136:139], v[86:89]\n"
"ds_read_b128 v[194:197], v7 offset:7168\n"
"v_cvt_pk_bf16_f32 v47, v48, v49\n"
"v_mfma_f32_16x16x32_bf16 v[90:93], v[202:205], a[140:143], v[90:93]\n"
"s_waitcnt lgkmcnt(4)\n"
"v_fma_f32 v50, v50, v146, v154\n"
"v_mfma_f32_16x16x32_bf16 v[94:97], v[206:209], a[136:139], v[94:97]\n"
"ds_read_b128 v[198:201], v7 offset:8192\n"
"v_fma_f32 v51, v51, v147, v155\n"
"v_mfma_f32_16x16x32_bf16 v[98:101], v[206:209], a[140:143], v[98:101]\n"
"buffer_load_dwordx4 v3, s[28:31], s65 offen offset:1024 sc1 lds\n"
"v_mfma_f32_16x16x32_bf16 v[102:105], v[210:213], a[136:139], v[102:105]\n"
"ds_read_b128 v[202:205], v7 offset:9216\n"
"v_cvt_pk_bf16_f32 v48, v50, v51\n"
"v_fma_f32 v52, v52, v148, v156\n"
"v_mfma_f32_16x16x32_bf16 v[106:109], v[210:213], a[140:143], v[106:109]\n"
"buffer_load_dwordx4 v3, s[28:31], s65 offen offset:2048 sc1 lds\n"
"v_fma_f32 v53, v53, v149, v157\n"
"v_cvt_pk_bf16_f32 v49, v52, v53\n"
"v_mfma_f32_16x16x32_bf16 v[110:113], v[214:217], a[136:139], v[110:113]\n"
"ds_read_b128 v[206:209], v7 offset:10240\n"
"v_fma_f32 v54, v54, v142, v150\n"
"v_mfma_f32_16x16x32_bf16 v[114:117], v[214:217], a[140:143], v[114:117]\n"
"v_fma_f32 v55, v55, v143, v151\n"
"v_cvt_pk_bf16_f32 v54, v54, v55\n"
"v_mfma_f32_16x16x32_bf16 v[118:121], v[218:221], a[136:139], v[118:121]\n"
"ds_read_b128 v[210:213], v7 offset:11264\n"
"v_fma_f32 v56, v56, v144, v152\n"
"v_mfma_f32_16x16x32_bf16 v[122:125], v[218:221], a[140:143], v[122:125]\n"
"s_waitcnt lgkmcnt(4)\n"
"v_fma_f32 v57, v57, v145, v153\n"
"v_mfma_f32_16x16x32_bf16 v[126:129], v[222:225], a[136:139], v[126:129]\n"
"ds_read_b128 v[214:217], v7 offset:12288\n"
"v_cvt_pk_bf16_f32 v55, v56, v57\n"
"v_mfma_f32_16x16x32_bf16 v[130:133], v[222:225], a[140:143], v[130:133]\n"
"buffer_store_dwordx4 v[46:49], v2, s[28:31], s88 offen offset:2048 sc1\n"
".Lmk_pg_13:\n"
"s_cbranch_execz .Lmk_pg_17\n"
"v_mfma_f32_16x16x32_bf16 v[134:137], v[226:229], a[136:139], v[134:137]\n"
"ds_read_b128 v[218:221], v7 offset:13312\n"
"v_fma_f32 v58, v58, v146, v154\n"
"v_mfma_f32_16x16x32_bf16 v[138:141], v[226:229], a[140:143], v[138:141]\n"
"v_fma_f32 v59, v59, v147, v155\n"
"v_cvt_pk_bf16_f32 v56, v58, v59\n"
"v_mfma_f32_16x16x32_bf16 v[86:89], v[190:193], a[144:147], v[86:89]\n"
"ds_read_b128 v[222:225], v7 offset:14336\n"
"v_fma_f32 v60, v60, v148, v156\n"
"v_mfma_f32_16x16x32_bf16 v[90:93], v[190:193], a[148:151], v[90:93]\n"
"v_fma_f32 v61, v61, v149, v157\n"
"v_mfma_f32_16x16x32_bf16 v[94:97], v[194:197], a[144:147], v[94:97]\n"
"ds_read_b128 v[226:229], v7 offset:15360\n"
"v_cvt_pk_bf16_f32 v57, v60, v61\n"
"v_mfma_f32_16x16x32_bf16 v[98:101], v[194:197], a[148:151], v[98:101]\n"
"s_waitcnt lgkmcnt(4)\n"
"v_fma_f32 v62, v62, v142, v150\n"
"v_mfma_f32_16x16x32_bf16 v[102:105], v[198:201], a[144:147], v[102:105]\n"
"ds_read_b128 v[190:193], v7 offset:16384\n"
"v_fma_f32 v63, v63, v143, v151\n"
"v_mfma_f32_16x16x32_bf16 v[106:109], v[198:201], a[148:151], v[106:109]\n"
"buffer_store_dwordx4 v[54:57], v2, s[28:31], s88 offen offset:3072 sc1\n"
"v_mfma_f32_16x16x32_bf16 v[110:113], v[202:205], a[144:147], v[110:113]\n"
"ds_read_b128 v[194:197], v7 offset:17408\n"
"v_cvt_pk_bf16_f32 v62, v62, v63\n"
"v_mfma_f32_16x16x32_bf16 v[114:117], v[202:205], a[148:151], v[114:117]\n"
"v_fma_f32 v64, v64, v144, v152\n"
"v_fma_f32 v65, v65, v145, v153\n"
"v_mfma_f32_16x16x32_bf16 v[118:121], v[206:209], a[144:147], v[118:121]\n"
"ds_read_b128 v[198:201], v7 offset:18432\n"
"v_cvt_pk_bf16_f32 v63, v64, v65\n"
"v_mfma_f32_16x16x32_bf16 v[122:125], v[206:209], a[148:151], v[122:125]\n"
"v_fma_f32 v66, v66, v146, v154\n"
"v_mfma_f32_16x16x32_bf16 v[126:129], v[210:213], a[144:147], v[126:129]\n"
"ds_read_b128 v[202:205], v7 offset:19456\n"
"v_fma_f32 v67, v67, v147, v155\n"
"v_mfma_f32_16x16x32_bf16 v[130:133], v[210:213], a[148:151], v[130:133]\n"
"s_waitcnt lgkmcnt(4)\n"
"v_cvt_pk_bf16_f32 v64, v66, v67\n"
"v_mfma_f32_16x16x32_bf16 v[134:137], v[214:217], a[144:147], v[134:137]\n"
"s_waitcnt vmcnt(23)\n"
"s_barrier\n"
"ds_read_b128 v[206:209], v7 offset:20480\n"
"v_mfma_f32_16x16x32_bf16 v[138:141], v[214:217], a[148:151], v[138:141]\n"
"v_fma_f32 v68, v68, v148, v156\n"
"v_fma_f32 v69, v69, v149, v157\n"
"v_mfma_f32_16x16x32_bf16 v[86:89], v[218:221], a[152:155], v[86:89]\n"
"ds_read_b128 v[210:213], v7 offset:21504\n"
"v_cvt_pk_bf16_f32 v65, v68, v69\n"
"v_mfma_f32_16x16x32_bf16 v[90:93], v[218:221], a[156:159], v[90:93]\n"
"v_fma_f32 v70, v70, v142, v150\n"
"v_fma_f32 v71, v71, v143, v151\n"
"v_mfma_f32_16x16x32_bf16 v[94:97], v[222:225], a[152:155], v[94:97]\n"
"ds_read_b128 v[214:217], v7 offset:22528\n"
"v_cvt_pk_bf16_f32 v70, v70, v71\n"
"v_mfma_f32_16x16x32_bf16 v[98:101], v[222:225], a[156:159], v[98:101]\n"
"buffer_store_dwordx4 v[62:65], v3, s[28:31], s88 offen offset:0 sc1\n"
"v_mfma_f32_16x16x32_bf16 v[102:105], v[226:229], a[152:155], v[102:105]\n"
"ds_read_b128 v[218:221], v7 offset:23552\n"
"v_fma_f32 v72, v72, v144, v152\n"
"v_mfma_f32_16x16x32_bf16 v[106:109], v[226:229], a[156:159], v[106:109]\n"
"s_waitcnt lgkmcnt(4)\n"
"v_fma_f32 v73, v73, v145, v153\n"
"v_mfma_f32_16x16x32_bf16 v[110:113], v[190:193], a[152:155], v[110:113]\n"
"ds_read_b128 v[222:225], v7 offset:24576\n"
"v_cvt_pk_bf16_f32 v71, v72, v73\n"
"v_mfma_f32_16x16x32_bf16 v[114:117], v[190:193], a[156:159], v[114:117]\n"
"v_fma_f32 v74, v74, v146, v154\n"
"v_fma_f32 v75, v75, v147, v155\n"
"v_mfma_f32_16x16x32_bf16 v[118:121], v[194:197], a[152:155], v[118:121]\n"
"ds_read_b128 v[226:229], v7 offset:25600\n"
"v_cvt_pk_bf16_f32 v72, v74, v75\n"
"v_mfma_f32_16x16x32_bf16 v[122:125], v[194:197], a[156:159], v[122:125]\n"
"v_fma_f32 v76, v76, v148, v156\n"
"v_mfma_f32_16x16x32_bf16 v[126:129], v[198:201], a[152:155], v[126:129]\n"
"ds_read_b128 v[190:193], v7 offset:26624\n"
"v_fma_f32 v77, v77, v149, v157\n"
"v_mfma_f32_16x16x32_bf16 v[130:133], v[198:201], a[156:159], v[130:133]\n"
"v_cvt_pk_bf16_f32 v73, v76, v77\n"
"v_mfma_f32_16x16x32_bf16 v[134:137], v[202:205], a[152:155], v[134:137]\n"
"ds_read_b128 v[194:197], v7 offset:27648\n"
"v_fma_f32 v78, v78, v142, v150\n"
"v_mfma_f32_16x16x32_bf16 v[138:141], v[202:205], a[156:159], v[138:141]\n"
"s_waitcnt lgkmcnt(4)\n"
"v_fma_f32 v79, v79, v143, v151\n"
"v_mfma_f32_16x16x32_bf16 v[86:89], v[206:209], a[160:163], v[86:89]\n"
"ds_read_b128 v[198:201], v7 offset:28672\n"
"v_cvt_pk_bf16_f32 v78, v78, v79\n"
"v_mfma_f32_16x16x32_bf16 v[90:93], v[206:209], a[164:167], v[90:93]\n"
"buffer_store_dwordx4 v[70:73], v3, s[28:31], s88 offen offset:1024 sc1\n"
"v_mfma_f32_16x16x32_bf16 v[94:97], v[210:213], a[160:163], v[94:97]\n"
"ds_read_b128 v[202:205], v7 offset:29696\n"
"v_fma_f32 v80, v80, v144, v152\n"
"v_mfma_f32_16x16x32_bf16 v[98:101], v[210:213], a[164:167], v[98:101]\n"
"v_fma_f32 v81, v81, v145, v153\n"
"v_cvt_pk_bf16_f32 v79, v80, v81\n"
"v_mfma_f32_16x16x32_bf16 v[102:105], v[214:217], a[160:163], v[102:105]\n"
"ds_read_b128 v[206:209], v7 offset:30720\n"
"v_fma_f32 v82, v82, v146, v154\n"
"v_mfma_f32_16x16x32_bf16 v[106:109], v[214:217], a[164:167], v[106:109]\n"
"v_fma_f32 v83, v83, v147, v155\n"
"v_mfma_f32_16x16x32_bf16 v[110:113], v[218:221], a[160:163], v[110:113]\n"
"ds_read_b128 v[210:213], v7 offset:31744\n"
"v_cvt_pk_bf16_f32 v80, v82, v83\n"
"v_mfma_f32_16x16x32_bf16 v[114:117], v[218:221], a[164:167], v[114:117]\n"
"s_waitcnt lgkmcnt(4)\n"
"v_fma_f32 v84, v84, v148, v156\n"
"v_mfma_f32_16x16x32_bf16 v[118:121], v[222:225], a[160:163], v[118:121]\n"
"ds_read_b128 v[214:217], v7 offset:32768\n"
"v_fma_f32 v85, v85, v149, v157\n"
"v_mfma_f32_16x16x32_bf16 v[122:125], v[222:225], a[164:167], v[122:125]\n"
"v_cvt_pk_bf16_f32 v81, v84, v85\n"
"v_mfma_f32_16x16x32_bf16 v[126:129], v[226:229], a[160:163], v[126:129]\n"
"ds_read_b128 v[218:221], v7 offset:33792\n"
"v_mfma_f32_16x16x32_bf16 v[130:133], v[226:229], a[164:167], v[130:133]\n"
"v_mfma_f32_16x16x32_bf16 v[134:137], v[190:193], a[160:163], v[134:137]\n"
"ds_read_b128 v[222:225], v7 offset:34816\n"
"v_mfma_f32_16x16x32_bf16 v[138:141], v[190:193], a[164:167], v[138:141]\n"
"buffer_store_dwordx4 v[78:81], v3, s[28:31], s88 offen offset:2048 sc1\n"
"v_mfma_f32_16x16x32_bf16 v[86:89], v[194:197], a[168:171], v[86:89]\n"
"ds_read_b128 v[226:229], v7 offset:35840\n"
"v_mfma_f32_16x16x32_bf16 v[90:93], v[194:197], a[172:175], v[90:93]\n"
"s_waitcnt lgkmcnt(4)\n"
"v_mfma_f32_16x16x32_bf16 v[94:97], v[198:201], a[168:171], v[94:97]\n"
"ds_read_b128 v[190:193], v7 offset:36864\n"
"v_mfma_f32_16x16x32_bf16 v[98:101], v[198:201], a[172:175], v[98:101]\n"
"v_mfma_f32_16x16x32_bf16 v[102:105], v[202:205], a[168:171], v[102:105]\n"
"ds_read_b128 v[194:197], v7 offset:37888\n"
"v_mfma_f32_16x16x32_bf16 v[106:109], v[202:205], a[172:175], v[106:109]\n"
"v_mfma_f32_16x16x32_bf16 v[110:113], v[206:209], a[168:171], v[110:113]\n"
"ds_read_b128 v[198:201], v7 offset:38912\n"
"v_mfma_f32_16x16x32_bf16 v[114:117], v[206:209], a[172:175], v[114:117]\n"
"v_mfma_f32_16x16x32_bf16 v[118:121], v[210:213], a[168:171], v[118:121]\n"
"ds_read_b128 v[202:205], v7 offset:39936\n"
"s_mov_b32 s32, s18\n"
"v_mfma_f32_16x16x32_bf16 v[122:125], v[210:213], a[172:175], v[122:125]\n"
"s_waitcnt lgkmcnt(4)\n"
"s_and_b32 s33, s19, 0xffff\n"
"v_mfma_f32_16x16x32_bf16 v[126:129], v[214:217], a[168:171], v[126:129]\n"
"ds_read_b128 v[206:209], v7 offset:40960\n"
"s_mov_b32 s34, 4096\n"
"v_mfma_f32_16x16x32_bf16 v[130:133], v[214:217], a[172:175], v[130:133]\n"
"v_bfe_u32 v15, v1, 0, 2\n"
"v_mfma_f32_16x16x32_bf16 v[134:137], v[218:221], a[168:171], v[134:137]\n"
"ds_read_b128 v[210:213], v7 offset:41984\n"
"v_mfma_f32_16x16x32_bf16 v[138:141], v[218:221], a[172:175], v[138:141]\n"
"v_mul_u32_u24 v14, 2, v15\n"
"v_mfma_f32_16x16x32_bf16 v[86:89], v[222:225], a[176:179], v[86:89]\n"
"ds_read_b128 v[214:217], v7 offset:43008\n"
"v_bfe_u32 v15, v1, 2, 2\n"
"v_mfma_f32_16x16x32_bf16 v[90:93], v[222:225], a[180:183], v[90:93]\n"
"v_lshl_add_u32 v14, v15, 4, v14\n"
"v_mfma_f32_16x16x32_bf16 v[94:97], v[226:229], a[176:179], v[94:97]\n"
"ds_read_b128 v[218:221], v7 offset:44032\n"
"v_mfma_f32_16x16x32_bf16 v[98:101], v[226:229], a[180:183], v[98:101]\n"
"s_waitcnt lgkmcnt(4)\n"
"s_lshl_b32 s58, s50, 6\n"
"v_mfma_f32_16x16x32_bf16 v[102:105], v[190:193], a[176:179], v[102:105]\n"
"ds_read_b128 v[222:225], v7 offset:45056\n"
"v_mfma_f32_16x16x32_bf16 v[106:109], v[190:193], a[180:183], v[106:109]\n"
"buffer_load_dwordx4 a[40:43], v2, s[28:31], s64 offen offset:2048 sc1\n"
"v_mfma_f32_16x16x32_bf16 v[110:113], v[194:197], a[176:179], v[110:113]\n"
"ds_read_b128 v[226:229], v7 offset:46080\n"
"v_mfma_f32_16x16x32_bf16 v[114:117], v[194:197], a[180:183], v[114:117]\n"
"buffer_load_dwordx4 a[44:47], v2, s[28:31], s64 offen offset:3072 sc1\n"
"v_mfma_f32_16x16x32_bf16 v[118:121], v[198:201], a[176:179], v[118:121]\n"
"ds_read_b128 v[190:193], v7 offset:47104\n"
"v_mfma_f32_16x16x32_bf16 v[122:125], v[198:201], a[180:183], v[122:125]\n"
"buffer_load_ushort v162, v14, s[32:35], s58 offen offset:0\n"
"v_mfma_f32_16x16x32_bf16 v[126:129], v[202:205], a[176:179], v[126:129]\n"
"ds_read_b128 v[194:197], v7 offset:48128\n"
"buffer_load_ushort v166, v14, s[32:35], s58 offen offset:8\n"
"v_mfma_f32_16x16x32_bf16 v[130:133], v[202:205], a[180:183], v[130:133]\n"
"s_waitcnt lgkmcnt(4)\n"
"ds_read_b128 v[236:239], v231 offset:0\n"
"ds_read_b128 v[240:243], v231 offset:64\n"
"ds_read_b128 v[244:247], v231 offset:128\n"
"ds_read_b128 v[248:251], v231 offset:192\n"
"v_mfma_f32_16x16x32_bf16 v[134:137], v[206:209], a[176:179], v[134:137]\n"
"s_waitcnt vmcnt(23)\n"
"s_barrier\n"
"ds_read_b128 v[198:201], v7 offset:49152\n"
"v_mfma_f32_16x16x32_bf16 v[138:141], v[206:209], a[180:183], v[138:141]\n"
"buffer_load_dwordx4 a[48:51], v3, s[28:31], s64 offen offset:0 sc1\n"
"v_mfma_f32_16x16x32_bf16 v[86:89], v[210:213], a[184:187], v[86:89]\n"
"ds_read_b128 v[202:205], v7 offset:50176\n"
"v_add_u32 v13, s54, v2\n"
"v_mfma_f32_16x16x32_bf16 v[90:93], v[210:213], a[188:191], v[90:93]\n"
"buffer_load_dwordx4 a[52:55], v3, s[28:31], s64 offen offset:1024 sc1\n"
"v_mfma_f32_16x16x32_bf16 v[94:97], v[214:217], a[184:187], v[94:97]\n"
"ds_read_b128 v[206:209], v7 offset:51200\n"
"v_mfma_f32_16x16x32_bf16 v[98:101], v[214:217], a[188:191], v[98:101]\n"
"v_mfma_f32_16x16x32_bf16 v[102:105], v[218:221], a[184:187], v[102:105]\n"
"ds_read_b128 v[210:213], v7 offset:52224\n"
"v_mfma_f32_16x16x32_bf16 v[106:109], v[218:221], a[188:191], v[106:109]\n"
"s_waitcnt lgkmcnt(8)\n"
"ds_write_b128 v13, v[30:33] offset:28672\n"
"v_mfma_f32_16x16x32_bf16 v[110:113], v[222:225], a[184:187], v[110:113]\n"
"ds_read_b128 v[214:217], v7 offset:53248\n"
"v_mfma_f32_16x16x32_bf16 v[114:117], v[222:225], a[188:191], v[114:117]\n"
"buffer_load_dwordx4 a[56:59], v3, s[28:31], s64 offen offset:2048 sc1\n"
"v_mfma_f32_16x16x32_bf16 v[118:121], v[226:229], a[184:187], v[118:121]\n"
"ds_read_b128 v[218:221], v7 offset:54272\n"
"v_mfma_f32_16x16x32_bf16 v[122:125], v[226:229], a[188:191], v[122:125]\n"
"buffer_load_dwordx4 a[60:63], v3, s[28:31], s64 offen offset:3072 sc1\n"
"v_mfma_f32_16x16x32_bf16 v[126:129], v[190:193], a[184:187], v[126:129]\n"
"ds_read_b128 v[222:225], v7 offset:55296\n"
"ds_write_b128 v13, v[38:41] offset:29696\n"
"v_mfma_f32_16x16x32_bf16 v[130:133], v[190:193], a[188:191], v[130:133]\n"
"v_mfma_f32_16x16x32_bf16 v[134:137], v[194:197], a[184:187], v[134:137]\n"
"ds_read_b128 v[226:229], v7 offset:56320\n"
"v_mfma_f32_16x16x32_bf16 v[138:141], v[194:197], a[188:191], v[138:141]\n"
"s_waitcnt lgkmcnt(6)\n"
"v_mfma_f32_16x16x32_bf16 v[86:89], v[198:201], a[192:195], v[86:89]\n"
"ds_read_b128 v[190:193], v7 offset:57344\n"
"v_mfma_f32_16x16x32_bf16 v[90:93], v[198:201], a[196:199], v[90:93]\n"
"s_bfe_u32 s64, s82, 262152\n"
"s_mul_i32 s64, s64, 8192\n"
"v_mfma_f32_16x16x32_bf16 v[94:97], v[202:205], a[192:195], v[94:97]\n"
"ds_read_b128 v[194:197], v7 offset:58368\n"
"s_add_u32 s64, s64, s66\n"
"buffer_load_dwordx4 a[64:67], v2, s[28:31], s64 offen offset:0 sc1\n"
"v_mfma_f32_16x16x32_bf16 v[98:101], v[202:205], a[196:199], v[98:101]\n"
"buffer_load_dwordx4 a[68:71], v2, s[28:31], s64 offen offset:1024 sc1\n"
"v_mfma_f32_16x16x32_bf16 v[102:105], v[206:209], a[192:195], v[102:105]\n"
"ds_read_b128 v[198:201], v7 offset:59392\n"
"ds_write_b128 v13, v[46:49] offset:30720\n"
"v_mfma_f32_16x16x32_bf16 v[106:109], v[206:209], a[196:199], v[106:109]\n"
"v_mfma_f32_16x16x32_bf16 v[110:113], v[210:213], a[192:195], v[110:113]\n"
"ds_read_b128 v[202:205], v7 offset:60416\n"
"ds_write_b128 v13, v[54:57] offset:31744\n"
"v_mfma_f32_16x16x32_bf16 v[114:117], v[210:213], a[196:199], v[114:117]\n"
"s_waitcnt lgkmcnt(6)\n"
"v_mfma_f32_16x16x32_bf16 v[118:121], v[214:217], a[192:195], v[118:121]\n"
"ds_read_b128 v[206:209], v7 offset:61440\n"
"v_mfma_f32_16x16x32_bf16 v[122:125], v[214:217], a[196:199], v[122:125]\n"
"buffer_load_dwordx4 a[72:75], v2, s[28:31], s64 offen offset:2048 sc1\n"
"v_mfma_f32_16x16x32_bf16 v[126:129], v[218:221], a[192:195], v[126:129]\n"
"ds_read_b128 v[210:213], v7 offset:62464\n"
"v_mfma_f32_16x16x32_bf16 v[130:133], v[218:221], a[196:199], v[130:133]\n"
"buffer_load_dwordx4 a[76:79], v2, s[28:31], s64 offen offset:3072 sc1\n"
"v_mfma_f32_16x16x32_bf16 v[134:137], v[222:225], a[192:195], v[134:137]\n"
"ds_read_b128 v[214:217], v7 offset:63488\n"
"ds_write_b128 v13, v[62:65] offset:32768\n"
"v_mfma_f32_16x16x32_bf16 v[138:141], v[222:225], a[196:199], v[138:141]\n"
"v_mfma_f32_16x16x32_bf16 v[86:89], v[226:229], a[200:203], v[86:89]\n"
"ds_read_b128 v[218:221], v7 offset:64512\n"
"v_mfma_f32_16x16x32_bf16 v[90:93], v[226:229], a[204:207], v[90:93]\n"
"s_waitcnt lgkmcnt(6)\n"
"v_mfma_f32_16x16x32_bf16 v[94:97], v[190:193], a[200:203], v[94:97]\n"
"ds_read_b128 v[222:225], v8 offset:0\n"
"ds_write_b128 v13, v[70:73] offset:33792\n"
"v_mfma_f32_16x16x32_bf16 v[98:101], v[190:193], a[204:207], v[98:101]\n"
"buffer_load_dwordx4 a[80:83], v3, s[28:31], s64 offen offset:0 sc1\n"
"v_mfma_f32_16x16x32_bf16 v[102:105], v[194:197], a[200:203], v[102:105]\n"
"ds_read_b128 v[226:229], v8 offset:1024\n"
"v_mfma_f32_16x16x32_bf16 v[106:109], v[194:197], a[204:207], v[106:109]\n"
"buffer_load_dwordx4 a[84:87], v3, s[28:31], s64 offen offset:1024 sc1\n"
"v_mfma_f32_16x16x32_bf16 v[110:113], v[198:201], a[200:203], v[110:113]\n"
"ds_read_b128 v[190:193], v8 offset:2048\n"
"v_mfma_f32_16x16x32_bf16 v[114:117], v[198:201], a[204:207], v[114:117]\n"
"ds_write_b128 v13, v[78:81] offset:34816\n"
"v_mfma_f32_16x16x32_bf16 v[118:121], v[202:205], a[200:203], v[118:121]\n"
"ds_read_b128 v[194:197], v8 offset:3072\n"
"v_mfma_f32_16x16x32_bf16 v[122:125], v[202:205], a[204:207], v[122:125]\n"
"s_waitcnt lgkmcnt(6)\n"
"v_mfma_f32_16x16x32_bf16 v[126:129], v[206:209], a[200:203], v[126:129]\n"
"ds_read_b128 v[198:201], v8 offset:4096\n"
"v_mfma_f32_16x16x32_bf16 v[130:133], v[206:209], a[204:207], v[130:133]\n"
"buffer_load_dwordx4 a[88:91], v3, s[28:31], s64 offen offset:2048 sc1\n"
"v_mfma_f32_16x16x32_bf16 v[134:137], v[210:213], a[200:203], v[134:137]\n"
"ds_read_b128 v[202:205], v8 offset:5120\n"
"v_mfma_f32_16x16x32_bf16 v[138:141], v[210:213], a[204:207], v[138:141]\n"
"buffer_load_dwordx4 a[92:95], v3, s[28:31], s64 offen offset:3072 sc1\n"
"v_mfma_f32_16x16x32_bf16 v[86:89], v[214:217], a[208:211], v[86:89]\n"
"ds_read_b128 v[206:209], v8 offset:6144\n"
"v_mfma_f32_16x16x32_bf16 v[90:93], v[214:217], a[212:215], v[90:93]\n"
"s_waitcnt vmcnt(16)\n"
"s_mov_b64 exec, 1\n"
"buffer_atomic_add v10, v9, s[28:31], s85 offen sc1\n"
"s_mov_b64 exec, -1\n"
"s_nop 1\n"
"v_mfma_f32_16x16x32_bf16 v[94:97], v[218:221], a[208:211], v[94:97]\n"
"ds_read_b128 v[210:213], v8 offset:7168\n"
"v_mfma_f32_16x16x32_bf16 v[98:101], v[218:221], a[212:215], v[98:101]\n"
"s_waitcnt lgkmcnt(4)\n"
"v_mfma_f32_16x16x32_bf16 v[102:105], v[222:225], a[208:211], v[102:105]\n"
"ds_read_b128 v[214:217], v8 offset:8192\n"
"v_mfma_f32_16x16x32_bf16 v[106:109], v[222:225], a[212:215], v[106:109]\n"
"s_bfe_u32 s64, s82, 262156\n"
"s_mul_i32 s64, s64, 8192\n"
"v_mfma_f32_16x16x32_bf16 v[110:113], v[226:229], a[208:211], v[110:113]\n"
"ds_read_b128 v[218:221], v8 offset:9216\n"
"s_add_u32 s64, s64, s66\n"
"v_mfma_f32_16x16x32_bf16 v[114:117], v[226:229], a[212:215], v[114:117]\n"
"buffer_load_dwordx4 a[96:99], v2, s[28:31], s64 offen offset:0 sc1\n"
"v_mfma_f32_16x16x32_bf16 v[118:121], v[190:193], a[208:211], v[118:121]\n"
"ds_read_b128 v[222:225], v8 offset:10240\n"
"v_mul_f32 v22, v18, v158\n"
"v_mfma_f32_16x16x32_bf16 v[122:125], v[190:193], a[212:215], v[122:125]\n"
"buffer_load_dwordx4 a[100:103], v2, s[28:31], s64 offen offset:1024 sc1\n"
"v_mfma_f32_16x16x32_bf16 v[126:129], v[194:197], a[208:211], v[126:129]\n"
"ds_read_b128 v[226:229], v8 offset:11264\n"
"v_mul_f32 v23, v18, v160\n"
"v_mfma_f32_16x16x32_bf16 v[130:133], v[194:197], a[212:215], v[130:133]\n"
"s_waitcnt lgkmcnt(4)\n"
"v_mov_b32 v24, 0x00000000\n"
"v_mfma_f32_16x16x32_bf16 v[134:137], v[198:201], a[208:211], v[134:137]\n"
"s_barrier\n"
"ds_read_b128 v[190:193], v6 offset:0\n"
"v_mfma_f32_16x16x32_bf16 v[138:141], v[198:201], a[212:215], v[138:141]\n"
"buffer_load_dwordx4 a[104:107], v2, s[28:31], s64 offen offset:2048 sc1\n"
"v_mfma_f32_16x16x32_bf16 v[86:89], v[202:205], a[216:219], v[86:89]\n"
"ds_read_b128 v[194:197], v6 offset:1024\n"
"v_mov_b32 v25, v24\n"
"v_mfma_f32_16x16x32_bf16 v[90:93], v[202:205], a[220:223], v[90:93]\n"
"s_waitcnt vmcnt(12)\n"
"v_lshlrev_b32 v162, 16, v162\n"
"v_lshlrev_b32 v166, 16, v166\n"
"v_mfma_f32_16x16x32_bf16 v[94:97], v[206:209], a[216:219], v[94:97]\n"
"ds_read_b128 v[198:201], v6 offset:2048\n"
"v_add_f32 v165, v24, v162\n"
"v_mfma_f32_16x16x32_bf16 v[98:101], v[206:209], a[220:223], v[98:101]\n"
"buffer_load_dwordx4 a[108:111], v2, s[28:31], s64 offen offset:3072 sc1\n"
"v_mfma_f32_16x16x32_bf16 v[102:105], v[210:213], a[216:219], v[102:105]\n"
"ds_read_b128 v[202:205], v6 offset:3072\n"
"v_add_f32 v164, v24, v162\n"
"v_mfma_f32_16x16x32_bf16 v[106:109], v[210:213], a[220:223], v[106:109]\n"
"s_waitcnt lgkmcnt(4)\n"
"v_add_f32 v163, v24, v162\n"
"v_mfma_f32_16x16x32_bf16 v[110:113], v[214:217], a[216:219], v[110:113]\n"
"ds_read_b128 v[206:209], v6 offset:4096\n"
"v_add_f32 v162, v24, v162\n"
"v_mfma_f32_16x16x32_bf16 v[114:117], v[214:217], a[220:223], v[114:117]\n"
"buffer_load_dwordx4 a[112:115], v3, s[28:31], s64 offen offset:0 sc1\n"
"v_mfma_f32_16x16x32_bf16 v[118:121], v[218:221], a[216:219], v[118:121]\n"
"ds_read_b128 v[210:213], v6 offset:5120\n"
"v_add_f32 v169, v25, v166\n"
"v_mfma_f32_16x16x32_bf16 v[122:125], v[218:221], a[220:223], v[122:125]\n"
"v_add_f32 v168, v25, v166\n"
"v_add_f32 v167, v25, v166\n"
"v_mfma_f32_16x16x32_bf16 v[126:129], v[222:225], a[216:219], v[126:129]\n"
"ds_read_b128 v[214:217], v6 offset:6144\n"
"v_add_f32 v166, v25, v166\n"
"v_mfma_f32_16x16x32_bf16 v[130:133], v[222:225], a[220:223], v[130:133]\n"
"buffer_load_dwordx4 a[116:119], v3, s[28:31], s64 offen offset:1024 sc1\n"
"v_mfma_f32_16x16x32_bf16 v[134:137], v[226:229], a[216:219], v[134:137]\n"
"ds_read_b128 v[218:221], v6 offset:7168\n"
"v_mfma_f32_16x16x32_bf16 v[138:141], v[226:229], a[220:223], v[138:141]\n"
"s_waitcnt lgkmcnt(4)\n"
"v_mfma_f32_16x16x32_bf16 v[86:89], v[190:193], a[224:227], v[86:89]\n"
"ds_read_b128 v[222:225], v6 offset:8192\n"
"v_mfma_f32_16x16x32_bf16 v[90:93], v[190:193], a[228:231], v[90:93]\n"
"buffer_load_dwordx4 a[120:123], v3, s[28:31], s64 offen offset:2048 sc1\n"
"v_mfma_f32_16x16x32_bf16 v[94:97], v[194:197], a[224:227], v[94:97]\n"
"ds_read_b128 v[226:229], v6 offset:9216\n"
"v_mfma_f32_16x16x32_bf16 v[98:101], v[194:197], a[228:231], v[98:101]\n"
"v_mfma_f32_16x16x32_bf16 v[102:105], v[198:201], a[224:227], v[102:105]\n"
"ds_read_b128 v[190:193], v6 offset:10240\n"
"v_mfma_f32_16x16x32_bf16 v[106:109], v[198:201], a[228:231], v[106:109]\n"
"buffer_load_dwordx4 a[124:127], v3, s[28:31], s64 offen offset:3072 sc1\n"
"v_mfma_f32_16x16x32_bf16 v[110:113], v[202:205], a[224:227], v[110:113]\n"
"ds_read_b128 v[194:197], v6 offset:11264\n"
"v_mfma_f32_16x16x32_bf16 v[114:117], v[202:205], a[228:231], v[114:117]\n"
"s_waitcnt lgkmcnt(4)\n"
"v_mfma_f32_16x16x32_bf16 v[118:121], v[206:209], a[224:227], v[118:121]\n"
"ds_read_b128 v[198:201], v6 offset:12288\n"
"v_mfma_f32_16x16x32_bf16 v[122:125], v[206:209], a[228:231], v[122:125]\n"
"s_bfe_u32 s64, s82, 262160\n"
"s_mul_i32 s64, s64, 8192\n"
"v_mfma_f32_16x16x32_bf16 v[126:129], v[210:213], a[224:227], v[126:129]\n"
"ds_read_b128 v[202:205], v6 offset:13312\n"
"s_add_u32 s64, s64, s66\n"
"v_mfma_f32_16x16x32_bf16 v[130:133], v[210:213], a[228:231], v[130:133]\n"
"buffer_load_dwordx4 a[128:131], v2, s[28:31], s64 offen offset:0 sc1\n"
"v_mfma_f32_16x16x32_bf16 v[134:137], v[214:217], a[224:227], v[134:137]\n"
"ds_read_b128 v[206:209], v6 offset:14336\n"
"v_mfma_f32_16x16x32_bf16 v[138:141], v[214:217], a[228:231], v[138:141]\n"
"buffer_load_dwordx4 a[132:135], v2, s[28:31], s64 offen offset:1024 sc1\n"
"v_mfma_f32_16x16x32_bf16 v[86:89], v[218:221], a[232:235], v[86:89]\n"
"ds_read_b128 v[210:213], v6 offset:15360\n"
"v_mfma_f32_16x16x32_bf16 v[90:93], v[218:221], a[236:239], v[90:93]\n"
"s_waitcnt lgkmcnt(4)\n"
"v_mfma_f32_16x16x32_bf16 v[94:97], v[222:225], a[232:235], v[94:97]\n"
"ds_read_b128 v[214:217], v6 offset:16384\n"
"v_mfma_f32_16x16x32_bf16 v[98:101], v[222:225], a[236:239], v[98:101]\n"
"buffer_load_dwordx4 a[136:139], v2, s[28:31], s64 offen offset:2048 sc1\n"
"v_mfma_f32_16x16x32_bf16 v[102:105], v[226:229], a[232:235], v[102:105]\n"
"ds_read_b128 v[218:221], v6 offset:17408\n"
"v_mfma_f32_16x16x32_bf16 v[106:109], v[226:229], a[236:239], v[106:109]\n"
"v_mfma_f32_16x16x32_bf16 v[110:113], v[190:193], a[232:235], v[110:113]\n"
"ds_read_b128 v[222:225], v6 offset:18432\n"
"v_mfma_f32_16x16x32_bf16 v[114:117], v[190:193], a[236:239], v[114:117]\n"
"buffer_load_dwordx4 a[140:143], v2, s[28:31], s64 offen offset:3072 sc1\n"
"v_mfma_f32_16x16x32_bf16 v[118:121], v[194:197], a[232:235], v[118:121]\n"
"ds_read_b128 v[226:229], v6 offset:19456\n"
"v_mfma_f32_16x16x32_bf16 v[122:125], v[194:197], a[236:239], v[122:125]\n"
"s_waitcnt lgkmcnt(4)\n"
"v_mov_b32 v11, 0\n"
"s_mov_b64 exec, 1\n"
"buffer_atomic_add v11, v9, s[28:31], s85 offen sc0 sc1\n"
"s_mov_b64 exec, -1\n"
"s_nop 1\n"
"v_mfma_f32_16x16x32_bf16 v[126:129], v[198:201], a[232:235], v[126:129]\n"
"ds_read_b128 v[190:193], v6 offset:20480\n"
"v_mfma_f32_16x16x32_bf16 v[130:133], v[198:201], a[236:239], v[130:133]\n"
"buffer_load_dwordx4 a[144:147], v3, s[28:31], s64 offen offset:0 sc1\n"
"v_mfma_f32_16x16x32_bf16 v[134:137], v[202:205], a[232:235], v[134:137]\n"
"ds_read_b128 v[194:197], v6 offset:21504\n"
"v_mfma_f32_16x16x32_bf16 v[138:141], v[202:205], a[236:239], v[138:141]\n"
"v_mfma_f32_16x16x32_bf16 v[86:89], v[206:209], a[240:243], v[86:89]\n"
"ds_read_b128 v[198:201], v6 offset:22528\n"
"v_mfma_f32_16x16x32_bf16 v[90:93], v[206:209], a[244:247], v[90:93]\n"
"buffer_load_dwordx4 a[148:151], v3, s[28:31], s64 offen offset:1024 sc1\n"
"v_mfma_f32_16x16x32_bf16 v[94:97], v[210:213], a[240:243], v[94:97]\n"
"ds_read_b128 v[202:205], v6 offset:23552\n"
"v_mfma_f32_16x16x32_bf16 v[98:101], v[210:213], a[244:247], v[98:101]\n"
"s_waitcnt lgkmcnt(4)\n"
"v_mfma_f32_16x16x32_bf16 v[102:105], v[214:217], a[240:243], v[102:105]\n"
"ds_read_b128 v[206:209], v6 offset:24576\n"
"v_mfma_f32_16x16x32_bf16 v[106:109], v[214:217], a[244:247], v[106:109]\n"
"buffer_load_dwordx4 a[152:155], v3, s[28:31], s64 offen offset:2048 sc1\n"
"v_mfma_f32_16x16x32_bf16 v[110:113], v[218:221], a[240:243], v[110:113]\n"
"ds_read_b128 v[210:213], v6 offset:25600\n"
"v_mfma_f32_16x16x32_bf16 v[114:117], v[218:221], a[244:247], v[114:117]\n"
"v_mfma_f32_16x16x32_bf16 v[118:121], v[222:225], a[240:243], v[118:121]\n"
"ds_read_b128 v[214:217], v6 offset:26624\n"
"v_mfma_f32_16x16x32_bf16 v[122:125], v[222:225], a[244:247], v[122:125]\n"
"buffer_load_dwordx4 a[156:159], v3, s[28:31], s64 offen offset:3072 sc1\n"
"v_mfma_f32_16x16x32_bf16 v[126:129], v[226:229], a[240:243], v[126:129]\n"
"ds_read_b128 v[218:221], v6 offset:27648\n"
"v_mfma_f32_16x16x32_bf16 v[130:133], v[226:229], a[244:247], v[130:133]\n"
"s_waitcnt lgkmcnt(4)\n"
"v_mfma_f32_16x16x32_bf16 v[134:137], v[190:193], a[240:243], v[134:137]\n"
"s_barrier\n"
"ds_read_b128 v[222:225], v6 offset:28672\n"
"v_mfma_f32_16x16x32_bf16 v[138:141], v[190:193], a[244:247], v[138:141]\n"
"s_bfe_u32 s64, s82, 262164\n"
"s_mul_i32 s64, s64, 8192\n"
"v_mfma_f32_16x16x32_bf16 v[86:89], v[194:197], a[248:251], v[86:89]\n"
"ds_read_b128 v[226:229], v6 offset:29696\n"
"s_add_u32 s64, s64, s66\n"
"v_mfma_f32_16x16x32_bf16 v[90:93], v[194:197], a[252:255], v[90:93]\n"
"buffer_load_dwordx4 a[160:163], v2, s[28:31], s64 offen offset:0 sc1\n"
"v_mfma_f32_16x16x32_bf16 v[94:97], v[198:201], a[248:251], v[94:97]\n"
"ds_read_b128 v[190:193], v6 offset:30720\n"
"v_mfma_f32_16x16x32_bf16 v[98:101], v[198:201], a[252:255], v[98:101]\n"
"buffer_load_dwordx4 a[164:167], v2, s[28:31], s64 offen offset:1024 sc1\n"
"v_mfma_f32_16x16x32_bf16 v[102:105], v[202:205], a[248:251], v[102:105]\n"
"ds_read_b128 v[194:197], v6 offset:31744\n"
"v_mfma_f32_16x16x32_bf16 v[106:109], v[202:205], a[252:255], v[106:109]\n"
"s_waitcnt lgkmcnt(4)\n"
"v_mfma_f32_16x16x32_bf16 v[110:113], v[206:209], a[248:251], v[110:113]\n"
"ds_read_b128 v[198:201], v6 offset:32768\n"
"v_mfma_f32_16x16x32_bf16 v[114:117], v[206:209], a[252:255], v[114:117]\n"
"buffer_load_dwordx4 a[168:171], v2, s[28:31], s64 offen offset:2048 sc1\n"
"v_mfma_f32_16x16x32_bf16 v[118:121], v[210:213], a[248:251], v[118:121]\n"
"ds_read_b128 v[202:205], v6 offset:33792\n"
"v_mfma_f32_16x16x32_bf16 v[122:125], v[210:213], a[252:255], v[122:125]\n"
"v_mfma_f32_16x16x32_bf16 v[126:129], v[214:217], a[248:251], v[126:129]\n"
"ds_read_b128 v[206:209], v6 offset:34816\n"
"v_mfma_f32_16x16x32_bf16 v[130:133], v[214:217], a[252:255], v[130:133]\n"
"buffer_load_dwordx4 a[172:175], v2, s[28:31], s64 offen offset:3072 sc1\n"
"v_mfma_f32_16x16x32_bf16 v[134:137], v[218:221], a[248:251], v[134:137]\n"
"ds_read_b128 v[210:213], v6 offset:35840\n"
"v_mfma_f32_16x16x32_bf16 v[138:141], v[218:221], a[252:255], v[138:141]\n"
"v_pk_fma_f32 v[170:171], v[236:237], v[158:159], v[22:23] op_sel:[0,1,0] op_sel_hi:[1,1,0] neg_lo:[0,0,1] neg_hi:[0,0,1]\n"
"v_pk_fma_f32 v[172:173], v[238:239], v[158:159], v[22:23] op_sel:[0,1,0] op_sel_hi:[1,1,0] neg_lo:[0,0,1] neg_hi:[0,0,1]\n"
"v_pk_fma_f32 v[174:175], v[236:237], v[160:161], v[22:23] op_sel:[0,1,1] op_sel_hi:[1,1,1] neg_lo:[0,0,1] neg_hi:[0,0,1]\n"
"v_pk_fma_f32 v[176:177], v[238:239], v[160:161], v[22:23] op_sel:[0,1,1] op_sel_hi:[1,1,1] neg_lo:[0,0,1] neg_hi:[0,0,1]\n"
"v_pk_fma_f32 v[86:87], v[86:87], v[158:159], v[170:171] op_sel:[0,0,0] op_sel_hi:[1,0,1]\n"
"v_pk_fma_f32 v[88:89], v[88:89], v[158:159], v[172:173] op_sel:[0,0,0] op_sel_hi:[1,0,1]\n"
"v_pk_fma_f32 v[90:91], v[90:91], v[160:161], v[174:175] op_sel:[0,0,0] op_sel_hi:[1,0,1]\n"
"v_pk_fma_f32 v[92:93], v[92:93], v[160:161], v[176:177] op_sel:[0,0,0] op_sel_hi:[1,0,1]\n"
"ds_read_b128 v[236:239], v231 offset:256\n"
"v_pk_mul_f32 v[170:171], v[86:87], v[86:87]\n"
"v_pk_mul_f32 v[172:173], v[88:89], v[88:89]\n"
"v_pk_mul_f32 v[174:175], v[90:91], v[90:91]\n"
"v_pk_mul_f32 v[176:177], v[92:93], v[92:93]\n"
"v_pk_fma_f32 v[170:171], v[170:171], s[86:87], v[178:179]\n"
"v_pk_fma_f32 v[172:173], v[172:173], s[86:87], v[178:179]\n"
"v_pk_fma_f32 v[174:175], v[174:175], s[86:87], v[178:179]\n"
"v_pk_fma_f32 v[176:177], v[176:177], s[86:87], v[178:179]\n"
"v_pk_mul_f32 v[170:171], v[170:171], v[86:87]\n"
"v_pk_mul_f32 v[172:173], v[172:173], v[88:89]\n"
"v_pk_mul_f32 v[174:175], v[174:175], v[90:91]\n"
"v_pk_mul_f32 v[176:177], v[176:177], v[92:93]\n"
"v_exp_f32 v170, v170\n"
"v_exp_f32 v171, v171\n"
"v_exp_f32 v172, v172\n"
"v_exp_f32 v173, v173\n"
"v_exp_f32 v174, v174\n"
"v_exp_f32 v175, v175\n"
"v_exp_f32 v176, v176\n"
"v_exp_f32 v177, v177\n"
"v_pk_add_f32 v[170:171], v[170:171], 1.0 op_sel_hi:[1,0]\n"
"v_pk_add_f32 v[172:173], v[172:173], 1.0 op_sel_hi:[1,0]\n"
"v_pk_add_f32 v[174:175], v[174:175], 1.0 op_sel_hi:[1,0]\n"
"v_pk_add_f32 v[176:177], v[176:177], 1.0 op_sel_hi:[1,0]\n"
"v_rcp_f32 v170, v170\n"
"v_rcp_f32 v171, v171\n"
"v_rcp_f32 v172, v172\n"
"v_rcp_f32 v173, v173\n"
"v_rcp_f32 v174, v174\n"
"v_rcp_f32 v175, v175\n"
"v_rcp_f32 v176, v176\n"
"v_rcp_f32 v177, v177\n"
"v_pk_mul_f32 v[86:87], v[86:87], v[170:171]\n"
"v_pk_mul_f32 v[88:89], v[88:89], v[172:173]\n"
"v_pk_mul_f32 v[90:91], v[90:91], v[174:175]\n"
"s_waitcnt vmcnt(7)\n"
"v_readfirstlane_b32 s61, v11\n"
"s_nop 0\n"
"s_cmp_ge_u32 s61, 16\n"
"s_cbranch_scc0 .Lmk_spin_35\n"
".Lmk_back_36:\n"
"s_bfe_u32 s65, s82, 262148\n"
"s_mul_i32 s65, s65, 28672\n"
"s_add_u32 s65, s65, s67\n"
"s_add_u32 m0, s54, 57344\n"
"s_nop 0\n"
"s_nop 0\n"
"buffer_load_dwordx4 v2, s[28:31], s65 offen offset:0 sc1 lds\n"
"buffer_load_dwordx4 v2, s[28:31], s65 offen offset:1024 sc1 lds\n"
"buffer_load_dwordx4 v2, s[28:31], s65 offen offset:2048 sc1 lds\n"
"buffer_load_dwordx4 v2, s[28:31], s65 offen offset:3072 sc1 lds\n"
"s_add_u32 m0, s54, 61440\n"
"s_nop 0\n"
"s_nop 0\n"
"buffer_load_dwordx4 v3, s[28:31], s65 offen offset:0 sc1 lds\n"
"buffer_load_dwordx4 v3, s[28:31], s65 offen offset:1024 sc1 lds\n"
"buffer_load_dwordx4 v3, s[28:31], s65 offen offset:2048 sc1 lds\n"
"v_pk_mul_f32 v[92:93], v[92:93], v[176:177]\n"
"s_waitcnt lgkmcnt(0)\n"
"v_mfma_f32_16x16x32_bf16 v[30:33], v[222:225], a[0:3], v[162:165]\n"
"ds_read_b128 v[214:217], v6 offset:36864\n"
"v_mfma_f32_16x16x32_bf16 v[34:37], v[222:225], a[4:7], v[166:169]\n"
"s_bfe_u32 s65, s82, 262152\n"
"s_mul_i32 s65, s65, 28672\n"
"v_mfma_f32_16x16x32_bf16 v[38:41], v[226:229], a[0:3], v[162:165]\n"
"ds_read_b128 v[218:221], v6 offset:37888\n"
"s_add_u32 s65, s65, s67\n"
"v_mfma_f32_16x16x32_bf16 v[42:45], v[226:229], a[4:7], v[166:169]\n"
"s_add_u32 m0, s54, 86016\n"
"v_mfma_f32_16x16x32_bf16 v[46:49], v[190:193], a[0:3], v[162:165]\n"
"ds_read_b128 v[222:225], v6 offset:38912\n"
"buffer_load_dwordx4 v2, s[28:31], s65 offen offset:0 sc1 lds\n"
"v_mfma_f32_16x16x32_bf16 v[50:53], v[190:193], a[4:7], v[166:169]\n"
"buffer_load_dwordx4 v2, s[28:31], s65 offen offset:1024 sc1 lds\n"
"v_mfma_f32_16x16x32_bf16 v[54:57], v[194:197], a[0:3], v[162:165]\n"
"ds_read_b128 v[226:229], v6 offset:39936\n"
"v_mfma_f32_16x16x32_bf16 v[58:61], v[194:197], a[4:7], v[166:169]\n"
"v_mfma_f32_16x16x32_bf16 v[62:65], v[198:201], a[0:3], v[162:165]\n"
"ds_read_b128 v[190:193], v6 offset:40960\n"
"v_mfma_f32_16x16x32_bf16 v[66:69], v[198:201], a[4:7], v[166:169]\n"
"buffer_load_dwordx4 v2, s[28:31], s65 offen offset:2048 sc1 lds\n"
"v_mfma_f32_16x16x32_bf16 v[70:73], v[202:205], a[0:3], v[162:165]\n"
".Lmk_pg_14:\n"
"s_cbranch_execz .Lmk_pg_18\n"
"ds_read_b128 v[194:197], v6 offset:41984\n"
"v_fma_f32 v170, v240, v159, -v22\n"
"v_mfma_f32_16x16x32_bf16 v[74:77], v[202:205], a[4:7], v[166:169]\n"
"v_fma_f32 v171, v241, v159, -v22\n"
"v_mfma_f32_16x16x32_bf16 v[78:81], v[206:209], a[0:3], v[162:165]\n"
"ds_read_b128 v[198:201], v6 offset:43008\n"
"v_mfma_f32_16x16x32_bf16 v[82:85], v[206:209], a[4:7], v[166:169]\n"
"buffer_load_dwordx4 v2, s[28:31], s65 offen offset:3072 sc1 lds\n"
"v_mfma_f32_16x16x32_bf16 v[30:33], v[210:213], a[8:11], v[30:33]\n"
"ds_read_b128 v[202:205], v6 offset:44032\n"
"v_fma_f32 v172, v242, v159, -v22\n"
"v_mfma_f32_16x16x32_bf16 v[34:37], v[210:213], a[12:15], v[34:37]\n"
"s_waitcnt lgkmcnt(4)\n"
"v_fma_f32 v173, v243, v159, -v22\n"
"v_mfma_f32_16x16x32_bf16 v[38:41], v[214:217], a[8:11], v[38:41]\n"
"ds_read_b128 v[206:209], v6 offset:45056\n"
"v_fma_f32 v94, v94, v158, v170\n"
"v_mfma_f32_16x16x32_bf16 v[42:45], v[214:217], a[12:15], v[42:45]\n"
"s_add_u32 m0, s54, 90112\n"
"v_mfma_f32_16x16x32_bf16 v[46:49], v[218:221], a[8:11], v[46:49]\n"
"ds_read_b128 v[210:213], v6 offset:46080\n"
"v_fma_f32 v95, v95, v158, v171\n"
"v_mfma_f32_16x16x32_bf16 v[50:53], v[218:221], a[12:15], v[50:53]\n"
"buffer_load_dwordx4 v3, s[28:31], s65 offen offset:0 sc1 lds\n"
"v_mfma_f32_16x16x32_bf16 v[54:57], v[222:225], a[8:11], v[54:57]\n"
"ds_read_b128 v[214:217], v6 offset:47104\n"
"v_fma_f32 v96, v96, v158, v172\n"
"v_mfma_f32_16x16x32_bf16 v[58:61], v[222:225], a[12:15], v[58:61]\n"
"buffer_load_dwordx4 v3, s[28:31], s65 offen offset:1024 sc1 lds\n"
"v_mfma_f32_16x16x32_bf16 v[62:65], v[226:229], a[8:11], v[62:65]\n"
"ds_read_b128 v[218:221], v6 offset:48128\n"
"v_fma_f32 v97, v97, v158, v173\n"
"v_mfma_f32_16x16x32_bf16 v[66:69], v[226:229], a[12:15], v[66:69]\n"
"s_waitcnt lgkmcnt(4)\n"
"v_mul_f32 v170, v94, v94\n"
"v_mfma_f32_16x16x32_bf16 v[70:73], v[190:193], a[8:11], v[70:73]\n"
"ds_read_b128 v[222:225], v6 offset:49152\n"
"v_mfma_f32_16x16x32_bf16 v[74:77], v[190:193], a[12:15], v[74:77]\n"
"buffer_load_dwordx4 v3, s[28:31], s65 offen offset:2048 sc1 lds\n"
"v_mfma_f32_16x16x32_bf16 v[78:81], v[194:197], a[8:11], v[78:81]\n"
"ds_read_b128 v[226:229], v6 offset:50176\n"
"v_mul_f32 v171, v95, v95\n"
"v_mfma_f32_16x16x32_bf16 v[82:85], v[194:197], a[12:15], v[82:85]\n"
"v_mul_f32 v172, v96, v96\n"
"v_mfma_f32_16x16x32_bf16 v[30:33], v[198:201], a[16:19], v[30:33]\n"
"ds_read_b128 v[190:193], v6 offset:51200\n"
"v_mfma_f32_16x16x32_bf16 v[34:37], v[198:201], a[20:23], v[34:37]\n"
"s_bfe_u32 s65, s82, 262156\n"
"s_mul_i32 s65, s65, 28672\n"
"v_mfma_f32_16x16x32_bf16 v[38:41], v[202:205], a[16:19], v[38:41]\n"
"ds_read_b128 v[194:197], v6 offset:52224\n"
"s_add_u32 s65, s65, s67\n"
"v_mfma_f32_16x16x32_bf16 v[42:45], v[202:205], a[20:23], v[42:45]\n"
"s_waitcnt lgkmcnt(4)\n"
"s_add_u32 m0, s54, 114688\n"
"v_mfma_f32_16x16x32_bf16 v[46:49], v[206:209], a[16:19], v[46:49]\n"
"ds_read_b128 v[198:201], v6 offset:53248\n"
"buffer_load_dwordx4 v2, s[28:31], s65 offen offset:0 sc1 lds\n"
"v_mfma_f32_16x16x32_bf16 v[50:53], v[206:209], a[20:23], v[50:53]\n"
"buffer_load_dwordx4 v2, s[28:31], s65 offen offset:1024 sc1 lds\n"
"v_mfma_f32_16x16x32_bf16 v[54:57], v[210:213], a[16:19], v[54:57]\n"
"ds_read_b128 v[202:205], v6 offset:54272\n"
"v_mul_f32 v173, v97, v97\n"
"v_mfma_f32_16x16x32_bf16 v[58:61], v[210:213], a[20:23], v[58:61]\n"
"v_fmamk_f32 v170, v170, 0xbdd2d3e8, v178\n"
"v_fmamk_f32 v171, v171, 0xbdd2d3e8, v178\n"
"v_mfma_f32_16x16x32_bf16 v[62:65], v[214:217], a[16:19], v[62:65]\n"
"ds_read_b128 v[206:209], v6 offset:55296\n"
"v_fmamk_f32 v172, v172, 0xbdd2d3e8, v178\n"
"v_mfma_f32_16x16x32_bf16 v[66:69], v[214:217], a[20:23], v[66:69]\n"
"buffer_load_dwordx4 v2, s[28:31], s65 offen offset:2048 sc1 lds\n"
"v_mfma_f32_16x16x32_bf16 v[70:73], v[218:221], a[16:19], v[70:73]\n"
"ds_read_b128 v[210:213], v6 offset:56320\n"
"v_fmamk_f32 v173, v173, 0xbdd2d3e8, v178\n"
"v_mfma_f32_16x16x32_bf16 v[74:77], v[218:221], a[20:23], v[74:77]\n"
"s_waitcnt lgkmcnt(4)\n"
"v_mul_f32 v170, v170, v94\n"
"v_mfma_f32_16x16x32_bf16 v[78:81], v[222:225], a[16:19], v[78:81]\n"
"s_waitcnt vmcnt(10)\n"
"s_barrier\n"
"ds_read_b128 v[214:217], v6 offset:57344\n"
"v_mfma_f32_16x16x32_bf16 v[82:85], v[222:225], a[20:23], v[82:85]\n"
"v_fma_f32 v174, v240, v161, -v23\n"
"v_mul_f32 v171, v171, v95\n"
"v_mfma_f32_16x16x32_bf16 v[30:33], v[226:229], a[24:27], v[30:33]\n"
"ds_read_b128 v[218:221], v6 offset:58368\n"
"v_fma_f32 v175, v241, v161, -v23\n"
"v_mfma_f32_16x16x32_bf16 v[34:37], v[226:229], a[28:31], v[34:37]\n"
"buffer_load_dwordx4 v2, s[28:31], s65 offen offset:3072 sc1 lds\n"
"v_mfma_f32_16x16x32_bf16 v[38:41], v[190:193], a[24:27], v[38:41]\n"
"ds_read_b128 v[222:225], v6 offset:59392\n"
"v_mul_f32 v172, v172, v96\n"
"v_mfma_f32_16x16x32_bf16 v[42:45], v[190:193], a[28:31], v[42:45]\n"
"v_mfma_f32_16x16x32_bf16 v[46:49], v[194:197], a[24:27], v[46:49]\n"
"ds_read_b128 v[226:229], v6 offset:60416\n"
"s_add_u32 m0, s54, 118784\n"
"v_mfma_f32_16x16x32_bf16 v[50:53], v[194:197], a[28:31], v[50:53]\n"
"s_waitcnt lgkmcnt(4)\n"
"v_fma_f32 v176, v242, v161, -v23\n"
"v_mfma_f32_16x16x32_bf16 v[54:57], v[198:201], a[24:27], v[54:57]\n"
"ds_read_b128 v[190:193], v6 offset:61440\n"
"v_mul_f32 v173, v173, v97\n"
"v_mfma_f32_16x16x32_bf16 v[58:61], v[198:201], a[28:31], v[58:61]\n"
"buffer_load_dwordx4 v3, s[28:31], s65 offen offset:0 sc1 lds\n"
"v_mfma_f32_16x16x32_bf16 v[62:65], v[202:205], a[24:27], v[62:65]\n"
"ds_read_b128 v[194:197], v6 offset:62464\n"
"v_fma_f32 v177, v243, v161, -v23\n"
"v_mfma_f32_16x16x32_bf16 v[66:69], v[202:205], a[28:31], v[66:69]\n"
"buffer_load_dwordx4 v3, s[28:31], s65 offen offset:1024 sc1 lds\n"
"v_mfma_f32_16x16x32_bf16 v[70:73], v[206:209], a[24:27], v[70:73]\n"
"ds_read_b128 v[198:201], v6 offset:63488\n"
"v_mfma_f32_16x16x32_bf16 v[74:77], v[206:209], a[28:31], v[74:77]\n"
"v_exp_f32 v170, v170\n"
"v_mfma_f32_16x16x32_bf16 v[78:81], v[210:213], a[24:27], v[78:81]\n"
"ds_read_b128 v[202:205], v6 offset:64512\n"
"ds_read_b128 v[240:243], v231 offset:320\n"
"v_mfma_f32_16x16x32_bf16 v[82:85], v[210:213], a[28:31], v[82:85]\n"
"s_waitcnt lgkmcnt(5)\n"
"v_exp_f32 v171, v171\n"
"v_mfma_f32_16x16x32_bf16 v[30:33], v[214:217], a[32:35], v[30:33]\n"
"ds_read_b128 v[206:209], v7 offset:0\n"
"v_fma_f32 v98, v98, v160, v174\n"
"v_mfma_f32_16x16x32_bf16 v[34:37], v[214:217], a[36:39], v[34:37]\n"
"buffer_load_dwordx4 v3, s[28:31], s65 offen offset:2048 sc1 lds\n"
"v_mfma_f32_16x16x32_bf16 v[38:41], v[218:221], a[32:35], v[38:41]\n"
"ds_read_b128 v[210:213], v7 offset:1024\n"
"v_exp_f32 v172, v172\n"
"v_mfma_f32_16x16x32_bf16 v[42:45], v[218:221], a[36:39], v[42:45]\n"
"s_bfe_u32 s65, s82, 262160\n"
"s_mul_i32 s65, s65, 28672\n"
"v_mfma_f32_16x16x32_bf16 v[46:49], v[222:225], a[32:35], v[46:49]\n"
"ds_read_b128 v[214:217], v7 offset:2048\n"
"s_add_u32 s65, s65, s67\n"
"v_mfma_f32_16x16x32_bf16 v[50:53], v[222:225], a[36:39], v[50:53]\n"
"s_add_u32 m0, s54, 0\n"
"s_nop 0\n"
"buffer_load_dwordx4 v2, s[28:31], s65 offen offset:0 sc1 lds\n"
"v_mfma_f32_16x16x32_bf16 v[54:57], v[226:229], a[32:35], v[54:57]\n"
"ds_read_b128 v[218:221], v7 offset:3072\n"
"v_fma_f32 v99, v99, v160, v175\n"
"v_mfma_f32_16x16x32_bf16 v[58:61], v[226:229], a[36:39], v[58:61]\n"
"s_waitcnt lgkmcnt(5)\n"
"v_exp_f32 v173, v173\n"
"v_mfma_f32_16x16x32_bf16 v[62:65], v[190:193], a[32:35], v[62:65]\n"
"ds_read_b128 v[222:225], v7 offset:4096\n"
"v_fma_f32 v100, v100, v160, v176\n"
"v_mfma_f32_16x16x32_bf16 v[66:69], v[190:193], a[36:39], v[66:69]\n"
"buffer_load_dwordx4 v2, s[28:31], s65 offen offset:1024 sc1 lds\n"
"v_mfma_f32_16x16x32_bf16 v[70:73], v[194:197], a[32:35], v[70:73]\n"
"ds_read_b128 v[226:229], v7 offset:5120\n"
"v_add_f32 v170, 1.0, v170\n"
"v_mfma_f32_16x16x32_bf16 v[74:77], v[194:197], a[36:39], v[74:77]\n"
"buffer_load_dwordx4 v2, s[28:31], s65 offen offset:2048 sc1 lds\n"
"v_mfma_f32_16x16x32_bf16 v[78:81], v[198:201], a[32:35], v[78:81]\n"
"ds_read_b128 v[190:193], v7 offset:6144\n"
"v_fma_f32 v101, v101, v160, v177\n"
"v_mfma_f32_16x16x32_bf16 v[82:85], v[198:201], a[36:39], v[82:85]\n"
"v_add_f32 v171, 1.0, v171\n"
"v_mfma_f32_16x16x32_bf16 v[30:33], v[202:205], a[40:43], v[30:33]\n"
"ds_read_b128 v[194:197], v7 offset:7168\n"
"v_mul_f32 v174, v98, v98\n"
"v_mfma_f32_16x16x32_bf16 v[34:37], v[202:205], a[44:47], v[34:37]\n"
"s_waitcnt lgkmcnt(4)\n"
"v_mfma_f32_16x16x32_bf16 v[38:41], v[206:209], a[40:43], v[38:41]\n"
"ds_read_b128 v[198:201], v7 offset:8192\n"
"v_add_f32 v172, 1.0, v172\n"
"v_mfma_f32_16x16x32_bf16 v[42:45], v[206:209], a[44:47], v[42:45]\n"
"buffer_load_dwordx4 v2, s[28:31], s65 offen offset:3072 sc1 lds\n"
"v_mfma_f32_16x16x32_bf16 v[46:49], v[210:213], a[40:43], v[46:49]\n"
"ds_read_b128 v[202:205], v7 offset:9216\n"
"s_add_u32 m0, s54, 4096\n"
"v_mfma_f32_16x16x32_bf16 v[50:53], v[210:213], a[44:47], v[50:53]\n"
"v_mul_f32 v175, v99, v99\n"
"v_add_f32 v173, 1.0, v173\n"
"v_mfma_f32_16x16x32_bf16 v[54:57], v[214:217], a[40:43], v[54:57]\n"
"ds_read_b128 v[206:209], v7 offset:10240\n"
"v_mfma_f32_16x16x32_bf16 v[58:61], v[214:217], a[44:47], v[58:61]\n"
"buffer_load_dwordx4 v3, s[28:31], s65 offen offset:0 sc1 lds\n"
"v_mfma_f32_16x16x32_bf16 v[62:65], v[218:221], a[40:43], v[62:65]\n"
"ds_read_b128 v[210:213], v7 offset:11264\n"
"v_mul_f32 v176, v100, v100\n"
"v_mfma_f32_16x16x32_bf16 v[66:69], v[218:221], a[44:47], v[66:69]\n"
"s_waitcnt lgkmcnt(4)\n"
"v_rcp_f32 v170, v170\n"
"v_mfma_f32_16x16x32_bf16 v[70:73], v[222:225], a[40:43], v[70:73]\n"
"ds_read_b128 v[214:217], v7 offset:12288\n"
"v_mul_f32 v177, v101, v101\n"
"v_mfma_f32_16x16x32_bf16 v[74:77], v[222:225], a[44:47], v[74:77]\n"
"buffer_load_dwordx4 v3, s[28:31], s65 offen offset:1024 sc1 lds\n"
"v_mfma_f32_16x16x32_bf16 v[78:81], v[226:229], a[40:43], v[78:81]\n"
"ds_read_b128 v[218:221], v7 offset:13312\n"
"v_mfma_f32_16x16x32_bf16 v[82:85], v[226:229], a[44:47], v[82:85]\n"
"buffer_load_dwordx4 v3, s[28:31], s65 offen offset:2048 sc1 lds\n"
"v_mfma_f32_16x16x32_bf16 v[30:33], v[190:193], a[48:51], v[30:33]\n"
"ds_read_b128 v[222:225], v7 offset:14336\n"
"v_mfma_f32_16x16x32_bf16 v[34:37], v[190:193], a[52:55], v[34:37]\n"
"v_rcp_f32 v171, v171\n"
"v_mfma_f32_16x16x32_bf16 v[38:41], v[194:197], a[48:51], v[38:41]\n"
"ds_read_b128 v[226:229], v7 offset:15360\n"
"v_fmamk_f32 v174, v174, 0xbdd2d3e8, v178\n"
"v_mfma_f32_16x16x32_bf16 v[42:45], v[194:197], a[52:55], v[42:45]\n"
"s_waitcnt lgkmcnt(4)\n"
"v_rcp_f32 v172, v172\n"
"v_mfma_f32_16x16x32_bf16 v[46:49], v[198:201], a[48:51], v[46:49]\n"
"ds_read_b128 v[190:193], v7 offset:16384\n"
"v_fmamk_f32 v175, v175, 0xbdd2d3e8, v178\n"
"v_mfma_f32_16x16x32_bf16 v[50:53], v[198:201], a[52:55], v[50:53]\n"
"s_bfe_u32 s64, s82, 262164\n"
"s_mul_i32 s64, s64, 8192\n"
"v_mfma_f32_16x16x32_bf16 v[54:57], v[202:205], a[48:51], v[54:57]\n"
"ds_read_b128 v[194:197], v7 offset:17408\n"
"s_add_u32 s64, s64, s66\n"
"buffer_load_dwordx4 a[176:179], v3, s[28:31], s64 offen offset:0 sc1\n"
"v_mfma_f32_16x16x32_bf16 v[58:61], v[202:205], a[52:55], v[58:61]\n"
"buffer_load_dwordx4 a[180:183], v3, s[28:31], s64 offen offset:1024 sc1\n"
"v_mfma_f32_16x16x32_bf16 v[62:65], v[206:209], a[48:51], v[62:65]\n"
"ds_read_b128 v[198:201], v7 offset:18432\n"
"v_mfma_f32_16x16x32_bf16 v[66:69], v[206:209], a[52:55], v[66:69]\n"
"v_rcp_f32 v173, v173\n"
"v_mfma_f32_16x16x32_bf16 v[70:73], v[210:213], a[48:51], v[70:73]\n"
"ds_read_b128 v[202:205], v7 offset:19456\n"
"v_fmamk_f32 v176, v176, 0xbdd2d3e8, v178\n"
"v_mfma_f32_16x16x32_bf16 v[74:77], v[210:213], a[52:55], v[74:77]\n"
"s_waitcnt lgkmcnt(4)\n"
"v_mul_f32 v94, v94, v170\n"
"v_mfma_f32_16x16x32_bf16 v[78:81], v[214:217], a[48:51], v[78:81]\n"
"s_waitcnt vmcnt(16)\n"
"s_barrier\n"
"ds_read_b128 v[206:209], v7 offset:20480\n"
"v_mfma_f32_16x16x32_bf16 v[82:85], v[214:217], a[52:55], v[82:85]\n"
"buffer_load_dwordx4 a[184:187], v3, s[28:31], s64 offen offset:2048 sc1\n"
"v_mfma_f32_16x16x32_bf16 v[30:33], v[218:221], a[56:59], v[30:33]\n"
"ds_read_b128 v[210:213], v7 offset:21504\n"
"v_fmamk_f32 v177, v177, 0xbdd2d3e8, v178\n"
"v_mfma_f32_16x16x32_bf16 v[34:37], v[218:221], a[60:63], v[34:37]\n"
"s_bfe_u32 s65, s82, 262164\n"
"s_mul_i32 s65, s65, 28672\n"
"v_mfma_f32_16x16x32_bf16 v[38:41], v[222:225], a[56:59], v[38:41]\n"
"ds_read_b128 v[214:217], v7 offset:22528\n"
"s_add_u32 s65, s65, s67\n"
"v_mfma_f32_16x16x32_bf16 v[42:45], v[222:225], a[60:63], v[42:45]\n"
"s_add_u32 m0, s54, 28672\n"
"v_mul_f32 v95, v95, v171\n"
"v_mfma_f32_16x16x32_bf16 v[46:49], v[226:229], a[56:59], v[46:49]\n"
"ds_read_b128 v[218:221], v7 offset:23552\n"
"buffer_load_dwordx4 v2, s[28:31], s65 offen offset:0 sc1 lds\n"
"v_mfma_f32_16x16x32_bf16 v[50:53], v[226:229], a[60:63], v[50:53]\n"
"s_waitcnt lgkmcnt(4)\n"
"v_mul_f32 v174, v174, v98\n"
"v_mfma_f32_16x16x32_bf16 v[54:57], v[190:193], a[56:59], v[54:57]\n"
"ds_read_b128 v[222:225], v7 offset:24576\n"
"v_mul_f32 v96, v96, v172\n"
"v_mfma_f32_16x16x32_bf16 v[58:61], v[190:193], a[60:63], v[58:61]\n"
"buffer_load_dwordx4 v2, s[28:31], s65 offen offset:1024 sc1 lds\n"
"v_mfma_f32_16x16x32_bf16 v[62:65], v[194:197], a[56:59], v[62:65]\n"
"ds_read_b128 v[226:229], v7 offset:25600\n"
"v_mul_f32 v175, v175, v99\n"
"v_mfma_f32_16x16x32_bf16 v[66:69], v[194:197], a[60:63], v[66:69]\n"
"buffer_load_dwordx4 v2, s[28:31], s65 offen offset:2048 sc1 lds\n"
"v_mfma_f32_16x16x32_bf16 v[70:73], v[198:201], a[56:59], v[70:73]\n"
"ds_read_b128 v[190:193], v7 offset:26624\n"
"v_mul_f32 v97, v97, v173\n"
"v_mfma_f32_16x16x32_bf16 v[74:77], v[198:201], a[60:63], v[74:77]\n"
"v_mul_f32 v176, v176, v100\n"
"v_fma_f32 v170, v244, v159, -v22\n"
"v_mfma_f32_16x16x32_bf16 v[78:81], v[202:205], a[56:59], v[78:81]\n"
"ds_read_b128 v[194:197], v7 offset:27648\n"
"v_mul_f32 v177, v177, v101\n"
"v_mfma_f32_16x16x32_bf16 v[82:85], v[202:205], a[60:63], v[82:85]\n"
"s_waitcnt lgkmcnt(4)\n"
"v_fma_f32 v171, v245, v159, -v22\n"
"v_mfma_f32_16x16x32_bf16 v[30:33], v[206:209], a[64:67], v[30:33]\n"
"ds_read_b128 v[198:201], v7 offset:28672\n"
"v_exp_f32 v174, v174\n"
"v_mfma_f32_16x16x32_bf16 v[34:37], v[206:209], a[68:71], v[34:37]\n"
"buffer_load_dwordx4 v2, s[28:31], s65 offen offset:3072 sc1 lds\n"
"v_mfma_f32_16x16x32_bf16 v[38:41], v[210:213], a[64:67], v[38:41]\n"
"ds_read_b128 v[202:205], v7 offset:29696\n"
"v_fma_f32 v172, v246, v159, -v22\n"
"v_mfma_f32_16x16x32_bf16 v[42:45], v[210:213], a[68:71], v[42:45]\n"
"s_add_u32 m0, s54, 32768\n"
"v_exp_f32 v175, v175\n"
"v_mfma_f32_16x16x32_bf16 v[46:49], v[214:217], a[64:67], v[46:49]\n"
"ds_read_b128 v[206:209], v7 offset:30720\n"
"v_mfma_f32_16x16x32_bf16 v[50:53], v[214:217], a[68:71], v[50:53]\n"
"buffer_load_dwordx4 v3, s[28:31], s65 offen offset:0 sc1 lds\n"
"v_mfma_f32_16x16x32_bf16 v[54:57], v[218:221], a[64:67], v[54:57]\n"
"ds_read_b128 v[210:213], v7 offset:31744\n"
"v_fma_f32 v173, v247, v159, -v22\n"
"v_mfma_f32_16x16x32_bf16 v[58:61], v[218:221], a[68:71], v[58:61]\n"
"s_waitcnt lgkmcnt(4)\n"
"v_mfma_f32_16x16x32_bf16 v[62:65], v[222:225], a[64:67], v[62:65]\n"
"ds_read_b128 v[214:217], v7 offset:32768\n"
"v_mfma_f32_16x16x32_bf16 v[66:69], v[222:225], a[68:71], v[66:69]\n"
"buffer_load_dwordx4 v3, s[28:31], s65 offen offset:1024 sc1 lds\n"
"v_mfma_f32_16x16x32_bf16 v[70:73], v[226:229], a[64:67], v[70:73]\n"
"ds_read_b128 v[218:221], v7 offset:33792\n"
"v_mfma_f32_16x16x32_bf16 v[74:77], v[226:229], a[68:71], v[74:77]\n"
"v_exp_f32 v176, v176\n"
"v_mfma_f32_16x16x32_bf16 v[78:81], v[190:193], a[64:67], v[78:81]\n"
"ds_read_b128 v[222:225], v7 offset:34816\n"
"v_fma_f32 v102, v102, v158, v170\n"
"v_mfma_f32_16x16x32_bf16 v[82:85], v[190:193], a[68:71], v[82:85]\n"
"buffer_load_dwordx4 v3, s[28:31], s65 offen offset:2048 sc1 lds\n"
"v_mfma_f32_16x16x32_bf16 v[30:33], v[194:197], a[72:75], v[30:33]\n"
"ds_read_b128 v[226:229], v7 offset:35840\n"
"v_exp_f32 v177, v177\n"
"v_mfma_f32_16x16x32_bf16 v[34:37], v[194:197], a[76:79], v[34:37]\n"
"s_waitcnt lgkmcnt(4)\n"
"v_fma_f32 v103, v103, v158, v171\n"
"v_mfma_f32_16x16x32_bf16 v[38:41], v[198:201], a[72:75], v[38:41]\n"
"ds_read_b128 v[190:193], v7 offset:36864\n"
"v_add_f32 v174, 1.0, v174\n"
"v_mfma_f32_16x16x32_bf16 v[42:45], v[198:201], a[76:79], v[42:45]\n"
"buffer_load_dwordx4 a[188:191], v3, s[28:31], s64 offen offset:3072 sc1\n"
"v_mfma_f32_16x16x32_bf16 v[46:49], v[202:205], a[72:75], v[46:49]\n"
"ds_read_b128 v[194:197], v7 offset:37888\n"
"v_fma_f32 v104, v104, v158, v172\n"
"v_mfma_f32_16x16x32_bf16 v[50:53], v[202:205], a[76:79], v[50:53]\n"
"v_add_f32 v175, 1.0, v175\n"
"v_fma_f32 v105, v105, v158, v173\n"
"v_mfma_f32_16x16x32_bf16 v[54:57], v[206:209], a[72:75], v[54:57]\n"
"ds_read_b128 v[198:201], v7 offset:38912\n"
"v_add_f32 v176, 1.0, v176\n"
"v_mfma_f32_16x16x32_bf16 v[58:61], v[206:209], a[76:79], v[58:61]\n"
"s_bfe_u32 s64, s82, 262168\n"
"s_mul_i32 s64, s64, 8192\n"
"v_mfma_f32_16x16x32_bf16 v[62:65], v[210:213], a[72:75], v[62:65]\n"
"ds_read_b128 v[202:205], v7 offset:39936\n"
"s_add_u32 s64, s64, s66\n"
"v_mfma_f32_16x16x32_bf16 v[66:69], v[210:213], a[76:79], v[66:69]\n"
"s_waitcnt lgkmcnt(4)\n"
"buffer_load_dwordx4 a[192:195], v2, s[28:31], s64 offen offset:0 sc1\n"
"v_mfma_f32_16x16x32_bf16 v[70:73], v[214:217], a[72:75], v[70:73]\n"
"ds_read_b128 v[206:209], v7 offset:40960\n"
"v_mul_f32 v170, v102, v102\n"
"v_mfma_f32_16x16x32_bf16 v[74:77], v[214:217], a[76:79], v[74:77]\n"
"buffer_load_dwordx4 a[196:199], v2, s[28:31], s64 offen offset:1024 sc1\n"
"v_mfma_f32_16x16x32_bf16 v[78:81], v[218:221], a[72:75], v[78:81]\n"
"ds_read_b128 v[210:213], v7 offset:41984\n"
"v_add_f32 v177, 1.0, v177\n"
"v_mfma_f32_16x16x32_bf16 v[82:85], v[218:221], a[76:79], v[82:85]\n"
"v_mul_f32 v171, v103, v103\n"
"v_rcp_f32 v174, v174\n"
"v_mfma_f32_16x16x32_bf16 v[30:33], v[222:225], a[80:83], v[30:33]\n"
"ds_read_b128 v[214:217], v7 offset:43008\n"
"v_mfma_f32_16x16x32_bf16 v[34:37], v[222:225], a[84:87], v[34:37]\n"
"buffer_load_dwordx4 a[200:203], v2, s[28:31], s64 offen offset:2048 sc1\n"
"v_mfma_f32_16x16x32_bf16 v[38:41], v[226:229], a[80:83], v[38:41]\n"
"ds_read_b128 v[218:221], v7 offset:44032\n"
"v_mul_f32 v172, v104, v104\n"
"v_mfma_f32_16x16x32_bf16 v[42:45], v[226:229], a[84:87], v[42:45]\n"
"s_waitcnt lgkmcnt(4)\n"
"v_mfma_f32_16x16x32_bf16 v[46:49], v[190:193], a[80:83], v[46:49]\n"
"ds_read_b128 v[222:225], v7 offset:45056\n"
"v_mfma_f32_16x16x32_bf16 v[50:53], v[190:193], a[84:87], v[50:53]\n"
"buffer_load_dwordx4 a[204:207], v2, s[28:31], s64 offen offset:3072 sc1\n"
"v_mfma_f32_16x16x32_bf16 v[54:57], v[194:197], a[80:83], v[54:57]\n"
"ds_read_b128 v[226:229], v7 offset:46080\n"
"v_mfma_f32_16x16x32_bf16 v[58:61], v[194:197], a[84:87], v[58:61]\n"
"v_rcp_f32 v175, v175\n"
"v_mfma_f32_16x16x32_bf16 v[62:65], v[198:201], a[80:83], v[62:65]\n"
"ds_read_b128 v[190:193], v7 offset:47104\n"
"v_mul_f32 v173, v105, v105\n"
"v_mfma_f32_16x16x32_bf16 v[66:69], v[198:201], a[84:87], v[66:69]\n"
"buffer_load_dwordx4 a[208:211], v3, s[28:31], s64 offen offset:0 sc1\n"
"v_mfma_f32_16x16x32_bf16 v[70:73], v[202:205], a[80:83], v[70:73]\n"
"ds_read_b128 v[194:197], v7 offset:48128\n"
"v_rcp_f32 v176, v176\n"
"v_mfma_f32_16x16x32_bf16 v[74:77], v[202:205], a[84:87], v[74:77]\n"
"s_waitcnt lgkmcnt(4)\n"
"v_fmamk_f32 v170, v170, 0xbdd2d3e8, v178\n"
"v_mfma_f32_16x16x32_bf16 v[78:81], v[206:209], a[80:83], v[78:81]\n"
"s_waitcnt vmcnt(23)\n"
"s_barrier\n"
"ds_read_b128 v[198:201], v7 offset:49152\n"
"v_mfma_f32_16x16x32_bf16 v[82:85], v[206:209], a[84:87], v[82:85]\n"
"buffer_load_dwordx4 a[212:215], v3, s[28:31], s64 offen offset:1024 sc1\n"
"v_mfma_f32_16x16x32_bf16 v[30:33], v[210:213], a[88:91], v[30:33]\n"
"ds_read_b128 v[202:205], v7 offset:50176\n"
"v_mfma_f32_16x16x32_bf16 v[34:37], v[210:213], a[92:95], v[34:37]\n"
"v_rcp_f32 v177, v177\n"
"v_mfma_f32_16x16x32_bf16 v[38:41], v[214:217], a[88:91], v[38:41]\n"
"ds_read_b128 v[206:209], v7 offset:51200\n"
"v_fmamk_f32 v171, v171, 0xbdd2d3e8, v178\n"
"v_mfma_f32_16x16x32_bf16 v[42:45], v[214:217], a[92:95], v[42:45]\n"
"s_bfe_u32 s65, s82, 262168\n"
"s_mul_i32 s65, s65, 28672\n"
"v_mfma_f32_16x16x32_bf16 v[46:49], v[218:221], a[88:91], v[46:49]\n"
"ds_read_b128 v[210:213], v7 offset:52224\n"
"s_add_u32 s65, s65, s67\n"
"v_mfma_f32_16x16x32_bf16 v[50:53], v[218:221], a[92:95], v[50:53]\n"
"s_waitcnt lgkmcnt(4)\n"
"s_add_u32 m0, s54, 57344\n"
"s_nop 0\n"
"buffer_load_dwordx4 v2, s[28:31], s65 offen offset:0 sc1 lds\n"
"v_mfma_f32_16x16x32_bf16 v[54:57], v[222:225], a[88:91], v[54:57]\n"
"ds_read_b128 v[214:217], v7 offset:53248\n"
"v_mul_f32 v98, v98, v174\n"
"v_mfma_f32_16x16x32_bf16 v[58:61], v[222:225], a[92:95], v[58:61]\n"
"buffer_load_dwordx4 v2, s[28:31], s65 offen offset:1024 sc1 lds\n"
"v_mfma_f32_16x16x32_bf16 v[62:65], v[226:229], a[88:91], v[62:65]\n"
"ds_read_b128 v[218:221], v7 offset:54272\n"
"v_fmamk_f32 v172, v172, 0xbdd2d3e8, v178\n"
"v_mfma_f32_16x16x32_bf16 v[66:69], v[226:229], a[92:95], v[66:69]\n"
"v_mul_f32 v99, v99, v175\n"
"v_fmamk_f32 v173, v173, 0xbdd2d3e8, v178\n"
"v_mfma_f32_16x16x32_bf16 v[70:73], v[190:193], a[88:91], v[70:73]\n"
"ds_read_b128 v[222:225], v7 offset:55296\n"
"v_mul_f32 v100, v100, v176\n"
"v_mfma_f32_16x16x32_bf16 v[74:77], v[190:193], a[92:95], v[74:77]\n"
"buffer_load_dwordx4 v2, s[28:31], s65 offen offset:2048 sc1 lds\n"
"v_mfma_f32_16x16x32_bf16 v[78:81], v[194:197], a[88:91], v[78:81]\n"
"ds_read_b128 v[226:229], v7 offset:56320\n"
"v_mul_f32 v170, v170, v102\n"
"v_mfma_f32_16x16x32_bf16 v[82:85], v[194:197], a[92:95], v[82:85]\n"
"s_waitcnt lgkmcnt(4)\n"
"v_mul_f32 v101, v101, v177\n"
"v_mfma_f32_16x16x32_bf16 v[30:33], v[198:201], a[96:99], v[30:33]\n"
"ds_read_b128 v[190:193], v7 offset:57344\n"
"v_mul_f32 v171, v171, v103\n"
"v_mfma_f32_16x16x32_bf16 v[34:37], v[198:201], a[100:103], v[34:37]\n"
"buffer_load_dwordx4 v2, s[28:31], s65 offen offset:3072 sc1 lds\n"
"v_mfma_f32_16x16x32_bf16 v[38:41], v[202:205], a[96:99], v[38:41]\n"
"ds_read_b128 v[194:197], v7 offset:58368\n"
"v_fma_f32 v174, v244, v161, -v23\n"
"v_mfma_f32_16x16x32_bf16 v[42:45], v[202:205], a[100:103], v[42:45]\n"
"v_mul_f32 v172, v172, v104\n"
"v_fma_f32 v175, v245, v161, -v23\n"
"v_mfma_f32_16x16x32_bf16 v[46:49], v[206:209], a[96:99], v[46:49]\n"
"ds_read_b128 v[198:201], v7 offset:59392\n"
"v_mul_f32 v173, v173, v105\n"
"v_mfma_f32_16x16x32_bf16 v[50:53], v[206:209], a[100:103], v[50:53]\n"
"s_add_u32 m0, s54, 61440\n"
"v_mfma_f32_16x16x32_bf16 v[54:57], v[210:213], a[96:99], v[54:57]\n"
"ds_read_b128 v[202:205], v7 offset:60416\n"
"v_fma_f32 v176, v246, v161, -v23\n"
"v_mfma_f32_16x16x32_bf16 v[58:61], v[210:213], a[100:103], v[58:61]\n"
"s_waitcnt lgkmcnt(4)\n"
"buffer_load_dwordx4 v3, s[28:31], s65 offen offset:0 sc1 lds\n"
"v_mfma_f32_16x16x32_bf16 v[62:65], v[214:217], a[96:99], v[62:65]\n"
"ds_read_b128 v[206:209], v7 offset:61440\n"
"v_mfma_f32_16x16x32_bf16 v[66:69], v[214:217], a[100:103], v[66:69]\n"
"buffer_load_dwordx4 v3, s[28:31], s65 offen offset:1024 sc1 lds\n"
"v_mfma_f32_16x16x32_bf16 v[70:73], v[218:221], a[96:99], v[70:73]\n"
"ds_read_b128 v[210:213], v7 offset:62464\n"
"v_mfma_f32_16x16x32_bf16 v[74:77], v[218:221], a[100:103], v[74:77]\n"
"v_exp_f32 v170, v170\n"
"v_mfma_f32_16x16x32_bf16 v[78:81], v[222:225], a[96:99], v[78:81]\n"
"ds_read_b128 v[214:217], v7 offset:63488\n"
"v_fma_f32 v177, v247, v161, -v23\n"
"v_mfma_f32_16x16x32_bf16 v[82:85], v[222:225], a[100:103], v[82:85]\n"
"buffer_load_dwordx4 v3, s[28:31], s65 offen offset:2048 sc1 lds\n"
"v_mfma_f32_16x16x32_bf16 v[30:33], v[226:229], a[104:107], v[30:33]\n"
"ds_read_b128 v[218:221], v7 offset:64512\n"
"v_exp_f32 v171, v171\n"
"v_mfma_f32_16x16x32_bf16 v[34:37], v[226:229], a[108:111], v[34:37]\n"
"s_waitcnt lgkmcnt(4)\n"
"ds_read_b128 v[244:247], v231 offset:384\n"
"v_mfma_f32_16x16x32_bf16 v[38:41], v[190:193], a[104:107], v[38:41]\n"
"ds_read_b128 v[222:225], v8 offset:0\n"
"v_mfma_f32_16x16x32_bf16 v[42:45], v[190:193], a[108:111], v[42:45]\n"
"buffer_load_dwordx4 a[216:219], v3, s[28:31], s64 offen offset:2048 sc1\n"
"v_mfma_f32_16x16x32_bf16 v[46:49], v[194:197], a[104:107], v[46:49]\n"
"ds_read_b128 v[226:229], v8 offset:1024\n"
"v_mfma_f32_16x16x32_bf16 v[50:53], v[194:197], a[108:111], v[50:53]\n"
"v_exp_f32 v172, v172\n"
"v_mfma_f32_16x16x32_bf16 v[54:57], v[198:201], a[104:107], v[54:57]\n"
"ds_read_b128 v[190:193], v8 offset:2048\n"
"v_fma_f32 v106, v106, v160, v174\n"
"v_mfma_f32_16x16x32_bf16 v[58:61], v[198:201], a[108:111], v[58:61]\n"
"buffer_load_dwordx4 a[220:223], v3, s[28:31], s64 offen offset:3072 sc1\n"
"v_mfma_f32_16x16x32_bf16 v[62:65], v[202:205], a[104:107], v[62:65]\n"
"ds_read_b128 v[194:197], v8 offset:3072\n"
"v_exp_f32 v173, v173\n"
"v_mfma_f32_16x16x32_bf16 v[66:69], v[202:205], a[108:111], v[66:69]\n"
"s_waitcnt lgkmcnt(5)\n"
"v_fma_f32 v107, v107, v160, v175\n"
"v_mfma_f32_16x16x32_bf16 v[70:73], v[206:209], a[104:107], v[70:73]\n"
"ds_read_b128 v[198:201], v8 offset:4096\n"
"v_add_f32 v170, 1.0, v170\n"
"v_mfma_f32_16x16x32_bf16 v[74:77], v[206:209], a[108:111], v[74:77]\n"
"s_bfe_u32 s64, s82, 262172\n"
"s_mul_i32 s64, s64, 8192\n"
"v_mfma_f32_16x16x32_bf16 v[78:81], v[210:213], a[104:107], v[78:81]\n"
"ds_read_b128 v[202:205], v8 offset:5120\n"
"s_add_u32 s64, s64, s66\n"
"v_mfma_f32_16x16x32_bf16 v[82:85], v[210:213], a[108:111], v[82:85]\n"
"buffer_load_dwordx4 a[224:227], v2, s[28:31], s64 offen offset:0 sc1\n"
"v_mfma_f32_16x16x32_bf16 v[30:33], v[214:217], a[112:115], v[30:33]\n"
"ds_read_b128 v[206:209], v8 offset:6144\n"
"v_fma_f32 v108, v108, v160, v176\n"
"v_mfma_f32_16x16x32_bf16 v[34:37], v[214:217], a[116:119], v[34:37]\n"
"buffer_load_dwordx4 a[228:231], v2, s[28:31], s64 offen offset:1024 sc1\n"
"v_mfma_f32_16x16x32_bf16 v[38:41], v[218:221], a[112:115], v[38:41]\n"
"ds_read_b128 v[210:213], v8 offset:7168\n"
"v_add_f32 v171, 1.0, v171\n"
"v_mfma_f32_16x16x32_bf16 v[42:45], v[218:221], a[116:119], v[42:45]\n"
"s_waitcnt lgkmcnt(4)\n"
"v_fma_f32 v109, v109, v160, v177\n"
"v_mfma_f32_16x16x32_bf16 v[46:49], v[222:225], a[112:115], v[46:49]\n"
"ds_read_b128 v[214:217], v8 offset:8192\n"
"v_add_f32 v172, 1.0, v172\n"
"v_mfma_f32_16x16x32_bf16 v[50:53], v[222:225], a[116:119], v[50:53]\n"
"buffer_load_dwordx4 a[232:235], v2, s[28:31], s64 offen offset:2048 sc1\n"
"v_mfma_f32_16x16x32_bf16 v[54:57], v[226:229], a[112:115], v[54:57]\n"
"ds_read_b128 v[218:221], v8 offset:9216\n"
"v_mul_f32 v174, v106, v106\n"
"v_mfma_f32_16x16x32_bf16 v[58:61], v[226:229], a[116:119], v[58:61]\n"
"v_add_f32 v173, 1.0, v173\n"
"v_mul_f32 v175, v107, v107\n"
"v_mfma_f32_16x16x32_bf16 v[62:65], v[190:193], a[112:115], v[62:65]\n"
"ds_read_b128 v[222:225], v8 offset:10240\n"
"v_mfma_f32_16x16x32_bf16 v[66:69], v[190:193], a[116:119], v[66:69]\n"
"v_rcp_f32 v170, v170\n"
"v_mfma_f32_16x16x32_bf16 v[70:73], v[194:197], a[112:115], v[70:73]\n"
"ds_read_b128 v[226:229], v8 offset:11264\n"
"v_mul_f32 v176, v108, v108\n"
"v_mfma_f32_16x16x32_bf16 v[74:77], v[194:197], a[116:119], v[74:77]\n"
"s_waitcnt lgkmcnt(4)\n"
"v_rcp_f32 v171, v171\n"
"v_mfma_f32_16x16x32_bf16 v[78:81], v[198:201], a[112:115], v[78:81]\n"
"s_waitcnt vmcnt(29)\n"
"s_barrier\n"
"ds_read_b128 v[190:193], v6 offset:0\n"
"v_mfma_f32_16x16x32_bf16 v[82:85], v[198:201], a[116:119], v[82:85]\n"
"buffer_load_dwordx4 a[236:239], v2, s[28:31], s64 offen offset:3072 sc1\n"
"v_mfma_f32_16x16x32_bf16 v[30:33], v[202:205], a[120:123], v[30:33]\n"
"ds_read_b128 v[194:197], v6 offset:1024\n"
"v_mul_f32 v177, v109, v109\n"
"v_mfma_f32_16x16x32_bf16 v[34:37], v[202:205], a[124:127], v[34:37]\n"
"s_bfe_u32 s65, s82, 262172\n"
"s_mul_i32 s65, s65, 28672\n"
"v_mfma_f32_16x16x32_bf16 v[38:41], v[206:209], a[120:123], v[38:41]\n"
"ds_read_b128 v[198:201], v6 offset:2048\n"
"s_add_u32 s65, s65, s67\n"
"v_mfma_f32_16x16x32_bf16 v[42:45], v[206:209], a[124:127], v[42:45]\n"
"s_add_u32 m0, s54, 86016\n"
"s_nop 0\n"
"buffer_load_dwordx4 v2, s[28:31], s65 offen offset:0 sc1 lds\n"
"v_mfma_f32_16x16x32_bf16 v[46:49], v[210:213], a[120:123], v[46:49]\n"
"ds_read_b128 v[202:205], v6 offset:3072\n"
"v_rcp_f32 v172, v172\n"
"v_mfma_f32_16x16x32_bf16 v[50:53], v[210:213], a[124:127], v[50:53]\n"
"s_waitcnt lgkmcnt(4)\n"
"v_fmamk_f32 v174, v174, 0xbdd2d3e8, v178\n"
"v_mfma_f32_16x16x32_bf16 v[54:57], v[214:217], a[120:123], v[54:57]\n"
"ds_read_b128 v[206:209], v6 offset:4096\n"
"v_rcp_f32 v173, v173\n"
"v_mfma_f32_16x16x32_bf16 v[58:61], v[214:217], a[124:127], v[58:61]\n"
"buffer_load_dwordx4 v2, s[28:31], s65 offen offset:1024 sc1 lds\n"
"v_mfma_f32_16x16x32_bf16 v[62:65], v[218:221], a[120:123], v[62:65]\n"
"ds_read_b128 v[210:213], v6 offset:5120\n"
"v_fmamk_f32 v175, v175, 0xbdd2d3e8, v178\n"
"v_mfma_f32_16x16x32_bf16 v[66:69], v[218:221], a[124:127], v[66:69]\n"
"buffer_load_dwordx4 v2, s[28:31], s65 offen offset:2048 sc1 lds\n"
"v_mfma_f32_16x16x32_bf16 v[70:73], v[222:225], a[120:123], v[70:73]\n"
"ds_read_b128 v[214:217], v6 offset:6144\n"
"v_mul_f32 v102, v102, v170\n"
"v_mfma_f32_16x16x32_bf16 v[74:77], v[222:225], a[124:127], v[74:77]\n"
"v_fmamk_f32 v176, v176, 0xbdd2d3e8, v178\n"
"v_mul_f32 v103, v103, v171\n"
"v_mfma_f32_16x16x32_bf16 v[78:81], v[226:229], a[120:123], v[78:81]\n"
"ds_read_b128 v[218:221], v6 offset:7168\n"
"v_fmamk_f32 v177, v177, 0xbdd2d3e8, v178\n"
"v_mfma_f32_16x16x32_bf16 v[82:85], v[226:229], a[124:127], v[82:85]\n"
"s_waitcnt lgkmcnt(4)\n"
"v_mfma_f32_16x16x32_bf16 v[30:33], v[190:193], a[128:131], v[30:33]\n"
"ds_read_b128 v[222:225], v6 offset:8192\n"
"v_mul_f32 v104, v104, v172\n"
"v_mfma_f32_16x16x32_bf16 v[34:37], v[190:193], a[132:135], v[34:37]\n"
"buffer_load_dwordx4 v2, s[28:31], s65 offen offset:3072 sc1 lds\n"
"v_mfma_f32_16x16x32_bf16 v[38:41], v[194:197], a[128:131], v[38:41]\n"
".Lmk_pg_15:\n"
"s_cbranch_execz .Lmk_ret_3\n"
"ds_read_b128 v[226:229], v6 offset:9216\n"
"s_add_u32 m0, s54, 90112\n"
"v_mfma_f32_16x16x32_bf16 v[42:45], v[194:197], a[132:135], v[42:45]\n"
"v_mul_f32 v174, v174, v106\n"
"v_mul_f32 v105, v105, v173\n"
"v_mfma_f32_16x16x32_bf16 v[46:49], v[198:201], a[128:131], v[46:49]\n"
"ds_read_b128 v[190:193], v6 offset:10240\n"
"v_mfma_f32_16x16x32_bf16 v[50:53], v[198:201], a[132:135], v[50:53]\n"
"buffer_load_dwordx4 v3, s[28:31], s65 offen offset:0 sc1 lds\n"
"v_mfma_f32_16x16x32_bf16 v[54:57], v[202:205], a[128:131], v[54:57]\n"
"ds_read_b128 v[194:197], v6 offset:11264\n"
"v_mul_f32 v175, v175, v107\n"
"v_mfma_f32_16x16x32_bf16 v[58:61], v[202:205], a[132:135], v[58:61]\n"
"s_waitcnt lgkmcnt(4)\n"
"v_fma_f32 v170, v248, v159, -v22\n"
"v_mfma_f32_16x16x32_bf16 v[62:65], v[206:209], a[128:131], v[62:65]\n"
"ds_read_b128 v[198:201], v6 offset:12288\n"
"v_mul_f32 v176, v176, v108\n"
"v_mfma_f32_16x16x32_bf16 v[66:69], v[206:209], a[132:135], v[66:69]\n"
"buffer_load_dwordx4 v3, s[28:31], s65 offen offset:1024 sc1 lds\n"
"v_mfma_f32_16x16x32_bf16 v[70:73], v[210:213], a[128:131], v[70:73]\n"
"ds_read_b128 v[202:205], v6 offset:13312\n"
"v_fma_f32 v171, v249, v159, -v22\n"
"v_mfma_f32_16x16x32_bf16 v[74:77], v[210:213], a[132:135], v[74:77]\n"
"buffer_load_dwordx4 v3, s[28:31], s65 offen offset:2048 sc1 lds\n"
"v_mfma_f32_16x16x32_bf16 v[78:81], v[214:217], a[128:131], v[78:81]\n"
"ds_read_b128 v[206:209], v6 offset:14336\n"
"v_mul_f32 v177, v177, v109\n"
"v_mfma_f32_16x16x32_bf16 v[82:85], v[214:217], a[132:135], v[82:85]\n"
"v_mfma_f32_16x16x32_bf16 v[30:33], v[218:221], a[136:139], v[30:33]\n"
"ds_read_b128 v[210:213], v6 offset:15360\n"
"v_fma_f32 v172, v250, v159, -v22\n"
"v_mfma_f32_16x16x32_bf16 v[34:37], v[218:221], a[140:143], v[34:37]\n"
"s_waitcnt lgkmcnt(4)\n"
"v_exp_f32 v174, v174\n"
"v_mfma_f32_16x16x32_bf16 v[38:41], v[222:225], a[136:139], v[38:41]\n"
"ds_read_b128 v[214:217], v6 offset:16384\n"
"v_mfma_f32_16x16x32_bf16 v[42:45], v[222:225], a[140:143], v[42:45]\n"
"buffer_load_dwordx4 a[240:243], v3, s[28:31], s64 offen offset:0 sc1\n"
"v_mfma_f32_16x16x32_bf16 v[46:49], v[226:229], a[136:139], v[46:49]\n"
"ds_read_b128 v[218:221], v6 offset:17408\n"
"v_fma_f32 v173, v251, v159, -v22\n"
"v_mfma_f32_16x16x32_bf16 v[50:53], v[226:229], a[140:143], v[50:53]\n"
"buffer_load_dwordx4 a[244:247], v3, s[28:31], s64 offen offset:1024 sc1\n"
"v_mfma_f32_16x16x32_bf16 v[54:57], v[190:193], a[136:139], v[54:57]\n"
"ds_read_b128 v[222:225], v6 offset:18432\n"
"v_mfma_f32_16x16x32_bf16 v[58:61], v[190:193], a[140:143], v[58:61]\n"
"v_exp_f32 v175, v175\n"
"v_mfma_f32_16x16x32_bf16 v[62:65], v[194:197], a[136:139], v[62:65]\n"
"ds_read_b128 v[226:229], v6 offset:19456\n"
"v_fma_f32 v110, v110, v158, v170\n"
"v_mfma_f32_16x16x32_bf16 v[66:69], v[194:197], a[140:143], v[66:69]\n"
"s_waitcnt lgkmcnt(4)\n"
"v_exp_f32 v176, v176\n"
"v_mfma_f32_16x16x32_bf16 v[70:73], v[198:201], a[136:139], v[70:73]\n"
"ds_read_b128 v[190:193], v6 offset:20480\n"
"v_fma_f32 v111, v111, v158, v171\n"
"v_mfma_f32_16x16x32_bf16 v[74:77], v[198:201], a[140:143], v[74:77]\n"
"buffer_load_dwordx4 a[248:251], v3, s[28:31], s64 offen offset:2048 sc1\n"
"v_mfma_f32_16x16x32_bf16 v[78:81], v[202:205], a[136:139], v[78:81]\n"
"ds_read_b128 v[194:197], v6 offset:21504\n"
"v_mfma_f32_16x16x32_bf16 v[82:85], v[202:205], a[140:143], v[82:85]\n"
"buffer_load_dwordx4 a[252:255], v3, s[28:31], s64 offen offset:3072 sc1\n"
"v_mfma_f32_16x16x32_bf16 v[30:33], v[206:209], a[144:147], v[30:33]\n"
"ds_read_b128 v[198:201], v6 offset:22528\n"
"v_mfma_f32_16x16x32_bf16 v[34:37], v[206:209], a[148:151], v[34:37]\n"
"v_exp_f32 v177, v177\n"
"v_mfma_f32_16x16x32_bf16 v[38:41], v[210:213], a[144:147], v[38:41]\n"
"ds_read_b128 v[202:205], v6 offset:23552\n"
"v_fma_f32 v112, v112, v158, v172\n"
"v_mfma_f32_16x16x32_bf16 v[42:45], v[210:213], a[148:151], v[42:45]\n"
"s_waitcnt lgkmcnt(4)\n"
"v_add_f32 v174, 1.0, v174\n"
"v_mfma_f32_16x16x32_bf16 v[46:49], v[214:217], a[144:147], v[46:49]\n"
"ds_read_b128 v[206:209], v6 offset:24576\n"
"v_fma_f32 v113, v113, v158, v173\n"
"v_mfma_f32_16x16x32_bf16 v[50:53], v[214:217], a[148:151], v[50:53]\n"
"s_bfe_u32 s64, s83, 262144\n"
"s_mul_i32 s64, s64, 8192\n"
"v_mfma_f32_16x16x32_bf16 v[54:57], v[218:221], a[144:147], v[54:57]\n"
"ds_read_b128 v[210:213], v6 offset:25600\n"
"s_add_u32 s64, s64, s66\n"
"buffer_load_dwordx4 a[0:3], v2, s[28:31], s64 offen offset:0 sc1\n"
"v_mfma_f32_16x16x32_bf16 v[58:61], v[218:221], a[148:151], v[58:61]\n"
"buffer_load_dwordx4 a[4:7], v2, s[28:31], s64 offen offset:1024 sc1\n"
"v_mfma_f32_16x16x32_bf16 v[62:65], v[222:225], a[144:147], v[62:65]\n"
"ds_read_b128 v[214:217], v6 offset:26624\n"
"v_add_f32 v175, 1.0, v175\n"
"v_mfma_f32_16x16x32_bf16 v[66:69], v[222:225], a[148:151], v[66:69]\n"
"v_mul_f32 v170, v110, v110\n"
"v_add_f32 v176, 1.0, v176\n"
"v_mfma_f32_16x16x32_bf16 v[70:73], v[226:229], a[144:147], v[70:73]\n"
"ds_read_b128 v[218:221], v6 offset:27648\n"
"v_mul_f32 v171, v111, v111\n"
"v_mfma_f32_16x16x32_bf16 v[74:77], v[226:229], a[148:151], v[74:77]\n"
"s_waitcnt lgkmcnt(4)\n"
"v_mfma_f32_16x16x32_bf16 v[78:81], v[190:193], a[144:147], v[78:81]\n"
"s_waitcnt vmcnt(33)\n"
"s_barrier\n"
"ds_read_b128 v[222:225], v6 offset:28672\n"
"v_mfma_f32_16x16x32_bf16 v[82:85], v[190:193], a[148:151], v[82:85]\n"
"buffer_load_dwordx4 a[8:11], v2, s[28:31], s64 offen offset:2048 sc1\n"
"v_mfma_f32_16x16x32_bf16 v[30:33], v[194:197], a[152:155], v[30:33]\n"
"ds_read_b128 v[226:229], v6 offset:29696\n"
"v_add_f32 v177, 1.0, v177\n"
"v_mfma_f32_16x16x32_bf16 v[34:37], v[194:197], a[156:159], v[34:37]\n"
"s_bfe_u32 s65, s83, 262144\n"
"s_mul_i32 s65, s65, 28672\n"
"v_mfma_f32_16x16x32_bf16 v[38:41], v[198:201], a[152:155], v[38:41]\n"
"ds_read_b128 v[190:193], v6 offset:30720\n"
"s_add_u32 s65, s65, s67\n"
"v_mfma_f32_16x16x32_bf16 v[42:45], v[198:201], a[156:159], v[42:45]\n"
"s_add_u32 m0, s54, 114688\n"
"v_mul_f32 v172, v112, v112\n"
"v_mfma_f32_16x16x32_bf16 v[46:49], v[202:205], a[152:155], v[46:49]\n"
"ds_read_b128 v[194:197], v6 offset:31744\n"
"buffer_load_dwordx4 v2, s[28:31], s65 offen offset:0 sc1 lds\n"
"v_mfma_f32_16x16x32_bf16 v[50:53], v[202:205], a[156:159], v[50:53]\n"
"s_waitcnt lgkmcnt(4)\n"
"v_rcp_f32 v174, v174\n"
"v_mfma_f32_16x16x32_bf16 v[54:57], v[206:209], a[152:155], v[54:57]\n"
"ds_read_b128 v[198:201], v6 offset:32768\n"
"v_mul_f32 v173, v113, v113\n"
"v_mfma_f32_16x16x32_bf16 v[58:61], v[206:209], a[156:159], v[58:61]\n"
"buffer_load_dwordx4 v2, s[28:31], s65 offen offset:1024 sc1 lds\n"
"v_mfma_f32_16x16x32_bf16 v[62:65], v[210:213], a[152:155], v[62:65]\n"
"ds_read_b128 v[202:205], v6 offset:33792\n"
"v_mfma_f32_16x16x32_bf16 v[66:69], v[210:213], a[156:159], v[66:69]\n"
"buffer_load_dwordx4 v2, s[28:31], s65 offen offset:2048 sc1 lds\n"
"v_mfma_f32_16x16x32_bf16 v[70:73], v[214:217], a[152:155], v[70:73]\n"
"ds_read_b128 v[206:209], v6 offset:34816\n"
"v_mfma_f32_16x16x32_bf16 v[74:77], v[214:217], a[156:159], v[74:77]\n"
"v_rcp_f32 v175, v175\n"
"v_mfma_f32_16x16x32_bf16 v[78:81], v[218:221], a[152:155], v[78:81]\n"
"ds_read_b128 v[210:213], v6 offset:35840\n"
"v_fmamk_f32 v170, v170, 0xbdd2d3e8, v178\n"
"v_mfma_f32_16x16x32_bf16 v[82:85], v[218:221], a[156:159], v[82:85]\n"
"s_waitcnt lgkmcnt(4)\n"
"v_rcp_f32 v176, v176\n"
"v_mfma_f32_16x16x32_bf16 v[30:33], v[222:225], a[160:163], v[30:33]\n"
"ds_read_b128 v[214:217], v6 offset:36864\n"
"v_fmamk_f32 v171, v171, 0xbdd2d3e8, v178\n"
"v_mfma_f32_16x16x32_bf16 v[34:37], v[222:225], a[164:167], v[34:37]\n"
"buffer_load_dwordx4 v2, s[28:31], s65 offen offset:3072 sc1 lds\n"
"v_mfma_f32_16x16x32_bf16 v[38:41], v[226:229], a[160:163], v[38:41]\n"
"ds_read_b128 v[218:221], v6 offset:37888\n"
"v_rcp_f32 v177, v177\n"
"v_mfma_f32_16x16x32_bf16 v[42:45], v[226:229], a[164:167], v[42:45]\n"
"s_add_u32 m0, s54, 118784\n"
"v_fmamk_f32 v172, v172, 0xbdd2d3e8, v178\n"
"v_mfma_f32_16x16x32_bf16 v[46:49], v[190:193], a[160:163], v[46:49]\n"
"ds_read_b128 v[222:225], v6 offset:38912\n"
"v_mul_f32 v106, v106, v174\n"
"v_mfma_f32_16x16x32_bf16 v[50:53], v[190:193], a[164:167], v[50:53]\n"
"buffer_load_dwordx4 v3, s[28:31], s65 offen offset:0 sc1 lds\n"
"v_mfma_f32_16x16x32_bf16 v[54:57], v[194:197], a[160:163], v[54:57]\n"
"ds_read_b128 v[226:229], v6 offset:39936\n"
"v_fmamk_f32 v173, v173, 0xbdd2d3e8, v178\n"
"v_mfma_f32_16x16x32_bf16 v[58:61], v[194:197], a[164:167], v[58:61]\n"
"s_waitcnt lgkmcnt(4)\n"
"v_mul_f32 v107, v107, v175\n"
"v_mfma_f32_16x16x32_bf16 v[62:65], v[198:201], a[160:163], v[62:65]\n"
"ds_read_b128 v[190:193], v6 offset:40960\n"
"v_mul_f32 v170, v170, v110\n"
"v_mfma_f32_16x16x32_bf16 v[66:69], v[198:201], a[164:167], v[66:69]\n"
"buffer_load_dwordx4 v3, s[28:31], s65 offen offset:1024 sc1 lds\n"
"v_mfma_f32_16x16x32_bf16 v[70:73], v[202:205], a[160:163], v[70:73]\n"
"ds_read_b128 v[194:197], v6 offset:41984\n"
"v_mul_f32 v108, v108, v176\n"
"v_mfma_f32_16x16x32_bf16 v[74:77], v[202:205], a[164:167], v[74:77]\n"
"v_mul_f32 v171, v171, v111\n"
"v_mul_f32 v109, v109, v177\n"
"v_mfma_f32_16x16x32_bf16 v[78:81], v[206:209], a[160:163], v[78:81]\n"
"ds_read_b128 v[198:201], v6 offset:43008\n"
"v_mfma_f32_16x16x32_bf16 v[82:85], v[206:209], a[164:167], v[82:85]\n"
"buffer_load_dwordx4 v3, s[28:31], s65 offen offset:2048 sc1 lds\n"
"v_mfma_f32_16x16x32_bf16 v[30:33], v[210:213], a[168:171], v[30:33]\n"
"ds_read_b128 v[202:205], v6 offset:44032\n"
"v_mul_f32 v172, v172, v112\n"
"v_mfma_f32_16x16x32_bf16 v[34:37], v[210:213], a[172:175], v[34:37]\n"
"s_waitcnt lgkmcnt(4)\n"
"v_fma_f32 v174, v248, v161, -v23\n"
"v_mfma_f32_16x16x32_bf16 v[38:41], v[214:217], a[168:171], v[38:41]\n"
"ds_read_b128 v[206:209], v6 offset:45056\n"
"v_mul_f32 v173, v173, v113\n"
"v_mfma_f32_16x16x32_bf16 v[42:45], v[214:217], a[172:175], v[42:45]\n"
"buffer_load_dwordx4 a[12:15], v2, s[28:31], s64 offen offset:3072 sc1\n"
"v_mfma_f32_16x16x32_bf16 v[46:49], v[218:221], a[168:171], v[46:49]\n"
"ds_read_b128 v[210:213], v6 offset:46080\n"
"v_fma_f32 v175, v249, v161, -v23\n"
"v_mfma_f32_16x16x32_bf16 v[50:53], v[218:221], a[172:175], v[50:53]\n"
"v_mfma_f32_16x16x32_bf16 v[54:57], v[222:225], a[168:171], v[54:57]\n"
"ds_read_b128 v[214:217], v6 offset:47104\n"
"s_lshr_b32 s57, s50, 1\n"
"v_mfma_f32_16x16x32_bf16 v[58:61], v[222:225], a[172:175], v[58:61]\n"
"buffer_load_dwordx4 a[16:19], v3, s[28:31], s64 offen offset:0 sc1\n"
"v_mfma_f32_16x16x32_bf16 v[62:65], v[226:229], a[168:171], v[62:65]\n"
"ds_read_b128 v[218:221], v6 offset:48128\n"
"v_exp_f32 v170, v170\n"
"v_mfma_f32_16x16x32_bf16 v[66:69], v[226:229], a[172:175], v[66:69]\n"
"s_waitcnt lgkmcnt(4)\n"
"s_lshr_b32 s58, s49, 1\n"
"v_mfma_f32_16x16x32_bf16 v[70:73], v[190:193], a[168:171], v[70:73]\n"
"ds_read_b128 v[222:225], v6 offset:49152\n"
"v_fma_f32 v176, v250, v161, -v23\n"
"v_mfma_f32_16x16x32_bf16 v[74:77], v[190:193], a[172:175], v[74:77]\n"
"buffer_load_dwordx4 a[20:23], v3, s[28:31], s64 offen offset:1024 sc1\n"
"v_mfma_f32_16x16x32_bf16 v[78:81], v[194:197], a[168:171], v[78:81]\n"
"ds_read_b128 v[226:229], v6 offset:50176\n"
"s_and_b32 s59, s49, 1\n"
"v_mfma_f32_16x16x32_bf16 v[82:85], v[194:197], a[172:175], v[82:85]\n"
"v_exp_f32 v171, v171\n"
"v_mfma_f32_16x16x32_bf16 v[30:33], v[198:201], a[176:179], v[30:33]\n"
"ds_read_b128 v[190:193], v6 offset:51200\n"
"v_fma_f32 v177, v251, v161, -v23\n"
"v_mfma_f32_16x16x32_bf16 v[34:37], v[198:201], a[180:183], v[34:37]\n"
"buffer_load_dwordx4 a[24:27], v3, s[28:31], s64 offen offset:2048 sc1\n"
"v_mfma_f32_16x16x32_bf16 v[38:41], v[202:205], a[176:179], v[38:41]\n"
"ds_read_b128 v[194:197], v6 offset:52224\n"
"s_lshl_b32 s59, s59, 1\n"
"v_mfma_f32_16x16x32_bf16 v[42:45], v[202:205], a[180:183], v[42:45]\n"
"s_waitcnt lgkmcnt(4)\n"
"v_exp_f32 v172, v172\n"
"v_mfma_f32_16x16x32_bf16 v[46:49], v[206:209], a[176:179], v[46:49]\n"
"ds_read_b128 v[198:201], v6 offset:53248\n"
"s_and_b32 s60, s41, 1\n"
"v_mfma_f32_16x16x32_bf16 v[50:53], v[206:209], a[180:183], v[50:53]\n"
"buffer_load_dwordx4 a[28:31], v3, s[28:31], s64 offen offset:3072 sc1\n"
"v_mfma_f32_16x16x32_bf16 v[54:57], v[210:213], a[176:179], v[54:57]\n"
"ds_read_b128 v[202:205], v6 offset:54272\n"
"v_fma_f32 v114, v114, v160, v174\n"
"v_mfma_f32_16x16x32_bf16 v[58:61], v[210:213], a[180:183], v[58:61]\n"
"s_add_u32 s59, s59, s60\n"
"v_exp_f32 v173, v173\n"
"v_mfma_f32_16x16x32_bf16 v[62:65], v[214:217], a[176:179], v[62:65]\n"
"ds_read_b128 v[206:209], v6 offset:55296\n"
"v_fma_f32 v115, v115, v160, v175\n"
"v_add_f32 v170, 1.0, v170\n"
"v_mfma_f32_16x16x32_bf16 v[66:69], v[214:217], a[180:183], v[66:69]\n"
"s_bfe_u32 s64, s83, 262148\n"
"s_mul_i32 s64, s64, 8192\n"
"v_fma_f32 v116, v116, v160, v176\n"
"v_mfma_f32_16x16x32_bf16 v[70:73], v[218:221], a[176:179], v[70:73]\n"
"ds_read_b128 v[210:213], v6 offset:56320\n"
"s_add_u32 s64, s64, s66\n"
"v_add_f32 v171, 1.0, v171\n"
"v_mfma_f32_16x16x32_bf16 v[74:77], v[218:221], a[180:183], v[74:77]\n"
"s_waitcnt lgkmcnt(4)\n"
"buffer_load_dwordx4 a[32:35], v2, s[28:31], s64 offen offset:0 sc1\n"
"v_mfma_f32_16x16x32_bf16 v[78:81], v[222:225], a[176:179], v[78:81]\n"
"s_waitcnt vmcnt(33)\n"
"s_barrier\n"
"ds_read_b128 v[214:217], v6 offset:57344\n"
"v_mfma_f32_16x16x32_bf16 v[82:85], v[222:225], a[180:183], v[82:85]\n"
"buffer_load_dwordx4 a[36:39], v2, s[28:31], s64 offen offset:1024 sc1\n"
"v_mfma_f32_16x16x32_bf16 v[30:33], v[226:229], a[184:187], v[30:33]\n"
"ds_read_b128 v[218:221], v6 offset:58368\n"
"s_mul_i32 s88, s57, 229376\n"
"v_mfma_f32_16x16x32_bf16 v[34:37], v[226:229], a[188:191], v[34:37]\n"
"s_mul_i32 s56, s58, 28672\n"
"v_fma_f32 v117, v117, v160, v177\n"
"v_add_f32 v172, 1.0, v172\n"
"v_mfma_f32_16x16x32_bf16 v[38:41], v[190:193], a[184:187], v[38:41]\n"
"ds_read_b128 v[222:225], v6 offset:59392\n"
"s_add_u32 s88, s88, s56\n"
"v_mul_f32 v174, v114, v114\n"
"v_mfma_f32_16x16x32_bf16 v[42:45], v[190:193], a[188:191], v[42:45]\n"
"s_bfe_u32 s65, s83, 262148\n"
"s_mul_i32 s65, s65, 28672\n"
"v_add_f32 v173, 1.0, v173\n"
"v_mfma_f32_16x16x32_bf16 v[46:49], v[194:197], a[184:187], v[46:49]\n"
"ds_read_b128 v[226:229], v6 offset:60416\n"
"s_add_u32 s65, s65, s67\n"
"v_mfma_f32_16x16x32_bf16 v[50:53], v[194:197], a[188:191], v[50:53]\n"
"s_waitcnt lgkmcnt(4)\n"
"s_add_u32 m0, s54, 0\n"
"v_mfma_f32_16x16x32_bf16 v[54:57], v[198:201], a[184:187], v[54:57]\n"
"ds_read_b128 v[190:193], v6 offset:61440\n"
"buffer_load_dwordx4 v2, s[28:31], s65 offen offset:0 sc1 lds\n"
"v_mfma_f32_16x16x32_bf16 v[58:61], v[198:201], a[188:191], v[58:61]\n"
"buffer_load_dwordx4 v2, s[28:31], s65 offen offset:1024 sc1 lds\n"
"v_mfma_f32_16x16x32_bf16 v[62:65], v[202:205], a[184:187], v[62:65]\n"
"ds_read_b128 v[194:197], v6 offset:62464\n"
"s_mul_i32 s56, s59, 7168\n"
"v_mfma_f32_16x16x32_bf16 v[66:69], v[202:205], a[188:191], v[66:69]\n"
"s_add_u32 s88, s88, s56\n"
"v_mul_f32 v175, v115, v115\n"
"v_mfma_f32_16x16x32_bf16 v[70:73], v[206:209], a[184:187], v[70:73]\n"
"ds_read_b128 v[198:201], v6 offset:63488\n"
"v_rcp_f32 v170, v170\n"
"v_mfma_f32_16x16x32_bf16 v[74:77], v[206:209], a[188:191], v[74:77]\n"
"buffer_load_dwordx4 v2, s[28:31], s65 offen offset:2048 sc1 lds\n"
"v_mfma_f32_16x16x32_bf16 v[78:81], v[210:213], a[184:187], v[78:81]\n"
"ds_read_b128 v[202:205], v6 offset:64512\n"
"s_add_u32 s88, s88, 45088768\n"
"v_mfma_f32_16x16x32_bf16 v[82:85], v[210:213], a[188:191], v[82:85]\n"
"s_waitcnt lgkmcnt(4)\n"
"v_mul_f32 v176, v116, v116\n"
"v_mfma_f32_16x16x32_bf16 v[30:33], v[214:217], a[192:195], v[30:33]\n"
"ds_read_b128 v[206:209], v7 offset:0\n"
"s_mul_i32 s70, s57, 512\n"
"v_mfma_f32_16x16x32_bf16 v[34:37], v[214:217], a[196:199], v[34:37]\n"
"buffer_load_dwordx4 v2, s[28:31], s65 offen offset:3072 sc1 lds\n"
"v_mfma_f32_16x16x32_bf16 v[38:41], v[218:221], a[192:195], v[38:41]\n"
"ds_read_b128 v[210:213], v7 offset:1024\n"
"v_rcp_f32 v171, v171\n"
"v_mfma_f32_16x16x32_bf16 v[42:45], v[218:221], a[196:199], v[42:45]\n"
"s_mul_i32 s56, s59, 128\n"
"v_mul_f32 v177, v117, v117\n"
"v_mfma_f32_16x16x32_bf16 v[46:49], v[222:225], a[192:195], v[46:49]\n"
"ds_read_b128 v[214:217], v7 offset:2048\n"
"v_rcp_f32 v172, v172\n"
"v_mfma_f32_16x16x32_bf16 v[50:53], v[222:225], a[196:199], v[50:53]\n"
"s_add_u32 m0, s54, 4096\n"
"v_fmamk_f32 v174, v174, 0xbdd2d3e8, v178\n"
"v_mfma_f32_16x16x32_bf16 v[54:57], v[226:229], a[192:195], v[54:57]\n"
"ds_read_b128 v[218:221], v7 offset:3072\n"
"s_add_u32 s70, s70, s56\n"
"v_mfma_f32_16x16x32_bf16 v[58:61], v[226:229], a[196:199], v[58:61]\n"
"s_waitcnt lgkmcnt(4)\n"
"buffer_load_dwordx4 v3, s[28:31], s65 offen offset:0 sc1 lds\n"
"v_mfma_f32_16x16x32_bf16 v[62:65], v[190:193], a[192:195], v[62:65]\n"
"ds_read_b128 v[222:225], v7 offset:4096\n"
"v_rcp_f32 v173, v173\n"
"v_mfma_f32_16x16x32_bf16 v[66:69], v[190:193], a[196:199], v[66:69]\n"
"buffer_load_dwordx4 v3, s[28:31], s65 offen offset:1024 sc1 lds\n"
"v_mfma_f32_16x16x32_bf16 v[70:73], v[194:197], a[192:195], v[70:73]\n"
"ds_read_b128 v[226:229], v7 offset:5120\n"
"s_add_u32 s70, s70, 49152\n"
"v_mfma_f32_16x16x32_bf16 v[74:77], v[194:197], a[196:199], v[74:77]\n"
"v_fmamk_f32 v175, v175, 0xbdd2d3e8, v178\n"
"v_mul_f32 v110, v110, v170\n"
"v_mfma_f32_16x16x32_bf16 v[78:81], v[198:201], a[192:195], v[78:81]\n"
"ds_read_b128 v[190:193], v7 offset:6144\n"
"s_mul_i32 s71, s51, 512\n"
"v_mfma_f32_16x16x32_bf16 v[82:85], v[198:201], a[196:199], v[82:85]\n"
"buffer_load_dwordx4 v3, s[28:31], s65 offen offset:2048 sc1 lds\n"
"v_mfma_f32_16x16x32_bf16 v[30:33], v[202:205], a[200:203], v[30:33]\n"
"ds_read_b128 v[194:197], v7 offset:7168\n"
"v_fmamk_f32 v176, v176, 0xbdd2d3e8, v178\n"
"v_mfma_f32_16x16x32_bf16 v[34:37], v[202:205], a[204:207], v[34:37]\n"
"s_waitcnt lgkmcnt(4)\n"
"s_mul_i32 s56, s41, 128\n"
"v_mfma_f32_16x16x32_bf16 v[38:41], v[206:209], a[200:203], v[38:41]\n"
"ds_read_b128 v[198:201], v7 offset:8192\n"
"v_mul_f32 v111, v111, v171\n"
"v_mfma_f32_16x16x32_bf16 v[42:45], v[206:209], a[204:207], v[42:45]\n"
"buffer_load_dwordx4 a[40:43], v2, s[28:31], s64 offen offset:2048 sc1\n"
"v_mfma_f32_16x16x32_bf16 v[46:49], v[210:213], a[200:203], v[46:49]\n"
"ds_read_b128 v[202:205], v7 offset:9216\n"
"s_add_u32 s71, s71, s56\n"
"v_mfma_f32_16x16x32_bf16 v[50:53], v[210:213], a[204:207], v[50:53]\n"
"v_fmamk_f32 v177, v177, 0xbdd2d3e8, v178\n"
"v_mul_f32 v112, v112, v172\n"
"v_mfma_f32_16x16x32_bf16 v[54:57], v[214:217], a[200:203], v[54:57]\n"
"ds_read_b128 v[206:209], v7 offset:10240\n"
"v_mul_f32 v174, v174, v114\n"
"v_mfma_f32_16x16x32_bf16 v[58:61], v[214:217], a[204:207], v[58:61]\n"
"buffer_load_dwordx4 a[44:47], v2, s[28:31], s64 offen offset:3072 sc1\n"
"v_mfma_f32_16x16x32_bf16 v[62:65], v[218:221], a[200:203], v[62:65]\n"
"ds_read_b128 v[210:213], v7 offset:11264\n"
"s_add_u32 s71, s71, 49152\n"
"v_mfma_f32_16x16x32_bf16 v[66:69], v[218:221], a[204:207], v[66:69]\n"
"s_waitcnt lgkmcnt(4)\n"
"v_mul_f32 v113, v113, v173\n"
"v_mfma_f32_16x16x32_bf16 v[70:73], v[222:225], a[200:203], v[70:73]\n"
"ds_read_b128 v[214:217], v7 offset:12288\n"
"s_mul_i32 s69, s51, 229376\n"
"v_mfma_f32_16x16x32_bf16 v[74:77], v[222:225], a[204:207], v[74:77]\n"
"buffer_load_dwordx4 a[48:51], v3, s[28:31], s64 offen offset:0 sc1\n"
"v_mfma_f32_16x16x32_bf16 v[78:81], v[226:229], a[200:203], v[78:81]\n"
"ds_read_b128 v[218:221], v7 offset:13312\n"
"v_mul_f32 v175, v175, v115\n"
"v_mfma_f32_16x16x32_bf16 v[82:85], v[226:229], a[204:207], v[82:85]\n"
"s_mul_i32 s56, s41, 7168\n"
"v_fma_f32 v170, v236, v159, -v22\n"
"v_mfma_f32_16x16x32_bf16 v[30:33], v[190:193], a[208:211], v[30:33]\n"
"ds_read_b128 v[222:225], v7 offset:14336\n"
"v_mul_f32 v176, v176, v116\n"
"v_mfma_f32_16x16x32_bf16 v[34:37], v[190:193], a[212:215], v[34:37]\n"
"buffer_load_dwordx4 a[52:55], v3, s[28:31], s64 offen offset:1024 sc1\n"
"v_mfma_f32_16x16x32_bf16 v[38:41], v[194:197], a[208:211], v[38:41]\n"
"ds_read_b128 v[226:229], v7 offset:15360\n"
"s_add_u32 s69, s69, s56\n"
"v_mfma_f32_16x16x32_bf16 v[42:45], v[194:197], a[212:215], v[42:45]\n"
"s_waitcnt lgkmcnt(4)\n"
"v_fma_f32 v171, v237, v159, -v22\n"
"v_mfma_f32_16x16x32_bf16 v[46:49], v[198:201], a[208:211], v[46:49]\n"
"ds_read_b128 v[190:193], v7 offset:16384\n"
"v_mul_f32 v177, v177, v117\n"
"v_mfma_f32_16x16x32_bf16 v[50:53], v[198:201], a[212:215], v[50:53]\n"
"s_add_u32 s69, s69, 45088768\n"
"v_fma_f32 v172, v238, v159, -v22\n"
"v_mfma_f32_16x16x32_bf16 v[54:57], v[202:205], a[208:211], v[54:57]\n"
"ds_read_b128 v[194:197], v7 offset:17408\n"
"v_mfma_f32_16x16x32_bf16 v[58:61], v[202:205], a[212:215], v[58:61]\n"
"buffer_load_dwordx4 a[56:59], v3, s[28:31], s64 offen offset:2048 sc1\n"
"v_mfma_f32_16x16x32_bf16 v[62:65], v[206:209], a[208:211], v[62:65]\n"
"ds_read_b128 v[198:201], v7 offset:18432\n"
"s_mov_b32 s32, s22\n"
"v_mfma_f32_16x16x32_bf16 v[66:69], v[206:209], a[212:215], v[66:69]\n"
"v_exp_f32 v174, v174\n"
"v_mfma_f32_16x16x32_bf16 v[70:73], v[210:213], a[208:211], v[70:73]\n"
"ds_read_b128 v[202:205], v7 offset:19456\n"
"v_fma_f32 v173, v239, v159, -v22\n"
"v_exp_f32 v175, v175\n"
"v_mfma_f32_16x16x32_bf16 v[74:77], v[210:213], a[212:215], v[74:77]\n"
"s_waitcnt lgkmcnt(4)\n"
"s_and_b32 s33, s23, 0xffff\n"
"v_fma_f32 v118, v118, v158, v170\n"
"v_mfma_f32_16x16x32_bf16 v[78:81], v[214:217], a[208:211], v[78:81]\n"
"s_waitcnt vmcnt(33)\n"
"s_barrier\n"
"ds_read_b128 v[206:209], v7 offset:20480\n"
"v_mfma_f32_16x16x32_bf16 v[82:85], v[214:217], a[212:215], v[82:85]\n"
"buffer_load_dwordx4 a[60:63], v3, s[28:31], s64 offen offset:3072 sc1\n"
"v_exp_f32 v176, v176\n"
"v_mfma_f32_16x16x32_bf16 v[30:33], v[218:221], a[216:219], v[30:33]\n"
"ds_read_b128 v[210:213], v7 offset:21504\n"
"s_mov_b32 s34, 4194304\n"
"v_fma_f32 v119, v119, v158, v171\n"
"v_mfma_f32_16x16x32_bf16 v[34:37], v[218:221], a[220:223], v[34:37]\n"
"s_bfe_u32 s65, s83, 262152\n"
"s_mul_i32 s65, s65, 28672\n"
"v_mfma_f32_16x16x32_bf16 v[38:41], v[222:225], a[216:219], v[38:41]\n"
"ds_read_b128 v[214:217], v7 offset:22528\n"
"s_add_u32 s65, s65, s67\n"
"v_mfma_f32_16x16x32_bf16 v[42:45], v[222:225], a[220:223], v[42:45]\n"
"s_add_u32 m0, s54, 28672\n"
"s_nop 0\n"
"buffer_load_dwordx4 v2, s[28:31], s65 offen offset:0 sc1 lds\n"
"v_mfma_f32_16x16x32_bf16 v[46:49], v[226:229], a[216:219], v[46:49]\n"
"ds_read_b128 v[218:221], v7 offset:23552\n"
"s_mul_i32 s89, s51, 131072\n"
"v_mfma_f32_16x16x32_bf16 v[50:53], v[226:229], a[220:223], v[50:53]\n"
"s_waitcnt lgkmcnt(4)\n"
"v_exp_f32 v177, v177\n"
"v_mfma_f32_16x16x32_bf16 v[54:57], v[190:193], a[216:219], v[54:57]\n"
"ds_read_b128 v[222:225], v7 offset:24576\n"
"s_mul_i32 s56, s52, 256\n"
"v_mfma_f32_16x16x32_bf16 v[58:61], v[190:193], a[220:223], v[58:61]\n"
"buffer_load_dwordx4 v2, s[28:31], s65 offen offset:1024 sc1 lds\n"
"v_mfma_f32_16x16x32_bf16 v[62:65], v[194:197], a[216:219], v[62:65]\n"
"ds_read_b128 v[226:229], v7 offset:25600\n"
"v_fma_f32 v120, v120, v158, v172\n"
"v_mfma_f32_16x16x32_bf16 v[66:69], v[194:197], a[220:223], v[66:69]\n"
"buffer_load_dwordx4 v2, s[28:31], s65 offen offset:2048 sc1 lds\n"
"v_mfma_f32_16x16x32_bf16 v[70:73], v[198:201], a[216:219], v[70:73]\n"
"ds_read_b128 v[190:193], v7 offset:26624\n"
"s_add_u32 s89, s89, s56\n"
"v_mfma_f32_16x16x32_bf16 v[74:77], v[198:201], a[220:223], v[74:77]\n"
"v_add_f32 v174, 1.0, v174\n"
"v_fma_f32 v121, v121, v158, v173\n"
"v_mfma_f32_16x16x32_bf16 v[78:81], v[202:205], a[216:219], v[78:81]\n"
"ds_read_b128 v[194:197], v7 offset:27648\n"
"s_mul_i32 s56, s41, 64\n"
"v_mfma_f32_16x16x32_bf16 v[82:85], v[202:205], a[220:223], v[82:85]\n"
"s_waitcnt lgkmcnt(4)\n"
"v_add_f32 v175, 1.0, v175\n"
"v_mfma_f32_16x16x32_bf16 v[30:33], v[206:209], a[224:227], v[30:33]\n"
"ds_read_b128 v[198:201], v7 offset:28672\n"
"v_mul_f32 v170, v118, v118\n"
"v_mfma_f32_16x16x32_bf16 v[34:37], v[206:209], a[228:231], v[34:37]\n"
"buffer_load_dwordx4 v2, s[28:31], s65 offen offset:3072 sc1 lds\n"
"v_mfma_f32_16x16x32_bf16 v[38:41], v[210:213], a[224:227], v[38:41]\n"
"ds_read_b128 v[202:205], v7 offset:29696\n"
"s_add_u32 m0, s54, 32768\n"
"v_mfma_f32_16x16x32_bf16 v[42:45], v[210:213], a[228:231], v[42:45]\n"
"s_add_u32 s89, s89, s56\n"
"v_add_f32 v176, 1.0, v176\n"
"v_mfma_f32_16x16x32_bf16 v[46:49], v[214:217], a[224:227], v[46:49]\n"
"ds_read_b128 v[206:209], v7 offset:30720\n"
"s_add_u32 s89, s89, 0\n"
"v_mfma_f32_16x16x32_bf16 v[50:53], v[214:217], a[228:231], v[50:53]\n"
"buffer_load_dwordx4 v3, s[28:31], s65 offen offset:0 sc1 lds\n"
"v_mfma_f32_16x16x32_bf16 v[54:57], v[218:221], a[224:227], v[54:57]\n"
"ds_read_b128 v[210:213], v7 offset:31744\n"
"v_mul_f32 v171, v119, v119\n"
"v_mfma_f32_16x16x32_bf16 v[58:61], v[218:221], a[228:231], v[58:61]\n"
"s_waitcnt lgkmcnt(4)\n"
"v_add_f32 v177, 1.0, v177\n"
"v_mfma_f32_16x16x32_bf16 v[62:65], v[222:225], a[224:227], v[62:65]\n"
"ds_read_b128 v[214:217], v7 offset:32768\n"
"v_mul_f32 v172, v120, v120\n"
"v_mfma_f32_16x16x32_bf16 v[66:69], v[222:225], a[228:231], v[66:69]\n"
"buffer_load_dwordx4 v3, s[28:31], s65 offen offset:1024 sc1 lds\n"
"v_mfma_f32_16x16x32_bf16 v[70:73], v[226:229], a[224:227], v[70:73]\n"
"ds_read_b128 v[218:221], v7 offset:33792\n"
"v_mfma_f32_16x16x32_bf16 v[74:77], v[226:229], a[228:231], v[74:77]\n"
"buffer_load_dwordx4 v3, s[28:31], s65 offen offset:2048 sc1 lds\n"
"v_mfma_f32_16x16x32_bf16 v[78:81], v[190:193], a[224:227], v[78:81]\n"
"ds_read_b128 v[222:225], v7 offset:34816\n"
"v_mfma_f32_16x16x32_bf16 v[82:85], v[190:193], a[228:231], v[82:85]\n"
"v_rcp_f32 v174, v174\n"
"v_mfma_f32_16x16x32_bf16 v[30:33], v[194:197], a[232:235], v[30:33]\n"
"ds_read_b128 v[226:229], v7 offset:35840\n"
"v_mul_f32 v173, v121, v121\n"
"v_mfma_f32_16x16x32_bf16 v[34:37], v[194:197], a[236:239], v[34:37]\n"
"s_waitcnt lgkmcnt(4)\n"
"v_rcp_f32 v175, v175\n"
"v_mfma_f32_16x16x32_bf16 v[38:41], v[198:201], a[232:235], v[38:41]\n"
"ds_read_b128 v[190:193], v7 offset:36864\n"
"v_fmamk_f32 v170, v170, 0xbdd2d3e8, v178\n"
"v_mfma_f32_16x16x32_bf16 v[42:45], v[198:201], a[236:239], v[42:45]\n"
"s_bfe_u32 s64, s83, 262152\n"
"s_mul_i32 s64, s64, 8192\n"
"v_mfma_f32_16x16x32_bf16 v[46:49], v[202:205], a[232:235], v[46:49]\n"
"ds_read_b128 v[194:197], v7 offset:37888\n"
"s_add_u32 s64, s64, s66\n"
"buffer_load_dwordx4 a[64:67], v2, s[28:31], s64 offen offset:0 sc1\n"
"v_mfma_f32_16x16x32_bf16 v[50:53], v[202:205], a[236:239], v[50:53]\n"
"buffer_load_dwordx4 a[68:71], v2, s[28:31], s64 offen offset:1024 sc1\n"
"v_mfma_f32_16x16x32_bf16 v[54:57], v[206:209], a[232:235], v[54:57]\n"
"ds_read_b128 v[198:201], v7 offset:38912\n"
"v_mfma_f32_16x16x32_bf16 v[58:61], v[206:209], a[236:239], v[58:61]\n"
"v_rcp_f32 v176, v176\n"
"v_mfma_f32_16x16x32_bf16 v[62:65], v[210:213], a[232:235], v[62:65]\n"
"ds_read_b128 v[202:205], v7 offset:39936\n"
"v_fmamk_f32 v171, v171, 0xbdd2d3e8, v178\n"
"v_mfma_f32_16x16x32_bf16 v[66:69], v[210:213], a[236:239], v[66:69]\n"
"s_waitcnt lgkmcnt(4)\n"
"v_rcp_f32 v177, v177\n"
"v_mfma_f32_16x16x32_bf16 v[70:73], v[214:217], a[232:235], v[70:73]\n"
"ds_read_b128 v[206:209], v7 offset:40960\n"
"v_fmamk_f32 v172, v172, 0xbdd2d3e8, v178\n"
"v_mfma_f32_16x16x32_bf16 v[74:77], v[214:217], a[236:239], v[74:77]\n"
"buffer_load_dwordx4 a[72:75], v2, s[28:31], s64 offen offset:2048 sc1\n"
"v_mfma_f32_16x16x32_bf16 v[78:81], v[218:221], a[232:235], v[78:81]\n"
"ds_read_b128 v[210:213], v7 offset:41984\n"
"v_mul_f32 v114, v114, v174\n"
"v_mfma_f32_16x16x32_bf16 v[82:85], v[218:221], a[236:239], v[82:85]\n"
"buffer_load_dwordx4 a[76:79], v2, s[28:31], s64 offen offset:3072 sc1\n"
"s_waitcnt vmcnt(38)\n"
"v_mfma_f32_16x16x32_bf16 v[30:33], v[222:225], a[240:243], v[30:33]\n"
"ds_read_b128 v[214:217], v7 offset:43008\n"
"v_fmamk_f32 v173, v173, 0xbdd2d3e8, v178\n"
"v_mfma_f32_16x16x32_bf16 v[34:37], v[222:225], a[244:247], v[34:37]\n"
"v_mul_f32 v115, v115, v175\n"
"v_mul_f32 v170, v170, v118\n"
"v_mfma_f32_16x16x32_bf16 v[38:41], v[226:229], a[240:243], v[38:41]\n"
"ds_read_b128 v[218:221], v7 offset:44032\n"
"v_mul_f32 v116, v116, v176\n"
"v_mfma_f32_16x16x32_bf16 v[42:45], v[226:229], a[244:247], v[42:45]\n"
"s_waitcnt lgkmcnt(4)\n"
"v_mul_f32 v171, v171, v119\n"
"v_mfma_f32_16x16x32_bf16 v[46:49], v[190:193], a[240:243], v[46:49]\n"
"ds_read_b128 v[222:225], v7 offset:45056\n"
"v_mul_f32 v117, v117, v177\n"
"v_mfma_f32_16x16x32_bf16 v[50:53], v[190:193], a[244:247], v[50:53]\n"
"buffer_load_dwordx4 a[80:83], v3, s[28:31], s64 offen offset:0 sc1\n"
"v_mfma_f32_16x16x32_bf16 v[54:57], v[194:197], a[240:243], v[54:57]\n"
"ds_read_b128 v[226:229], v7 offset:46080\n"
"v_mul_f32 v172, v172, v120\n"
"v_mfma_f32_16x16x32_bf16 v[58:61], v[194:197], a[244:247], v[58:61]\n"
"buffer_load_dwordx4 a[84:87], v3, s[28:31], s64 offen offset:1024 sc1\n"
"v_mfma_f32_16x16x32_bf16 v[62:65], v[198:201], a[240:243], v[62:65]\n"
"ds_read_b128 v[190:193], v7 offset:47104\n"
"v_fma_f32 v174, v236, v161, -v23\n"
"v_mfma_f32_16x16x32_bf16 v[66:69], v[198:201], a[244:247], v[66:69]\n"
"v_mul_f32 v173, v173, v121\n"
"v_fma_f32 v175, v237, v161, -v23\n"
"v_mfma_f32_16x16x32_bf16 v[70:73], v[202:205], a[240:243], v[70:73]\n"
"ds_read_b128 v[194:197], v7 offset:48128\n"
"v_exp_f32 v170, v170\n"
"v_mfma_f32_16x16x32_bf16 v[74:77], v[202:205], a[244:247], v[74:77]\n"
"s_waitcnt lgkmcnt(4)\n"
"v_fma_f32 v176, v238, v161, -v23\n"
"v_mfma_f32_16x16x32_bf16 v[78:81], v[206:209], a[240:243], v[78:81]\n"
"s_waitcnt vmcnt(33)\n"
"s_barrier\n"
"ds_read_b128 v[198:201], v7 offset:49152\n"
"v_mfma_f32_16x16x32_bf16 v[82:85], v[206:209], a[244:247], v[82:85]\n"
"buffer_load_dwordx4 a[88:91], v3, s[28:31], s64 offen offset:2048 sc1\n"
"v_mfma_f32_16x16x32_bf16 v[30:33], v[210:213], a[248:251], v[30:33]\n"
"ds_read_b128 v[202:205], v7 offset:50176\n"
"v_exp_f32 v171, v171\n"
"v_mfma_f32_16x16x32_bf16 v[34:37], v[210:213], a[252:255], v[34:37]\n"
"s_bfe_u32 s65, s83, 262156\n"
"s_mul_i32 s65, s65, 28672\n"
"v_mfma_f32_16x16x32_bf16 v[38:41], v[214:217], a[248:251], v[38:41]\n"
".Lmk_pg_16:\n"
"s_cbranch_execz .Lmk_ret_3\n"
"ds_read_b128 v[206:209], v7 offset:51200\n"
"s_add_u32 s65, s65, s67\n"
"v_mfma_f32_16x16x32_bf16 v[42:45], v[214:217], a[252:255], v[42:45]\n"
"s_add_u32 m0, s54, 57344\n"
"v_fma_f32 v177, v239, v161, -v23\n"
"v_mfma_f32_16x16x32_bf16 v[46:49], v[218:221], a[248:251], v[46:49]\n"
"ds_read_b128 v[210:213], v7 offset:52224\n"
"buffer_load_dwordx4 v2, s[28:31], s65 offen offset:0 sc1 lds\n"
"v_mfma_f32_16x16x32_bf16 v[50:53], v[218:221], a[252:255], v[50:53]\n"
"s_waitcnt lgkmcnt(4)\n"
"v_exp_f32 v172, v172\n"
"v_mfma_f32_16x16x32_bf16 v[54:57], v[222:225], a[248:251], v[54:57]\n"
"ds_read_b128 v[214:217], v7 offset:53248\n"
"v_fma_f32 v122, v122, v160, v174\n"
"v_mfma_f32_16x16x32_bf16 v[58:61], v[222:225], a[252:255], v[58:61]\n"
"buffer_load_dwordx4 v2, s[28:31], s65 offen offset:1024 sc1 lds\n"
"v_mfma_f32_16x16x32_bf16 v[62:65], v[226:229], a[248:251], v[62:65]\n"
"ds_read_b128 v[218:221], v7 offset:54272\n"
"v_mfma_f32_16x16x32_bf16 v[66:69], v[226:229], a[252:255], v[66:69]\n"
"buffer_load_dwordx4 v2, s[28:31], s65 offen offset:2048 sc1 lds\n"
"v_mfma_f32_16x16x32_bf16 v[70:73], v[190:193], a[248:251], v[70:73]\n"
"ds_read_b128 v[222:225], v7 offset:55296\n"
"v_mfma_f32_16x16x32_bf16 v[74:77], v[190:193], a[252:255], v[74:77]\n"
"v_exp_f32 v173, v173\n"
"v_mfma_f32_16x16x32_bf16 v[78:81], v[194:197], a[248:251], v[78:81]\n"
"ds_read_b128 v[226:229], v7 offset:56320\n"
"v_fma_f32 v123, v123, v160, v175\n"
"v_mfma_f32_16x16x32_bf16 v[82:85], v[194:197], a[252:255], v[82:85]\n"
"s_waitcnt lgkmcnt(4)\n"
"v_add_f32 v170, 1.0, v170\n"
"v_mfma_f32_16x16x32_bf16 v[30:33], v[198:201], a[0:3], v[30:33]\n"
"ds_read_b128 v[190:193], v7 offset:57344\n"
"v_fma_f32 v124, v124, v160, v176\n"
"v_mfma_f32_16x16x32_bf16 v[34:37], v[198:201], a[4:7], v[34:37]\n"
"buffer_load_dwordx4 v2, s[28:31], s65 offen offset:3072 sc1 lds\n"
"v_mfma_f32_16x16x32_bf16 v[38:41], v[202:205], a[0:3], v[38:41]\n"
"ds_read_b128 v[194:197], v7 offset:58368\n"
"v_add_f32 v171, 1.0, v171\n"
"v_mfma_f32_16x16x32_bf16 v[42:45], v[202:205], a[4:7], v[42:45]\n"
"v_fma_f32 v125, v125, v160, v177\n"
"v_add_f32 v172, 1.0, v172\n"
"v_mfma_f32_16x16x32_bf16 v[46:49], v[206:209], a[0:3], v[46:49]\n"
"ds_read_b128 v[198:201], v7 offset:59392\n"
"s_add_u32 m0, s54, 61440\n"
"v_mfma_f32_16x16x32_bf16 v[50:53], v[206:209], a[4:7], v[50:53]\n"
"v_mul_f32 v174, v122, v122\n"
"v_add_f32 v173, 1.0, v173\n"
"v_mfma_f32_16x16x32_bf16 v[54:57], v[210:213], a[0:3], v[54:57]\n"
"ds_read_b128 v[202:205], v7 offset:60416\n"
"buffer_load_dwordx4 v3, s[28:31], s65 offen offset:0 sc1 lds\n"
"v_mfma_f32_16x16x32_bf16 v[58:61], v[210:213], a[4:7], v[58:61]\n"
"s_waitcnt lgkmcnt(4)\n"
"v_mul_f32 v175, v123, v123\n"
"v_mfma_f32_16x16x32_bf16 v[62:65], v[214:217], a[0:3], v[62:65]\n"
"ds_read_b128 v[206:209], v7 offset:61440\n"
"v_mfma_f32_16x16x32_bf16 v[66:69], v[214:217], a[4:7], v[66:69]\n"
"buffer_load_dwordx4 v3, s[28:31], s65 offen offset:1024 sc1 lds\n"
"v_mfma_f32_16x16x32_bf16 v[70:73], v[218:221], a[0:3], v[70:73]\n"
"ds_read_b128 v[210:213], v7 offset:62464\n"
"v_mfma_f32_16x16x32_bf16 v[74:77], v[218:221], a[4:7], v[74:77]\n"
"v_rcp_f32 v170, v170\n"
"v_mfma_f32_16x16x32_bf16 v[78:81], v[222:225], a[0:3], v[78:81]\n"
"ds_read_b128 v[214:217], v7 offset:63488\n"
"v_mul_f32 v176, v124, v124\n"
"v_mfma_f32_16x16x32_bf16 v[82:85], v[222:225], a[4:7], v[82:85]\n"
"buffer_load_dwordx4 v3, s[28:31], s65 offen offset:2048 sc1 lds\n"
"v_mfma_f32_16x16x32_bf16 v[30:33], v[226:229], a[8:11], v[30:33]\n"
"ds_read_b128 v[218:221], v7 offset:64512\n"
"v_rcp_f32 v171, v171\n"
"s_waitcnt vmcnt(34)\n"
"v_mfma_f32_16x16x32_bf16 v[34:37], v[226:229], a[12:15], v[34:37]\n"
"s_waitcnt lgkmcnt(4)\n"
"v_mul_f32 v177, v125, v125\n"
"v_mfma_f32_16x16x32_bf16 v[38:41], v[190:193], a[8:11], v[38:41]\n"
"ds_read_b128 v[222:225], v8 offset:0\n"
"v_mfma_f32_16x16x32_bf16 v[42:45], v[190:193], a[12:15], v[42:45]\n"
"buffer_load_dwordx4 a[92:95], v3, s[28:31], s64 offen offset:3072 sc1\n"
"v_mfma_f32_16x16x32_bf16 v[46:49], v[194:197], a[8:11], v[46:49]\n"
"ds_read_b128 v[226:229], v8 offset:1024\n"
"v_mfma_f32_16x16x32_bf16 v[50:53], v[194:197], a[12:15], v[50:53]\n"
"v_rcp_f32 v172, v172\n"
"v_mfma_f32_16x16x32_bf16 v[54:57], v[198:201], a[8:11], v[54:57]\n"
"ds_read_b128 v[190:193], v8 offset:2048\n"
"v_fmamk_f32 v174, v174, 0xbdd2d3e8, v178\n"
"v_mfma_f32_16x16x32_bf16 v[58:61], v[198:201], a[12:15], v[58:61]\n"
"s_bfe_u32 s64, s83, 262156\n"
"s_mul_i32 s64, s64, 8192\n"
"v_mfma_f32_16x16x32_bf16 v[62:65], v[202:205], a[8:11], v[62:65]\n"
"ds_read_b128 v[194:197], v8 offset:3072\n"
"s_add_u32 s64, s64, s66\n"
"v_mfma_f32_16x16x32_bf16 v[66:69], v[202:205], a[12:15], v[66:69]\n"
"s_waitcnt lgkmcnt(4)\n"
"buffer_load_dwordx4 a[96:99], v2, s[28:31], s64 offen offset:0 sc1\n"
"v_mfma_f32_16x16x32_bf16 v[70:73], v[206:209], a[8:11], v[70:73]\n"
"ds_read_b128 v[198:201], v8 offset:4096\n"
"v_rcp_f32 v173, v173\n"
"v_mfma_f32_16x16x32_bf16 v[74:77], v[206:209], a[12:15], v[74:77]\n"
"buffer_load_dwordx4 a[100:103], v2, s[28:31], s64 offen offset:1024 sc1\n"
"v_mfma_f32_16x16x32_bf16 v[78:81], v[210:213], a[8:11], v[78:81]\n"
"ds_read_b128 v[202:205], v8 offset:5120\n"
"v_fmamk_f32 v175, v175, 0xbdd2d3e8, v178\n"
"v_mfma_f32_16x16x32_bf16 v[82:85], v[210:213], a[12:15], v[82:85]\n"
"v_mul_f32 v118, v118, v170\n"
"v_fmamk_f32 v176, v176, 0xbdd2d3e8, v178\n"
"v_mfma_f32_16x16x32_bf16 v[30:33], v[214:217], a[16:19], v[30:33]\n"
"ds_read_b128 v[206:209], v8 offset:6144\n"
"v_mul_f32 v119, v119, v171\n"
"v_mfma_f32_16x16x32_bf16 v[34:37], v[214:217], a[20:23], v[34:37]\n"
"buffer_load_dwordx4 a[104:107], v2, s[28:31], s64 offen offset:2048 sc1\n"
"v_mfma_f32_16x16x32_bf16 v[38:41], v[218:221], a[16:19], v[38:41]\n"
"ds_read_b128 v[210:213], v8 offset:7168\n"
"v_fmamk_f32 v177, v177, 0xbdd2d3e8, v178\n"
"v_mfma_f32_16x16x32_bf16 v[42:45], v[218:221], a[20:23], v[42:45]\n"
"s_waitcnt lgkmcnt(4)\n"
"v_mul_f32 v120, v120, v172\n"
"v_mfma_f32_16x16x32_bf16 v[46:49], v[222:225], a[16:19], v[46:49]\n"
"ds_read_b128 v[214:217], v8 offset:8192\n"
"v_mul_f32 v174, v174, v122\n"
"v_mfma_f32_16x16x32_bf16 v[50:53], v[222:225], a[20:23], v[50:53]\n"
"buffer_load_dwordx4 a[108:111], v2, s[28:31], s64 offen offset:3072 sc1\n"
"v_mfma_f32_16x16x32_bf16 v[54:57], v[226:229], a[16:19], v[54:57]\n"
"ds_read_b128 v[218:221], v8 offset:9216\n"
"v_mul_f32 v121, v121, v173\n"
"v_mfma_f32_16x16x32_bf16 v[58:61], v[226:229], a[20:23], v[58:61]\n"
"v_mul_f32 v175, v175, v123\n"
"v_fma_f32 v170, v240, v159, -v22\n"
"v_mfma_f32_16x16x32_bf16 v[62:65], v[190:193], a[16:19], v[62:65]\n"
"ds_read_b128 v[222:225], v8 offset:10240\n"
"v_mul_f32 v176, v176, v124\n"
"v_mfma_f32_16x16x32_bf16 v[66:69], v[190:193], a[20:23], v[66:69]\n"
"buffer_load_dwordx4 a[112:115], v3, s[28:31], s64 offen offset:0 sc1\n"
"v_mfma_f32_16x16x32_bf16 v[70:73], v[194:197], a[16:19], v[70:73]\n"
"ds_read_b128 v[226:229], v8 offset:11264\n"
"v_fma_f32 v171, v241, v159, -v22\n"
"v_mfma_f32_16x16x32_bf16 v[74:77], v[194:197], a[20:23], v[74:77]\n"
"s_waitcnt lgkmcnt(4)\n"
"v_mul_f32 v177, v177, v125\n"
"v_mfma_f32_16x16x32_bf16 v[78:81], v[198:201], a[16:19], v[78:81]\n"
"s_waitcnt vmcnt(33)\n"
"s_barrier\n"
"ds_read_b128 v[190:193], v6 offset:0\n"
"v_mfma_f32_16x16x32_bf16 v[82:85], v[198:201], a[20:23], v[82:85]\n"
"buffer_load_dwordx4 a[116:119], v3, s[28:31], s64 offen offset:1024 sc1\n"
"v_mfma_f32_16x16x32_bf16 v[30:33], v[202:205], a[24:27], v[30:33]\n"
"ds_read_b128 v[194:197], v6 offset:1024\n"
"v_fma_f32 v172, v242, v159, -v22\n"
"v_mfma_f32_16x16x32_bf16 v[34:37], v[202:205], a[28:31], v[34:37]\n"
"v_exp_f32 v174, v174\n"
"v_mfma_f32_16x16x32_bf16 v[38:41], v[206:209], a[24:27], v[38:41]\n"
"ds_read_b128 v[198:201], v6 offset:2048\n"
"v_fma_f32 v173, v243, v159, -v22\n"
"v_exp_f32 v175, v175\n"
"v_mfma_f32_16x16x32_bf16 v[42:45], v[206:209], a[28:31], v[42:45]\n"
"s_bfe_u32 s65, s83, 262160\n"
"s_mul_i32 s65, s65, 28672\n"
"v_mfma_f32_16x16x32_bf16 v[46:49], v[210:213], a[24:27], v[46:49]\n"
"ds_read_b128 v[202:205], v6 offset:3072\n"
"s_add_u32 s65, s65, s67\n"
"v_mfma_f32_16x16x32_bf16 v[50:53], v[210:213], a[28:31], v[50:53]\n"
"s_waitcnt lgkmcnt(4)\n"
"s_add_u32 m0, s54, 86016\n"
"v_mfma_f32_16x16x32_bf16 v[54:57], v[214:217], a[24:27], v[54:57]\n"
"ds_read_b128 v[206:209], v6 offset:4096\n"
"buffer_load_dwordx4 v2, s[28:31], s65 offen offset:0 sc1 lds\n"
"v_mfma_f32_16x16x32_bf16 v[58:61], v[214:217], a[28:31], v[58:61]\n"
"buffer_load_dwordx4 v2, s[28:31], s65 offen offset:1024 sc1 lds\n"
"v_mfma_f32_16x16x32_bf16 v[62:65], v[218:221], a[24:27], v[62:65]\n"
"ds_read_b128 v[210:213], v6 offset:5120\n"
"v_fma_f32 v126, v126, v158, v170\n"
"v_mfma_f32_16x16x32_bf16 v[66:69], v[218:221], a[28:31], v[66:69]\n"
"v_exp_f32 v176, v176\n"
"v_mfma_f32_16x16x32_bf16 v[70:73], v[222:225], a[24:27], v[70:73]\n"
"ds_read_b128 v[214:217], v6 offset:6144\n"
"v_fma_f32 v127, v127, v158, v171\n"
"v_mfma_f32_16x16x32_bf16 v[74:77], v[222:225], a[28:31], v[74:77]\n"
"buffer_load_dwordx4 v2, s[28:31], s65 offen offset:2048 sc1 lds\n"
"v_mfma_f32_16x16x32_bf16 v[78:81], v[226:229], a[24:27], v[78:81]\n"
"ds_read_b128 v[218:221], v6 offset:7168\n"
"v_exp_f32 v177, v177\n"
"v_mfma_f32_16x16x32_bf16 v[82:85], v[226:229], a[28:31], v[82:85]\n"
"s_waitcnt lgkmcnt(4)\n"
"v_fma_f32 v128, v128, v158, v172\n"
"v_mfma_f32_16x16x32_bf16 v[30:33], v[190:193], a[32:35], v[30:33]\n"
"ds_read_b128 v[222:225], v6 offset:8192\n"
"v_add_f32 v174, 1.0, v174\n"
"v_mfma_f32_16x16x32_bf16 v[34:37], v[190:193], a[36:39], v[34:37]\n"
"buffer_load_dwordx4 v2, s[28:31], s65 offen offset:3072 sc1 lds\n"
"v_mfma_f32_16x16x32_bf16 v[38:41], v[194:197], a[32:35], v[38:41]\n"
"ds_read_b128 v[226:229], v6 offset:9216\n"
"v_fma_f32 v129, v129, v158, v173\n"
"v_mfma_f32_16x16x32_bf16 v[42:45], v[194:197], a[36:39], v[42:45]\n"
"v_add_f32 v175, 1.0, v175\n"
"v_mul_f32 v170, v126, v126\n"
"v_mfma_f32_16x16x32_bf16 v[46:49], v[198:201], a[32:35], v[46:49]\n"
"ds_read_b128 v[190:193], v6 offset:10240\n"
"v_mfma_f32_16x16x32_bf16 v[50:53], v[198:201], a[36:39], v[50:53]\n"
"s_add_u32 m0, s54, 90112\n"
"v_add_f32 v176, 1.0, v176\n"
"v_mfma_f32_16x16x32_bf16 v[54:57], v[202:205], a[32:35], v[54:57]\n"
"ds_read_b128 v[194:197], v6 offset:11264\n"
"v_mfma_f32_16x16x32_bf16 v[58:61], v[202:205], a[36:39], v[58:61]\n"
"s_waitcnt lgkmcnt(4)\n"
"buffer_load_dwordx4 v3, s[28:31], s65 offen offset:0 sc1 lds\n"
"v_mfma_f32_16x16x32_bf16 v[62:65], v[206:209], a[32:35], v[62:65]\n"
"ds_read_b128 v[198:201], v6 offset:12288\n"
"v_mul_f32 v171, v127, v127\n"
"v_mfma_f32_16x16x32_bf16 v[66:69], v[206:209], a[36:39], v[66:69]\n"
"buffer_load_dwordx4 v3, s[28:31], s65 offen offset:1024 sc1 lds\n"
"v_mfma_f32_16x16x32_bf16 v[70:73], v[210:213], a[32:35], v[70:73]\n"
"ds_read_b128 v[202:205], v6 offset:13312\n"
"v_add_f32 v177, 1.0, v177\n"
"v_mfma_f32_16x16x32_bf16 v[74:77], v[210:213], a[36:39], v[74:77]\n"
"v_mul_f32 v172, v128, v128\n"
"v_mfma_f32_16x16x32_bf16 v[78:81], v[214:217], a[32:35], v[78:81]\n"
"ds_read_b128 v[206:209], v6 offset:14336\n"
"v_rcp_f32 v174, v174\n"
"v_mfma_f32_16x16x32_bf16 v[82:85], v[214:217], a[36:39], v[82:85]\n"
"buffer_load_dwordx4 v3, s[28:31], s65 offen offset:2048 sc1 lds\n"
"s_waitcnt vmcnt(35)\n"
"v_mfma_f32_16x16x32_bf16 v[30:33], v[218:221], a[40:43], v[30:33]\n"
"ds_read_b128 v[210:213], v6 offset:15360\n"
"v_mul_f32 v173, v129, v129\n"
"v_mfma_f32_16x16x32_bf16 v[34:37], v[218:221], a[44:47], v[34:37]\n"
"s_waitcnt lgkmcnt(4)\n"
"v_mfma_f32_16x16x32_bf16 v[38:41], v[222:225], a[40:43], v[38:41]\n"
"ds_read_b128 v[214:217], v6 offset:16384\n"
"v_mfma_f32_16x16x32_bf16 v[42:45], v[222:225], a[44:47], v[42:45]\n"
"buffer_load_dwordx4 a[120:123], v3, s[28:31], s64 offen offset:2048 sc1\n"
"v_mfma_f32_16x16x32_bf16 v[46:49], v[226:229], a[40:43], v[46:49]\n"
"ds_read_b128 v[218:221], v6 offset:17408\n"
"v_mfma_f32_16x16x32_bf16 v[50:53], v[226:229], a[44:47], v[50:53]\n"
"v_rcp_f32 v175, v175\n"
"v_mfma_f32_16x16x32_bf16 v[54:57], v[190:193], a[40:43], v[54:57]\n"
"ds_read_b128 v[222:225], v6 offset:18432\n"
"v_fmamk_f32 v170, v170, 0xbdd2d3e8, v178\n"
"v_mfma_f32_16x16x32_bf16 v[58:61], v[190:193], a[44:47], v[58:61]\n"
"buffer_load_dwordx4 a[124:127], v3, s[28:31], s64 offen offset:3072 sc1\n"
"v_mfma_f32_16x16x32_bf16 v[62:65], v[194:197], a[40:43], v[62:65]\n"
"ds_read_b128 v[226:229], v6 offset:19456\n"
"v_rcp_f32 v176, v176\n"
"v_mfma_f32_16x16x32_bf16 v[66:69], v[194:197], a[44:47], v[66:69]\n"
"s_waitcnt lgkmcnt(4)\n"
"v_fmamk_f32 v171, v171, 0xbdd2d3e8, v178\n"
"v_mfma_f32_16x16x32_bf16 v[70:73], v[198:201], a[40:43], v[70:73]\n"
"ds_read_b128 v[190:193], v6 offset:20480\n"
"v_mfma_f32_16x16x32_bf16 v[74:77], v[198:201], a[44:47], v[74:77]\n"
"s_bfe_u32 s64, s83, 262160\n"
"s_mul_i32 s64, s64, 8192\n"
"v_mfma_f32_16x16x32_bf16 v[78:81], v[202:205], a[40:43], v[78:81]\n"
"ds_read_b128 v[194:197], v6 offset:21504\n"
"s_add_u32 s64, s64, s66\n"
"v_mfma_f32_16x16x32_bf16 v[82:85], v[202:205], a[44:47], v[82:85]\n"
"v_rcp_f32 v177, v177\n"
"v_mfma_f32_16x16x32_bf16 v[30:33], v[206:209], a[48:51], v[30:33]\n"
"ds_read_b128 v[198:201], v6 offset:22528\n"
"v_fmamk_f32 v172, v172, 0xbdd2d3e8, v178\n"
"v_mfma_f32_16x16x32_bf16 v[34:37], v[206:209], a[52:55], v[34:37]\n"
"buffer_load_dwordx4 a[128:131], v2, s[28:31], s64 offen offset:0 sc1\n"
"v_mfma_f32_16x16x32_bf16 v[38:41], v[210:213], a[48:51], v[38:41]\n"
"ds_read_b128 v[202:205], v6 offset:23552\n"
"v_mul_f32 v122, v122, v174\n"
"v_mfma_f32_16x16x32_bf16 v[42:45], v[210:213], a[52:55], v[42:45]\n"
"s_waitcnt lgkmcnt(4)\n"
"v_fmamk_f32 v173, v173, 0xbdd2d3e8, v178\n"
"v_mfma_f32_16x16x32_bf16 v[46:49], v[214:217], a[48:51], v[46:49]\n"
"ds_read_b128 v[206:209], v6 offset:24576\n"
"v_mul_f32 v123, v123, v175\n"
"v_mfma_f32_16x16x32_bf16 v[50:53], v[214:217], a[52:55], v[50:53]\n"
"buffer_load_dwordx4 a[132:135], v2, s[28:31], s64 offen offset:1024 sc1\n"
"v_mfma_f32_16x16x32_bf16 v[54:57], v[218:221], a[48:51], v[54:57]\n"
"ds_read_b128 v[210:213], v6 offset:25600\n"
"v_mul_f32 v170, v170, v126\n"
"v_mfma_f32_16x16x32_bf16 v[58:61], v[218:221], a[52:55], v[58:61]\n"
"buffer_load_dwordx4 a[136:139], v2, s[28:31], s64 offen offset:2048 sc1\n"
"v_mfma_f32_16x16x32_bf16 v[62:65], v[222:225], a[48:51], v[62:65]\n"
"ds_read_b128 v[214:217], v6 offset:26624\n"
"v_mul_f32 v124, v124, v176\n"
"v_mfma_f32_16x16x32_bf16 v[66:69], v[222:225], a[52:55], v[66:69]\n"
"v_mul_f32 v171, v171, v127\n"
"v_mul_f32 v125, v125, v177\n"
"v_mfma_f32_16x16x32_bf16 v[70:73], v[226:229], a[48:51], v[70:73]\n"
"ds_read_b128 v[218:221], v6 offset:27648\n"
"v_mul_f32 v172, v172, v128\n"
"v_mfma_f32_16x16x32_bf16 v[74:77], v[226:229], a[52:55], v[74:77]\n"
"s_waitcnt lgkmcnt(4)\n"
"v_fma_f32 v174, v240, v161, -v23\n"
"v_mfma_f32_16x16x32_bf16 v[78:81], v[190:193], a[48:51], v[78:81]\n"
"s_waitcnt vmcnt(33)\n"
"s_barrier\n"
"ds_read_b128 v[222:225], v6 offset:28672\n"
"v_mfma_f32_16x16x32_bf16 v[82:85], v[190:193], a[52:55], v[82:85]\n"
"buffer_load_dwordx4 a[140:143], v2, s[28:31], s64 offen offset:3072 sc1\n"
"v_mfma_f32_16x16x32_bf16 v[30:33], v[194:197], a[56:59], v[30:33]\n"
"ds_read_b128 v[226:229], v6 offset:29696\n"
"v_mul_f32 v173, v173, v129\n"
"v_mfma_f32_16x16x32_bf16 v[34:37], v[194:197], a[60:63], v[34:37]\n"
"s_bfe_u32 s65, s83, 262164\n"
"s_mul_i32 s65, s65, 28672\n"
"v_mfma_f32_16x16x32_bf16 v[38:41], v[198:201], a[56:59], v[38:41]\n"
"ds_read_b128 v[190:193], v6 offset:30720\n"
"s_add_u32 s65, s65, s67\n"
"v_mfma_f32_16x16x32_bf16 v[42:45], v[198:201], a[60:63], v[42:45]\n"
"s_add_u32 m0, s54, 114688\n"
"s_nop 0\n"
"buffer_load_dwordx4 v2, s[28:31], s65 offen offset:0 sc1 lds\n"
"v_mfma_f32_16x16x32_bf16 v[46:49], v[202:205], a[56:59], v[46:49]\n"
"ds_read_b128 v[194:197], v6 offset:31744\n"
"v_fma_f32 v175, v241, v161, -v23\n"
"v_mfma_f32_16x16x32_bf16 v[50:53], v[202:205], a[60:63], v[50:53]\n"
"s_waitcnt lgkmcnt(4)\n"
"v_exp_f32 v170, v170\n"
"v_mfma_f32_16x16x32_bf16 v[54:57], v[206:209], a[56:59], v[54:57]\n"
"ds_read_b128 v[198:201], v6 offset:32768\n"
"v_fma_f32 v176, v242, v161, -v23\n"
"v_mfma_f32_16x16x32_bf16 v[58:61], v[206:209], a[60:63], v[58:61]\n"
"buffer_load_dwordx4 v2, s[28:31], s65 offen offset:1024 sc1 lds\n"
"v_mfma_f32_16x16x32_bf16 v[62:65], v[210:213], a[56:59], v[62:65]\n"
"ds_read_b128 v[202:205], v6 offset:33792\n"
"v_mfma_f32_16x16x32_bf16 v[66:69], v[210:213], a[60:63], v[66:69]\n"
"buffer_load_dwordx4 v2, s[28:31], s65 offen offset:2048 sc1 lds\n"
"v_mfma_f32_16x16x32_bf16 v[70:73], v[214:217], a[56:59], v[70:73]\n"
"ds_read_b128 v[206:209], v6 offset:34816\n"
"v_mfma_f32_16x16x32_bf16 v[74:77], v[214:217], a[60:63], v[74:77]\n"
"v_exp_f32 v171, v171\n"
"v_mfma_f32_16x16x32_bf16 v[78:81], v[218:221], a[56:59], v[78:81]\n"
"ds_read_b128 v[210:213], v6 offset:35840\n"
"v_fma_f32 v177, v243, v161, -v23\n"
"v_mfma_f32_16x16x32_bf16 v[82:85], v[218:221], a[60:63], v[82:85]\n"
"s_waitcnt lgkmcnt(4)\n"
"v_exp_f32 v172, v172\n"
"s_waitcnt vmcnt(30)\n"
"v_mfma_f32_16x16x32_bf16 v[30:33], v[222:225], a[64:67], v[30:33]\n"
"ds_read_b128 v[214:217], v6 offset:36864\n"
"v_fma_f32 v130, v130, v160, v174\n"
"v_mfma_f32_16x16x32_bf16 v[34:37], v[222:225], a[68:71], v[34:37]\n"
"buffer_load_dwordx4 v2, s[28:31], s65 offen offset:3072 sc1 lds\n"
"v_mfma_f32_16x16x32_bf16 v[38:41], v[226:229], a[64:67], v[38:41]\n"
"ds_read_b128 v[218:221], v6 offset:37888\n"
"v_mfma_f32_16x16x32_bf16 v[42:45], v[226:229], a[68:71], v[42:45]\n"
"buffer_load_dwordx4 a[144:147], v3, s[28:31], s64 offen offset:0 sc1\n"
"v_mfma_f32_16x16x32_bf16 v[46:49], v[190:193], a[64:67], v[46:49]\n"
"ds_read_b128 v[222:225], v6 offset:38912\n"
"v_mfma_f32_16x16x32_bf16 v[50:53], v[190:193], a[68:71], v[50:53]\n"
"v_exp_f32 v173, v173\n"
"v_mfma_f32_16x16x32_bf16 v[54:57], v[194:197], a[64:67], v[54:57]\n"
"ds_read_b128 v[226:229], v6 offset:39936\n"
"s_add_u32 m0, s54, 118784\n"
"v_mfma_f32_16x16x32_bf16 v[58:61], v[194:197], a[68:71], v[58:61]\n"
"s_waitcnt lgkmcnt(4)\n"
"v_fma_f32 v131, v131, v160, v175\n"
"v_mfma_f32_16x16x32_bf16 v[62:65], v[198:201], a[64:67], v[62:65]\n"
"ds_read_b128 v[190:193], v6 offset:40960\n"
"v_add_f32 v170, 1.0, v170\n"
"v_mfma_f32_16x16x32_bf16 v[66:69], v[198:201], a[68:71], v[66:69]\n"
"buffer_load_dwordx4 v3, s[28:31], s65 offen offset:0 sc1 lds\n"
"v_mfma_f32_16x16x32_bf16 v[70:73], v[202:205], a[64:67], v[70:73]\n"
"ds_read_b128 v[194:197], v6 offset:41984\n"
"v_fma_f32 v132, v132, v160, v176\n"
"v_mfma_f32_16x16x32_bf16 v[74:77], v[202:205], a[68:71], v[74:77]\n"
"buffer_load_dwordx4 a[148:151], v3, s[28:31], s64 offen offset:1024 sc1\n"
"v_mfma_f32_16x16x32_bf16 v[78:81], v[206:209], a[64:67], v[78:81]\n"
"ds_read_b128 v[198:201], v6 offset:43008\n"
"v_add_f32 v171, 1.0, v171\n"
"v_mfma_f32_16x16x32_bf16 v[82:85], v[206:209], a[68:71], v[82:85]\n"
"v_fma_f32 v133, v133, v160, v177\n"
"v_add_f32 v172, 1.0, v172\n"
"v_mfma_f32_16x16x32_bf16 v[30:33], v[210:213], a[72:75], v[30:33]\n"
"ds_read_b128 v[202:205], v6 offset:44032\n"
"v_mul_f32 v174, v130, v130\n"
"v_mfma_f32_16x16x32_bf16 v[34:37], v[210:213], a[76:79], v[34:37]\n"
"s_waitcnt lgkmcnt(4)\n"
"v_add_f32 v173, 1.0, v173\n"
"v_mfma_f32_16x16x32_bf16 v[38:41], v[214:217], a[72:75], v[38:41]\n"
"ds_read_b128 v[206:209], v6 offset:45056\n"
"v_mul_f32 v175, v131, v131\n"
"v_mfma_f32_16x16x32_bf16 v[42:45], v[214:217], a[76:79], v[42:45]\n"
"buffer_load_dwordx4 v3, s[28:31], s65 offen offset:1024 sc1 lds\n"
"v_mfma_f32_16x16x32_bf16 v[46:49], v[218:221], a[72:75], v[46:49]\n"
"ds_read_b128 v[210:213], v6 offset:46080\n"
"v_mfma_f32_16x16x32_bf16 v[50:53], v[218:221], a[76:79], v[50:53]\n"
"buffer_load_dwordx4 v3, s[28:31], s65 offen offset:2048 sc1 lds\n"
"v_mfma_f32_16x16x32_bf16 v[54:57], v[222:225], a[72:75], v[54:57]\n"
"ds_read_b128 v[214:217], v6 offset:47104\n"
"v_mfma_f32_16x16x32_bf16 v[58:61], v[222:225], a[76:79], v[58:61]\n"
"v_rcp_f32 v170, v170\n"
"v_mfma_f32_16x16x32_bf16 v[62:65], v[226:229], a[72:75], v[62:65]\n"
"ds_read_b128 v[218:221], v6 offset:48128\n"
"v_mul_f32 v176, v132, v132\n"
"v_mfma_f32_16x16x32_bf16 v[66:69], v[226:229], a[76:79], v[66:69]\n"
"s_waitcnt lgkmcnt(4)\n"
"v_rcp_f32 v171, v171\n"
"v_mfma_f32_16x16x32_bf16 v[70:73], v[190:193], a[72:75], v[70:73]\n"
"ds_read_b128 v[222:225], v6 offset:49152\n"
"v_mul_f32 v177, v133, v133\n"
"v_mfma_f32_16x16x32_bf16 v[74:77], v[190:193], a[76:79], v[74:77]\n"
"buffer_load_dwordx4 a[152:155], v3, s[28:31], s64 offen offset:2048 sc1\n"
"v_mfma_f32_16x16x32_bf16 v[78:81], v[194:197], a[72:75], v[78:81]\n"
"ds_read_b128 v[226:229], v6 offset:50176\n"
"v_mfma_f32_16x16x32_bf16 v[82:85], v[194:197], a[76:79], v[82:85]\n"
"buffer_load_dwordx4 a[156:159], v3, s[28:31], s64 offen offset:3072 sc1\n"
"v_mfma_f32_16x16x32_bf16 v[30:33], v[198:201], a[80:83], v[30:33]\n"
"ds_read_b128 v[190:193], v6 offset:51200\n"
"v_mfma_f32_16x16x32_bf16 v[34:37], v[198:201], a[84:87], v[34:37]\n"
"v_rcp_f32 v172, v172\n"
"v_mfma_f32_16x16x32_bf16 v[38:41], v[202:205], a[80:83], v[38:41]\n"
"ds_read_b128 v[194:197], v6 offset:52224\n"
"v_fmamk_f32 v174, v174, 0xbdd2d3e8, v178\n"
"v_mfma_f32_16x16x32_bf16 v[42:45], v[202:205], a[84:87], v[42:45]\n"
"s_waitcnt lgkmcnt(4)\n"
"v_rcp_f32 v173, v173\n"
"v_mfma_f32_16x16x32_bf16 v[46:49], v[206:209], a[80:83], v[46:49]\n"
"ds_read_b128 v[198:201], v6 offset:53248\n"
"v_fmamk_f32 v175, v175, 0xbdd2d3e8, v178\n"
"v_mfma_f32_16x16x32_bf16 v[50:53], v[206:209], a[84:87], v[50:53]\n"
"s_bfe_u32 s64, s83, 262164\n"
"s_mul_i32 s64, s64, 8192\n"
"v_mfma_f32_16x16x32_bf16 v[54:57], v[210:213], a[80:83], v[54:57]\n"
"ds_read_b128 v[202:205], v6 offset:54272\n"
"s_add_u32 s64, s64, s66\n"
"buffer_load_dwordx4 a[160:163], v2, s[28:31], s64 offen offset:0 sc1\n"
"v_mfma_f32_16x16x32_bf16 v[58:61], v[210:213], a[84:87], v[58:61]\n"
"buffer_load_dwordx4 a[164:167], v2, s[28:31], s64 offen offset:1024 sc1\n"
"v_mfma_f32_16x16x32_bf16 v[62:65], v[214:217], a[80:83], v[62:65]\n"
"ds_read_b128 v[206:209], v6 offset:55296\n"
"v_mul_f32 v126, v126, v170\n"
"v_mfma_f32_16x16x32_bf16 v[66:69], v[214:217], a[84:87], v[66:69]\n"
"v_fmamk_f32 v176, v176, 0xbdd2d3e8, v178\n"
"v_mul_f32 v127, v127, v171\n"
"v_mfma_f32_16x16x32_bf16 v[70:73], v[218:221], a[80:83], v[70:73]\n"
"ds_read_b128 v[210:213], v6 offset:56320\n"
"v_fmamk_f32 v177, v177, 0xbdd2d3e8, v178\n"
"v_mfma_f32_16x16x32_bf16 v[74:77], v[218:221], a[84:87], v[74:77]\n"
"s_waitcnt lgkmcnt(4)\n"
"v_mul_f32 v128, v128, v172\n"
"v_mfma_f32_16x16x32_bf16 v[78:81], v[222:225], a[80:83], v[78:81]\n"
"s_waitcnt vmcnt(33)\n"
"s_barrier\n"
"ds_read_b128 v[214:217], v6 offset:57344\n"
"v_mfma_f32_16x16x32_bf16 v[82:85], v[222:225], a[84:87], v[82:85]\n"
"buffer_load_dwordx4 a[168:171], v2, s[28:31], s64 offen offset:2048 sc1\n"
"v_mfma_f32_16x16x32_bf16 v[30:33], v[226:229], a[88:91], v[30:33]\n"
"ds_read_b128 v[218:221], v6 offset:58368\n"
"v_mul_f32 v174, v174, v130\n"
"s_waitcnt vmcnt(27)\n"
"v_mfma_f32_16x16x32_bf16 v[34:37], v[226:229], a[92:95], v[34:37]\n"
"s_bfe_u32 s65, s83, 262168\n"
"s_mul_i32 s65, s65, 28672\n"
"v_mfma_f32_16x16x32_bf16 v[38:41], v[190:193], a[88:91], v[38:41]\n"
"ds_read_b128 v[222:225], v6 offset:59392\n"
"s_add_u32 s65, s65, s67\n"
"v_mfma_f32_16x16x32_bf16 v[42:45], v[190:193], a[92:95], v[42:45]\n"
"s_add_u32 m0, s54, 0\n"
"v_mul_f32 v129, v129, v173\n"
"v_mfma_f32_16x16x32_bf16 v[46:49], v[194:197], a[88:91], v[46:49]\n"
"ds_read_b128 v[226:229], v6 offset:60416\n"
"buffer_load_dwordx4 v2, s[28:31], s65 offen offset:0 sc1 lds\n"
"v_mfma_f32_16x16x32_bf16 v[50:53], v[194:197], a[92:95], v[50:53]\n"
"s_waitcnt lgkmcnt(4)\n"
"v_mul_f32 v175, v175, v131\n"
"v_mfma_f32_16x16x32_bf16 v[54:57], v[198:201], a[88:91], v[54:57]\n"
"ds_read_b128 v[190:193], v6 offset:61440\n"
"v_fma_f32 v170, v244, v159, -v22\n"
"v_mfma_f32_16x16x32_bf16 v[58:61], v[198:201], a[92:95], v[58:61]\n"
"buffer_load_dwordx4 v2, s[28:31], s65 offen offset:1024 sc1 lds\n"
"v_mfma_f32_16x16x32_bf16 v[62:65], v[202:205], a[88:91], v[62:65]\n"
"ds_read_b128 v[194:197], v6 offset:62464\n"
"v_mul_f32 v176, v176, v132\n"
"v_mfma_f32_16x16x32_bf16 v[66:69], v[202:205], a[92:95], v[66:69]\n"
"buffer_load_dwordx4 a[172:175], v2, s[28:31], s64 offen offset:3072 sc1\n"
"v_mfma_f32_16x16x32_bf16 v[70:73], v[206:209], a[88:91], v[70:73]\n"
"ds_read_b128 v[198:201], v6 offset:63488\n"
"v_fma_f32 v171, v245, v159, -v22\n"
"v_mfma_f32_16x16x32_bf16 v[74:77], v[206:209], a[92:95], v[74:77]\n"
"v_mul_f32 v177, v177, v133\n"
"v_fma_f32 v172, v246, v159, -v22\n"
"v_mfma_f32_16x16x32_bf16 v[78:81], v[210:213], a[88:91], v[78:81]\n"
"ds_read_b128 v[202:205], v6 offset:64512\n"
"v_exp_f32 v174, v174\n"
"v_mfma_f32_16x16x32_bf16 v[82:85], v[210:213], a[92:95], v[82:85]\n"
"s_waitcnt lgkmcnt(4)\n"
"v_fma_f32 v173, v247, v159, -v22\n"
"v_mfma_f32_16x16x32_bf16 v[30:33], v[214:217], a[96:99], v[30:33]\n"
"ds_read_b128 v[206:209], v7 offset:0\n"
"v_mfma_f32_16x16x32_bf16 v[34:37], v[214:217], a[100:103], v[34:37]\n"
"buffer_load_dwordx4 v2, s[28:31], s65 offen offset:2048 sc1 lds\n"
"v_mfma_f32_16x16x32_bf16 v[38:41], v[218:221], a[96:99], v[38:41]\n"
"ds_read_b128 v[210:213], v7 offset:1024\n"
"v_mfma_f32_16x16x32_bf16 v[42:45], v[218:221], a[100:103], v[42:45]\n"
"v_exp_f32 v175, v175\n"
"v_mfma_f32_16x16x32_bf16 v[46:49], v[222:225], a[96:99], v[46:49]\n"
"ds_read_b128 v[214:217], v7 offset:2048\n"
"v_fma_f32 v134, v134, v158, v170\n"
"v_mfma_f32_16x16x32_bf16 v[50:53], v[222:225], a[100:103], v[50:53]\n"
"buffer_load_dwordx4 v2, s[28:31], s65 offen offset:3072 sc1 lds\n"
"v_mfma_f32_16x16x32_bf16 v[54:57], v[226:229], a[96:99], v[54:57]\n"
"ds_read_b128 v[218:221], v7 offset:3072\n"
"v_exp_f32 v176, v176\n"
"v_mfma_f32_16x16x32_bf16 v[58:61], v[226:229], a[100:103], v[58:61]\n"
"s_waitcnt lgkmcnt(4)\n"
"v_fma_f32 v135, v135, v158, v171\n"
"v_mfma_f32_16x16x32_bf16 v[62:65], v[190:193], a[96:99], v[62:65]\n"
"ds_read_b128 v[222:225], v7 offset:4096\n"
"v_mfma_f32_16x16x32_bf16 v[66:69], v[190:193], a[100:103], v[66:69]\n"
"buffer_load_dwordx4 a[176:179], v3, s[28:31], s64 offen offset:0 sc1\n"
"v_mfma_f32_16x16x32_bf16 v[70:73], v[194:197], a[96:99], v[70:73]\n"
"ds_read_b128 v[226:229], v7 offset:5120\n"
"v_mfma_f32_16x16x32_bf16 v[74:77], v[194:197], a[100:103], v[74:77]\n"
"v_exp_f32 v177, v177\n"
"v_mfma_f32_16x16x32_bf16 v[78:81], v[198:201], a[96:99], v[78:81]\n"
"ds_read_b128 v[190:193], v7 offset:6144\n"
"s_add_u32 m0, s54, 4096\n"
"v_mfma_f32_16x16x32_bf16 v[82:85], v[198:201], a[100:103], v[82:85]\n"
"v_fma_f32 v136, v136, v158, v172\n"
"v_add_f32 v174, 1.0, v174\n"
"v_mfma_f32_16x16x32_bf16 v[30:33], v[202:205], a[104:107], v[30:33]\n"
"ds_read_b128 v[194:197], v7 offset:7168\n"
"buffer_load_dwordx4 v3, s[28:31], s65 offen offset:0 sc1 lds\n"
"v_mfma_f32_16x16x32_bf16 v[34:37], v[202:205], a[108:111], v[34:37]\n"
"s_waitcnt lgkmcnt(4)\n"
"v_fma_f32 v137, v137, v158, v173\n"
"v_mfma_f32_16x16x32_bf16 v[38:41], v[206:209], a[104:107], v[38:41]\n"
"ds_read_b128 v[198:201], v7 offset:8192\n"
"v_add_f32 v175, 1.0, v175\n"
"v_mfma_f32_16x16x32_bf16 v[42:45], v[206:209], a[108:111], v[42:45]\n"
"buffer_load_dwordx4 a[180:183], v3, s[28:31], s64 offen offset:1024 sc1\n"
"v_mfma_f32_16x16x32_bf16 v[46:49], v[210:213], a[104:107], v[46:49]\n"
"ds_read_b128 v[202:205], v7 offset:9216\n"
"v_mul_f32 v170, v134, v134\n"
"v_mfma_f32_16x16x32_bf16 v[50:53], v[210:213], a[108:111], v[50:53]\n"
"v_add_f32 v176, 1.0, v176\n"
"v_mul_f32 v171, v135, v135\n"
"v_mfma_f32_16x16x32_bf16 v[54:57], v[214:217], a[104:107], v[54:57]\n"
"ds_read_b128 v[206:209], v7 offset:10240\n"
"v_mfma_f32_16x16x32_bf16 v[58:61], v[214:217], a[108:111], v[58:61]\n"
"buffer_load_dwordx4 v3, s[28:31], s65 offen offset:1024 sc1 lds\n"
"v_mfma_f32_16x16x32_bf16 v[62:65], v[218:221], a[104:107], v[62:65]\n"
"ds_read_b128 v[210:213], v7 offset:11264\n"
"v_add_f32 v177, 1.0, v177\n"
"v_mfma_f32_16x16x32_bf16 v[66:69], v[218:221], a[108:111], v[66:69]\n"
"s_waitcnt lgkmcnt(4)\n"
"v_mul_f32 v172, v136, v136\n"
"v_mfma_f32_16x16x32_bf16 v[70:73], v[222:225], a[104:107], v[70:73]\n"
"ds_read_b128 v[214:217], v7 offset:12288\n"
"v_mfma_f32_16x16x32_bf16 v[74:77], v[222:225], a[108:111], v[74:77]\n"
"buffer_load_dwordx4 v3, s[28:31], s65 offen offset:2048 sc1 lds\n"
"v_mfma_f32_16x16x32_bf16 v[78:81], v[226:229], a[104:107], v[78:81]\n"
"ds_read_b128 v[218:221], v7 offset:13312\n"
"v_mfma_f32_16x16x32_bf16 v[82:85], v[226:229], a[108:111], v[82:85]\n"
"v_rcp_f32 v174, v174\n"
"v_mfma_f32_16x16x32_bf16 v[30:33], v[190:193], a[112:115], v[30:33]\n"
"ds_read_b128 v[222:225], v7 offset:14336\n"
"v_mul_f32 v173, v137, v137\n"
"v_mfma_f32_16x16x32_bf16 v[34:37], v[190:193], a[116:119], v[34:37]\n"
"buffer_load_dwordx4 a[184:187], v3, s[28:31], s64 offen offset:2048 sc1\n"
"v_mfma_f32_16x16x32_bf16 v[38:41], v[194:197], a[112:115], v[38:41]\n"
"ds_read_b128 v[226:229], v7 offset:15360\n"
"v_rcp_f32 v175, v175\n"
"v_mfma_f32_16x16x32_bf16 v[42:45], v[194:197], a[116:119], v[42:45]\n"
"s_waitcnt lgkmcnt(4)\n"
"v_fmamk_f32 v170, v170, 0xbdd2d3e8, v178\n"
"v_mfma_f32_16x16x32_bf16 v[46:49], v[198:201], a[112:115], v[46:49]\n"
"ds_read_b128 v[190:193], v7 offset:16384\n"
"v_mfma_f32_16x16x32_bf16 v[50:53], v[198:201], a[116:119], v[50:53]\n"
"buffer_load_dwordx4 a[188:191], v3, s[28:31], s64 offen offset:3072 sc1\n"
"v_mfma_f32_16x16x32_bf16 v[54:57], v[202:205], a[112:115], v[54:57]\n"
"ds_read_b128 v[194:197], v7 offset:17408\n"
"v_mfma_f32_16x16x32_bf16 v[58:61], v[202:205], a[116:119], v[58:61]\n"
"v_rcp_f32 v176, v176\n"
"v_mfma_f32_16x16x32_bf16 v[62:65], v[206:209], a[112:115], v[62:65]\n"
"ds_read_b128 v[198:201], v7 offset:18432\n"
"v_fmamk_f32 v171, v171, 0xbdd2d3e8, v178\n"
"v_rcp_f32 v177, v177\n"
"v_mfma_f32_16x16x32_bf16 v[66:69], v[206:209], a[116:119], v[66:69]\n"
"s_bfe_u32 s64, s83, 262168\n"
"s_mul_i32 s64, s64, 8192\n"
"v_fmamk_f32 v172, v172, 0xbdd2d3e8, v178\n"
"v_mfma_f32_16x16x32_bf16 v[70:73], v[210:213], a[112:115], v[70:73]\n"
"ds_read_b128 v[202:205], v7 offset:19456\n"
".Lmk_pg_17:\n"
"s_cbranch_execz .Lmk_ret_3\n"
"s_add_u32 s64, s64, s66\n"
"v_mfma_f32_16x16x32_bf16 v[74:77], v[210:213], a[116:119], v[74:77]\n"
"s_waitcnt lgkmcnt(4)\n"
"buffer_load_dwordx4 a[192:195], v2, s[28:31], s64 offen offset:0 sc1\n"
"v_mfma_f32_16x16x32_bf16 v[78:81], v[214:217], a[112:115], v[78:81]\n"
"s_waitcnt vmcnt(33)\n"
"s_barrier\n"
"ds_read_b128 v[206:209], v7 offset:20480\n"
"v_mfma_f32_16x16x32_bf16 v[82:85], v[214:217], a[116:119], v[82:85]\n"
"s_bfe_u32 s65, s83, 262172\n"
"s_mul_i32 s65, s65, 28672\n"
"s_waitcnt vmcnt(27)\n"
"v_mfma_f32_16x16x32_bf16 v[30:33], v[218:221], a[120:123], v[30:33]\n"
"ds_read_b128 v[210:213], v7 offset:21504\n"
"s_add_u32 s65, s65, s67\n"
"v_mfma_f32_16x16x32_bf16 v[34:37], v[218:221], a[124:127], v[34:37]\n"
"s_add_u32 m0, s54, 28672\n"
"v_mul_f32 v130, v130, v174\n"
"v_mfma_f32_16x16x32_bf16 v[38:41], v[222:225], a[120:123], v[38:41]\n"
"ds_read_b128 v[214:217], v7 offset:22528\n"
"buffer_load_dwordx4 v2, s[28:31], s65 offen offset:0 sc1 lds\n"
"v_mfma_f32_16x16x32_bf16 v[42:45], v[222:225], a[124:127], v[42:45]\n"
"buffer_load_dwordx4 v2, s[28:31], s65 offen offset:1024 sc1 lds\n"
"v_mfma_f32_16x16x32_bf16 v[46:49], v[226:229], a[120:123], v[46:49]\n"
"ds_read_b128 v[218:221], v7 offset:23552\n"
"v_fmamk_f32 v173, v173, 0xbdd2d3e8, v178\n"
"v_mfma_f32_16x16x32_bf16 v[50:53], v[226:229], a[124:127], v[50:53]\n"
"s_waitcnt lgkmcnt(4)\n"
"v_mul_f32 v131, v131, v175\n"
"v_mfma_f32_16x16x32_bf16 v[54:57], v[190:193], a[120:123], v[54:57]\n"
"ds_read_b128 v[222:225], v7 offset:24576\n"
"v_mul_f32 v170, v170, v134\n"
"v_mfma_f32_16x16x32_bf16 v[58:61], v[190:193], a[124:127], v[58:61]\n"
"buffer_load_dwordx4 v2, s[28:31], s65 offen offset:2048 sc1 lds\n"
"v_mfma_f32_16x16x32_bf16 v[62:65], v[194:197], a[120:123], v[62:65]\n"
"ds_read_b128 v[226:229], v7 offset:25600\n"
"v_mul_f32 v132, v132, v176\n"
"v_mfma_f32_16x16x32_bf16 v[66:69], v[194:197], a[124:127], v[66:69]\n"
"v_mul_f32 v171, v171, v135\n"
"v_mul_f32 v133, v133, v177\n"
"v_mfma_f32_16x16x32_bf16 v[70:73], v[198:201], a[120:123], v[70:73]\n"
"ds_read_b128 v[190:193], v7 offset:26624\n"
"v_mul_f32 v172, v172, v136\n"
"v_mfma_f32_16x16x32_bf16 v[74:77], v[198:201], a[124:127], v[74:77]\n"
"buffer_load_dwordx4 v2, s[28:31], s65 offen offset:3072 sc1 lds\n"
"v_mfma_f32_16x16x32_bf16 v[78:81], v[202:205], a[120:123], v[78:81]\n"
"ds_read_b128 v[194:197], v7 offset:27648\n"
"v_fma_f32 v174, v244, v161, -v23\n"
"v_mfma_f32_16x16x32_bf16 v[82:85], v[202:205], a[124:127], v[82:85]\n"
"s_waitcnt lgkmcnt(4)\n"
"v_mul_f32 v173, v173, v137\n"
"v_mfma_f32_16x16x32_bf16 v[30:33], v[206:209], a[128:131], v[30:33]\n"
"ds_read_b128 v[198:201], v7 offset:28672\n"
"v_fma_f32 v175, v245, v161, -v23\n"
"v_mfma_f32_16x16x32_bf16 v[34:37], v[206:209], a[132:135], v[34:37]\n"
"s_add_u32 m0, s54, 32768\n"
"v_exp_f32 v170, v170\n"
"v_mfma_f32_16x16x32_bf16 v[38:41], v[210:213], a[128:131], v[38:41]\n"
"ds_read_b128 v[202:205], v7 offset:29696\n"
"v_mfma_f32_16x16x32_bf16 v[42:45], v[210:213], a[132:135], v[42:45]\n"
"buffer_load_dwordx4 v3, s[28:31], s65 offen offset:0 sc1 lds\n"
"v_mfma_f32_16x16x32_bf16 v[46:49], v[214:217], a[128:131], v[46:49]\n"
"ds_read_b128 v[206:209], v7 offset:30720\n"
"v_fma_f32 v176, v246, v161, -v23\n"
"v_mfma_f32_16x16x32_bf16 v[50:53], v[214:217], a[132:135], v[50:53]\n"
"buffer_load_dwordx4 a[196:199], v2, s[28:31], s64 offen offset:1024 sc1\n"
"v_mfma_f32_16x16x32_bf16 v[54:57], v[218:221], a[128:131], v[54:57]\n"
"ds_read_b128 v[210:213], v7 offset:31744\n"
"v_exp_f32 v171, v171\n"
"v_mfma_f32_16x16x32_bf16 v[58:61], v[218:221], a[132:135], v[58:61]\n"
"s_waitcnt lgkmcnt(4)\n"
"v_fma_f32 v177, v247, v161, -v23\n"
"v_mfma_f32_16x16x32_bf16 v[62:65], v[222:225], a[128:131], v[62:65]\n"
"ds_read_b128 v[214:217], v7 offset:32768\n"
"v_mfma_f32_16x16x32_bf16 v[66:69], v[222:225], a[132:135], v[66:69]\n"
"buffer_load_dwordx4 v3, s[28:31], s65 offen offset:1024 sc1 lds\n"
"v_mfma_f32_16x16x32_bf16 v[70:73], v[226:229], a[128:131], v[70:73]\n"
"ds_read_b128 v[218:221], v7 offset:33792\n"
"v_mfma_f32_16x16x32_bf16 v[74:77], v[226:229], a[132:135], v[74:77]\n"
"v_exp_f32 v172, v172\n"
"v_mfma_f32_16x16x32_bf16 v[78:81], v[190:193], a[128:131], v[78:81]\n"
"ds_read_b128 v[222:225], v7 offset:34816\n"
"v_fma_f32 v138, v138, v160, v174\n"
"v_mfma_f32_16x16x32_bf16 v[82:85], v[190:193], a[132:135], v[82:85]\n"
"buffer_load_dwordx4 a[200:203], v2, s[28:31], s64 offen offset:2048 sc1\n"
"v_mfma_f32_16x16x32_bf16 v[30:33], v[194:197], a[136:139], v[30:33]\n"
"ds_read_b128 v[226:229], v7 offset:35840\n"
"v_exp_f32 v173, v173\n"
"v_mfma_f32_16x16x32_bf16 v[34:37], v[194:197], a[140:143], v[34:37]\n"
"s_waitcnt lgkmcnt(4)\n"
"v_fma_f32 v139, v139, v160, v175\n"
"v_mfma_f32_16x16x32_bf16 v[38:41], v[198:201], a[136:139], v[38:41]\n"
"ds_read_b128 v[190:193], v7 offset:36864\n"
"v_add_f32 v170, 1.0, v170\n"
"v_mfma_f32_16x16x32_bf16 v[42:45], v[198:201], a[140:143], v[42:45]\n"
"buffer_load_dwordx4 v3, s[28:31], s65 offen offset:2048 sc1 lds\n"
"v_mfma_f32_16x16x32_bf16 v[46:49], v[202:205], a[136:139], v[46:49]\n"
"ds_read_b128 v[194:197], v7 offset:37888\n"
"v_fma_f32 v140, v140, v160, v176\n"
"v_mfma_f32_16x16x32_bf16 v[50:53], v[202:205], a[140:143], v[50:53]\n"
"v_add_f32 v171, 1.0, v171\n"
"v_mfma_f32_16x16x32_bf16 v[54:57], v[206:209], a[136:139], v[54:57]\n"
"ds_read_b128 v[198:201], v7 offset:38912\n"
"v_fma_f32 v141, v141, v160, v177\n"
"v_mfma_f32_16x16x32_bf16 v[58:61], v[206:209], a[140:143], v[58:61]\n"
"buffer_load_dwordx4 a[204:207], v2, s[28:31], s64 offen offset:3072 sc1\n"
"v_mfma_f32_16x16x32_bf16 v[62:65], v[210:213], a[136:139], v[62:65]\n"
"ds_read_b128 v[202:205], v7 offset:39936\n"
"v_add_f32 v172, 1.0, v172\n"
"v_mfma_f32_16x16x32_bf16 v[66:69], v[210:213], a[140:143], v[66:69]\n"
"s_waitcnt lgkmcnt(4)\n"
"v_mfma_f32_16x16x32_bf16 v[70:73], v[214:217], a[136:139], v[70:73]\n"
"ds_read_b128 v[206:209], v7 offset:40960\n"
"v_mul_f32 v174, v138, v138\n"
"v_mfma_f32_16x16x32_bf16 v[74:77], v[214:217], a[140:143], v[74:77]\n"
"v_add_f32 v173, 1.0, v173\n"
"v_mfma_f32_16x16x32_bf16 v[78:81], v[218:221], a[136:139], v[78:81]\n"
"ds_read_b128 v[210:213], v7 offset:41984\n"
"v_mfma_f32_16x16x32_bf16 v[82:85], v[218:221], a[140:143], v[82:85]\n"
"buffer_load_dwordx4 a[208:211], v3, s[28:31], s64 offen offset:0 sc1\n"
"s_waitcnt vmcnt(33)\n"
"v_mfma_f32_16x16x32_bf16 v[30:33], v[222:225], a[144:147], v[30:33]\n"
"ds_read_b128 v[214:217], v7 offset:43008\n"
"v_mul_f32 v175, v139, v139\n"
"s_waitcnt vmcnt(31)\n"
"v_mfma_f32_16x16x32_bf16 v[34:37], v[222:225], a[148:151], v[34:37]\n"
"v_rcp_f32 v170, v170\n"
"v_mfma_f32_16x16x32_bf16 v[38:41], v[226:229], a[144:147], v[38:41]\n"
"ds_read_b128 v[218:221], v7 offset:44032\n"
"v_mul_f32 v176, v140, v140\n"
"v_mfma_f32_16x16x32_bf16 v[42:45], v[226:229], a[148:151], v[42:45]\n"
"s_waitcnt lgkmcnt(4)\n"
"v_mfma_f32_16x16x32_bf16 v[46:49], v[190:193], a[144:147], v[46:49]\n"
"ds_read_b128 v[222:225], v7 offset:45056\n"
"v_mfma_f32_16x16x32_bf16 v[50:53], v[190:193], a[148:151], v[50:53]\n"
"buffer_load_dwordx4 a[212:215], v3, s[28:31], s64 offen offset:1024 sc1\n"
"v_mfma_f32_16x16x32_bf16 v[54:57], v[194:197], a[144:147], v[54:57]\n"
"ds_read_b128 v[226:229], v7 offset:46080\n"
"v_mfma_f32_16x16x32_bf16 v[58:61], v[194:197], a[148:151], v[58:61]\n"
"buffer_load_dwordx4 a[216:219], v3, s[28:31], s64 offen offset:2048 sc1\n"
"v_mfma_f32_16x16x32_bf16 v[62:65], v[198:201], a[144:147], v[62:65]\n"
"ds_read_b128 v[190:193], v7 offset:47104\n"
"v_mfma_f32_16x16x32_bf16 v[66:69], v[198:201], a[148:151], v[66:69]\n"
"v_rcp_f32 v171, v171\n"
"v_mfma_f32_16x16x32_bf16 v[70:73], v[202:205], a[144:147], v[70:73]\n"
"ds_read_b128 v[194:197], v7 offset:48128\n"
"v_mul_f32 v177, v141, v141\n"
"v_mfma_f32_16x16x32_bf16 v[74:77], v[202:205], a[148:151], v[74:77]\n"
"s_waitcnt lgkmcnt(4)\n"
"v_rcp_f32 v172, v172\n"
"v_mfma_f32_16x16x32_bf16 v[78:81], v[206:209], a[144:147], v[78:81]\n"
"s_waitcnt vmcnt(31)\n"
"s_barrier\n"
"ds_read_b128 v[198:201], v7 offset:49152\n"
"v_mfma_f32_16x16x32_bf16 v[82:85], v[206:209], a[148:151], v[82:85]\n"
"buffer_load_dwordx4 a[220:223], v3, s[28:31], s64 offen offset:3072 sc1\n"
"s_waitcnt vmcnt(27)\n"
"v_mfma_f32_16x16x32_bf16 v[30:33], v[210:213], a[152:155], v[30:33]\n"
"ds_read_b128 v[202:205], v7 offset:50176\n"
"v_fmamk_f32 v174, v174, 0xbdd2d3e8, v178\n"
"v_mfma_f32_16x16x32_bf16 v[34:37], v[210:213], a[156:159], v[34:37]\n"
"s_bfe_u32 s64, s83, 262172\n"
"s_mul_i32 s64, s64, 8192\n"
"v_mfma_f32_16x16x32_bf16 v[38:41], v[214:217], a[152:155], v[38:41]\n"
"ds_read_b128 v[206:209], v7 offset:51200\n"
"s_add_u32 s64, s64, s66\n"
"v_mfma_f32_16x16x32_bf16 v[42:45], v[214:217], a[156:159], v[42:45]\n"
"buffer_load_dwordx4 a[224:227], v2, s[28:31], s64 offen offset:0 sc1\n"
"v_mfma_f32_16x16x32_bf16 v[46:49], v[218:221], a[152:155], v[46:49]\n"
"ds_read_b128 v[210:213], v7 offset:52224\n"
"v_rcp_f32 v173, v173\n"
"v_mfma_f32_16x16x32_bf16 v[50:53], v[218:221], a[156:159], v[50:53]\n"
"s_waitcnt lgkmcnt(4)\n"
"v_fmamk_f32 v175, v175, 0xbdd2d3e8, v178\n"
"v_mfma_f32_16x16x32_bf16 v[54:57], v[222:225], a[152:155], v[54:57]\n"
"ds_read_b128 v[214:217], v7 offset:53248\n"
"v_mul_f32 v134, v134, v170\n"
"v_mfma_f32_16x16x32_bf16 v[58:61], v[222:225], a[156:159], v[58:61]\n"
"buffer_load_dwordx4 a[228:231], v2, s[28:31], s64 offen offset:1024 sc1\n"
"v_mfma_f32_16x16x32_bf16 v[62:65], v[226:229], a[152:155], v[62:65]\n"
"ds_read_b128 v[218:221], v7 offset:54272\n"
"v_fmamk_f32 v176, v176, 0xbdd2d3e8, v178\n"
"v_mfma_f32_16x16x32_bf16 v[66:69], v[226:229], a[156:159], v[66:69]\n"
"buffer_load_dwordx4 a[232:235], v2, s[28:31], s64 offen offset:2048 sc1\n"
"v_mfma_f32_16x16x32_bf16 v[70:73], v[190:193], a[152:155], v[70:73]\n"
"ds_read_b128 v[222:225], v7 offset:55296\n"
"v_mul_f32 v135, v135, v171\n"
"v_mfma_f32_16x16x32_bf16 v[74:77], v[190:193], a[156:159], v[74:77]\n"
"v_fmamk_f32 v177, v177, 0xbdd2d3e8, v178\n"
"v_mul_f32 v136, v136, v172\n"
"v_mfma_f32_16x16x32_bf16 v[78:81], v[194:197], a[152:155], v[78:81]\n"
"ds_read_b128 v[226:229], v7 offset:56320\n"
"v_mul_f32 v174, v174, v138\n"
"v_mfma_f32_16x16x32_bf16 v[82:85], v[194:197], a[156:159], v[82:85]\n"
"s_waitcnt lgkmcnt(4)\n"
"v_mul_f32 v137, v137, v173\n"
"v_mfma_f32_16x16x32_bf16 v[30:33], v[198:201], a[160:163], v[30:33]\n"
"ds_read_b128 v[190:193], v7 offset:57344\n"
"v_mul_f32 v175, v175, v139\n"
"v_mfma_f32_16x16x32_bf16 v[34:37], v[198:201], a[164:167], v[34:37]\n"
"buffer_load_dwordx4 a[236:239], v2, s[28:31], s64 offen offset:3072 sc1\n"
"v_mfma_f32_16x16x32_bf16 v[38:41], v[202:205], a[160:163], v[38:41]\n"
"ds_read_b128 v[194:197], v7 offset:58368\n"
"v_mul_f32 v176, v176, v140\n"
"v_mfma_f32_16x16x32_bf16 v[42:45], v[202:205], a[164:167], v[42:45]\n"
"buffer_load_dwordx4 a[240:243], v3, s[28:31], s64 offen offset:0 sc1\n"
"v_mfma_f32_16x16x32_bf16 v[46:49], v[206:209], a[160:163], v[46:49]\n"
"ds_read_b128 v[198:201], v7 offset:59392\n"
"v_mul_f32 v177, v177, v141\n"
"v_mfma_f32_16x16x32_bf16 v[50:53], v[206:209], a[164:167], v[50:53]\n"
"v_exp_f32 v174, v174\n"
"v_mfma_f32_16x16x32_bf16 v[54:57], v[210:213], a[160:163], v[54:57]\n"
"ds_read_b128 v[202:205], v7 offset:60416\n"
"v_exp_f32 v175, v175\n"
"v_mfma_f32_16x16x32_bf16 v[58:61], v[210:213], a[164:167], v[58:61]\n"
"s_waitcnt lgkmcnt(4)\n"
"v_exp_f32 v176, v176\n"
"v_mfma_f32_16x16x32_bf16 v[62:65], v[214:217], a[160:163], v[62:65]\n"
"ds_read_b128 v[206:209], v7 offset:61440\n"
"v_mfma_f32_16x16x32_bf16 v[66:69], v[214:217], a[164:167], v[66:69]\n"
"buffer_load_dwordx4 a[244:247], v3, s[28:31], s64 offen offset:1024 sc1\n"
"v_mfma_f32_16x16x32_bf16 v[70:73], v[218:221], a[160:163], v[70:73]\n"
"ds_read_b128 v[210:213], v7 offset:62464\n"
"v_mfma_f32_16x16x32_bf16 v[74:77], v[218:221], a[164:167], v[74:77]\n"
"buffer_load_dwordx4 a[248:251], v3, s[28:31], s64 offen offset:2048 sc1\n"
"v_mfma_f32_16x16x32_bf16 v[78:81], v[222:225], a[160:163], v[78:81]\n"
"ds_read_b128 v[214:217], v7 offset:63488\n"
"v_mfma_f32_16x16x32_bf16 v[82:85], v[222:225], a[164:167], v[82:85]\n"
"v_exp_f32 v177, v177\n"
"v_mfma_f32_16x16x32_bf16 v[30:33], v[226:229], a[168:171], v[30:33]\n"
"ds_read_b128 v[218:221], v7 offset:64512\n"
"v_add_f32 v174, 1.0, v174\n"
"s_waitcnt vmcnt(31)\n"
"v_mfma_f32_16x16x32_bf16 v[34:37], v[226:229], a[172:175], v[34:37]\n"
"s_waitcnt lgkmcnt(4)\n"
"v_add_f32 v175, 1.0, v175\n"
"v_mfma_f32_16x16x32_bf16 v[38:41], v[190:193], a[168:171], v[38:41]\n"
"ds_read_b128 v[222:225], v8 offset:0\n"
"v_add_f32 v176, 1.0, v176\n"
"v_mfma_f32_16x16x32_bf16 v[42:45], v[190:193], a[172:175], v[42:45]\n"
"buffer_load_dwordx4 a[252:255], v3, s[28:31], s64 offen offset:3072 sc1\n"
"v_mfma_f32_16x16x32_bf16 v[46:49], v[194:197], a[168:171], v[46:49]\n"
"ds_read_b128 v[226:229], v8 offset:1024\n"
"v_add_f32 v177, 1.0, v177\n"
"v_mfma_f32_16x16x32_bf16 v[50:53], v[194:197], a[172:175], v[50:53]\n"
"s_mul_i32 s66, s52, 458752\n"
"s_mul_i32 s64, s41, 114688\n"
"v_mfma_f32_16x16x32_bf16 v[54:57], v[198:201], a[168:171], v[54:57]\n"
"ds_read_b128 v[190:193], v8 offset:2048\n"
"v_rcp_f32 v174, v174\n"
"v_mfma_f32_16x16x32_bf16 v[58:61], v[198:201], a[172:175], v[58:61]\n"
"s_add_u32 s66, s66, s64\n"
"s_add_u32 s66, s66, 17825792\n"
"v_mfma_f32_16x16x32_bf16 v[62:65], v[202:205], a[168:171], v[62:65]\n"
"ds_read_b128 v[194:197], v8 offset:3072\n"
"s_bfe_u32 s64, s84, 262144\n"
"s_mul_i32 s64, s64, 14336\n"
"s_add_u32 s64, s64, s66\n"
"buffer_load_dwordx4 a[0:3], v2, s[28:31], s64 offen offset:0 sc1\n"
"v_mfma_f32_16x16x32_bf16 v[66:69], v[202:205], a[172:175], v[66:69]\n"
"s_waitcnt lgkmcnt(4)\n"
"v_rcp_f32 v175, v175\n"
"v_mfma_f32_16x16x32_bf16 v[70:73], v[206:209], a[168:171], v[70:73]\n"
"ds_read_b128 v[198:201], v8 offset:4096\n"
"v_rcp_f32 v176, v176\n"
"v_mfma_f32_16x16x32_bf16 v[74:77], v[206:209], a[172:175], v[74:77]\n"
"buffer_load_dwordx4 a[4:7], v2, s[28:31], s64 offen offset:1024 sc1\n"
"v_mfma_f32_16x16x32_bf16 v[78:81], v[210:213], a[168:171], v[78:81]\n"
"ds_read_b128 v[202:205], v8 offset:5120\n"
"v_mfma_f32_16x16x32_bf16 v[82:85], v[210:213], a[172:175], v[82:85]\n"
"buffer_load_dwordx4 a[8:11], v2, s[28:31], s64 offen offset:2048 sc1\n"
"s_waitcnt vmcnt(32)\n"
"v_mfma_f32_16x16x32_bf16 v[30:33], v[214:217], a[176:179], v[30:33]\n"
"ds_read_b128 v[206:209], v8 offset:6144\n"
"s_waitcnt vmcnt(30)\n"
"v_mfma_f32_16x16x32_bf16 v[34:37], v[214:217], a[180:183], v[34:37]\n"
"v_rcp_f32 v177, v177\n"
"v_mfma_f32_16x16x32_bf16 v[38:41], v[218:221], a[176:179], v[38:41]\n"
"ds_read_b128 v[210:213], v8 offset:7168\n"
"v_mul_f32 v138, v138, v174\n"
"v_mfma_f32_16x16x32_bf16 v[42:45], v[218:221], a[180:183], v[42:45]\n"
"s_waitcnt lgkmcnt(4)\n"
"v_mul_f32 v139, v139, v175\n"
"v_mul_f32 v140, v140, v176\n"
"v_mul_f32 v141, v141, v177\n"
"v_mfma_f32_16x16x32_bf16 v[46:49], v[222:225], a[176:179], v[46:49]\n"
"ds_read_b128 v[214:217], v8 offset:8192\n"
"v_mfma_f32_16x16x32_bf16 v[50:53], v[222:225], a[180:183], v[50:53]\n"
"buffer_load_dwordx4 a[12:15], v2, s[28:31], s64 offen offset:3072 sc1\n"
"v_mfma_f32_16x16x32_bf16 v[54:57], v[226:229], a[176:179], v[54:57]\n"
"ds_read_b128 v[218:221], v8 offset:9216\n"
"v_mfma_f32_16x16x32_bf16 v[58:61], v[226:229], a[180:183], v[58:61]\n"
"buffer_load_dwordx4 a[16:19], v3, s[28:31], s64 offen offset:0 sc1\n"
"v_mfma_f32_16x16x32_bf16 v[62:65], v[190:193], a[176:179], v[62:65]\n"
"ds_read_b128 v[222:225], v8 offset:10240\n"
"v_mfma_f32_16x16x32_bf16 v[66:69], v[190:193], a[180:183], v[66:69]\n"
"v_mfma_f32_16x16x32_bf16 v[70:73], v[194:197], a[176:179], v[70:73]\n"
"ds_read_b128 v[226:229], v8 offset:11264\n"
"v_mfma_f32_16x16x32_bf16 v[74:77], v[194:197], a[180:183], v[74:77]\n"
"s_waitcnt lgkmcnt(4)\n"
"v_mfma_f32_16x16x32_bf16 v[78:81], v[198:201], a[176:179], v[78:81]\n"
"s_waitcnt vmcnt(30)\n"
"s_barrier\n"
"ds_read_b128 v[190:193], v6 offset:0\n"
"v_mfma_f32_16x16x32_bf16 v[82:85], v[198:201], a[180:183], v[82:85]\n"
"buffer_load_dwordx4 a[20:23], v3, s[28:31], s64 offen offset:1024 sc1\n"
"s_waitcnt vmcnt(28)\n"
"v_mfma_f32_16x16x32_bf16 v[30:33], v[202:205], a[184:187], v[30:33]\n"
"ds_read_b128 v[194:197], v6 offset:1024\n"
"v_mfma_f32_16x16x32_bf16 v[34:37], v[202:205], a[188:191], v[34:37]\n"
"buffer_load_dwordx4 a[24:27], v3, s[28:31], s64 offen offset:2048 sc1\n"
"v_mfma_f32_16x16x32_bf16 v[38:41], v[206:209], a[184:187], v[38:41]\n"
"ds_read_b128 v[198:201], v6 offset:6144\n"
"v_mfma_f32_16x16x32_bf16 v[42:45], v[206:209], a[188:191], v[42:45]\n"
"v_mfma_f32_16x16x32_bf16 v[46:49], v[210:213], a[184:187], v[46:49]\n"
"ds_read_b128 v[202:205], v6 offset:7168\n"
"v_mfma_f32_16x16x32_bf16 v[50:53], v[210:213], a[188:191], v[50:53]\n"
"s_waitcnt lgkmcnt(4)\n"
"ds_read_b128 v[236:239], v16 offset:12288\n"
"v_mfma_f32_16x16x32_bf16 v[54:57], v[214:217], a[184:187], v[54:57]\n"
"ds_read_b128 v[206:209], v6 offset:8192\n"
"v_mfma_f32_16x16x32_bf16 v[58:61], v[214:217], a[188:191], v[58:61]\n"
"buffer_load_dwordx4 a[28:31], v3, s[28:31], s64 offen offset:3072 sc1\n"
"v_mfma_f32_16x16x32_bf16 v[62:65], v[218:221], a[184:187], v[62:65]\n"
"ds_read_b128 v[210:213], v6 offset:13312\n"
"v_mfma_f32_16x16x32_bf16 v[66:69], v[218:221], a[188:191], v[66:69]\n"
"v_mfma_f32_16x16x32_bf16 v[70:73], v[222:225], a[184:187], v[70:73]\n"
"ds_read_b128 v[214:217], v6 offset:14336\n"
"v_mfma_f32_16x16x32_bf16 v[74:77], v[222:225], a[188:191], v[74:77]\n"
"buffer_load_dwordx4 a[32:35], v4, s[28:31], s64 offen offset:0 sc1\n"
"v_mfma_f32_16x16x32_bf16 v[78:81], v[226:229], a[184:187], v[78:81]\n"
"ds_read_b128 v[218:221], v6 offset:15360\n"
"ds_read_b128 v[240:243], v16 offset:13312\n"
"v_mfma_f32_16x16x32_bf16 v[82:85], v[226:229], a[188:191], v[82:85]\n"
"s_waitcnt lgkmcnt(6)\n"
"v_mfma_f32_16x16x32_bf16 v[30:33], v[190:193], a[192:195], v[30:33]\n"
"ds_read_b128 v[222:225], v6 offset:20480\n"
"s_waitcnt vmcnt(25)\n"
"v_mfma_f32_16x16x32_bf16 v[34:37], v[190:193], a[196:199], v[34:37]\n"
"buffer_load_dwordx4 a[36:39], v4, s[28:31], s64 offen offset:1024 sc1\n"
"v_mfma_f32_16x16x32_bf16 v[38:41], v[194:197], a[192:195], v[38:41]\n"
"ds_read_b128 v[226:229], v6 offset:21504\n"
"v_mfma_f32_16x16x32_bf16 v[42:45], v[194:197], a[196:199], v[42:45]\n"
"ds_read_b128 v[244:247], v16 offset:14336\n"
"v_mfma_f32_16x16x32_bf16 v[78:81], v[198:201], a[192:195], v[78:81]\n"
"ds_read_b128 v[190:193], v6 offset:22528\n"
"v_mfma_f32_16x16x32_bf16 v[82:85], v[198:201], a[196:199], v[82:85]\n"
"buffer_load_dwordx4 a[40:43], v4, s[28:31], s64 offen offset:2048 sc1\n"
"s_waitcnt vmcnt(25)\n"
"v_mfma_f32_16x16x32_bf16 v[30:33], v[202:205], a[200:203], v[30:33]\n"
"ds_read_b128 v[194:197], v6 offset:27648\n"
"s_waitcnt vmcnt(11)\n"
"v_mfma_f32_16x16x32_bf16 v[34:37], v[202:205], a[204:207], v[34:37]\n"
"s_waitcnt lgkmcnt(6)\n"
"v_mfma_f32_16x16x32_bf16 v[38:41], v[206:209], a[200:203], v[38:41]\n"
"s_barrier\n"
"ds_read_b128 v[198:201], v6 offset:28672\n"
"v_mfma_f32_16x16x32_bf16 v[42:45], v[206:209], a[204:207], v[42:45]\n"
"buffer_load_dwordx4 a[44:47], v4, s[28:31], s64 offen offset:3072 sc1\n"
"v_mfma_f32_16x16x32_bf16 v[78:81], v[210:213], a[200:203], v[78:81]\n"
"ds_read_b128 v[202:205], v6 offset:29696\n"
"ds_read_b128 v[248:251], v16 offset:15360\n"
"v_mfma_f32_16x16x32_bf16 v[82:85], v[210:213], a[204:207], v[82:85]\n"
"v_mfma_f32_16x16x32_bf16 v[30:33], v[214:217], a[208:211], v[30:33]\n"
"ds_read_b128 v[206:209], v6 offset:34816\n"
"v_mfma_f32_16x16x32_bf16 v[34:37], v[214:217], a[212:215], v[34:37]\n"
"buffer_load_dwordx4 a[48:51], v5, s[28:31], s64 offen offset:0 sc1\n"
"v_mfma_f32_16x16x32_bf16 v[38:41], v[218:221], a[208:211], v[38:41]\n"
"ds_read_b128 v[210:213], v6 offset:35840\n"
"v_mfma_f32_16x16x32_bf16 v[42:45], v[218:221], a[212:215], v[42:45]\n"
"s_waitcnt lgkmcnt(5)\n"
"v_mfma_f32_16x16x32_bf16 v[78:81], v[222:225], a[208:211], v[78:81]\n"
"ds_read_b128 v[214:217], v6 offset:36864\n"
"v_mfma_f32_16x16x32_bf16 v[82:85], v[222:225], a[212:215], v[82:85]\n"
"buffer_load_dwordx4 a[52:55], v5, s[28:31], s64 offen offset:1024 sc1\n"
"v_mfma_f32_16x16x32_bf16 v[30:33], v[226:229], a[216:219], v[30:33]\n"
"ds_read_b128 v[218:221], v6 offset:41984\n"
"v_mfma_f32_16x16x32_bf16 v[34:37], v[226:229], a[220:223], v[34:37]\n"
"v_mfma_f32_16x16x32_bf16 v[38:41], v[190:193], a[216:219], v[38:41]\n"
"ds_read_b128 v[222:225], v6 offset:43008\n"
"v_mfma_f32_16x16x32_bf16 v[42:45], v[190:193], a[220:223], v[42:45]\n"
"s_bfe_u32 s64, s84, 262148\n"
"s_mul_i32 s64, s64, 14336\n"
"v_mfma_f32_16x16x32_bf16 v[78:81], v[194:197], a[216:219], v[78:81]\n"
"ds_read_b128 v[226:229], v6 offset:44032\n"
"s_add_u32 s64, s64, s66\n"
"v_mfma_f32_16x16x32_bf16 v[82:85], v[194:197], a[220:223], v[82:85]\n"
"s_waitcnt lgkmcnt(4)\n"
"buffer_load_dwordx4 a[56:59], v2, s[28:31], s64 offen offset:0 sc1\n"
"v_mfma_f32_16x16x32_bf16 v[30:33], v[198:201], a[224:227], v[30:33]\n"
"ds_read_b128 v[190:193], v6 offset:49152\n"
"v_mfma_f32_16x16x32_bf16 v[34:37], v[198:201], a[228:231], v[34:37]\n"
"buffer_load_dwordx4 a[60:63], v2, s[28:31], s64 offen offset:1024 sc1\n"
"v_mfma_f32_16x16x32_bf16 v[38:41], v[202:205], a[224:227], v[38:41]\n"
"ds_read_b128 v[194:197], v6 offset:50176\n"
"v_mfma_f32_16x16x32_bf16 v[42:45], v[202:205], a[228:231], v[42:45]\n"
"v_mfma_f32_16x16x32_bf16 v[78:81], v[206:209], a[224:227], v[78:81]\n"
"ds_read_b128 v[198:201], v6 offset:51200\n"
"v_mfma_f32_16x16x32_bf16 v[82:85], v[206:209], a[228:231], v[82:85]\n"
"buffer_load_dwordx4 a[64:67], v2, s[28:31], s64 offen offset:2048 sc1\n"
"v_mfma_f32_16x16x32_bf16 v[30:33], v[210:213], a[232:235], v[30:33]\n"
"ds_read_b128 v[202:205], v6 offset:56320\n"
"v_mfma_f32_16x16x32_bf16 v[34:37], v[210:213], a[236:239], v[34:37]\n"
"s_waitcnt lgkmcnt(4)\n"
"v_mfma_f32_16x16x32_bf16 v[38:41], v[214:217], a[232:235], v[38:41]\n"
"ds_read_b128 v[206:209], v6 offset:2048\n"
"v_mfma_f32_16x16x32_bf16 v[42:45], v[214:217], a[236:239], v[42:45]\n"
"buffer_load_dwordx4 a[68:71], v2, s[28:31], s64 offen offset:3072 sc1\n"
"v_mfma_f32_16x16x32_bf16 v[78:81], v[218:221], a[232:235], v[78:81]\n"
"ds_read_b128 v[210:213], v6 offset:3072\n"
"v_mfma_f32_16x16x32_bf16 v[82:85], v[218:221], a[236:239], v[82:85]\n"
"v_mfma_f32_16x16x32_bf16 v[30:33], v[222:225], a[240:243], v[30:33]\n"
"ds_read_b128 v[214:217], v6 offset:9216\n"
"v_mfma_f32_16x16x32_bf16 v[34:37], v[222:225], a[244:247], v[34:37]\n"
"buffer_load_dwordx4 a[72:75], v3, s[28:31], s64 offen offset:0 sc1\n"
"v_mfma_f32_16x16x32_bf16 v[38:41], v[226:229], a[240:243], v[38:41]\n"
"ds_read_b128 v[218:221], v6 offset:10240\n"
"v_mfma_f32_16x16x32_bf16 v[42:45], v[226:229], a[244:247], v[42:45]\n"
"s_waitcnt lgkmcnt(4)\n"
"v_mfma_f32_16x16x32_bf16 v[78:81], v[190:193], a[240:243], v[78:81]\n"
"ds_read_b128 v[222:225], v6 offset:16384\n"
"v_mfma_f32_16x16x32_bf16 v[82:85], v[190:193], a[244:247], v[82:85]\n"
"buffer_load_dwordx4 a[76:79], v3, s[28:31], s64 offen offset:1024 sc1\n"
"v_mfma_f32_16x16x32_bf16 v[30:33], v[194:197], a[248:251], v[30:33]\n"
"ds_read_b128 v[226:229], v6 offset:17408\n"
"v_mfma_f32_16x16x32_bf16 v[34:37], v[194:197], a[252:255], v[34:37]\n"
"v_mfma_f32_16x16x32_bf16 v[38:41], v[198:201], a[248:251], v[38:41]\n"
"ds_read_b128 v[190:193], v6 offset:23552\n"
"v_mfma_f32_16x16x32_bf16 v[42:45], v[198:201], a[252:255], v[42:45]\n"
"buffer_load_dwordx4 a[80:83], v3, s[28:31], s64 offen offset:2048 sc1\n"
"v_mfma_f32_16x16x32_bf16 v[78:81], v[202:205], a[248:251], v[78:81]\n"
"ds_read_b128 v[194:197], v6 offset:24576\n"
"v_mfma_f32_16x16x32_bf16 v[82:85], v[202:205], a[252:255], v[82:85]\n"
"s_waitcnt lgkmcnt(4)\n"
"v_mfma_f32_16x16x32_bf16 v[46:49], v[206:209], a[192:195], v[46:49]\n"
"ds_read_b128 v[198:201], v6 offset:30720\n"
"v_mfma_f32_16x16x32_bf16 v[50:53], v[206:209], a[196:199], v[50:53]\n"
"buffer_load_dwordx4 a[84:87], v3, s[28:31], s64 offen offset:3072 sc1\n"
"v_mfma_f32_16x16x32_bf16 v[54:57], v[210:213], a[192:195], v[54:57]\n"
"ds_read_b128 v[202:205], v6 offset:31744\n"
"v_mfma_f32_16x16x32_bf16 v[58:61], v[210:213], a[196:199], v[58:61]\n"
"v_mfma_f32_16x16x32_bf16 v[46:49], v[214:217], a[200:203], v[46:49]\n"
"ds_read_b128 v[206:209], v6 offset:37888\n"
"v_mfma_f32_16x16x32_bf16 v[50:53], v[214:217], a[204:207], v[50:53]\n"
"v_mfma_f32_16x16x32_bf16 v[54:57], v[218:221], a[200:203], v[54:57]\n"
"ds_read_b128 v[210:213], v6 offset:38912\n"
"v_mul_f32 v30, v30, v86\n"
"v_mfma_f32_16x16x32_bf16 v[58:61], v[218:221], a[204:207], v[58:61]\n"
"s_waitcnt lgkmcnt(4)\n"
"v_mul_f32 v31, v31, v87\n"
"v_mfma_f32_16x16x32_bf16 v[46:49], v[222:225], a[208:211], v[46:49]\n"
"ds_read_b128 v[214:217], v6 offset:45056\n"
"v_mul_f32 v32, v32, v88\n"
"v_mfma_f32_16x16x32_bf16 v[50:53], v[222:225], a[212:215], v[50:53]\n"
"v_mul_f32 v33, v33, v89\n"
"v_mul_f32 v34, v34, v90\n"
"v_mfma_f32_16x16x32_bf16 v[54:57], v[226:229], a[208:211], v[54:57]\n"
"ds_read_b128 v[218:221], v6 offset:46080\n"
"v_mul_f32 v35, v35, v91\n"
"v_mfma_f32_16x16x32_bf16 v[58:61], v[226:229], a[212:215], v[58:61]\n"
"v_mul_f32 v36, v36, v92\n"
"v_mul_f32 v37, v37, v93\n"
"v_mfma_f32_16x16x32_bf16 v[46:49], v[190:193], a[216:219], v[46:49]\n"
"ds_read_b128 v[222:225], v6 offset:52224\n"
"v_mul_f32 v38, v38, v94\n"
"v_mfma_f32_16x16x32_bf16 v[50:53], v[190:193], a[220:223], v[50:53]\n"
"v_mul_f32 v39, v39, v95\n"
"v_mul_f32 v40, v40, v96\n"
"v_mfma_f32_16x16x32_bf16 v[54:57], v[194:197], a[216:219], v[54:57]\n"
"ds_read_b128 v[226:229], v6 offset:53248\n"
"v_mul_f32 v41, v41, v97\n"
"v_mfma_f32_16x16x32_bf16 v[58:61], v[194:197], a[220:223], v[58:61]\n"
"s_waitcnt lgkmcnt(4)\n"
"v_mul_f32 v42, v42, v98\n"
"v_mfma_f32_16x16x32_bf16 v[46:49], v[198:201], a[224:227], v[46:49]\n"
"ds_read_b128 v[190:193], v6 offset:4096\n"
"v_mul_f32 v43, v43, v99\n"
"v_mfma_f32_16x16x32_bf16 v[50:53], v[198:201], a[228:231], v[50:53]\n"
"v_mul_f32 v44, v44, v100\n"
"v_mul_f32 v45, v45, v101\n"
"v_mfma_f32_16x16x32_bf16 v[54:57], v[202:205], a[224:227], v[54:57]\n"
"ds_read_b128 v[194:197], v6 offset:5120\n"
"v_cvt_pk_bf16_f32 v86, v30, v31\n"
"v_mfma_f32_16x16x32_bf16 v[58:61], v[202:205], a[228:231], v[58:61]\n"
"v_cvt_pk_bf16_f32 v87, v32, v33\n"
"v_cvt_pk_bf16_f32 v88, v38, v39\n"
"v_mfma_f32_16x16x32_bf16 v[46:49], v[206:209], a[232:235], v[46:49]\n"
"ds_read_b128 v[198:201], v6 offset:11264\n"
"v_cvt_pk_bf16_f32 v89, v40, v41\n"
"v_mfma_f32_16x16x32_bf16 v[50:53], v[206:209], a[236:239], v[50:53]\n"
"buffer_store_dwordx4 v[86:89], v2, s[28:31], s88 offen offset:0 sc1\n"
"v_mfma_f32_16x16x32_bf16 v[54:57], v[210:213], a[232:235], v[54:57]\n"
"ds_read_b128 v[202:205], v6 offset:12288\n"
"v_cvt_pk_bf16_f32 v90, v34, v35\n"
"v_mfma_f32_16x16x32_bf16 v[58:61], v[210:213], a[236:239], v[58:61]\n"
"s_waitcnt lgkmcnt(4)\n"
"v_cvt_pk_bf16_f32 v91, v36, v37\n"
"v_mfma_f32_16x16x32_bf16 v[46:49], v[214:217], a[240:243], v[46:49]\n"
"ds_read_b128 v[206:209], v6 offset:18432\n"
"v_cvt_pk_bf16_f32 v92, v42, v43\n"
"v_mfma_f32_16x16x32_bf16 v[50:53], v[214:217], a[244:247], v[50:53]\n"
"v_cvt_pk_bf16_f32 v93, v44, v45\n"
"v_mfma_f32_16x16x32_bf16 v[54:57], v[218:221], a[240:243], v[54:57]\n"
"ds_read_b128 v[210:213], v6 offset:19456\n"
"v_mfma_f32_16x16x32_bf16 v[58:61], v[218:221], a[244:247], v[58:61]\n"
"buffer_store_dwordx4 v[90:93], v2, s[28:31], s88 offen offset:1024 sc1\n"
"v_mfma_f32_16x16x32_bf16 v[46:49], v[222:225], a[248:251], v[46:49]\n"
"ds_read_b128 v[214:217], v6 offset:25600\n"
"v_mul_f32 v78, v78, v134\n"
"v_mfma_f32_16x16x32_bf16 v[50:53], v[222:225], a[252:255], v[50:53]\n"
"v_mul_f32 v79, v79, v135\n"
"v_mul_f32 v80, v80, v136\n"
"v_mfma_f32_16x16x32_bf16 v[54:57], v[226:229], a[248:251], v[54:57]\n"
"ds_read_b128 v[218:221], v6 offset:26624\n"
"v_mul_f32 v81, v81, v137\n"
"v_mfma_f32_16x16x32_bf16 v[58:61], v[226:229], a[252:255], v[58:61]\n"
"s_waitcnt lgkmcnt(4)\n"
"v_mul_f32 v82, v82, v138\n"
"v_mul_f32 v83, v83, v139\n"
"v_mul_f32 v84, v84, v140\n"
"v_mul_f32 v85, v85, v141\n"
"v_cvt_pk_bf16_f32 v134, v78, v79\n"
"v_cvt_pk_bf16_f32 v135, v80, v81\n"
"v_cvt_pk_bf16_f32 v136, v82, v83\n"
"v_cvt_pk_bf16_f32 v137, v84, v85\n"
"buffer_store_dwordx4 v[134:137], v3, s[28:31], s88 offen offset:2048 sc1\n"
"v_mfma_f32_16x16x32_bf16 v[62:65], v[190:193], a[192:195], v[62:65]\n"
"ds_read_b128 v[222:225], v6 offset:32768\n"
"v_mfma_f32_16x16x32_bf16 v[66:69], v[190:193], a[196:199], v[66:69]\n"
"v_mfma_f32_16x16x32_bf16 v[70:73], v[194:197], a[192:195], v[70:73]\n"
"ds_read_b128 v[226:229], v6 offset:33792\n"
"v_mfma_f32_16x16x32_bf16 v[74:77], v[194:197], a[196:199], v[74:77]\n"
"v_mfma_f32_16x16x32_bf16 v[62:65], v[198:201], a[200:203], v[62:65]\n"
"ds_read_b128 v[190:193], v6 offset:39936\n"
"v_mfma_f32_16x16x32_bf16 v[66:69], v[198:201], a[204:207], v[66:69]\n"
"v_mfma_f32_16x16x32_bf16 v[70:73], v[202:205], a[200:203], v[70:73]\n"
"ds_read_b128 v[194:197], v6 offset:40960\n"
"v_mul_f32 v46, v46, v102\n"
"v_mfma_f32_16x16x32_bf16 v[74:77], v[202:205], a[204:207], v[74:77]\n"
"s_waitcnt lgkmcnt(4)\n"
"v_mul_f32 v47, v47, v103\n"
"v_mfma_f32_16x16x32_bf16 v[62:65], v[206:209], a[208:211], v[62:65]\n"
"ds_read_b128 v[198:201], v6 offset:47104\n"
"v_mul_f32 v48, v48, v104\n"
"v_mfma_f32_16x16x32_bf16 v[66:69], v[206:209], a[212:215], v[66:69]\n"
"v_mul_f32 v49, v49, v105\n"
"v_mul_f32 v50, v50, v106\n"
"v_mfma_f32_16x16x32_bf16 v[70:73], v[210:213], a[208:211], v[70:73]\n"
"ds_read_b128 v[202:205], v6 offset:48128\n"
"v_mul_f32 v51, v51, v107\n"
"v_mfma_f32_16x16x32_bf16 v[74:77], v[210:213], a[212:215], v[74:77]\n"
"v_mul_f32 v52, v52, v108\n"
"v_mfma_f32_16x16x32_bf16 v[62:65], v[214:217], a[216:219], v[62:65]\n"
"ds_read_b128 v[206:209], v6 offset:54272\n"
"v_mul_f32 v53, v53, v109\n"
"v_mfma_f32_16x16x32_bf16 v[66:69], v[214:217], a[220:223], v[66:69]\n"
"v_mul_f32 v54, v54, v110\n"
"v_mfma_f32_16x16x32_bf16 v[70:73], v[218:221], a[216:219], v[70:73]\n"
"ds_read_b128 v[210:213], v6 offset:55296\n"
"v_mul_f32 v55, v55, v111\n"
"v_mfma_f32_16x16x32_bf16 v[74:77], v[218:221], a[220:223], v[74:77]\n"
"s_waitcnt lgkmcnt(4)\n"
"v_mul_f32 v56, v56, v112\n"
"v_mfma_f32_16x16x32_bf16 v[62:65], v[222:225], a[224:227], v[62:65]\n"
"v_mul_f32 v57, v57, v113\n"
"v_mfma_f32_16x16x32_bf16 v[66:69], v[222:225], a[228:231], v[66:69]\n"
"v_mul_f32 v58, v58, v114\n"
"v_mfma_f32_16x16x32_bf16 v[70:73], v[226:229], a[224:227], v[70:73]\n"
"v_mul_f32 v59, v59, v115\n"
"v_mfma_f32_16x16x32_bf16 v[74:77], v[226:229], a[228:231], v[74:77]\n"
"v_mul_f32 v60, v60, v116\n"
"v_mfma_f32_16x16x32_bf16 v[62:65], v[190:193], a[232:235], v[62:65]\n"
"v_mul_f32 v61, v61, v117\n"
"v_mfma_f32_16x16x32_bf16 v[66:69], v[190:193], a[236:239], v[66:69]\n"
"v_cvt_pk_bf16_f32 v102, v46, v47\n"
"v_mfma_f32_16x16x32_bf16 v[70:73], v[194:197], a[232:235], v[70:73]\n"
"v_cvt_pk_bf16_f32 v103, v48, v49\n"
"v_mfma_f32_16x16x32_bf16 v[74:77], v[194:197], a[236:239], v[74:77]\n"
"s_waitcnt lgkmcnt(0)\n"
"v_cvt_pk_bf16_f32 v104, v54, v55\n"
"v_mfma_f32_16x16x32_bf16 v[62:65], v[198:201], a[240:243], v[62:65]\n"
"v_cvt_pk_bf16_f32 v105, v56, v57\n"
".Lmk_pg_18:\n"
"s_cbranch_execz .Lmk_ret_3\n"
"v_mfma_f32_16x16x32_bf16 v[66:69], v[198:201], a[244:247], v[66:69]\n"
"buffer_store_dwordx4 v[102:105], v2, s[28:31], s88 offen offset:2048 sc1\n"
"v_mfma_f32_16x16x32_bf16 v[70:73], v[202:205], a[240:243], v[70:73]\n"
"v_cvt_pk_bf16_f32 v106, v50, v51\n"
"v_mfma_f32_16x16x32_bf16 v[74:77], v[202:205], a[244:247], v[74:77]\n"
"v_cvt_pk_bf16_f32 v107, v52, v53\n"
"v_mfma_f32_16x16x32_bf16 v[62:65], v[206:209], a[248:251], v[62:65]\n"
"v_cvt_pk_bf16_f32 v108, v58, v59\n"
"v_mfma_f32_16x16x32_bf16 v[66:69], v[206:209], a[252:255], v[66:69]\n"
"v_cvt_pk_bf16_f32 v109, v60, v61\n"
"v_mfma_f32_16x16x32_bf16 v[70:73], v[210:213], a[248:251], v[70:73]\n"
"buffer_store_dwordx4 v[106:109], v2, s[28:31], s88 offen offset:3072 sc1\n"
"v_mfma_f32_16x16x32_bf16 v[74:77], v[210:213], a[252:255], v[74:77]\n"
"s_nop 1\n"
"v_pk_mul_f32 v[62:63], v[62:63], v[118:119]\n"
"v_pk_mul_f32 v[64:65], v[64:65], v[120:121]\n"
"v_pk_mul_f32 v[66:67], v[66:67], v[122:123]\n"
"v_pk_mul_f32 v[68:69], v[68:69], v[124:125]\n"
"v_pk_mul_f32 v[70:71], v[70:71], v[126:127]\n"
"v_pk_mul_f32 v[72:73], v[72:73], v[128:129]\n"
"v_pk_mul_f32 v[74:75], v[74:75], v[130:131]\n"
"v_pk_mul_f32 v[76:77], v[76:77], v[132:133]\n"
"v_cvt_pk_bf16_f32 v118, v62, v63\n"
"v_cvt_pk_bf16_f32 v119, v64, v65\n"
"v_cvt_pk_bf16_f32 v120, v70, v71\n"
"v_cvt_pk_bf16_f32 v121, v72, v73\n"
"buffer_store_dwordx4 v[118:121], v3, s[28:31], s88 offen offset:0 sc1\n"
"v_cvt_pk_bf16_f32 v122, v66, v67\n"
"v_cvt_pk_bf16_f32 v123, v68, v69\n"
"v_cvt_pk_bf16_f32 v124, v74, v75\n"
"v_cvt_pk_bf16_f32 v125, v76, v77\n"
"buffer_store_dwordx4 v[122:125], v3, s[28:31], s88 offen offset:1024 sc1\n"
"s_bfe_u32 s64, s84, 262148\n"
"s_mul_i32 s64, s64, 14336\n"
"s_add_u32 s64, s64, s66\n"
"buffer_load_dwordx4 a[88:91], v4, s[28:31], s64 offen offset:0 sc1\n"
"buffer_load_dwordx4 a[92:95], v4, s[28:31], s64 offen offset:1024 sc1\n"
"buffer_load_dwordx4 a[96:99], v4, s[28:31], s64 offen offset:2048 sc1\n"
"buffer_load_dwordx4 a[100:103], v4, s[28:31], s64 offen offset:3072 sc1\n"
"buffer_load_dwordx4 a[104:107], v5, s[28:31], s64 offen offset:0 sc1\n"
"buffer_load_dwordx4 a[108:111], v5, s[28:31], s64 offen offset:1024 sc1\n"
"s_bfe_u32 s64, s84, 262152\n"
"s_mul_i32 s64, s64, 14336\n"
"s_add_u32 s64, s64, s66\n"
"buffer_load_dwordx4 a[112:115], v2, s[28:31], s64 offen offset:0 sc1\n"
"buffer_load_dwordx4 a[116:119], v2, s[28:31], s64 offen offset:1024 sc1\n"
"buffer_load_dwordx4 a[120:123], v2, s[28:31], s64 offen offset:2048 sc1\n"
"buffer_load_dwordx4 a[124:127], v2, s[28:31], s64 offen offset:3072 sc1\n"
"buffer_load_dwordx4 a[128:131], v3, s[28:31], s64 offen offset:0 sc1\n"
"buffer_load_dwordx4 a[132:135], v3, s[28:31], s64 offen offset:1024 sc1\n"
"buffer_load_dwordx4 a[136:139], v3, s[28:31], s64 offen offset:2048 sc1\n"
"buffer_load_dwordx4 a[140:143], v3, s[28:31], s64 offen offset:3072 sc1\n"
"buffer_load_dwordx4 a[144:147], v4, s[28:31], s64 offen offset:0 sc1\n"
"buffer_load_dwordx4 a[148:151], v4, s[28:31], s64 offen offset:1024 sc1\n"
"v_lshlrev_b32 v18, 16, v236\n"
"v_and_b32 v19, 0xffff0000, v236\n"
"v_lshlrev_b32 v20, 16, v237\n"
"v_and_b32 v21, 0xffff0000, v237\n"
"v_lshlrev_b32 v34, 16, v238\n"
"v_and_b32 v35, 0xffff0000, v238\n"
"v_lshlrev_b32 v36, 16, v239\n"
"v_and_b32 v37, 0xffff0000, v239\n"
"v_lshlrev_b32 v22, 16, v240\n"
"v_and_b32 v23, 0xffff0000, v240\n"
"v_lshlrev_b32 v24, 16, v241\n"
"v_and_b32 v25, 0xffff0000, v241\n"
"v_lshlrev_b32 v38, 16, v242\n"
"v_and_b32 v39, 0xffff0000, v242\n"
"v_lshlrev_b32 v40, 16, v243\n"
"v_and_b32 v41, 0xffff0000, v243\n"
"v_lshlrev_b32 v26, 16, v244\n"
"v_and_b32 v27, 0xffff0000, v244\n"
"v_lshlrev_b32 v28, 16, v245\n"
"v_and_b32 v29, 0xffff0000, v245\n"
"v_lshlrev_b32 v42, 16, v246\n"
"v_and_b32 v43, 0xffff0000, v246\n"
"v_lshlrev_b32 v44, 16, v247\n"
"v_and_b32 v45, 0xffff0000, v247\n"
"v_lshlrev_b32 v30, 16, v248\n"
"v_and_b32 v31, 0xffff0000, v248\n"
"v_lshlrev_b32 v32, 16, v249\n"
"v_and_b32 v33, 0xffff0000, v249\n"
"v_lshlrev_b32 v46, 16, v250\n"
"v_and_b32 v47, 0xffff0000, v250\n"
"v_lshlrev_b32 v48, 16, v251\n"
"v_and_b32 v49, 0xffff0000, v251\n"
"s_mov_b64 exec, 0\n"
"s_cmp_eq_u32 s41, 0\n"
"s_cbranch_scc1 .Lmk_pg_19\n"
"s_cmp_eq_u32 s41, 1\n"
"s_cbranch_scc1 .Lmk_pg_20\n"
"s_cmp_eq_u32 s41, 2\n"
"s_cbranch_scc1 .Lmk_ret_4\n"
"s_branch .Lmk_ret_4\n"
".Lmk_ret_4:\n"
"s_mov_b64 exec, -1\n"
"s_nop 0\n"
"s_waitcnt vmcnt(16)\n"
"s_mov_b64 exec, 1\n"
"buffer_atomic_add v10, v9, s[28:31], s70 offen sc1\n"
"s_mov_b64 exec, -1\n"
"v_mov_b32 v11, 0\n"
"s_mov_b64 exec, 1\n"
"buffer_atomic_add v11, v9, s[28:31], s71 offen sc0 sc1\n"
"s_mov_b64 exec, -1\n"
"s_bfe_u32 s64, s84, 262152\n"
"s_mul_i32 s64, s64, 14336\n"
"s_add_u32 s64, s64, s66\n"
"buffer_load_dwordx4 a[152:155], v4, s[28:31], s64 offen offset:2048 sc1\n"
"buffer_load_dwordx4 a[156:159], v4, s[28:31], s64 offen offset:3072 sc1\n"
"buffer_load_dwordx4 a[160:163], v5, s[28:31], s64 offen offset:0 sc1\n"
"buffer_load_dwordx4 a[164:167], v5, s[28:31], s64 offen offset:1024 sc1\n"
"s_bfe_u32 s64, s84, 262156\n"
"s_mul_i32 s64, s64, 14336\n"
"s_add_u32 s64, s64, s66\n"
"buffer_load_dwordx4 a[168:171], v2, s[28:31], s64 offen offset:0 sc1\n"
"buffer_load_dwordx4 a[172:175], v2, s[28:31], s64 offen offset:1024 sc1\n"
"buffer_load_dwordx4 a[176:179], v2, s[28:31], s64 offen offset:2048 sc1\n"
"buffer_load_dwordx4 a[180:183], v2, s[28:31], s64 offen offset:3072 sc1\n"
"buffer_load_dwordx4 a[184:187], v3, s[28:31], s64 offen offset:0 sc1\n"
"buffer_load_dwordx4 a[188:191], v3, s[28:31], s64 offen offset:1024 sc1\n"
"s_waitcnt vmcnt(10)\n"
"v_readfirstlane_b32 s61, v11\n"
"s_nop 0\n"
"s_cmp_ge_u32 s61, 8\n"
"s_cbranch_scc0 .Lmk_spin_40\n"
".Lmk_back_41:\n"
"s_bfe_u32 s65, s84, 262144\n"
"s_mul_i32 s65, s65, 28672\n"
"s_add_u32 s65, s65, s69\n"
"s_add_u32 m0, s54, 57344\n"
"s_nop 0\n"
"buffer_load_dwordx4 v2, s[28:31], s65 offen offset:0 sc1 lds\n"
"buffer_load_dwordx4 v2, s[28:31], s65 offen offset:1024 sc1 lds\n"
"buffer_load_dwordx4 v2, s[28:31], s65 offen offset:2048 sc1 lds\n"
"buffer_load_dwordx4 v2, s[28:31], s65 offen offset:3072 sc1 lds\n"
"s_add_u32 m0, s54, 61440\n"
"s_nop 0\n"
"buffer_load_dwordx4 v3, s[28:31], s65 offen offset:0 sc1 lds\n"
"buffer_load_dwordx4 v3, s[28:31], s65 offen offset:1024 sc1 lds\n"
"buffer_load_dwordx4 v3, s[28:31], s65 offen offset:2048 sc1 lds\n"
"s_bfe_u32 s65, s84, 262148\n"
"s_mul_i32 s65, s65, 28672\n"
"s_add_u32 s65, s65, s69\n"
"s_add_u32 m0, s54, 86016\n"
"s_nop 0\n"
"buffer_load_dwordx4 v2, s[28:31], s65 offen offset:0 sc1 lds\n"
"buffer_load_dwordx4 v2, s[28:31], s65 offen offset:1024 sc1 lds\n"
"buffer_load_dwordx4 v2, s[28:31], s65 offen offset:2048 sc1 lds\n"
"buffer_load_dwordx4 v2, s[28:31], s65 offen offset:3072 sc1 lds\n"
"s_add_u32 m0, s54, 90112\n"
"s_nop 0\n"
"buffer_load_dwordx4 v3, s[28:31], s65 offen offset:0 sc1 lds\n"
"buffer_load_dwordx4 v3, s[28:31], s65 offen offset:1024 sc1 lds\n"
"buffer_load_dwordx4 v3, s[28:31], s65 offen offset:2048 sc1 lds\n"
"s_waitcnt vmcnt(7)\n"
"s_barrier\n"
"ds_read_b128 v[190:193], v6 offset:57344\n"
"ds_read_b128 v[194:197], v6 offset:58368\n"
"ds_read_b128 v[198:201], v6 offset:64512\n"
"ds_read_b128 v[202:205], v7 offset:0\n"
"ds_read_b128 v[206:209], v6 offset:59392\n"
"ds_read_b128 v[210:213], v6 offset:60416\n"
"ds_read_b128 v[214:217], v7 offset:1024\n"
"ds_read_b128 v[218:221], v7 offset:2048\n"
"s_waitcnt lgkmcnt(7)\n"
"v_mfma_f32_16x16x32_bf16 v[18:21], a[0:3], v[190:193], v[18:21]\n"
"ds_read_b128 v[222:225], v6 offset:61440\n"
"v_mfma_f32_16x16x32_bf16 v[34:37], a[4:7], v[190:193], v[34:37]\n"
"s_bfe_u32 s65, s84, 262152\n"
"s_mul_i32 s65, s65, 28672\n"
"s_waitcnt lgkmcnt(7)\n"
"v_mfma_f32_16x16x32_bf16 v[22:25], a[0:3], v[194:197], v[22:25]\n"
"ds_read_b128 v[226:229], v6 offset:62464\n"
"s_add_u32 s65, s65, s69\n"
"v_mfma_f32_16x16x32_bf16 v[38:41], a[4:7], v[194:197], v[38:41]\n"
"s_add_u32 m0, s54, 114688\n"
"s_nop 0\n"
"buffer_load_dwordx4 v2, s[28:31], s65 offen offset:0 sc1 lds\n"
"s_waitcnt lgkmcnt(7)\n"
"v_mfma_f32_16x16x32_bf16 v[26:29], a[0:3], v[198:201], v[26:29]\n"
"ds_read_b128 v[190:193], v7 offset:3072\n"
"v_mfma_f32_16x16x32_bf16 v[42:45], a[4:7], v[198:201], v[42:45]\n"
"buffer_load_dwordx4 v2, s[28:31], s65 offen offset:1024 sc1 lds\n"
"s_waitcnt lgkmcnt(7)\n"
"v_mfma_f32_16x16x32_bf16 v[30:33], a[0:3], v[202:205], v[30:33]\n"
"ds_read_b128 v[194:197], v7 offset:4096\n"
"v_mfma_f32_16x16x32_bf16 v[46:49], a[4:7], v[202:205], v[46:49]\n"
"s_waitcnt lgkmcnt(4)\n"
"v_mfma_f32_16x16x32_bf16 v[18:21], a[8:11], v[206:209], v[18:21]\n"
"ds_read_b128 v[198:201], v7 offset:6144\n"
"v_mfma_f32_16x16x32_bf16 v[34:37], a[12:15], v[206:209], v[34:37]\n"
"buffer_load_dwordx4 v2, s[28:31], s65 offen offset:2048 sc1 lds\n"
"v_mfma_f32_16x16x32_bf16 v[22:25], a[8:11], v[210:213], v[22:25]\n"
"ds_read_b128 v[202:205], v7 offset:7168\n"
"v_mfma_f32_16x16x32_bf16 v[38:41], a[12:15], v[210:213], v[38:41]\n"
"v_mfma_f32_16x16x32_bf16 v[26:29], a[8:11], v[214:217], v[26:29]\n"
"ds_read_b128 v[206:209], v7 offset:13312\n"
"v_mfma_f32_16x16x32_bf16 v[42:45], a[12:15], v[214:217], v[42:45]\n"
"buffer_load_dwordx4 v2, s[28:31], s65 offen offset:3072 sc1 lds\n"
"v_mfma_f32_16x16x32_bf16 v[30:33], a[8:11], v[218:221], v[30:33]\n"
"ds_read_b128 v[210:213], v7 offset:14336\n"
"v_mfma_f32_16x16x32_bf16 v[46:49], a[12:15], v[218:221], v[46:49]\n"
"s_waitcnt lgkmcnt(4)\n"
"v_mfma_f32_16x16x32_bf16 v[18:21], a[16:19], v[222:225], v[18:21]\n"
"ds_read_b128 v[214:217], v7 offset:8192\n"
"s_add_u32 m0, s54, 118784\n"
"v_mfma_f32_16x16x32_bf16 v[34:37], a[20:23], v[222:225], v[34:37]\n"
"v_mfma_f32_16x16x32_bf16 v[22:25], a[16:19], v[226:229], v[22:25]\n"
"ds_read_b128 v[218:221], v7 offset:9216\n"
"v_mfma_f32_16x16x32_bf16 v[38:41], a[20:23], v[226:229], v[38:41]\n"
"buffer_load_dwordx4 v3, s[28:31], s65 offen offset:0 sc1 lds\n"
"v_mfma_f32_16x16x32_bf16 v[26:29], a[16:19], v[190:193], v[26:29]\n"
"ds_read_b128 v[222:225], v7 offset:15360\n"
"v_mfma_f32_16x16x32_bf16 v[42:45], a[20:23], v[190:193], v[42:45]\n"
"buffer_load_dwordx4 v3, s[28:31], s65 offen offset:1024 sc1 lds\n"
"v_mfma_f32_16x16x32_bf16 v[30:33], a[16:19], v[194:197], v[30:33]\n"
"ds_read_b128 v[226:229], v7 offset:16384\n"
"v_mfma_f32_16x16x32_bf16 v[46:49], a[20:23], v[194:197], v[46:49]\n"
"s_waitcnt lgkmcnt(4)\n"
"v_mfma_f32_16x16x32_bf16 v[18:21], a[24:27], v[198:201], v[18:21]\n"
"ds_read_b128 v[190:193], v7 offset:10240\n"
"v_mfma_f32_16x16x32_bf16 v[34:37], a[28:31], v[198:201], v[34:37]\n"
"buffer_load_dwordx4 v3, s[28:31], s65 offen offset:2048 sc1 lds\n"
"v_mfma_f32_16x16x32_bf16 v[22:25], a[24:27], v[202:205], v[22:25]\n"
"ds_read_b128 v[194:197], v7 offset:11264\n"
"v_mfma_f32_16x16x32_bf16 v[38:41], a[28:31], v[202:205], v[38:41]\n"
"s_bfe_u32 s65, s84, 262156\n"
"s_mul_i32 s65, s65, 28672\n"
"v_mfma_f32_16x16x32_bf16 v[26:29], a[24:27], v[206:209], v[26:29]\n"
"ds_read_b128 v[198:201], v7 offset:17408\n"
"s_add_u32 s65, s65, s69\n"
"v_mfma_f32_16x16x32_bf16 v[42:45], a[28:31], v[206:209], v[42:45]\n"
"s_add_u32 m0, s54, 0\n"
"v_mfma_f32_16x16x32_bf16 v[30:33], a[24:27], v[210:213], v[30:33]\n"
"ds_read_b128 v[202:205], v7 offset:18432\n"
"buffer_load_dwordx4 v2, s[28:31], s65 offen offset:0 sc1 lds\n"
"v_mfma_f32_16x16x32_bf16 v[46:49], a[28:31], v[210:213], v[46:49]\n"
"s_waitcnt lgkmcnt(4)\n"
"v_mfma_f32_16x16x32_bf16 v[18:21], a[32:35], v[214:217], v[18:21]\n"
"ds_read_b64 v[206:207], v6 offset:63488\n"
"ds_read_b64 v[208:209], v7 offset:12288\n"
"v_mfma_f32_16x16x32_bf16 v[34:37], a[36:39], v[214:217], v[34:37]\n"
"buffer_load_dwordx4 v2, s[28:31], s65 offen offset:1024 sc1 lds\n"
"v_mfma_f32_16x16x32_bf16 v[22:25], a[32:35], v[218:221], v[22:25]\n"
"ds_read_b64 v[210:211], v6 offset:63496\n"
"ds_read_b64 v[212:213], v7 offset:12296\n"
"v_mfma_f32_16x16x32_bf16 v[38:41], a[36:39], v[218:221], v[38:41]\n"
"buffer_load_dwordx4 v2, s[28:31], s65 offen offset:2048 sc1 lds\n"
"v_mfma_f32_16x16x32_bf16 v[26:29], a[32:35], v[222:225], v[26:29]\n"
"ds_read_b64 v[214:215], v7 offset:5120\n"
"ds_read_b64 v[216:217], v7 offset:19456\n"
"v_mfma_f32_16x16x32_bf16 v[42:45], a[36:39], v[222:225], v[42:45]\n"
"v_mfma_f32_16x16x32_bf16 v[30:33], a[32:35], v[226:229], v[30:33]\n"
"ds_read_b64 v[218:219], v7 offset:5128\n"
"ds_read_b64 v[220:221], v7 offset:19464\n"
"v_mfma_f32_16x16x32_bf16 v[46:49], a[36:39], v[226:229], v[46:49]\n"
"s_waitcnt lgkmcnt(8)\n"
"v_mfma_f32_16x16x32_bf16 v[18:21], a[40:43], v[190:193], v[18:21]\n"
"s_waitcnt vmcnt(10)\n"
"s_barrier\n"
"ds_read_b128 v[222:225], v7 offset:20480\n"
"v_mfma_f32_16x16x32_bf16 v[34:37], a[44:47], v[190:193], v[34:37]\n"
"buffer_load_dwordx4 v2, s[28:31], s65 offen offset:3072 sc1 lds\n"
"v_mfma_f32_16x16x32_bf16 v[22:25], a[40:43], v[194:197], v[22:25]\n"
"ds_read_b128 v[226:229], v7 offset:21504\n"
"s_add_u32 m0, s54, 4096\n"
"v_mfma_f32_16x16x32_bf16 v[38:41], a[44:47], v[194:197], v[38:41]\n"
"v_mfma_f32_16x16x32_bf16 v[26:29], a[40:43], v[198:201], v[26:29]\n"
"ds_read_b128 v[190:193], v7 offset:27648\n"
"v_mfma_f32_16x16x32_bf16 v[42:45], a[44:47], v[198:201], v[42:45]\n"
"buffer_load_dwordx4 v3, s[28:31], s65 offen offset:0 sc1 lds\n"
"v_mfma_f32_16x16x32_bf16 v[30:33], a[40:43], v[202:205], v[30:33]\n"
"ds_read_b128 v[194:197], v7 offset:28672\n"
"v_mfma_f32_16x16x32_bf16 v[46:49], a[44:47], v[202:205], v[46:49]\n"
"s_waitcnt lgkmcnt(4)\n"
"v_mfma_f32_16x16x32_bf16 v[18:21], a[48:51], v[206:209], v[18:21]\n"
"ds_read_b128 v[198:201], v7 offset:22528\n"
"v_mfma_f32_16x16x32_bf16 v[34:37], a[52:55], v[206:209], v[34:37]\n"
"buffer_load_dwordx4 v3, s[28:31], s65 offen offset:1024 sc1 lds\n"
"v_mfma_f32_16x16x32_bf16 v[22:25], a[48:51], v[210:213], v[22:25]\n"
"ds_read_b128 v[202:205], v7 offset:23552\n"
"v_lshrrev_b32 v50, 4, v1\n"
"v_mfma_f32_16x16x32_bf16 v[38:41], a[52:55], v[210:213], v[38:41]\n"
"buffer_load_dwordx4 v3, s[28:31], s65 offen offset:2048 sc1 lds\n"
"v_mfma_f32_16x16x32_bf16 v[26:29], a[48:51], v[214:217], v[26:29]\n"
"ds_read_b128 v[206:209], v7 offset:29696\n"
"v_mfma_f32_16x16x32_bf16 v[42:45], a[52:55], v[214:217], v[42:45]\n"
"v_mfma_f32_16x16x32_bf16 v[30:33], a[48:51], v[218:221], v[30:33]\n"
"ds_read_b128 v[210:213], v7 offset:30720\n"
"s_lshl_b32 s57, s41, 2\n"
"v_mfma_f32_16x16x32_bf16 v[46:49], a[52:55], v[218:221], v[46:49]\n"
"s_waitcnt lgkmcnt(4)\n"
"v_mfma_f32_16x16x32_bf16 v[18:21], a[56:59], v[222:225], v[18:21]\n"
"ds_read_b128 v[214:217], v7 offset:24576\n"
"v_mfma_f32_16x16x32_bf16 v[34:37], a[60:63], v[222:225], v[34:37]\n"
"s_bfe_u32 s65, s84, 262160\n"
"s_mul_i32 s65, s65, 28672\n"
"s_add_u32 s65, s65, s69\n"
"s_add_u32 m0, s54, 28672\n"
"s_nop 0\n"
"buffer_load_dwordx4 v2, s[28:31], s65 offen offset:0 sc1 lds\n"
"v_mfma_f32_16x16x32_bf16 v[22:25], a[56:59], v[226:229], v[22:25]\n"
"ds_read_b128 v[218:221], v7 offset:25600\n"
"v_add_u32 v50, s57, v50\n"
"v_mfma_f32_16x16x32_bf16 v[38:41], a[60:63], v[226:229], v[38:41]\n"
"buffer_load_dwordx4 v2, s[28:31], s65 offen offset:1024 sc1 lds\n"
"v_mfma_f32_16x16x32_bf16 v[26:29], a[56:59], v[190:193], v[26:29]\n"
"ds_read_b128 v[222:225], v7 offset:31744\n"
"v_mfma_f32_16x16x32_bf16 v[42:45], a[60:63], v[190:193], v[42:45]\n"
"buffer_load_dwordx4 v2, s[28:31], s65 offen offset:2048 sc1 lds\n"
"v_mfma_f32_16x16x32_bf16 v[30:33], a[56:59], v[194:197], v[30:33]\n"
"ds_read_b128 v[226:229], v7 offset:32768\n"
"v_xor_b32 v50, v50, v1\n"
"v_mfma_f32_16x16x32_bf16 v[46:49], a[60:63], v[194:197], v[46:49]\n"
"s_waitcnt lgkmcnt(4)\n"
"v_mfma_f32_16x16x32_bf16 v[18:21], a[64:67], v[198:201], v[18:21]\n"
"ds_read_b128 v[190:193], v7 offset:34816\n"
"v_mfma_f32_16x16x32_bf16 v[34:37], a[68:71], v[198:201], v[34:37]\n"
"s_bfe_u32 s64, s84, 262156\n"
"s_mul_i32 s64, s64, 14336\n"
"v_mfma_f32_16x16x32_bf16 v[22:25], a[64:67], v[202:205], v[22:25]\n"
"ds_read_b128 v[194:197], v7 offset:35840\n"
"s_add_u32 s64, s64, s66\n"
"v_mfma_f32_16x16x32_bf16 v[38:41], a[68:71], v[202:205], v[38:41]\n"
"buffer_load_dwordx4 a[192:195], v3, s[28:31], s64 offen offset:2048 sc1\n"
"v_mfma_f32_16x16x32_bf16 v[26:29], a[64:67], v[206:209], v[26:29]\n"
"ds_read_b128 v[198:201], v7 offset:41984\n"
"v_and_b32 v50, 15, v50\n"
"v_mfma_f32_16x16x32_bf16 v[42:45], a[68:71], v[206:209], v[42:45]\n"
"buffer_load_dwordx4 a[196:199], v3, s[28:31], s64 offen offset:3072 sc1\n"
"v_mfma_f32_16x16x32_bf16 v[30:33], a[64:67], v[210:213], v[30:33]\n"
"ds_read_b128 v[202:205], v7 offset:43008\n"
"v_mfma_f32_16x16x32_bf16 v[46:49], a[68:71], v[210:213], v[46:49]\n"
"s_waitcnt lgkmcnt(4)\n"
"v_bfe_u32 v15, v1, 0, 4\n"
"v_mfma_f32_16x16x32_bf16 v[18:21], a[72:75], v[214:217], v[18:21]\n"
"ds_read_b128 v[206:209], v7 offset:36864\n"
"v_mfma_f32_16x16x32_bf16 v[34:37], a[76:79], v[214:217], v[34:37]\n"
"buffer_load_dwordx4 v2, s[28:31], s65 offen offset:3072 sc1 lds\n"
"v_mfma_f32_16x16x32_bf16 v[22:25], a[72:75], v[218:221], v[22:25]\n"
"ds_read_b128 v[210:213], v7 offset:37888\n"
"v_mfma_f32_16x16x32_bf16 v[38:41], a[76:79], v[218:221], v[38:41]\n"
"v_mul_u32_u24 v51, 256, v15\n"
"v_mfma_f32_16x16x32_bf16 v[26:29], a[72:75], v[222:225], v[26:29]\n"
"ds_read_b128 v[214:217], v7 offset:44032\n"
"v_mfma_f32_16x16x32_bf16 v[42:45], a[76:79], v[222:225], v[42:45]\n"
"s_add_u32 m0, s54, 32768\n"
"v_mfma_f32_16x16x32_bf16 v[30:33], a[72:75], v[226:229], v[30:33]\n"
"ds_read_b128 v[218:221], v7 offset:45056\n"
"buffer_load_dwordx4 v3, s[28:31], s65 offen offset:0 sc1 lds\n"
"v_mfma_f32_16x16x32_bf16 v[46:49], a[76:79], v[226:229], v[46:49]\n"
"s_waitcnt lgkmcnt(4)\n"
"v_mfma_f32_16x16x32_bf16 v[18:21], a[80:83], v[190:193], v[18:21]\n"
"ds_read_b128 v[222:225], v7 offset:38912\n"
"v_add_u32 v51, 143360, v51\n"
"v_mfma_f32_16x16x32_bf16 v[34:37], a[84:87], v[190:193], v[34:37]\n"
"buffer_load_dwordx4 a[200:203], v4, s[28:31], s64 offen offset:0 sc1\n"
"v_mfma_f32_16x16x32_bf16 v[22:25], a[80:83], v[194:197], v[22:25]\n"
"ds_read_b128 v[226:229], v7 offset:39936\n"
"v_mfma_f32_16x16x32_bf16 v[38:41], a[84:87], v[194:197], v[38:41]\n"
"v_mfma_f32_16x16x32_bf16 v[26:29], a[80:83], v[198:201], v[26:29]\n"
"ds_read_b128 v[190:193], v7 offset:46080\n"
"v_lshl_add_u32 v50, v50, 4, v51\n"
"v_mfma_f32_16x16x32_bf16 v[42:45], a[84:87], v[198:201], v[42:45]\n"
"buffer_load_dwordx4 a[204:207], v4, s[28:31], s64 offen offset:1024 sc1\n"
"v_mfma_f32_16x16x32_bf16 v[30:33], a[80:83], v[202:205], v[30:33]\n"
"ds_read_b128 v[194:197], v7 offset:47104\n"
"v_mfma_f32_16x16x32_bf16 v[46:49], a[84:87], v[202:205], v[46:49]\n"
"s_waitcnt lgkmcnt(4)\n"
"v_mfma_f32_16x16x32_bf16 v[18:21], a[88:91], v[206:209], v[18:21]\n"
"ds_read_b64 v[198:199], v7 offset:26624\n"
"ds_read_b64 v[200:201], v7 offset:40960\n"
"v_mfma_f32_16x16x32_bf16 v[34:37], a[92:95], v[206:209], v[34:37]\n"
"buffer_load_dwordx4 v3, s[28:31], s65 offen offset:1024 sc1 lds\n"
"v_mfma_f32_16x16x32_bf16 v[22:25], a[88:91], v[210:213], v[22:25]\n"
"ds_read_b64 v[202:203], v7 offset:26632\n"
"ds_read_b64 v[204:205], v7 offset:40968\n"
"v_mfma_f32_16x16x32_bf16 v[38:41], a[92:95], v[210:213], v[38:41]\n"
"s_and_b32 s58, s41, 1\n"
"v_mfma_f32_16x16x32_bf16 v[26:29], a[88:91], v[214:217], v[26:29]\n"
"ds_read_b64 v[206:207], v7 offset:33792\n"
"ds_read_b64 v[208:209], v7 offset:48128\n"
"v_mfma_f32_16x16x32_bf16 v[42:45], a[92:95], v[214:217], v[42:45]\n"
"buffer_load_dwordx4 v3, s[28:31], s65 offen offset:2048 sc1 lds\n"
"v_mfma_f32_16x16x32_bf16 v[30:33], a[88:91], v[218:221], v[30:33]\n"
"ds_read_b64 v[210:211], v7 offset:33800\n"
"ds_read_b64 v[212:213], v7 offset:48136\n"
"v_mfma_f32_16x16x32_bf16 v[46:49], a[92:95], v[218:221], v[46:49]\n"
"s_waitcnt lgkmcnt(8)\n"
"s_lshl_b32 s57, s58, 3\n"
"v_mfma_f32_16x16x32_bf16 v[18:21], a[96:99], v[222:225], v[18:21]\n"
"s_waitcnt vmcnt(18)\n"
"s_barrier\n"
"ds_read_b128 v[214:217], v7 offset:49152\n"
"v_mfma_f32_16x16x32_bf16 v[34:37], a[100:103], v[222:225], v[34:37]\n"
"buffer_load_dwordx4 a[208:211], v4, s[28:31], s64 offen offset:2048 sc1\n"
"v_mfma_f32_16x16x32_bf16 v[22:25], a[96:99], v[226:229], v[22:25]\n"
"ds_read_b128 v[218:221], v7 offset:50176\n"
"v_lshrrev_b32 v51, 4, v1\n"
"v_mfma_f32_16x16x32_bf16 v[38:41], a[100:103], v[226:229], v[38:41]\n"
"buffer_load_dwordx4 a[212:215], v4, s[28:31], s64 offen offset:3072 sc1\n"
"v_mfma_f32_16x16x32_bf16 v[26:29], a[96:99], v[190:193], v[26:29]\n"
"ds_read_b128 v[222:225], v7 offset:56320\n"
"v_mfma_f32_16x16x32_bf16 v[42:45], a[100:103], v[190:193], v[42:45]\n"
"v_add_u32 v51, s57, v51\n"
"v_mfma_f32_16x16x32_bf16 v[30:33], a[96:99], v[194:197], v[30:33]\n"
"ds_read_b128 v[226:229], v7 offset:57344\n"
"v_mfma_f32_16x16x32_bf16 v[46:49], a[100:103], v[194:197], v[46:49]\n"
"s_waitcnt lgkmcnt(4)\n"
"v_mfma_f32_16x16x32_bf16 v[18:21], a[104:107], v[198:201], v[18:21]\n"
"ds_read_b128 v[190:193], v7 offset:51200\n"
"v_mfma_f32_16x16x32_bf16 v[34:37], a[108:111], v[198:201], v[34:37]\n"
"buffer_load_dwordx4 a[216:219], v5, s[28:31], s64 offen offset:0 sc1\n"
"v_mfma_f32_16x16x32_bf16 v[22:25], a[104:107], v[202:205], v[22:25]\n"
"ds_read_b128 v[194:197], v7 offset:52224\n"
"v_xor_b32 v51, v51, v1\n"
"v_mfma_f32_16x16x32_bf16 v[38:41], a[108:111], v[202:205], v[38:41]\n"
"buffer_load_dwordx4 a[220:223], v5, s[28:31], s64 offen offset:1024 sc1\n"
"v_mfma_f32_16x16x32_bf16 v[26:29], a[104:107], v[206:209], v[26:29]\n"
"ds_read_b128 v[198:201], v7 offset:58368\n"
"v_mfma_f32_16x16x32_bf16 v[42:45], a[108:111], v[206:209], v[42:45]\n"
"v_mfma_f32_16x16x32_bf16 v[30:33], a[104:107], v[210:213], v[30:33]\n"
"ds_read_b128 v[202:205], v7 offset:59392\n"
"v_and_b32 v51, 15, v51\n"
"v_mfma_f32_16x16x32_bf16 v[46:49], a[108:111], v[210:213], v[46:49]\n"
"s_waitcnt lgkmcnt(4)\n"
"v_mfma_f32_16x16x32_bf16 v[18:21], a[112:115], v[214:217], v[18:21]\n"
"ds_read_b128 v[206:209], v7 offset:53248\n"
"v_mfma_f32_16x16x32_bf16 v[34:37], a[116:119], v[214:217], v[34:37]\n"
"s_bfe_u32 s64, s84, 262160\n"
"s_mul_i32 s64, s64, 14336\n"
"v_mfma_f32_16x16x32_bf16 v[22:25], a[112:115], v[218:221], v[22:25]\n"
"ds_read_b128 v[210:213], v7 offset:54272\n"
"s_add_u32 s64, s64, s66\n"
"buffer_load_dwordx4 a[224:227], v2, s[28:31], s64 offen offset:0 sc1\n"
"v_mfma_f32_16x16x32_bf16 v[38:41], a[116:119], v[218:221], v[38:41]\n"
"buffer_load_dwordx4 a[228:231], v2, s[28:31], s64 offen offset:1024 sc1\n"
"v_mfma_f32_16x16x32_bf16 v[26:29], a[112:115], v[222:225], v[26:29]\n"
"ds_read_b128 v[214:217], v7 offset:60416\n"
"s_mul_i32 s57, s41, 2048\n"
"v_mfma_f32_16x16x32_bf16 v[42:45], a[116:119], v[222:225], v[42:45]\n"
"v_mfma_f32_16x16x32_bf16 v[30:33], a[112:115], v[226:229], v[30:33]\n"
"ds_read_b128 v[218:221], v7 offset:61440\n"
"s_add_u32 s57, s57, 143360\n"
"v_mfma_f32_16x16x32_bf16 v[46:49], a[116:119], v[226:229], v[46:49]\n"
"s_waitcnt lgkmcnt(4)\n"
"v_mfma_f32_16x16x32_bf16 v[18:21], a[120:123], v[190:193], v[18:21]\n"
"ds_read_b128 v[222:225], v7 offset:63488\n"
"v_mfma_f32_16x16x32_bf16 v[34:37], a[124:127], v[190:193], v[34:37]\n"
"buffer_load_dwordx4 a[232:235], v2, s[28:31], s64 offen offset:2048 sc1\n"
"v_mfma_f32_16x16x32_bf16 v[22:25], a[120:123], v[194:197], v[22:25]\n"
"ds_read_b128 v[226:229], v7 offset:64512\n"
"v_bfe_u32 v15, v1, 4, 2\n"
"v_mfma_f32_16x16x32_bf16 v[38:41], a[124:127], v[194:197], v[38:41]\n"
"buffer_load_dwordx4 a[236:239], v2, s[28:31], s64 offen offset:3072 sc1\n"
"v_mfma_f32_16x16x32_bf16 v[26:29], a[120:123], v[198:201], v[26:29]\n"
"ds_read_b128 v[190:193], v8 offset:5120\n"
"v_mfma_f32_16x16x32_bf16 v[42:45], a[124:127], v[198:201], v[42:45]\n"
"v_mfma_f32_16x16x32_bf16 v[30:33], a[120:123], v[202:205], v[30:33]\n"
"ds_read_b128 v[194:197], v8 offset:6144\n"
"v_mfma_f32_16x16x32_bf16 v[46:49], a[124:127], v[202:205], v[46:49]\n"
"s_waitcnt lgkmcnt(4)\n"
"v_mul_u32_u24 v52, 256, v15\n"
"v_mfma_f32_16x16x32_bf16 v[18:21], a[128:131], v[206:209], v[18:21]\n"
"ds_read_b128 v[198:201], v8 offset:0\n"
"v_mfma_f32_16x16x32_bf16 v[34:37], a[132:135], v[206:209], v[34:37]\n"
"s_bfe_u32 s65, s84, 262164\n"
"s_mul_i32 s65, s65, 28672\n"
"s_add_u32 s65, s65, s69\n"
"s_add_u32 m0, s54, 57344\n"
"s_nop 0\n"
"buffer_load_dwordx4 v2, s[28:31], s65 offen offset:0 sc1 lds\n"
"v_mfma_f32_16x16x32_bf16 v[22:25], a[128:131], v[210:213], v[22:25]\n"
"ds_read_b128 v[202:205], v8 offset:1024\n"
"v_mfma_f32_16x16x32_bf16 v[38:41], a[132:135], v[210:213], v[38:41]\n"
"buffer_load_dwordx4 a[240:243], v3, s[28:31], s64 offen offset:0 sc1\n"
"v_mfma_f32_16x16x32_bf16 v[26:29], a[128:131], v[214:217], v[26:29]\n"
"ds_read_b128 v[206:209], v8 offset:7168\n"
"v_add_u32 v52, s57, v52\n"
"v_mfma_f32_16x16x32_bf16 v[42:45], a[132:135], v[214:217], v[42:45]\n"
"v_mfma_f32_16x16x32_bf16 v[30:33], a[128:131], v[218:221], v[30:33]\n"
"ds_read_b128 v[210:213], v8 offset:8192\n"
"buffer_load_dwordx4 a[244:247], v3, s[28:31], s64 offen offset:1024 sc1\n"
"v_mfma_f32_16x16x32_bf16 v[46:49], a[132:135], v[218:221], v[46:49]\n"
"s_waitcnt lgkmcnt(4)\n"
"v_lshl_add_u32 v51, v51, 4, v52\n"
"v_mfma_f32_16x16x32_bf16 v[18:21], a[136:139], v[222:225], v[18:21]\n"
"ds_read_b128 v[214:217], v8 offset:2048\n"
"v_mfma_f32_16x16x32_bf16 v[34:37], a[140:143], v[222:225], v[34:37]\n"
"buffer_load_dwordx4 v2, s[28:31], s65 offen offset:1024 sc1 lds\n"
"v_mfma_f32_16x16x32_bf16 v[22:25], a[136:139], v[226:229], v[22:25]\n"
"ds_read_b128 v[218:221], v8 offset:3072\n"
"v_mfma_f32_16x16x32_bf16 v[38:41], a[140:143], v[226:229], v[38:41]\n"
"v_mfma_f32_16x16x32_bf16 v[26:29], a[136:139], v[190:193], v[26:29]\n"
"ds_read_b128 v[222:225], v8 offset:9216\n"
"v_xor_b32 v52, 64, v51\n"
"v_mfma_f32_16x16x32_bf16 v[42:45], a[140:143], v[190:193], v[42:45]\n"
"buffer_load_dwordx4 v2, s[28:31], s65 offen offset:2048 sc1 lds\n"
"v_mfma_f32_16x16x32_bf16 v[30:33], a[136:139], v[194:197], v[30:33]\n"
"ds_read_b128 v[226:229], v8 offset:10240\n"
"v_mfma_f32_16x16x32_bf16 v[46:49], a[140:143], v[194:197], v[46:49]\n"
"s_waitcnt lgkmcnt(4)\n"
"v_mfma_f32_16x16x32_bf16 v[18:21], a[144:147], v[198:201], v[18:21]\n"
"ds_read_b64 v[190:191], v7 offset:55296\n"
"ds_read_b64 v[192:193], v8 offset:4096\n"
"v_mfma_f32_16x16x32_bf16 v[34:37], a[148:151], v[198:201], v[34:37]\n"
"buffer_load_dwordx4 a[248:251], v3, s[28:31], s64 offen offset:2048 sc1\n"
"v_mfma_f32_16x16x32_bf16 v[22:25], a[144:147], v[202:205], v[22:25]\n"
"ds_read_b64 v[194:195], v7 offset:55304\n"
"ds_read_b64 v[196:197], v8 offset:4104\n"
"v_mfma_f32_16x16x32_bf16 v[38:41], a[148:151], v[202:205], v[38:41]\n"
"v_bfe_u32 v15, v1, 4, 2\n"
"v_mfma_f32_16x16x32_bf16 v[26:29], a[144:147], v[206:209], v[26:29]\n"
"ds_read_b64 v[198:199], v7 offset:62464\n"
"ds_read_b64 v[200:201], v8 offset:11264\n"
"v_mfma_f32_16x16x32_bf16 v[42:45], a[148:151], v[206:209], v[42:45]\n"
"buffer_load_dwordx4 a[252:255], v3, s[28:31], s64 offen offset:3072 sc1\n"
"v_mfma_f32_16x16x32_bf16 v[30:33], a[144:147], v[210:213], v[30:33]\n"
"ds_read_b64 v[202:203], v7 offset:62472\n"
"ds_read_b64 v[204:205], v8 offset:11272\n"
"v_mfma_f32_16x16x32_bf16 v[46:49], a[148:151], v[210:213], v[46:49]\n"
"s_waitcnt lgkmcnt(8)\n"
"v_mul_u32_u24 v53, 2048, v15\n"
"v_mfma_f32_16x16x32_bf16 v[18:21], a[152:155], v[214:217], v[18:21]\n"
"s_waitcnt vmcnt(26)\n"
"s_barrier\n"
"ds_read_b128 v[206:209], v6 offset:0\n"
"v_mfma_f32_16x16x32_bf16 v[34:37], a[156:159], v[214:217], v[34:37]\n"
"buffer_load_dwordx4 v2, s[28:31], s65 offen offset:3072 sc1 lds\n"
"v_mfma_f32_16x16x32_bf16 v[22:25], a[152:155], v[218:221], v[22:25]\n"
"ds_read_b128 v[210:213], v6 offset:1024\n"
"v_bfe_u32 v15, v1, 0, 4\n"
"v_mfma_f32_16x16x32_bf16 v[38:41], a[156:159], v[218:221], v[38:41]\n"
"v_mfma_f32_16x16x32_bf16 v[26:29], a[152:155], v[222:225], v[26:29]\n"
"ds_read_b128 v[214:217], v6 offset:7168\n"
"s_add_u32 m0, s54, 61440\n"
"v_mfma_f32_16x16x32_bf16 v[42:45], a[156:159], v[222:225], v[42:45]\n"
"v_lshl_add_u32 v53, v15, 4, v53\n"
"v_mfma_f32_16x16x32_bf16 v[30:33], a[152:155], v[226:229], v[30:33]\n"
"ds_read_b128 v[218:221], v6 offset:8192\n"
"buffer_load_dwordx4 v3, s[28:31], s65 offen offset:0 sc1 lds\n"
"v_mfma_f32_16x16x32_bf16 v[46:49], a[156:159], v[226:229], v[46:49]\n"
"s_waitcnt lgkmcnt(4)\n"
"v_mfma_f32_16x16x32_bf16 v[18:21], a[160:163], v[190:193], v[18:21]\n"
"ds_read_b128 v[222:225], v6 offset:2048\n"
"v_mfma_f32_16x16x32_bf16 v[34:37], a[164:167], v[190:193], v[34:37]\n"
"buffer_load_dwordx4 a[0:3], v4, s[28:31], s64 offen offset:0 sc1\n"
"v_mfma_f32_16x16x32_bf16 v[22:25], a[160:163], v[194:197], v[22:25]\n"
"ds_read_b128 v[226:229], v6 offset:3072\n"
"s_lshr_b32 s59, s41, 1\n"
"v_mfma_f32_16x16x32_bf16 v[38:41], a[164:167], v[194:197], v[38:41]\n"
"v_mfma_f32_16x16x32_bf16 v[26:29], a[160:163], v[198:201], v[26:29]\n"
"ds_read_b128 v[190:193], v6 offset:9216\n"
"v_mfma_f32_16x16x32_bf16 v[42:45], a[164:167], v[198:201], v[42:45]\n"
"buffer_load_dwordx4 a[4:7], v4, s[28:31], s64 offen offset:1024 sc1\n"
"v_mfma_f32_16x16x32_bf16 v[30:33], a[160:163], v[202:205], v[30:33]\n"
"ds_read_b128 v[194:197], v6 offset:10240\n"
"s_mul_i32 s89, s51, 131072\n"
"v_mfma_f32_16x16x32_bf16 v[46:49], a[164:167], v[202:205], v[46:49]\n"
"s_waitcnt lgkmcnt(4)\n"
"v_mfma_f32_16x16x32_bf16 v[18:21], a[168:171], v[206:209], v[18:21]\n"
"ds_read_b128 v[198:201], v6 offset:4096\n"
"v_mfma_f32_16x16x32_bf16 v[34:37], a[172:175], v[206:209], v[34:37]\n"
"buffer_load_dwordx4 v3, s[28:31], s65 offen offset:1024 sc1 lds\n"
"v_mfma_f32_16x16x32_bf16 v[22:25], a[168:171], v[210:213], v[22:25]\n"
".Lmk_pg_19:\n"
"s_cbranch_execz .Lmk_ret_4\n"
"ds_read_b128 v[202:205], v6 offset:5120\n"
"s_mul_i32 s56, s52, 256\n"
"v_mfma_f32_16x16x32_bf16 v[38:41], a[172:175], v[210:213], v[38:41]\n"
"v_mfma_f32_16x16x32_bf16 v[26:29], a[168:171], v[214:217], v[26:29]\n"
"ds_read_b128 v[206:209], v6 offset:11264\n"
"v_mfma_f32_16x16x32_bf16 v[42:45], a[172:175], v[214:217], v[42:45]\n"
"buffer_load_dwordx4 v3, s[28:31], s65 offen offset:2048 sc1 lds\n"
"v_mfma_f32_16x16x32_bf16 v[30:33], a[168:171], v[218:221], v[30:33]\n"
"ds_read_b128 v[210:213], v6 offset:12288\n"
"s_add_u32 s89, s89, s56\n"
"v_mfma_f32_16x16x32_bf16 v[46:49], a[172:175], v[218:221], v[46:49]\n"
"s_waitcnt lgkmcnt(4)\n"
"v_mfma_f32_16x16x32_bf16 v[18:21], a[176:179], v[222:225], v[18:21]\n"
"ds_read_b128 v[214:217], v6 offset:14336\n"
"v_mfma_f32_16x16x32_bf16 v[34:37], a[180:183], v[222:225], v[34:37]\n"
"buffer_load_dwordx4 a[8:11], v4, s[28:31], s64 offen offset:2048 sc1\n"
"v_mfma_f32_16x16x32_bf16 v[22:25], a[176:179], v[226:229], v[22:25]\n"
"ds_read_b128 v[218:221], v6 offset:15360\n"
"s_mul_i32 s56, s58, 32768\n"
"v_mfma_f32_16x16x32_bf16 v[38:41], a[180:183], v[226:229], v[38:41]\n"
"buffer_load_dwordx4 a[12:15], v4, s[28:31], s64 offen offset:3072 sc1\n"
"v_mfma_f32_16x16x32_bf16 v[26:29], a[176:179], v[190:193], v[26:29]\n"
"ds_read_b128 v[222:225], v6 offset:21504\n"
"v_mfma_f32_16x16x32_bf16 v[42:45], a[180:183], v[190:193], v[42:45]\n"
"v_mfma_f32_16x16x32_bf16 v[30:33], a[176:179], v[194:197], v[30:33]\n"
"ds_read_b128 v[226:229], v6 offset:22528\n"
"s_add_u32 s89, s89, s56\n"
"v_mfma_f32_16x16x32_bf16 v[46:49], a[180:183], v[194:197], v[46:49]\n"
"s_waitcnt lgkmcnt(4)\n"
"v_mfma_f32_16x16x32_bf16 v[18:21], a[184:187], v[198:201], v[18:21]\n"
"ds_read_b128 v[190:193], v6 offset:16384\n"
"v_mfma_f32_16x16x32_bf16 v[34:37], a[188:191], v[198:201], v[34:37]\n"
"buffer_load_dwordx4 a[16:19], v5, s[28:31], s64 offen offset:0 sc1\n"
"v_mfma_f32_16x16x32_bf16 v[22:25], a[184:187], v[202:205], v[22:25]\n"
"ds_read_b128 v[194:197], v6 offset:17408\n"
"v_mfma_f32_16x16x32_bf16 v[38:41], a[188:191], v[202:205], v[38:41]\n"
"buffer_load_dwordx4 a[20:23], v5, s[28:31], s64 offen offset:1024 sc1\n"
"v_mfma_f32_16x16x32_bf16 v[26:29], a[184:187], v[206:209], v[26:29]\n"
"ds_read_b128 v[198:201], v6 offset:23552\n"
"s_mul_i32 s56, s59, 8192\n"
"v_mfma_f32_16x16x32_bf16 v[42:45], a[188:191], v[206:209], v[42:45]\n"
"v_mfma_f32_16x16x32_bf16 v[30:33], a[184:187], v[210:213], v[30:33]\n"
"ds_read_b128 v[202:205], v6 offset:24576\n"
"v_mfma_f32_16x16x32_bf16 v[46:49], a[188:191], v[210:213], v[46:49]\n"
"s_waitcnt lgkmcnt(4)\n"
"s_add_u32 s89, s89, s56\n"
"s_waitcnt vmcnt(31)\n"
"v_mfma_f32_16x16x32_bf16 v[18:21], a[192:195], v[214:217], v[18:21]\n"
"ds_read_b128 v[206:209], v6 offset:18432\n"
"v_mfma_f32_16x16x32_bf16 v[34:37], a[196:199], v[214:217], v[34:37]\n"
"s_bfe_u32 s64, s84, 262164\n"
"s_mul_i32 s64, s64, 14336\n"
"s_add_u32 s64, s64, s66\n"
"buffer_load_dwordx4 a[24:27], v2, s[28:31], s64 offen offset:0 sc1\n"
"v_mfma_f32_16x16x32_bf16 v[22:25], a[192:195], v[218:221], v[22:25]\n"
"ds_read_b128 v[210:213], v6 offset:19456\n"
"v_mfma_f32_16x16x32_bf16 v[38:41], a[196:199], v[218:221], v[38:41]\n"
"buffer_load_dwordx4 a[28:31], v2, s[28:31], s64 offen offset:1024 sc1\n"
"v_mfma_f32_16x16x32_bf16 v[26:29], a[192:195], v[222:225], v[26:29]\n"
"ds_read_b128 v[214:217], v6 offset:25600\n"
"s_add_u32 s89, s89, 0\n"
"v_mfma_f32_16x16x32_bf16 v[42:45], a[196:199], v[222:225], v[42:45]\n"
"v_mfma_f32_16x16x32_bf16 v[30:33], a[192:195], v[226:229], v[30:33]\n"
"ds_read_b128 v[218:221], v6 offset:26624\n"
"v_mfma_f32_16x16x32_bf16 v[46:49], a[196:199], v[226:229], v[46:49]\n"
"s_waitcnt lgkmcnt(4)\n"
"s_waitcnt vmcnt(29)\n"
"v_mfma_f32_16x16x32_bf16 v[18:21], a[200:203], v[190:193], v[18:21]\n"
"ds_read_b64 v[222:223], v6 offset:6144\n"
"ds_read_b64 v[224:225], v6 offset:20480\n"
"v_mfma_f32_16x16x32_bf16 v[34:37], a[204:207], v[190:193], v[34:37]\n"
"buffer_load_dwordx4 a[32:35], v2, s[28:31], s64 offen offset:2048 sc1\n"
"v_mfma_f32_16x16x32_bf16 v[22:25], a[200:203], v[194:197], v[22:25]\n"
"ds_read_b64 v[226:227], v6 offset:6152\n"
"ds_read_b64 v[228:229], v6 offset:20488\n"
"v_mfma_f32_16x16x32_bf16 v[38:41], a[204:207], v[194:197], v[38:41]\n"
"buffer_load_dwordx4 a[36:39], v2, s[28:31], s64 offen offset:3072 sc1\n"
"v_mfma_f32_16x16x32_bf16 v[26:29], a[200:203], v[198:201], v[26:29]\n"
"ds_read_b64 v[190:191], v6 offset:13312\n"
"ds_read_b64 v[192:193], v6 offset:27648\n"
"v_mfma_f32_16x16x32_bf16 v[42:45], a[204:207], v[198:201], v[42:45]\n"
"v_mfma_f32_16x16x32_bf16 v[30:33], a[200:203], v[202:205], v[30:33]\n"
"ds_read_b64 v[194:195], v6 offset:13320\n"
"ds_read_b64 v[196:197], v6 offset:27656\n"
"v_mfma_f32_16x16x32_bf16 v[46:49], a[204:207], v[202:205], v[46:49]\n"
"s_waitcnt lgkmcnt(8)\n"
"s_waitcnt vmcnt(21)\n"
"v_mfma_f32_16x16x32_bf16 v[18:21], a[208:211], v[206:209], v[18:21]\n"
"s_barrier\n"
"ds_read_b128 v[198:201], v6 offset:28672\n"
"v_mfma_f32_16x16x32_bf16 v[34:37], a[212:215], v[206:209], v[34:37]\n"
"s_bfe_u32 s65, s84, 262168\n"
"s_mul_i32 s65, s65, 28672\n"
"s_add_u32 s65, s65, s69\n"
"s_add_u32 m0, s54, 86016\n"
"s_nop 0\n"
"buffer_load_dwordx4 v2, s[28:31], s65 offen offset:0 sc1 lds\n"
"v_mfma_f32_16x16x32_bf16 v[22:25], a[208:211], v[210:213], v[22:25]\n"
"ds_read_b128 v[202:205], v6 offset:29696\n"
"v_mfma_f32_16x16x32_bf16 v[38:41], a[212:215], v[210:213], v[38:41]\n"
"buffer_load_dwordx4 a[40:43], v3, s[28:31], s64 offen offset:0 sc1\n"
"v_mfma_f32_16x16x32_bf16 v[26:29], a[208:211], v[214:217], v[26:29]\n"
"ds_read_b128 v[206:209], v6 offset:35840\n"
"v_mfma_f32_16x16x32_bf16 v[42:45], a[212:215], v[214:217], v[42:45]\n"
"buffer_load_dwordx4 a[44:47], v3, s[28:31], s64 offen offset:1024 sc1\n"
"v_mfma_f32_16x16x32_bf16 v[30:33], a[208:211], v[218:221], v[30:33]\n"
"ds_read_b128 v[210:213], v6 offset:36864\n"
"v_mfma_f32_16x16x32_bf16 v[46:49], a[212:215], v[218:221], v[46:49]\n"
"s_waitcnt lgkmcnt(4)\n"
"v_mfma_f32_16x16x32_bf16 v[18:21], a[216:219], v[222:225], v[18:21]\n"
"ds_read_b128 v[214:217], v6 offset:30720\n"
"v_mfma_f32_16x16x32_bf16 v[34:37], a[220:223], v[222:225], v[34:37]\n"
"buffer_load_dwordx4 v2, s[28:31], s65 offen offset:1024 sc1 lds\n"
"v_mfma_f32_16x16x32_bf16 v[22:25], a[216:219], v[226:229], v[22:25]\n"
"ds_read_b128 v[218:221], v6 offset:31744\n"
"v_mfma_f32_16x16x32_bf16 v[38:41], a[220:223], v[226:229], v[38:41]\n"
"v_mfma_f32_16x16x32_bf16 v[26:29], a[216:219], v[190:193], v[26:29]\n"
"ds_read_b128 v[222:225], v6 offset:37888\n"
"v_mfma_f32_16x16x32_bf16 v[42:45], a[220:223], v[190:193], v[42:45]\n"
"buffer_load_dwordx4 v2, s[28:31], s65 offen offset:2048 sc1 lds\n"
"v_mfma_f32_16x16x32_bf16 v[30:33], a[216:219], v[194:197], v[30:33]\n"
"ds_read_b128 v[226:229], v6 offset:38912\n"
"v_mfma_f32_16x16x32_bf16 v[46:49], a[220:223], v[194:197], v[46:49]\n"
"s_waitcnt lgkmcnt(4)\n"
"v_mfma_f32_16x16x32_bf16 v[18:21], a[224:227], v[198:201], v[18:21]\n"
"ds_read_b128 v[190:193], v6 offset:32768\n"
"v_mfma_f32_16x16x32_bf16 v[34:37], a[228:231], v[198:201], v[34:37]\n"
"buffer_load_dwordx4 a[48:51], v3, s[28:31], s64 offen offset:2048 sc1\n"
"v_mfma_f32_16x16x32_bf16 v[22:25], a[224:227], v[202:205], v[22:25]\n"
"ds_read_b128 v[194:197], v6 offset:33792\n"
"v_mfma_f32_16x16x32_bf16 v[38:41], a[228:231], v[202:205], v[38:41]\n"
"v_mfma_f32_16x16x32_bf16 v[26:29], a[224:227], v[206:209], v[26:29]\n"
"ds_read_b128 v[198:201], v6 offset:39936\n"
"v_mfma_f32_16x16x32_bf16 v[42:45], a[228:231], v[206:209], v[42:45]\n"
"buffer_load_dwordx4 a[52:55], v3, s[28:31], s64 offen offset:3072 sc1\n"
"v_mfma_f32_16x16x32_bf16 v[30:33], a[224:227], v[210:213], v[30:33]\n"
"ds_read_b128 v[202:205], v6 offset:40960\n"
"v_mfma_f32_16x16x32_bf16 v[46:49], a[228:231], v[210:213], v[46:49]\n"
"s_waitcnt lgkmcnt(4)\n"
"v_mfma_f32_16x16x32_bf16 v[18:21], a[232:235], v[214:217], v[18:21]\n"
"ds_read_b128 v[206:209], v6 offset:43008\n"
"v_mfma_f32_16x16x32_bf16 v[34:37], a[236:239], v[214:217], v[34:37]\n"
"buffer_load_dwordx4 v2, s[28:31], s65 offen offset:3072 sc1 lds\n"
"v_mfma_f32_16x16x32_bf16 v[22:25], a[232:235], v[218:221], v[22:25]\n"
"ds_read_b128 v[210:213], v6 offset:44032\n"
"v_mfma_f32_16x16x32_bf16 v[38:41], a[236:239], v[218:221], v[38:41]\n"
"v_mfma_f32_16x16x32_bf16 v[26:29], a[232:235], v[222:225], v[26:29]\n"
"ds_read_b128 v[214:217], v6 offset:50176\n"
"s_add_u32 m0, s54, 90112\n"
"v_mfma_f32_16x16x32_bf16 v[42:45], a[236:239], v[222:225], v[42:45]\n"
"v_mfma_f32_16x16x32_bf16 v[30:33], a[232:235], v[226:229], v[30:33]\n"
"ds_read_b128 v[218:221], v6 offset:51200\n"
"buffer_load_dwordx4 v3, s[28:31], s65 offen offset:0 sc1 lds\n"
"v_mfma_f32_16x16x32_bf16 v[46:49], a[236:239], v[226:229], v[46:49]\n"
"s_waitcnt lgkmcnt(4)\n"
"s_waitcnt vmcnt(27)\n"
"v_mfma_f32_16x16x32_bf16 v[18:21], a[240:243], v[190:193], v[18:21]\n"
"ds_read_b128 v[222:225], v6 offset:45056\n"
"v_mfma_f32_16x16x32_bf16 v[34:37], a[244:247], v[190:193], v[34:37]\n"
"buffer_load_dwordx4 a[56:59], v4, s[28:31], s64 offen offset:0 sc1\n"
"v_mfma_f32_16x16x32_bf16 v[22:25], a[240:243], v[194:197], v[22:25]\n"
"ds_read_b128 v[226:229], v6 offset:46080\n"
"v_mfma_f32_16x16x32_bf16 v[38:41], a[244:247], v[194:197], v[38:41]\n"
"v_mfma_f32_16x16x32_bf16 v[26:29], a[240:243], v[198:201], v[26:29]\n"
"ds_read_b128 v[190:193], v6 offset:52224\n"
"v_mfma_f32_16x16x32_bf16 v[42:45], a[244:247], v[198:201], v[42:45]\n"
"buffer_load_dwordx4 a[60:63], v4, s[28:31], s64 offen offset:1024 sc1\n"
"v_mfma_f32_16x16x32_bf16 v[30:33], a[240:243], v[202:205], v[30:33]\n"
"ds_read_b128 v[194:197], v6 offset:53248\n"
"v_mfma_f32_16x16x32_bf16 v[46:49], a[244:247], v[202:205], v[46:49]\n"
"s_waitcnt lgkmcnt(4)\n"
"s_waitcnt vmcnt(25)\n"
"v_mfma_f32_16x16x32_bf16 v[18:21], a[248:251], v[206:209], v[18:21]\n"
"ds_read_b128 v[198:201], v6 offset:47104\n"
"v_mfma_f32_16x16x32_bf16 v[34:37], a[252:255], v[206:209], v[34:37]\n"
"buffer_load_dwordx4 v3, s[28:31], s65 offen offset:1024 sc1 lds\n"
"v_mfma_f32_16x16x32_bf16 v[22:25], a[248:251], v[210:213], v[22:25]\n"
"ds_read_b128 v[202:205], v6 offset:48128\n"
"v_mfma_f32_16x16x32_bf16 v[38:41], a[252:255], v[210:213], v[38:41]\n"
"buffer_load_dwordx4 v3, s[28:31], s65 offen offset:2048 sc1 lds\n"
"v_mfma_f32_16x16x32_bf16 v[26:29], a[248:251], v[214:217], v[26:29]\n"
"ds_read_b128 v[206:209], v6 offset:54272\n"
"v_mfma_f32_16x16x32_bf16 v[42:45], a[252:255], v[214:217], v[42:45]\n"
"v_mfma_f32_16x16x32_bf16 v[30:33], a[248:251], v[218:221], v[30:33]\n"
"ds_read_b128 v[210:213], v6 offset:55296\n"
"v_mfma_f32_16x16x32_bf16 v[46:49], a[252:255], v[218:221], v[46:49]\n"
"s_waitcnt lgkmcnt(4)\n"
"s_waitcnt vmcnt(23)\n"
"v_mfma_f32_16x16x32_bf16 v[18:21], a[0:3], v[222:225], v[18:21]\n"
"ds_read_b64 v[214:215], v6 offset:34816\n"
"ds_read_b64 v[216:217], v6 offset:49152\n"
"v_mfma_f32_16x16x32_bf16 v[34:37], a[4:7], v[222:225], v[34:37]\n"
"buffer_load_dwordx4 a[64:67], v4, s[28:31], s64 offen offset:2048 sc1\n"
"v_mfma_f32_16x16x32_bf16 v[22:25], a[0:3], v[226:229], v[22:25]\n"
"ds_read_b64 v[218:219], v6 offset:34824\n"
"ds_read_b64 v[220:221], v6 offset:49160\n"
"v_mfma_f32_16x16x32_bf16 v[38:41], a[4:7], v[226:229], v[38:41]\n"
"buffer_load_dwordx4 a[68:71], v4, s[28:31], s64 offen offset:3072 sc1\n"
"v_mfma_f32_16x16x32_bf16 v[26:29], a[0:3], v[190:193], v[26:29]\n"
"ds_read_b64 v[222:223], v6 offset:41984\n"
"ds_read_b64 v[224:225], v6 offset:56320\n"
"v_mfma_f32_16x16x32_bf16 v[42:45], a[4:7], v[190:193], v[42:45]\n"
"v_mfma_f32_16x16x32_bf16 v[30:33], a[0:3], v[194:197], v[30:33]\n"
"ds_read_b64 v[226:227], v6 offset:41992\n"
"ds_read_b64 v[228:229], v6 offset:56328\n"
"v_mfma_f32_16x16x32_bf16 v[46:49], a[4:7], v[194:197], v[46:49]\n"
"s_waitcnt lgkmcnt(8)\n"
"s_waitcnt vmcnt(15)\n"
"v_mfma_f32_16x16x32_bf16 v[18:21], a[8:11], v[198:201], v[18:21]\n"
"s_barrier\n"
"ds_read_b128 v[190:193], v6 offset:57344\n"
"v_mfma_f32_16x16x32_bf16 v[34:37], a[12:15], v[198:201], v[34:37]\n"
"buffer_load_dwordx4 a[72:75], v5, s[28:31], s64 offen offset:0 sc1\n"
"v_mfma_f32_16x16x32_bf16 v[22:25], a[8:11], v[202:205], v[22:25]\n"
"ds_read_b128 v[194:197], v6 offset:58368\n"
"v_mfma_f32_16x16x32_bf16 v[38:41], a[12:15], v[202:205], v[38:41]\n"
"buffer_load_dwordx4 a[76:79], v5, s[28:31], s64 offen offset:1024 sc1\n"
"v_mfma_f32_16x16x32_bf16 v[26:29], a[8:11], v[206:209], v[26:29]\n"
"ds_read_b128 v[198:201], v6 offset:64512\n"
"v_mfma_f32_16x16x32_bf16 v[42:45], a[12:15], v[206:209], v[42:45]\n"
"v_mfma_f32_16x16x32_bf16 v[30:33], a[8:11], v[210:213], v[30:33]\n"
"ds_read_b128 v[202:205], v7 offset:0\n"
"v_mfma_f32_16x16x32_bf16 v[46:49], a[12:15], v[210:213], v[46:49]\n"
"s_waitcnt lgkmcnt(4)\n"
"v_mfma_f32_16x16x32_bf16 v[18:21], a[16:19], v[214:217], v[18:21]\n"
"ds_read_b128 v[206:209], v6 offset:59392\n"
"v_mfma_f32_16x16x32_bf16 v[34:37], a[20:23], v[214:217], v[34:37]\n"
"s_bfe_u32 s64, s84, 262168\n"
"s_mul_i32 s64, s64, 14336\n"
"s_add_u32 s64, s64, s66\n"
"buffer_load_dwordx4 a[80:83], v2, s[28:31], s64 offen offset:0 sc1\n"
"v_mfma_f32_16x16x32_bf16 v[22:25], a[16:19], v[218:221], v[22:25]\n"
"ds_read_b128 v[210:213], v6 offset:60416\n"
"v_mfma_f32_16x16x32_bf16 v[38:41], a[20:23], v[218:221], v[38:41]\n"
"buffer_load_dwordx4 a[84:87], v2, s[28:31], s64 offen offset:1024 sc1\n"
"v_mfma_f32_16x16x32_bf16 v[26:29], a[16:19], v[222:225], v[26:29]\n"
"ds_read_b128 v[214:217], v7 offset:1024\n"
"v_mfma_f32_16x16x32_bf16 v[42:45], a[20:23], v[222:225], v[42:45]\n"
"v_mfma_f32_16x16x32_bf16 v[30:33], a[16:19], v[226:229], v[30:33]\n"
"ds_read_b128 v[218:221], v7 offset:2048\n"
"v_mfma_f32_16x16x32_bf16 v[46:49], a[20:23], v[226:229], v[46:49]\n"
"s_waitcnt lgkmcnt(4)\n"
"v_mfma_f32_16x16x32_bf16 v[18:21], a[24:27], v[190:193], v[18:21]\n"
"ds_read_b128 v[222:225], v6 offset:61440\n"
"v_mfma_f32_16x16x32_bf16 v[34:37], a[28:31], v[190:193], v[34:37]\n"
"buffer_load_dwordx4 a[88:91], v2, s[28:31], s64 offen offset:2048 sc1\n"
"v_mfma_f32_16x16x32_bf16 v[22:25], a[24:27], v[194:197], v[22:25]\n"
"ds_read_b128 v[226:229], v6 offset:62464\n"
"v_mfma_f32_16x16x32_bf16 v[38:41], a[28:31], v[194:197], v[38:41]\n"
"buffer_load_dwordx4 a[92:95], v2, s[28:31], s64 offen offset:3072 sc1\n"
"v_mfma_f32_16x16x32_bf16 v[26:29], a[24:27], v[198:201], v[26:29]\n"
"ds_read_b128 v[190:193], v7 offset:3072\n"
"v_mfma_f32_16x16x32_bf16 v[42:45], a[28:31], v[198:201], v[42:45]\n"
"v_mfma_f32_16x16x32_bf16 v[30:33], a[24:27], v[202:205], v[30:33]\n"
"ds_read_b128 v[194:197], v7 offset:4096\n"
"s_bfe_u32 s65, s84, 262172\n"
"s_mul_i32 s65, s65, 28672\n"
"v_mfma_f32_16x16x32_bf16 v[46:49], a[28:31], v[202:205], v[46:49]\n"
"s_waitcnt lgkmcnt(4)\n"
"s_add_u32 s65, s65, s69\n"
"v_mfma_f32_16x16x32_bf16 v[18:21], a[32:35], v[206:209], v[18:21]\n"
"ds_read_b128 v[198:201], v7 offset:6144\n"
"s_add_u32 m0, s54, 114688\n"
"s_nop 0\n"
"buffer_load_dwordx4 v2, s[28:31], s65 offen offset:0 sc1 lds\n"
"v_mfma_f32_16x16x32_bf16 v[34:37], a[36:39], v[206:209], v[34:37]\n"
"buffer_load_dwordx4 a[96:99], v3, s[28:31], s64 offen offset:0 sc1\n"
"v_mfma_f32_16x16x32_bf16 v[22:25], a[32:35], v[210:213], v[22:25]\n"
"ds_read_b128 v[202:205], v7 offset:7168\n"
"v_mfma_f32_16x16x32_bf16 v[38:41], a[36:39], v[210:213], v[38:41]\n"
"v_mfma_f32_16x16x32_bf16 v[26:29], a[32:35], v[214:217], v[26:29]\n"
"ds_read_b128 v[206:209], v7 offset:13312\n"
"v_mfma_f32_16x16x32_bf16 v[42:45], a[36:39], v[214:217], v[42:45]\n"
"buffer_load_dwordx4 a[100:103], v3, s[28:31], s64 offen offset:1024 sc1\n"
"v_mfma_f32_16x16x32_bf16 v[30:33], a[32:35], v[218:221], v[30:33]\n"
"ds_read_b128 v[210:213], v7 offset:14336\n"
"v_mfma_f32_16x16x32_bf16 v[46:49], a[36:39], v[218:221], v[46:49]\n"
"s_waitcnt lgkmcnt(4)\n"
"s_waitcnt vmcnt(21)\n"
"v_mfma_f32_16x16x32_bf16 v[18:21], a[40:43], v[222:225], v[18:21]\n"
"ds_read_b128 v[214:217], v7 offset:8192\n"
"v_mfma_f32_16x16x32_bf16 v[34:37], a[44:47], v[222:225], v[34:37]\n"
"buffer_load_dwordx4 v2, s[28:31], s65 offen offset:1024 sc1 lds\n"
"v_mfma_f32_16x16x32_bf16 v[22:25], a[40:43], v[226:229], v[22:25]\n"
"ds_read_b128 v[218:221], v7 offset:9216\n"
"v_mfma_f32_16x16x32_bf16 v[38:41], a[44:47], v[226:229], v[38:41]\n"
"v_mfma_f32_16x16x32_bf16 v[26:29], a[40:43], v[190:193], v[26:29]\n"
"ds_read_b128 v[222:225], v7 offset:15360\n"
"v_mfma_f32_16x16x32_bf16 v[42:45], a[44:47], v[190:193], v[42:45]\n"
"buffer_load_dwordx4 v2, s[28:31], s65 offen offset:2048 sc1 lds\n"
"v_mfma_f32_16x16x32_bf16 v[30:33], a[40:43], v[194:197], v[30:33]\n"
"ds_read_b128 v[226:229], v7 offset:16384\n"
"v_mfma_f32_16x16x32_bf16 v[46:49], a[44:47], v[194:197], v[46:49]\n"
"s_waitcnt lgkmcnt(4)\n"
"s_waitcnt vmcnt(19)\n"
"v_mfma_f32_16x16x32_bf16 v[18:21], a[48:51], v[198:201], v[18:21]\n"
"ds_read_b128 v[190:193], v7 offset:10240\n"
"v_mfma_f32_16x16x32_bf16 v[34:37], a[52:55], v[198:201], v[34:37]\n"
"buffer_load_dwordx4 a[104:107], v3, s[28:31], s64 offen offset:2048 sc1\n"
"v_mfma_f32_16x16x32_bf16 v[22:25], a[48:51], v[202:205], v[22:25]\n"
"ds_read_b128 v[194:197], v7 offset:11264\n"
"v_mfma_f32_16x16x32_bf16 v[38:41], a[52:55], v[202:205], v[38:41]\n"
"v_mfma_f32_16x16x32_bf16 v[26:29], a[48:51], v[206:209], v[26:29]\n"
"ds_read_b128 v[198:201], v7 offset:17408\n"
"v_mfma_f32_16x16x32_bf16 v[42:45], a[52:55], v[206:209], v[42:45]\n"
"buffer_load_dwordx4 a[108:111], v3, s[28:31], s64 offen offset:3072 sc1\n"
"v_mfma_f32_16x16x32_bf16 v[30:33], a[48:51], v[210:213], v[30:33]\n"
"ds_read_b128 v[202:205], v7 offset:18432\n"
"v_mfma_f32_16x16x32_bf16 v[46:49], a[52:55], v[210:213], v[46:49]\n"
"s_waitcnt lgkmcnt(4)\n"
"s_waitcnt vmcnt(17)\n"
"v_mfma_f32_16x16x32_bf16 v[18:21], a[56:59], v[214:217], v[18:21]\n"
"ds_read_b64 v[206:207], v6 offset:63488\n"
"ds_read_b64 v[208:209], v7 offset:12288\n"
"v_mfma_f32_16x16x32_bf16 v[34:37], a[60:63], v[214:217], v[34:37]\n"
"buffer_load_dwordx4 v2, s[28:31], s65 offen offset:3072 sc1 lds\n"
"v_mfma_f32_16x16x32_bf16 v[22:25], a[56:59], v[218:221], v[22:25]\n"
"ds_read_b64 v[210:211], v6 offset:63496\n"
"ds_read_b64 v[212:213], v7 offset:12296\n"
"v_mfma_f32_16x16x32_bf16 v[38:41], a[60:63], v[218:221], v[38:41]\n"
"v_mfma_f32_16x16x32_bf16 v[26:29], a[56:59], v[222:225], v[26:29]\n"
"ds_read_b64 v[214:215], v7 offset:5120\n"
"ds_read_b64 v[216:217], v7 offset:19456\n"
"v_mfma_f32_16x16x32_bf16 v[42:45], a[60:63], v[222:225], v[42:45]\n"
"s_add_u32 m0, s54, 118784\n"
"v_mfma_f32_16x16x32_bf16 v[30:33], a[56:59], v[226:229], v[30:33]\n"
"ds_read_b64 v[218:219], v7 offset:5128\n"
"ds_read_b64 v[220:221], v7 offset:19464\n"
"v_mfma_f32_16x16x32_bf16 v[46:49], a[60:63], v[226:229], v[46:49]\n"
"s_waitcnt lgkmcnt(8)\n"
"buffer_load_dwordx4 v3, s[28:31], s65 offen offset:0 sc1 lds\n"
"s_waitcnt vmcnt(13)\n"
"v_mfma_f32_16x16x32_bf16 v[18:21], a[64:67], v[190:193], v[18:21]\n"
"s_barrier\n"
"ds_read_b128 v[222:225], v7 offset:20480\n"
"v_mfma_f32_16x16x32_bf16 v[34:37], a[68:71], v[190:193], v[34:37]\n"
"buffer_load_dwordx4 a[112:115], v4, s[28:31], s64 offen offset:0 sc1\n"
"v_mfma_f32_16x16x32_bf16 v[22:25], a[64:67], v[194:197], v[22:25]\n"
"ds_read_b128 v[226:229], v7 offset:21504\n"
"v_mfma_f32_16x16x32_bf16 v[38:41], a[68:71], v[194:197], v[38:41]\n"
"v_mfma_f32_16x16x32_bf16 v[26:29], a[64:67], v[198:201], v[26:29]\n"
"ds_read_b128 v[190:193], v7 offset:27648\n"
"v_mfma_f32_16x16x32_bf16 v[42:45], a[68:71], v[198:201], v[42:45]\n"
"buffer_load_dwordx4 a[116:119], v4, s[28:31], s64 offen offset:1024 sc1\n"
"v_mfma_f32_16x16x32_bf16 v[30:33], a[64:67], v[202:205], v[30:33]\n"
"ds_read_b128 v[194:197], v7 offset:28672\n"
"v_mfma_f32_16x16x32_bf16 v[46:49], a[68:71], v[202:205], v[46:49]\n"
"s_waitcnt lgkmcnt(4)\n"
"v_mfma_f32_16x16x32_bf16 v[18:21], a[72:75], v[206:209], v[18:21]\n"
"ds_read_b128 v[198:201], v7 offset:22528\n"
"v_mfma_f32_16x16x32_bf16 v[34:37], a[76:79], v[206:209], v[34:37]\n"
"buffer_load_dwordx4 v3, s[28:31], s65 offen offset:1024 sc1 lds\n"
"v_mfma_f32_16x16x32_bf16 v[22:25], a[72:75], v[210:213], v[22:25]\n"
"ds_read_b128 v[202:205], v7 offset:23552\n"
"v_mfma_f32_16x16x32_bf16 v[38:41], a[76:79], v[210:213], v[38:41]\n"
"buffer_load_dwordx4 v3, s[28:31], s65 offen offset:2048 sc1 lds\n"
"v_mfma_f32_16x16x32_bf16 v[26:29], a[72:75], v[214:217], v[26:29]\n"
"ds_read_b128 v[206:209], v7 offset:29696\n"
"v_mfma_f32_16x16x32_bf16 v[42:45], a[76:79], v[214:217], v[42:45]\n"
"v_mfma_f32_16x16x32_bf16 v[30:33], a[72:75], v[218:221], v[30:33]\n"
"ds_read_b128 v[210:213], v7 offset:30720\n"
"v_mfma_f32_16x16x32_bf16 v[46:49], a[76:79], v[218:221], v[46:49]\n"
"s_waitcnt lgkmcnt(4)\n"
"s_waitcnt vmcnt(13)\n"
"v_mfma_f32_16x16x32_bf16 v[18:21], a[80:83], v[222:225], v[18:21]\n"
"ds_read_b128 v[214:217], v7 offset:24576\n"
"v_mfma_f32_16x16x32_bf16 v[34:37], a[84:87], v[222:225], v[34:37]\n"
"buffer_load_dwordx4 a[120:123], v4, s[28:31], s64 offen offset:2048 sc1\n"
"v_mfma_f32_16x16x32_bf16 v[22:25], a[80:83], v[226:229], v[22:25]\n"
"ds_read_b128 v[218:221], v7 offset:25600\n"
"v_mfma_f32_16x16x32_bf16 v[38:41], a[84:87], v[226:229], v[38:41]\n"
"buffer_load_dwordx4 a[124:127], v4, s[28:31], s64 offen offset:3072 sc1\n"
"v_mfma_f32_16x16x32_bf16 v[26:29], a[80:83], v[190:193], v[26:29]\n"
"ds_read_b128 v[222:225], v7 offset:31744\n"
"v_mfma_f32_16x16x32_bf16 v[42:45], a[84:87], v[190:193], v[42:45]\n"
"v_mfma_f32_16x16x32_bf16 v[30:33], a[80:83], v[194:197], v[30:33]\n"
"ds_read_b128 v[226:229], v7 offset:32768\n"
"v_mfma_f32_16x16x32_bf16 v[46:49], a[84:87], v[194:197], v[46:49]\n"
"s_waitcnt lgkmcnt(4)\n"
"v_mfma_f32_16x16x32_bf16 v[18:21], a[88:91], v[198:201], v[18:21]\n"
"ds_read_b128 v[190:193], v7 offset:34816\n"
"v_mfma_f32_16x16x32_bf16 v[34:37], a[92:95], v[198:201], v[34:37]\n"
"buffer_load_dwordx4 a[128:131], v5, s[28:31], s64 offen offset:0 sc1\n"
"v_mfma_f32_16x16x32_bf16 v[22:25], a[88:91], v[202:205], v[22:25]\n"
"ds_read_b128 v[194:197], v7 offset:35840\n"
"v_mfma_f32_16x16x32_bf16 v[38:41], a[92:95], v[202:205], v[38:41]\n"
"buffer_load_dwordx4 a[132:135], v5, s[28:31], s64 offen offset:1024 sc1\n"
"v_mfma_f32_16x16x32_bf16 v[26:29], a[88:91], v[206:209], v[26:29]\n"
"ds_read_b128 v[198:201], v7 offset:41984\n"
"v_mfma_f32_16x16x32_bf16 v[42:45], a[92:95], v[206:209], v[42:45]\n"
"v_mfma_f32_16x16x32_bf16 v[30:33], a[88:91], v[210:213], v[30:33]\n"
"ds_read_b128 v[202:205], v7 offset:43008\n"
"v_mfma_f32_16x16x32_bf16 v[46:49], a[92:95], v[210:213], v[46:49]\n"
"s_waitcnt lgkmcnt(4)\n"
"s_waitcnt vmcnt(14)\n"
"v_mfma_f32_16x16x32_bf16 v[18:21], a[96:99], v[214:217], v[18:21]\n"
"ds_read_b128 v[206:209], v7 offset:36864\n"
"v_mfma_f32_16x16x32_bf16 v[34:37], a[100:103], v[214:217], v[34:37]\n"
"s_bfe_u32 s64, s84, 262172\n"
"s_mul_i32 s64, s64, 14336\n"
"s_add_u32 s64, s64, s66\n"
"buffer_load_dwordx4 a[136:139], v2, s[28:31], s64 offen offset:0 sc1\n"
"v_mfma_f32_16x16x32_bf16 v[22:25], a[96:99], v[218:221], v[22:25]\n"
"ds_read_b128 v[210:213], v7 offset:37888\n"
"v_mfma_f32_16x16x32_bf16 v[38:41], a[100:103], v[218:221], v[38:41]\n"
"buffer_load_dwordx4 a[140:143], v2, s[28:31], s64 offen offset:1024 sc1\n"
"v_mfma_f32_16x16x32_bf16 v[26:29], a[96:99], v[222:225], v[26:29]\n"
"ds_read_b128 v[214:217], v7 offset:44032\n"
"v_mfma_f32_16x16x32_bf16 v[42:45], a[100:103], v[222:225], v[42:45]\n"
"v_mfma_f32_16x16x32_bf16 v[30:33], a[96:99], v[226:229], v[30:33]\n"
"ds_read_b128 v[218:221], v7 offset:45056\n"
"v_mfma_f32_16x16x32_bf16 v[46:49], a[100:103], v[226:229], v[46:49]\n"
"s_waitcnt lgkmcnt(4)\n"
"s_waitcnt vmcnt(12)\n"
"v_mfma_f32_16x16x32_bf16 v[18:21], a[104:107], v[190:193], v[18:21]\n"
"ds_read_b128 v[222:225], v7 offset:38912\n"
"v_mfma_f32_16x16x32_bf16 v[34:37], a[108:111], v[190:193], v[34:37]\n"
"buffer_load_dwordx4 a[144:147], v2, s[28:31], s64 offen offset:2048 sc1\n"
"v_mfma_f32_16x16x32_bf16 v[22:25], a[104:107], v[194:197], v[22:25]\n"
"ds_read_b128 v[226:229], v7 offset:39936\n"
"v_mfma_f32_16x16x32_bf16 v[38:41], a[108:111], v[194:197], v[38:41]\n"
"buffer_load_dwordx4 a[148:151], v2, s[28:31], s64 offen offset:3072 sc1\n"
"v_mfma_f32_16x16x32_bf16 v[26:29], a[104:107], v[198:201], v[26:29]\n"
"ds_read_b128 v[190:193], v7 offset:46080\n"
"v_mfma_f32_16x16x32_bf16 v[42:45], a[108:111], v[198:201], v[42:45]\n"
"buffer_load_dwordx4 a[152:155], v3, s[28:31], s64 offen offset:0 sc1\n"
"v_mfma_f32_16x16x32_bf16 v[30:33], a[104:107], v[202:205], v[30:33]\n"
"ds_read_b128 v[194:197], v7 offset:47104\n"
"v_mfma_f32_16x16x32_bf16 v[46:49], a[108:111], v[202:205], v[46:49]\n"
"s_waitcnt lgkmcnt(4)\n"
"s_waitcnt vmcnt(12)\n"
"v_mfma_f32_16x16x32_bf16 v[18:21], a[112:115], v[206:209], v[18:21]\n"
"ds_read_b64 v[198:199], v7 offset:26624\n"
"ds_read_b64 v[200:201], v7 offset:40960\n"
"s_waitcnt vmcnt(11)\n"
"v_mfma_f32_16x16x32_bf16 v[34:37], a[116:119], v[206:209], v[34:37]\n"
"buffer_load_dwordx4 a[156:159], v3, s[28:31], s64 offen offset:1024 sc1\n"
"v_mfma_f32_16x16x32_bf16 v[22:25], a[112:115], v[210:213], v[22:25]\n"
"ds_read_b64 v[202:203], v7 offset:26632\n"
"ds_read_b64 v[204:205], v7 offset:40968\n"
"v_mfma_f32_16x16x32_bf16 v[38:41], a[116:119], v[210:213], v[38:41]\n"
"v_mfma_f32_16x16x32_bf16 v[26:29], a[112:115], v[214:217], v[26:29]\n"
"ds_read_b64 v[206:207], v7 offset:33792\n"
"ds_read_b64 v[208:209], v7 offset:48128\n"
"v_mfma_f32_16x16x32_bf16 v[42:45], a[116:119], v[214:217], v[42:45]\n"
"buffer_load_dwordx4 a[160:163], v3, s[28:31], s64 offen offset:2048 sc1\n"
"v_mfma_f32_16x16x32_bf16 v[30:33], a[112:115], v[218:221], v[30:33]\n"
"ds_read_b64 v[210:211], v7 offset:33800\n"
"ds_read_b64 v[212:213], v7 offset:48136\n"
"v_mfma_f32_16x16x32_bf16 v[46:49], a[116:119], v[218:221], v[46:49]\n"
"s_waitcnt lgkmcnt(8)\n"
"s_waitcnt vmcnt(10)\n"
"v_mfma_f32_16x16x32_bf16 v[18:21], a[120:123], v[222:225], v[18:21]\n"
"s_barrier\n"
"ds_read_b128 v[214:217], v7 offset:49152\n"
"s_waitcnt vmcnt(9)\n"
"v_mfma_f32_16x16x32_bf16 v[34:37], a[124:127], v[222:225], v[34:37]\n"
"buffer_load_dwordx4 a[164:167], v3, s[28:31], s64 offen offset:3072 sc1\n"
"v_mfma_f32_16x16x32_bf16 v[22:25], a[120:123], v[226:229], v[22:25]\n"
"ds_read_b128 v[218:221], v7 offset:50176\n"
"v_mfma_f32_16x16x32_bf16 v[38:41], a[124:127], v[226:229], v[38:41]\n"
"v_mfma_f32_16x16x32_bf16 v[26:29], a[120:123], v[190:193], v[26:29]\n"
"ds_read_b128 v[222:225], v7 offset:56320\n"
"v_mfma_f32_16x16x32_bf16 v[42:45], a[124:127], v[190:193], v[42:45]\n"
"buffer_load_dwordx4 a[168:171], v4, s[28:31], s64 offen offset:0 sc1\n"
"v_mfma_f32_16x16x32_bf16 v[30:33], a[120:123], v[194:197], v[30:33]\n"
"ds_read_b128 v[226:229], v7 offset:57344\n"
"v_mfma_f32_16x16x32_bf16 v[46:49], a[124:127], v[194:197], v[46:49]\n"
"s_waitcnt lgkmcnt(4)\n"
"s_waitcnt vmcnt(10)\n"
"v_mfma_f32_16x16x32_bf16 v[18:21], a[128:131], v[198:201], v[18:21]\n"
"ds_read_b128 v[190:193], v7 offset:51200\n"
"s_waitcnt vmcnt(9)\n"
"v_mfma_f32_16x16x32_bf16 v[34:37], a[132:135], v[198:201], v[34:37]\n"
"buffer_load_dwordx4 a[172:175], v4, s[28:31], s64 offen offset:1024 sc1\n"
"v_mfma_f32_16x16x32_bf16 v[22:25], a[128:131], v[202:205], v[22:25]\n"
"ds_read_b128 v[194:197], v7 offset:52224\n"
"v_mfma_f32_16x16x32_bf16 v[38:41], a[132:135], v[202:205], v[38:41]\n"
"v_mfma_f32_16x16x32_bf16 v[26:29], a[128:131], v[206:209], v[26:29]\n"
"ds_read_b128 v[198:201], v7 offset:58368\n"
"v_mfma_f32_16x16x32_bf16 v[42:45], a[132:135], v[206:209], v[42:45]\n"
"buffer_load_dwordx4 a[176:179], v4, s[28:31], s64 offen offset:2048 sc1\n"
"v_mfma_f32_16x16x32_bf16 v[30:33], a[128:131], v[210:213], v[30:33]\n"
"ds_read_b128 v[202:205], v7 offset:59392\n"
"v_mfma_f32_16x16x32_bf16 v[46:49], a[132:135], v[210:213], v[46:49]\n"
"s_waitcnt lgkmcnt(4)\n"
"s_waitcnt vmcnt(10)\n"
"v_mfma_f32_16x16x32_bf16 v[18:21], a[136:139], v[214:217], v[18:21]\n"
"ds_read_b128 v[206:209], v7 offset:53248\n"
"s_waitcnt vmcnt(9)\n"
"v_mfma_f32_16x16x32_bf16 v[34:37], a[140:143], v[214:217], v[34:37]\n"
"buffer_load_dwordx4 a[180:183], v4, s[28:31], s64 offen offset:3072 sc1\n"
"v_mfma_f32_16x16x32_bf16 v[22:25], a[136:139], v[218:221], v[22:25]\n"
"ds_read_b128 v[210:213], v7 offset:54272\n"
"v_mfma_f32_16x16x32_bf16 v[38:41], a[140:143], v[218:221], v[38:41]\n"
"v_mfma_f32_16x16x32_bf16 v[26:29], a[136:139], v[222:225], v[26:29]\n"
"ds_read_b128 v[214:217], v7 offset:60416\n"
"v_mfma_f32_16x16x32_bf16 v[42:45], a[140:143], v[222:225], v[42:45]\n"
"buffer_load_dwordx4 a[184:187], v5, s[28:31], s64 offen offset:0 sc1\n"
"v_mfma_f32_16x16x32_bf16 v[30:33], a[136:139], v[226:229], v[30:33]\n"
"ds_read_b128 v[218:221], v7 offset:61440\n"
"v_mfma_f32_16x16x32_bf16 v[46:49], a[140:143], v[226:229], v[46:49]\n"
"s_waitcnt lgkmcnt(4)\n"
"s_waitcnt vmcnt(10)\n"
"v_mfma_f32_16x16x32_bf16 v[18:21], a[144:147], v[190:193], v[18:21]\n"
"ds_read_b128 v[222:225], v7 offset:63488\n"
"s_waitcnt vmcnt(9)\n"
"v_mfma_f32_16x16x32_bf16 v[34:37], a[148:151], v[190:193], v[34:37]\n"
"buffer_load_dwordx4 a[188:191], v5, s[28:31], s64 offen offset:1024 sc1\n"
"v_mfma_f32_16x16x32_bf16 v[22:25], a[144:147], v[194:197], v[22:25]\n"
"ds_read_b128 v[226:229], v7 offset:64512\n"
"v_mfma_f32_16x16x32_bf16 v[38:41], a[148:151], v[194:197], v[38:41]\n"
"v_mfma_f32_16x16x32_bf16 v[26:29], a[144:147], v[198:201], v[26:29]\n"
"ds_read_b128 v[190:193], v8 offset:0\n"
"v_mfma_f32_16x16x32_bf16 v[42:45], a[148:151], v[198:201], v[42:45]\n"
"v_mfma_f32_16x16x32_bf16 v[30:33], a[144:147], v[202:205], v[30:33]\n"
"ds_read_b128 v[194:197], v8 offset:1024\n"
"v_mfma_f32_16x16x32_bf16 v[46:49], a[148:151], v[202:205], v[46:49]\n"
"s_waitcnt lgkmcnt(4)\n"
"s_waitcnt vmcnt(9)\n"
"v_mfma_f32_16x16x32_bf16 v[18:21], a[152:155], v[206:209], v[18:21]\n"
"ds_read_b128 v[198:201], v8 offset:2048\n"
"s_waitcnt vmcnt(8)\n"
"v_mfma_f32_16x16x32_bf16 v[34:37], a[156:159], v[206:209], v[34:37]\n"
"v_mfma_f32_16x16x32_bf16 v[22:25], a[152:155], v[210:213], v[22:25]\n"
"ds_read_b128 v[202:205], v8 offset:3072\n"
"v_mfma_f32_16x16x32_bf16 v[38:41], a[156:159], v[210:213], v[38:41]\n"
"v_mfma_f32_16x16x32_bf16 v[26:29], a[152:155], v[214:217], v[26:29]\n"
"ds_read_b64 v[206:207], v7 offset:55296\n"
"ds_read_b64 v[208:209], v8 offset:4096\n"
"v_mfma_f32_16x16x32_bf16 v[42:45], a[156:159], v[214:217], v[42:45]\n"
"v_mfma_f32_16x16x32_bf16 v[30:33], a[152:155], v[218:221], v[30:33]\n"
"ds_read_b64 v[210:211], v7 offset:55304\n"
"ds_read_b64 v[212:213], v8 offset:4104\n"
"v_mfma_f32_16x16x32_bf16 v[46:49], a[156:159], v[218:221], v[46:49]\n"
"s_waitcnt lgkmcnt(6)\n"
"s_waitcnt vmcnt(7)\n"
"v_mfma_f32_16x16x32_bf16 v[18:21], a[160:163], v[222:225], v[18:21]\n"
"ds_read_b128 v[214:217], v8 offset:5120\n"
"s_waitcnt vmcnt(6)\n"
"v_mfma_f32_16x16x32_bf16 v[34:37], a[164:167], v[222:225], v[34:37]\n"
"v_mfma_f32_16x16x32_bf16 v[22:25], a[160:163], v[226:229], v[22:25]\n"
"ds_read_b128 v[218:221], v8 offset:6144\n"
"v_mfma_f32_16x16x32_bf16 v[38:41], a[164:167], v[226:229], v[38:41]\n"
"s_waitcnt vmcnt(5)\n"
"v_mfma_f32_16x16x32_bf16 v[18:21], a[168:171], v[190:193], v[18:21]\n"
"ds_read_b128 v[222:225], v8 offset:7168\n"
"s_waitcnt vmcnt(4)\n"
"v_mfma_f32_16x16x32_bf16 v[34:37], a[172:175], v[190:193], v[34:37]\n"
"v_mfma_f32_16x16x32_bf16 v[22:25], a[168:171], v[194:197], v[22:25]\n"
".Lmk_pg_20:\n"
"s_cbranch_execz .Lmk_ret_4\n"
"ds_read_b128 v[226:229], v8 offset:8192\n"
"v_mfma_f32_16x16x32_bf16 v[38:41], a[172:175], v[194:197], v[38:41]\n"
"s_waitcnt lgkmcnt(4)\n"
"s_waitcnt vmcnt(3)\n"
"v_mfma_f32_16x16x32_bf16 v[18:21], a[176:179], v[198:201], v[18:21]\n"
"ds_read_b128 v[190:193], v8 offset:9216\n"
"s_waitcnt vmcnt(2)\n"
"v_mfma_f32_16x16x32_bf16 v[34:37], a[180:183], v[198:201], v[34:37]\n"
"v_mfma_f32_16x16x32_bf16 v[22:25], a[176:179], v[202:205], v[22:25]\n"
"ds_read_b128 v[194:197], v8 offset:10240\n"
"v_mfma_f32_16x16x32_bf16 v[38:41], a[180:183], v[202:205], v[38:41]\n"
"s_waitcnt vmcnt(1)\n"
"v_mfma_f32_16x16x32_bf16 v[18:21], a[184:187], v[206:209], v[18:21]\n"
"ds_read_b64 v[198:199], v7 offset:62464\n"
"ds_read_b64 v[200:201], v8 offset:11264\n"
"s_waitcnt vmcnt(0)\n"
"v_mfma_f32_16x16x32_bf16 v[34:37], a[188:191], v[206:209], v[34:37]\n"
"v_mfma_f32_16x16x32_bf16 v[22:25], a[184:187], v[210:213], v[22:25]\n"
"ds_read_b64 v[202:203], v7 offset:62472\n"
"ds_read_b64 v[204:205], v8 offset:11272\n"
"v_mfma_f32_16x16x32_bf16 v[38:41], a[188:191], v[210:213], v[38:41]\n"
"s_waitcnt lgkmcnt(6)\n"
"v_mfma_f32_16x16x32_bf16 v[26:29], a[160:163], v[214:217], v[26:29]\n"
"v_mfma_f32_16x16x32_bf16 v[42:45], a[164:167], v[214:217], v[42:45]\n"
"v_mfma_f32_16x16x32_bf16 v[30:33], a[160:163], v[218:221], v[30:33]\n"
"v_mfma_f32_16x16x32_bf16 v[46:49], a[164:167], v[218:221], v[46:49]\n"
"v_mfma_f32_16x16x32_bf16 v[26:29], a[168:171], v[222:225], v[26:29]\n"
"v_cvt_pk_bf16_f32 v236, v18, v19\n"
"v_cvt_pk_bf16_f32 v237, v20, v21\n"
"v_mfma_f32_16x16x32_bf16 v[42:45], a[172:175], v[222:225], v[42:45]\n"
"v_cvt_pk_bf16_f32 v238, v34, v35\n"
"v_cvt_pk_bf16_f32 v239, v36, v37\n"
"v_mfma_f32_16x16x32_bf16 v[30:33], a[168:171], v[226:229], v[30:33]\n"
"ds_write_b128 v50, v[236:239] offset:0\n"
"v_cvt_pk_bf16_f32 v240, v22, v23\n"
"v_mfma_f32_16x16x32_bf16 v[46:49], a[172:175], v[226:229], v[46:49]\n"
"s_waitcnt lgkmcnt(1)\n"
"v_cvt_pk_bf16_f32 v241, v24, v25\n"
"v_mfma_f32_16x16x32_bf16 v[26:29], a[176:179], v[190:193], v[26:29]\n"
"v_cvt_pk_bf16_f32 v242, v38, v39\n"
"v_cvt_pk_bf16_f32 v243, v40, v41\n"
"v_mfma_f32_16x16x32_bf16 v[42:45], a[180:183], v[190:193], v[42:45]\n"
"ds_write_b128 v50, v[240:243] offset:4096\n"
"s_add_u32 s57, s89, 0\n"
"s_add_u32 s58, s89, 16384\n"
"s_waitcnt lgkmcnt(0)\n"
"s_barrier\n"
"ds_read_b128 v[54:57], v51 offset:0\n"
"ds_read_b128 v[58:61], v52 offset:1024\n"
"v_mfma_f32_16x16x32_bf16 v[30:33], a[176:179], v[194:197], v[30:33]\n"
"v_mfma_f32_16x16x32_bf16 v[46:49], a[180:183], v[194:197], v[46:49]\n"
"v_mfma_f32_16x16x32_bf16 v[26:29], a[184:187], v[198:201], v[26:29]\n"
"v_mfma_f32_16x16x32_bf16 v[42:45], a[188:191], v[198:201], v[42:45]\n"
"v_mfma_f32_16x16x32_bf16 v[30:33], a[184:187], v[202:205], v[30:33]\n"
"v_mfma_f32_16x16x32_bf16 v[46:49], a[188:191], v[202:205], v[46:49]\n"
"s_waitcnt lgkmcnt(1)\n"
"buffer_store_dwordx4 v[54:57], v53, s[32:35], s57 offen sc1\n"
"s_waitcnt lgkmcnt(0)\n"
"buffer_store_dwordx4 v[58:61], v53, s[32:35], s58 offen sc1\n"
"s_nop 0\n"
"v_cvt_pk_bf16_f32 v244, v26, v27\n"
"v_cvt_pk_bf16_f32 v245, v28, v29\n"
"v_cvt_pk_bf16_f32 v246, v42, v43\n"
"v_cvt_pk_bf16_f32 v247, v44, v45\n"
"ds_write_b128 v50, v[244:247] offset:8192\n"
"v_cvt_pk_bf16_f32 v248, v30, v31\n"
"v_cvt_pk_bf16_f32 v249, v32, v33\n"
"v_cvt_pk_bf16_f32 v250, v46, v47\n"
"v_cvt_pk_bf16_f32 v251, v48, v49\n"
"ds_write_b128 v50, v[248:251] offset:12288\n"
"s_add_u32 s57, s89, 65536\n"
"s_add_u32 s58, s89, 81920\n"
"s_waitcnt lgkmcnt(0)\n"
"s_barrier\n"
"ds_read_b128 v[54:57], v51 offset:8192\n"
"ds_read_b128 v[58:61], v52 offset:9216\n"
"s_waitcnt lgkmcnt(1)\n"
"buffer_store_dwordx4 v[54:57], v53, s[32:35], s57 offen sc1\n"
"s_waitcnt lgkmcnt(0)\n"
"buffer_store_dwordx4 v[58:61], v53, s[32:35], s58 offen sc1\n"
".Lmk_abort:\n"
"s_endpgm\n"
".Lmk_fspin_5:\n"
"s_waitcnt vmcnt(0)\n"
"s_sleep 2\n"
"s_add_u32 s55, s55, 1\n"
"s_cmp_gt_u32 s55, 0x40000\n"
"s_cbranch_scc1 .Lmk_abort\n"
"buffer_load_dwordx4 v[80:83], v2, s[28:31], s82 offen offset:0 sc1\n"
"buffer_load_dwordx4 v[84:87], v2, s[28:31], s82 offen offset:1024 sc1\n"
"s_waitcnt vmcnt(1)\n"
"v_bitop3_b32 v80, v80, v82, s26 bitop3:0x7e\n"
"s_waitcnt vmcnt(0)\n"
"v_bitop3_b32 v84, v84, v86, s26 bitop3:0x7e\n"
"v_or_b32 v80, v80, v84\n"
"v_cmp_ne_u32 vcc, 0, v80\n"
"s_nop 0\n"
"s_cbranch_vccnz .Lmk_fspin_5\n"
"s_branch .Lmk_fback_6\n"
".Lmk_slow_8:\n"
"s_waitcnt vmcnt(0) lgkmcnt(0)\n"
"s_nop 15\n"
"v_mov_b32 v192, 0xff800000\n"
"v_mov_b32 v208, 0xff800000\n"
"s_mov_b32 s56, 524288\n"
".Lmk_mx_10:\n"
"buffer_load_dwordx4 v[18:21], v2, s[28:31], s56 offen offset:0 sc1\n"
"buffer_load_dwordx4 v[22:25], v2, s[28:31], s56 offen offset:1024 sc1\n"
"s_waitcnt vmcnt(1)\n"
"v_mfma_f32_32x32x16_bf16 v[40:55], v[18:21], v[236:239], 0\n"
"s_waitcnt vmcnt(0)\n"
"v_mfma_f32_32x32x16_bf16 v[40:55], v[22:25], v[240:243], v[40:55]\n"
"v_mfma_f32_32x32x16_bf16 v[56:71], v[18:21], v[244:247], 0\n"
"v_mfma_f32_32x32x16_bf16 v[56:71], v[22:25], v[248:251], v[56:71]\n"
"s_nop 9\n"
"v_max3_f32 v192, v192, v40, v41\n"
"v_max3_f32 v192, v192, v42, v43\n"
"v_max3_f32 v192, v192, v44, v45\n"
"v_max3_f32 v192, v192, v46, v47\n"
"v_max3_f32 v192, v192, v48, v49\n"
"v_max3_f32 v192, v192, v50, v51\n"
"v_max3_f32 v192, v192, v52, v53\n"
"v_max3_f32 v192, v192, v54, v55\n"
"v_max3_f32 v208, v208, v56, v57\n"
"v_max3_f32 v208, v208, v58, v59\n"
"v_max3_f32 v208, v208, v60, v61\n"
"v_max3_f32 v208, v208, v62, v63\n"
"v_max3_f32 v208, v208, v64, v65\n"
"v_max3_f32 v208, v208, v66, v67\n"
"v_max3_f32 v208, v208, v68, v69\n"
"v_max3_f32 v208, v208, v70, v71\n"
"s_add_u32 s56, s56, 4096\n"
"s_cmp_lt_u32 s56, 786432\n"
"s_cbranch_scc1 .Lmk_mx_10\n"
"v_mov_b32 v193, v192\n"
"s_nop 1\n"
"v_permlane32_swap_b32 v192, v193\n"
"v_max_f32 v192, v192, v193\n"
"v_sub_f32 v192, 0, v192\n"
"v_mov_b32 v193, v192\n"
"v_mov_b32 v194, v192\n"
"v_mov_b32 v195, v192\n"
"v_mov_b32 v196, v192\n"
"v_mov_b32 v197, v192\n"
"v_mov_b32 v198, v192\n"
"v_mov_b32 v199, v192\n"
"v_mov_b32 v200, v192\n"
"v_mov_b32 v201, v192\n"
"v_mov_b32 v202, v192\n"
"v_mov_b32 v203, v192\n"
"v_mov_b32 v204, v192\n"
"v_mov_b32 v205, v192\n"
"v_mov_b32 v206, v192\n"
"v_mov_b32 v207, v192\n"
"v_mov_b32 v209, v208\n"
"s_nop 1\n"
"v_permlane32_swap_b32 v208, v209\n"
"v_max_f32 v208, v208, v209\n"
"v_sub_f32 v208, 0, v208\n"
"v_mov_b32 v209, v208\n"
"v_mov_b32 v210, v208\n"
"v_mov_b32 v211, v208\n"
"v_mov_b32 v212, v208\n"
"v_mov_b32 v213, v208\n"
"v_mov_b32 v214, v208\n"
"v_mov_b32 v215, v208\n"
"v_mov_b32 v216, v208\n"
"v_mov_b32 v217, v208\n"
"v_mov_b32 v218, v208\n"
"v_mov_b32 v219, v208\n"
"v_mov_b32 v220, v208\n"
"v_mov_b32 v221, v208\n"
"v_mov_b32 v222, v208\n"
"v_mov_b32 v223, v208\n"
"v_mfma_f32_32x32x16_bf16 v[18:33], v[128:131], v[236:239], v[192:207]\n"
"v_mfma_f32_32x32x16_bf16 v[18:33], v[132:135], v[240:243], v[18:33]\n"
"s_nop 15\n"
"s_branch .Lmk_fast_9\n"
".Lmk_fspin_13:\n"
"s_waitcnt vmcnt(0)\n"
"s_sleep 2\n"
"s_add_u32 s55, s55, 1\n"
"s_cmp_gt_u32 s55, 0x40000\n"
"s_cbranch_scc1 .Lmk_abort\n"
"buffer_load_dwordx4 v[34:37], v2, s[28:31], s82 offen sc1\n"
"s_waitcnt vmcnt(0)\n"
"v_bitop3_b32 v34, v34, v35, s26 bitop3:0x7e\n"
"v_bitop3_b32 v36, v36, v37, s26 bitop3:0x7e\n"
"v_or_b32 v34, v34, v36\n"
"v_cmp_ne_u32 vcc, 0, v34\n"
"s_nop 0\n"
"s_cbranch_vccnz .Lmk_fspin_13\n"
"s_branch .Lmk_fback_14\n"
".Lmk_fspin_16:\n"
"s_waitcnt vmcnt(0)\n"
"s_sleep 2\n"
"s_add_u32 s55, s55, 1\n"
"s_cmp_gt_u32 s55, 0x40000\n"
"s_cbranch_scc1 .Lmk_abort\n"
"buffer_load_dwordx4 v[34:37], v2, s[28:31], s82 offen sc1\n"
"s_waitcnt vmcnt(0)\n"
"v_bitop3_b32 v34, v34, v35, s26 bitop3:0x7e\n"
"v_bitop3_b32 v36, v36, v37, s26 bitop3:0x7e\n"
"v_or_b32 v34, v34, v36\n"
"v_cmp_ne_u32 vcc, 0, v34\n"
"s_nop 0\n"
"s_cbranch_vccnz .Lmk_fspin_16\n"
"s_branch .Lmk_fback_17\n"
".Lmk_spin_21:\n"
"s_add_u32 s55, s55, 1\n"
"s_cmp_gt_u32 s55, 0x40000\n"
"s_cbranch_scc1 .Lmk_abort\n"
"v_mov_b32 v11, 0\n"
"s_mov_b64 exec, 1\n"
"buffer_atomic_add v11, v9, s[28:31], s71 offen sc0 sc1\n"
"s_mov_b64 exec, -1\n"
"s_waitcnt vmcnt(0)\n"
"s_nop 0\n"
"v_readfirstlane_b32 s61, v11\n"
"s_nop 0\n"
"s_cmp_ge_u32 s61, 8\n"
"s_cbranch_scc0 .Lmk_spin_21\n"
"s_branch .Lmk_back_22\n"
".Lmk_spin_24:\n"
"s_add_u32 s55, s55, 1\n"
"s_cmp_gt_u32 s55, 0x40000\n"
"s_cbranch_scc1 .Lmk_abort\n"
"v_mov_b32 v11, 0\n"
"s_mov_b64 exec, 1\n"
"buffer_atomic_add v11, v9, s[28:31], s71 offen sc0 sc1\n"
"s_mov_b64 exec, -1\n"
"s_waitcnt vmcnt(0)\n"
"s_nop 0\n"
"v_readfirstlane_b32 s61, v11\n"
"s_nop 0\n"
"s_cmp_ge_u32 s61, 8\n"
"s_cbranch_scc0 .Lmk_spin_24\n"
"s_branch .Lmk_back_25\n"
".Lmk_fspin_26:\n"
"s_waitcnt vmcnt(0)\n"
"s_sleep 2\n"
"s_add_u32 s55, s55, 1\n"
"s_cmp_gt_u32 s55, 0x40000\n"
"s_cbranch_scc1 .Lmk_abort\n"
"buffer_load_dwordx4 v[236:239], v2, s[28:31], s80 offen offset:0 sc1\n"
"buffer_load_dwordx4 v[240:243], v2, s[28:31], s80 offen offset:1024 sc1\n"
"buffer_load_dwordx4 v[244:247], v2, s[28:31], s80 offen offset:2048 sc1\n"
"buffer_load_dwordx4 v[248:251], v2, s[28:31], s80 offen offset:3072 sc1\n"
"s_waitcnt vmcnt(3)\n"
"v_bitop3_b32 v237, v237, v239, s26 bitop3:0x7e\n"
"s_waitcnt vmcnt(2)\n"
"v_bitop3_b32 v241, v241, v243, s26 bitop3:0x7e\n"
"s_waitcnt vmcnt(1)\n"
"v_bitop3_b32 v245, v245, v247, s26 bitop3:0x7e\n"
"s_waitcnt vmcnt(0)\n"
"v_bitop3_b32 v249, v249, v251, s26 bitop3:0x7e\n"
"v_or3_b32 v237, v237, v249, v245\n"
"v_or_b32 v237, v237, v241\n"
"v_cmp_ne_u32 vcc, 0, v237\n"
"s_nop 0\n"
"s_cbranch_vccnz .Lmk_fspin_26\n"
"s_branch .Lmk_fback_27\n"
".Lmk_spin_30:\n"
"s_add_u32 s55, s55, 1\n"
"s_cmp_gt_u32 s55, 0x40000\n"
"s_cbranch_scc1 .Lmk_abort\n"
"v_mov_b32 v11, 0\n"
"s_mov_b64 exec, 1\n"
"buffer_atomic_add v11, v9, s[28:31], s70 offen sc0 sc1\n"
"s_mov_b64 exec, -1\n"
"s_waitcnt vmcnt(0)\n"
"s_nop 0\n"
"v_readfirstlane_b32 s61, v11\n"
"s_nop 0\n"
"s_cmp_ge_u32 s61, 16\n"
"s_cbranch_scc0 .Lmk_spin_30\n"
"s_branch .Lmk_back_31\n"
".Lmk_fspin_32:\n"
"s_waitcnt vmcnt(0)\n"
"s_sleep 2\n"
"s_add_u32 s55, s55, 1\n"
"s_cmp_gt_u32 s55, 0x40000\n"
"s_cbranch_scc1 .Lmk_abort\n"
"buffer_load_dwordx4 v[236:239], v2, s[28:31], s80 offen offset:0 sc1\n"
"buffer_load_dwordx4 v[240:243], v2, s[28:31], s80 offen offset:1024 sc1\n"
"buffer_load_dwordx4 v[244:247], v2, s[28:31], s80 offen offset:2048 sc1\n"
"buffer_load_dwordx4 v[248:251], v2, s[28:31], s80 offen offset:3072 sc1\n"
"buffer_load_dwordx4 v[170:173], v3, s[28:31], s80 offen offset:0 sc1\n"
"buffer_load_dwordx4 v[174:177], v3, s[28:31], s80 offen offset:1024 sc1\n"
"buffer_load_dwordx4 v[162:165], v3, s[28:31], s80 offen offset:2048 sc1\n"
"buffer_load_dwordx4 v[166:169], v3, s[28:31], s80 offen offset:3072 sc1\n"
"s_waitcnt vmcnt(7)\n"
"v_bitop3_b32 v237, v237, v239, s26 bitop3:0x7e\n"
"s_waitcnt vmcnt(6)\n"
"v_bitop3_b32 v241, v241, v243, s26 bitop3:0x7e\n"
"s_waitcnt vmcnt(5)\n"
"v_bitop3_b32 v245, v245, v247, s26 bitop3:0x7e\n"
"s_waitcnt vmcnt(4)\n"
"v_bitop3_b32 v249, v249, v251, s26 bitop3:0x7e\n"
"s_waitcnt vmcnt(3)\n"
"v_bitop3_b32 v171, v171, v173, s26 bitop3:0x7e\n"
"s_waitcnt vmcnt(2)\n"
"v_bitop3_b32 v175, v175, v177, s26 bitop3:0x7e\n"
"s_waitcnt vmcnt(1)\n"
"v_bitop3_b32 v163, v163, v165, s26 bitop3:0x7e\n"
"s_waitcnt vmcnt(0)\n"
"v_bitop3_b32 v167, v167, v169, s26 bitop3:0x7e\n"
"v_or3_b32 v237, v237, v167, v163\n"
"v_or3_b32 v237, v237, v175, v171\n"
"v_or3_b32 v237, v237, v249, v245\n"
"v_or_b32 v237, v237, v241\n"
"v_cmp_ne_u32 vcc, 0, v237\n"
"s_nop 0\n"
"s_cbranch_vccnz .Lmk_fspin_32\n"
"s_branch .Lmk_fback_33\n"
".Lmk_spin_35:\n"
"s_add_u32 s55, s55, 1\n"
"s_cmp_gt_u32 s55, 0x40000\n"
"s_cbranch_scc1 .Lmk_abort\n"
"v_mov_b32 v11, 0\n"
"s_mov_b64 exec, 1\n"
"buffer_atomic_add v11, v9, s[28:31], s85 offen sc0 sc1\n"
"s_mov_b64 exec, -1\n"
"s_waitcnt vmcnt(0)\n"
"s_nop 0\n"
"v_readfirstlane_b32 s61, v11\n"
"s_nop 0\n"
"s_cmp_ge_u32 s61, 16\n"
"s_cbranch_scc0 .Lmk_spin_35\n"
"s_branch .Lmk_back_36\n"
".Lmk_spin_40:\n"
"s_add_u32 s55, s55, 1\n"
"s_cmp_gt_u32 s55, 0x40000\n"
"s_cbranch_scc1 .Lmk_abort\n"
"v_mov_b32 v11, 0\n"
"s_mov_b64 exec, 1\n"
"buffer_atomic_add v11, v9, s[28:31], s71 offen sc0 sc1\n"
"s_mov_b64 exec, -1\n"
"s_waitcnt vmcnt(0)\n"
"s_nop 0\n"
"v_readfirstlane_b32 s61, v11\n"
"s_nop 0\n"
"s_cmp_ge_u32 s61, 8\n"
"s_cbranch_scc0 .Lmk_spin_40\n"
"s_branch .Lmk_back_41\n"
  :: "s"(__builtin_amdgcn_kernarg_segment_ptr()), "s"(blockIdx.x), "{v0}"(tid) : "v1","v2","v3","v4","v5","v6","v7","v8","v9","v10","v11","v12","v13","v14","v15","v16","v17","v18","v19","v20","v21","v22","v23","v24","v25","v26","v27","v28","v29","v30","v31","v32","v33","v34","v35","v36","v37","v38","v39","v40","v41","v42","v43","v44","v45","v46","v47","v48","v49","v50","v51","v52","v53","v54","v55","v56","v57","v58","v59","v60","v61","v62","v63","v64","v65","v66","v67","v68","v69","v70","v71","v72","v73","v74","v75","v76","v77","v78","v79","v80","v81","v82","v83","v84","v85","v86","v87","v88","v89","v90","v91","v92","v93","v94","v95","v96","v97","v98","v99","v100","v101","v102","v103","v104","v105","v106","v107","v108","v109","v110","v111","v112","v113","v114","v115","v116","v117","v118","v119","v120","v121","v122","v123","v124","v125","v126","v127","v128","v129","v130","v131","v132","v133","v134","v135","v136","v137","v138","v139","v140","v141","v142","v143","v144","v145","v146","v147","v148","v149","v150","v151","v152","v153","v154","v155","v156","v157","v158","v159","v160","v161","v162","v163","v164","v165","v166","v167","v168","v169","v170","v171","v172","v173","v174","v175","v176","v177","v178","v179","v180","v181","v182","v183","v184","v185","v186","v187","v188","v189","v190","v191","v192","v193","v194","v195","v196","v197","v198","v199","v200","v201","v202","v203","v204","v205","v206","v207","v208","v209","v210","v211","v212","v213","v214","v215","v216","v217","v218","v219","v220","v221","v222","v223","v224","v225","v226","v227","v228","v229","v230","v231","v232","v233","v234","v235","v236","v237","v238","v239","v240","v241","v242","v243","v244","v245","v246","v247","v248","v249","v250","v251","v252","v253","v254","v255","a0","a1","a2","a3","a4","a5","a6","a7","a8","a9","a10","a11","a12","a13","a14","a15","a16","a17","a18","a19","a20","a21","a22","a23","a24","a25","a26","a27","a28","a29","a30","a31","a32","a33","a34","a35","a36","a37","a38","a39","a40","a41","a42","a43","a44","a45","a46","a47","a48","a49","a50","a51","a52","a53","a54","a55","a56","a57","a58","a59","a60","a61","a62","a63","a64","a65","a66","a67","a68","a69","a70","a71","a72","a73","a74","a75","a76","a77","a78","a79","a80","a81","a82","a83","a84","a85","a86","a87","a88","a89","a90","a91","a92","a93","a94","a95","a96","a97","a98","a99","a100","a101","a102","a103","a104","a105","a106","a107","a108","a109","a110","a111","a112","a113","a114","a115","a116","a117","a118","a119","a120","a121","a122","a123","a124","a125","a126","a127","a128","a129","a130","a131","a132","a133","a134","a135","a136","a137","a138","a139","a140","a141","a142","a143","a144","a145","a146","a147","a148","a149","a150","a151","a152","a153","a154","a155","a156","a157","a158","a159","a160","a161","a162","a163","a164","a165","a166","a167","a168","a169","a170","a171","a172","a173","a174","a175","a176","a177","a178","a179","a180","a181","a182","a183","a184","a185","a186","a187","a188","a189","a190","a191","a192","a193","a194","a195","a196","a197","a198","a199","a200","a201","a202","a203","a204","a205","a206","a207","a208","a209","a210","a211","a212","a213","a214","a215","a216","a217","a218","a219","a220","a221","a222","a223","a224","a225","a226","a227","a228","a229","a230","a231","a232","a233","a234","a235","a236","a237","a238","a239","a240","a241","a242","a243","a244","a245","a246","a247","a248","a249","a250","a251","a252","a253","a254","a255","s4","s5","s6","s7","s8","s9","s10","s11","s12","s13","s14","s15","s16","s17","s18","s19","s20","s21","s22","s23","s24","s25","s26","s27","s28","s29","s30","s31","s32","s33","s34","s35","s36","s37","s38","s39","s40","s41","s42","s43","s44","s45","s46","s47","s48","s49","s50","s51","s52","s53","s54","s55","s56","s57","s58","s59","s60","s61","s62","s63","s64","s65","s66","s67","s68","s69","s70","s71","s72","s73","s74","s75","s76","s77","s78","s79","s80","s81","s82","s83","s84","s85","s86","s87","s88","s89","s90","s91","s92","s93","s94","s95","s96","s97","s98","s99","s100","s101","vcc","scc","memory");
}

extern "C" void kernel_launch(void* const* d_in, const int* in_sizes, int n_in, void* d_out, int out_size,
                              void* d_ws, size_t ws_size, hipStream_t stream) {
  (void)hipFuncSetAttribute((const void*)preln_mqa_gmlp_kernel, hipFuncAttributeMaxDynamicSharedMemorySize, 163840);
  preln_mqa_gmlp_kernel<<<256, 256, 163840, stream>>>(d_in[0], d_in[1], d_in[2], d_in[3], d_in[4], d_in[6], d_in[7],
                                                      d_in[8], d_in[9], d_out, d_ws);
}
```
